```python
import torch

def _softmax(x, axis=-1):
    return torch.softmax(x, dim=axis)
def _gelu(x):
    return torch.nn.functional.gelu(x, approximate="tanh")
def _silu(x):
    return torch.nn.functional.silu(x)
def _sigmoid(x):
    return torch.sigmoid(x)
def _layernorm(x, axis=-1, eps=1e-5):
    m = x.mean(dim=axis, keepdim=True)
    v = x.var(dim=axis, keepdim=True, unbiased=False)
    return (x - m) / torch.sqrt(v + eps)
def _scatter_add(tgt, idx, src, axis=0):
    return tgt.clone().index_add_(axis, idx, src)
def _segment_sum(src, seg, n):
    out = torch.zeros((n,) + src.shape[1:], dtype=src.dtype)
    return out.index_add_(0, seg, src)
def _pad(x, pw):
    flat = tuple(v for pair in reversed(pw) for v in pair)
    return torch.nn.functional.pad(x, flat)

kernel_name = 'parallel_sum__compressed_kv_cla__dense_activation_squared_relu__prompt'

def setup_inputs():

    _w = (lambda a: (lambda t: t.to(torch.bfloat16) if t.is_floating_point() else t)(torch.as_tensor(a)))
    def _A_setup():
        B, H, Sq, Sk, D, M = 1, 16, 2048, 2048, 256, 448
        Dc = 4 * D
        return {
            "x": _w((lambda *s: torch.randn(*s, dtype=torch.bfloat16) * 0.05)(B, Sq, M)),
            "c_kv": _w((lambda *s: torch.randn(*s, dtype=torch.bfloat16) * 1.6)(B, Sk, Dc)),
            "w_q": _w((lambda *s: torch.randn(*s, dtype=torch.bfloat16) * 0.8)(M, H, D) * (M ** -0.5)),
            "w_uk": _w((lambda *s: torch.randn(*s, dtype=torch.bfloat16) * 0.8)(Dc, H, D) * (Dc ** -0.5)),
            "w_uv": _w((lambda *s: torch.randn(*s, dtype=torch.bfloat16) * 1.6)(Dc, H, D) * (Dc ** -0.5)),
            "w_o": _w((lambda *s: torch.randn(*s, dtype=torch.bfloat16) * 1.6)(H, D, M) * ((H * D) ** -0.5)),
        }
    _A_in = _A_setup()

    def _B_setup():
        B, S, D, F = 1, 2048, 448, 1792
        return {
            "x": _w((lambda *s: torch.randn(*s, dtype=torch.bfloat16) * 0.05)(B, S, D)),
            "w1": _w((lambda *s: torch.randn(*s, dtype=torch.bfloat16) * 0.05)(D, F)),
            "b1": _w((lambda *s: torch.randn(*s, dtype=torch.bfloat16) * 0.2)(F)),
            "w2": _w((lambda *s: torch.randn(*s, dtype=torch.bfloat16) * 0.05)(F, D)),
            "b2": _w((lambda *s: torch.randn(*s, dtype=torch.bfloat16) * 0.8)(D)),
        }
    _B_in = _B_setup()

    return {
        "A_x": _A_in["x"],
        "A_c_kv": _A_in["c_kv"],
        "A_w_q": _A_in["w_q"],
        "A_w_uk": _A_in["w_uk"],
        "A_w_uv": _A_in["w_uv"],
        "A_w_o": _A_in["w_o"],
        "B_x": _B_in["x"],
        "B_w1": _B_in["w1"],
        "B_b1": _B_in["b1"],
        "B_w2": _B_in["w2"],
        "B_b2": _B_in["b2"],
    }

def reference(A_x, A_c_kv, A_w_q, A_w_uk, A_w_uv, A_w_o, B_x, B_w1, B_b1, B_w2, B_b2):
    def _A_ref(x, c_kv, w_q, w_uk, w_uv, w_o):
        B, Sq, M = x.shape[0], x.shape[1], x.shape[2]
        Sk = c_kv.shape[1]
        H, D = w_q.shape[1], w_q.shape[2]
        q = torch.einsum("bqm,mhd->bhqd", x, w_q)
        k = torch.einsum("bkc,chd->bhkd", c_kv, w_uk)
        v = torch.einsum("bkc,chd->bhkd", c_kv, w_uv)
        scores = torch.einsum("bhqd,bhkd->bhqk", q, k) * (D ** -0.5)
        off = Sk - Sq
        qi = torch.arange(Sq)[:, None]
        kj = torch.arange(Sk)[None, :]
        cmask = kj <= (qi + off)
        scores = torch.where(cmask[None, None], scores, -1e9)
        p = _softmax(scores, axis=-1)
        ctx = torch.einsum("bhqk,bhkd->bhqd", p, v)
        return torch.einsum("bhqd,hdm->bqm", ctx, w_o)

    def _B_ref(x, w1, b1, w2, b2):
        h = torch.einsum("bsd,df->bsf", x, w1) + b1
        zero = torch.zeros_like(h)
        r = torch.where(h > zero, h, zero)
        h = r * r
        out = torch.einsum("bsf,fd->bsd", h, w2) + b2
        return out

    x = A_x
    xn = _layernorm(x, axis=-1)
    return x + _A_ref(xn, A_c_kv, A_w_q, A_w_uk, A_w_uv, A_w_o) + _B_ref(xn, B_w1, B_b1, B_w2, B_b2)
```

```cpp
#include <hip/hip_runtime.h>
#include <hip/hip_bf16.h>
typedef unsigned short u16;
__device__ __forceinline__ float b2f(u16 b){ return __uint_as_float(((unsigned)b)<<16); }
__device__ __forceinline__ u16 f2b(float f){ unsigned u=__float_as_uint(f); u += 0x7fffu + ((u>>16)&1u); return (u16)(u>>16); }
constexpr int S=2048, M=448, H=16, D=256, DC=1024, F=1792;
__global__ void k_ln(const u16* x, u16* xn){
  int r=blockIdx.x, t=threadIdx.x;
  float v[7]; float s=0; for(int i=0;i<7;i++){ v[i]=b2f(x[r*M+t+64*i]); s+=v[i]; }
  for(int o=32;o;o>>=1) s+=__shfl_xor(s,o);
  float mean=s/M; float q=0; for(int i=0;i<7;i++){ float d=v[i]-mean; q+=d*d; }
  for(int o=32;o;o>>=1) q+=__shfl_xor(q,o);
  float rs=1.0f/sqrtf(q/M+1e-5f);
  for(int i=0;i<7;i++) xn[r*M+t+64*i]=f2b((v[i]-mean)*rs);
}
template<int MODE> __global__ void k_gemm(const u16* A,int lda,const u16* B,int ldb,int K,void* C,int ldc,const u16* bias){
  __shared__ float As[16][65], Bs[16][65];
  int tx=threadIdx.x&15, ty=threadIdx.x>>4; int m0=blockIdx.y*64, n0=blockIdx.x*64;
  float acc[4][4]={};
  for(int k0=0;k0<K;k0+=16){
    for(int i=threadIdx.x;i<1024;i+=256){ int r=i>>4,c=i&15; As[c][r]=b2f(A[(size_t)(m0+r)*lda+k0+c]); int kk=i>>6,nn=i&63; Bs[kk][nn]=b2f(B[(size_t)(k0+kk)*ldb+n0+nn]); }
    __syncthreads();
    for(int k=0;k<16;k++){ for(int i=0;i<4;i++) for(int j=0;j<4;j++) acc[i][j]+=As[k][ty*4+i]*Bs[k][tx*4+j]; }
    __syncthreads();
  }
  for(int i=0;i<4;i++) for(int j=0;j<4;j++){ int m=m0+ty*4+i,n=n0+tx*4+j; float v=acc[i][j];
    if(MODE==0) ((u16*)C)[(size_t)m*ldc+n]=f2b(v);
    if(MODE==1){ v+=b2f(bias[n]); v=v>0?v:0; ((u16*)C)[(size_t)m*ldc+n]=f2b(v*v);}
    if(MODE==2) ((float*)C)[(size_t)m*ldc+n]=v;
  }
}
__global__ void k_attn(const u16* q,const u16* k,const u16* v,u16* ctx){
  __shared__ float p[S]; __shared__ float qs[D]; __shared__ float red[256];
  int r=blockIdx.x,h=blockIdx.y,t=threadIdx.x;
  qs[t]=b2f(q[(size_t)r*4096+h*D+t]); __syncthreads();
  float mx=-1e30f;
  for(int j=t;j<=r;j+=256){ float s=0; const u16* kr=k+(size_t)j*4096+h*D; for(int d=0;d<D;d++) s+=qs[d]*b2f(kr[d]); s*=0.0625f; p[j]=s; mx=fmaxf(mx,s);}
  red[t]=mx; __syncthreads(); for(int o=128;o;o>>=1){ if(t<o) red[t]=fmaxf(red[t],red[t+o]); __syncthreads(); } mx=red[0]; __syncthreads();
  float l=0; for(int j=t;j<=r;j+=256){ float e=__expf(p[j]-mx); p[j]=e; l+=e; }
  red[t]=l; __syncthreads(); for(int o=128;o;o>>=1){ if(t<o) red[t]+=red[t+o]; __syncthreads(); } l=red[0];
  float a=0; for(int j=0;j<=r;j++) a+=p[j]*b2f(v[(size_t)j*4096+h*D+t]);
  ctx[(size_t)r*4096+h*D+t]=f2b(a/l);
}
__global__ void k_fin(const u16* x,const float* a,const float* b,const u16* b2,u16* out){ int i=blockIdx.x*256+threadIdx.x; out[i]=f2b(b2f(x[i])+a[i]+b[i]+b2f(b2[i%M])); }
extern "C" void kernel_launch(void* const* d_in, const int* in_sizes, int n_in, void* d_out, int out_size, void* d_ws, size_t ws_size, hipStream_t stream){
  const u16 *x=(const u16*)d_in[0],*ckv=(const u16*)d_in[1],*wq=(const u16*)d_in[2],*wuk=(const u16*)d_in[3],*wuv=(const u16*)d_in[4],*wo=(const u16*)d_in[5],*w1=(const u16*)d_in[7],*b1=(const u16*)d_in[8],*w2=(const u16*)d_in[9],*b2=(const u16*)d_in[10];
  char* ws=(char*)d_ws; u16* xn=(u16*)ws; u16* q=(u16*)(ws+(4<<20)); u16* k=(u16*)(ws+(24<<20)); u16* v=(u16*)(ws+(44<<20)); u16* ctx=(u16*)(ws+(64<<20)); u16* h1=(u16*)(ws+(84<<20)); float* a=(float*)(ws+(100<<20)); float* b=(float*)(ws+(110<<20));
  k_ln<<<S,64,0,stream>>>(x,xn);
  k_gemm<0><<<dim3(64,32),256,0,stream>>>(xn,M,wq,4096,M,q,4096,nullptr);
  k_gemm<0><<<dim3(64,32),256,0,stream>>>(ckv,DC,wuk,4096,DC,k,4096,nullptr);
  k_gemm<0><<<dim3(64,32),256,0,stream>>>(ckv,DC,wuv,4096,DC,v,4096,nullptr);
  k_gemm<1><<<dim3(28,32),256,0,stream>>>(xn,M,w1,F,M,h1,F,b1);
  k_attn<<<dim3(S,H),256,0,stream>>>(q,k,v,ctx);
  k_gemm<2><<<dim3(7,32),256,0,stream>>>(ctx,4096,wo,M,4096,a,M,nullptr);
  k_gemm<2><<<dim3(7,32),256,0,stream>>>(h1,F,w2,M,F,b,M,nullptr);
  k_fin<<<S*M/256,256,0,stream>>>(x,a,b,b2,(u16*)d_out);
}
```

```cpp
#include <hip/hip_runtime.h>
#include <hip/hip_bf16.h>
#include <utility>
typedef unsigned short u16;
typedef unsigned u32x4 __attribute__((ext_vector_type(4)));
typedef __attribute__((address_space(3))) char lds_char;
typedef __amdgpu_buffer_rsrc_t srd_t;
#define DI __device__ __forceinline__
template <int N> using iseq = std::make_integer_sequence<int, N>;
template <int... Is> using ilist = std::integer_sequence<int, Is...>;
constexpr int SEQ = 2048, DM = 448, NH = 16, HD = 256, DC = 1024, FF = 1792;

DI srd_t mksrd(const void* p, unsigned bytes) { return __builtin_amdgcn_make_buffer_rsrc((void*)p, (short)0, (int)bytes, 0x00020000); }
DI void setm0(unsigned v) { asm volatile("s_mov_b32 m0, %0\n\ts_nop 0" ::"s"(v) : "memory"); }
template <int IMM> DI void dma16(unsigned voff, srd_t srd, unsigned soff) {
  asm volatile("buffer_load_dwordx4 %0, %1, %2 offen offset:%3 lds" ::"v"(voff), "s"(srd), "s"(soff), "i"(IMM) : "memory");
}
DI void dma4(unsigned m0v, const unsigned (&v)[4], srd_t srd, unsigned soff) {
  setm0(m0v); dma16<0>(v[0], srd, soff); dma16<1024>(v[1], srd, soff); dma16<2048>(v[2], srd, soff); dma16<3072>(v[3], srd, soff);
}

#define ACLOB "a0","a1","a2","a3","a4","a5","a6","a7","a8","a9","a10","a11","a12","a13","a14","a15","a16","a17","a18","a19","a20","a21","a22","a23","a24","a25","a26","a27","a28","a29","a30","a31","a32","a33","a34","a35","a36","a37","a38","a39","a40","a41","a42","a43","a44","a45","a46","a47","a48","a49","a50","a51","a52","a53","a54","a55","a56","a57","a58","a59","a60","a61","a62","a63","a64","a65","a66","a67","a68","a69","a70","a71","a72","a73","a74","a75","a76","a77","a78","a79","a80","a81","a82","a83","a84","a85","a86","a87","a88","a89","a90","a91","a92","a93","a94","a95","a96","a97","a98","a99","a100","a101","a102","a103","a104","a105","a106","a107","a108","a109","a110","a111","a112","a113","a114","a115","a116","a117","a118","a119","a120","a121","a122","a123","a124","a125","a126","a127","a128","a129","a130","a131","a132","a133","a134","a135","a136","a137","a138","a139","a140","a141","a142","a143","a144","a145","a146","a147","a148","a149","a150","a151","a152","a153","a154","a155","a156","a157","a158","a159","a160","a161","a162","a163","a164","a165","a166","a167","a168","a169","a170","a171","a172","a173","a174","a175","a176","a177","a178","a179","a180","a181","a182","a183","a184","a185","a186","a187","a188","a189","a190","a191","a192","a193","a194","a195","a196","a197","a198","a199","a200","a201","a202","a203","a204","a205","a206","a207","a208","a209","a210","a211","a212","a213","a214","a215","a216","a217","a218","a219","a220","a221","a222","a223","a224","a225","a226","a227","a228","a229","a230","a231","a232","a233","a234","a235","a236","a237","a238","a239","a240","a241","a242","a243","a244","a245","a246","a247","a248","a249","a250","a251","a252","a253","a254","a255"
#define VSCLOB "v32","v33","v34","v35","v36","v37","v38","v39","v40","v41","v42","v43","v44","v45","v46","v47","v48","v49","v50","v51","v52","v53","v54","v55","v56","v57","v58","v59","v60","v61","v62","v63","v64","v65","v66","v67","v68","v69","v70","v71","v72","v73","v74","v75","v76","v77","v78","v79","v80","v81","v82","v83","v84","v85","v86","v87","v88","v89","v90","v91","v92","v93","v94","v95","v96","v97","v98","v99","v100","v101","v102","v103","v104","v105","v106","v107","v108","v109","v110","v111","v112","v113","v114","v115","v116","v117","v118","v119","v120","v121","v122","v123","v124","v125","v126","v127","v128","v129","v130","v131","v132","v133","v134","v135","v136","v137","v138","v139","v140","v141","v142","v143","v144","v145","v146","v147","v148","v149","v150","v151","v152","v153","v154","v155","v156","v157","v158","v159","v160","v161","v162","v163","v164","v165","v166","v167","v168","v169","v170","v171","v172","v173","v174","v175","v176","v177","v178","v179","v180","v181","v182","v183","v184","v185","v186","v187","v188","v189","v190","v191","v192","v193","v194","v195","v196","v197","v198","v199","v200","v201","v202","v203","v204","v205","v206","v207","v208","v209","v210","v211","v212","v213","v214","v215","v216","v217","v218","v219","v220","v221","v222","v223","v224","v225","v226","v227","v228","v229","v230","v231","v232","v233","v234","v235","v236","v237","v238","v239","v240","v241","v242","v243","v244","v245","v246","v247","v248","v249","v250","v251","v252","v253","v254","v255","s16","s17","s18","s19","s20","s21","s22","s23","s24","s25","s26","s27","s28","s29","s30","s31","s33","s34","s35","s36","s37","s38","s39","s40","s41","s42","s43","s44","s45","s46","s47","s48","s49","s50","s51","s52","s53","s54","s55","s56","s57","s58","s59","s60","s61","s62","s63","s64","s65","s66","s67","s68","s69","s70","s71","s72","s73","s74","s75","s76","s77","s78","s79","s80","s81","s82","s83","s84","s85","s86","s87","s88","s89","s90","s91","s92","s93","s94","s95"


constexpr unsigned WS_KF = 0;
constexpr unsigned WS_VF = WS_KF + NH * SEQ * HD * 2;
constexpr unsigned WS_XN = WS_VF + NH * SEQ * HD * 2;
constexpr unsigned WS_AF = WS_XN + SEQ * DM * 2;
constexpr int AKS = (NH * HD + FF) / 32;
constexpr unsigned PO_WAVE = 33 * 1024;
constexpr unsigned WS_PO = WS_AF + SEQ * AKS * 64;
constexpr unsigned WS_WQ = WS_PO + 128 * 4 * PO_WAVE;
constexpr unsigned WS_W1 = WS_WQ + DM * NH * HD * 2;
constexpr unsigned WS_WO = WS_W1 + DM * FF * 2;
constexpr unsigned WS_P3 = WS_WO + AKS * 28 * 1024 + 32768;
constexpr unsigned WS_FL = WS_P3 + 64 * 16 * 14336 + 8192;
constexpr unsigned FL_PREP = WS_FL + 1024, FL_KV = WS_FL + 2048, FL_PO = WS_FL + 4096, FL_CTX = WS_FL + 16384, FL_PREP2 = WS_FL + 20480;
constexpr unsigned OOB = 0x80000000u, NREC = 0x40000000u;

#define MK_ASM \
  "MK_BEG_%=:\n\t" \
  "s_getpc_b64 s[84:85]\n\t" \
  "v_readlane_b32 s56, %13, 0\n\t" \
  "v_readlane_b32 s57, %13, 1\n\t" \
  "v_readlane_b32 s60, %13, 2\n\t" \
  "v_readlane_b32 s61, %13, 3\n\t" \
  "v_readlane_b32 s29, %13, 22\n\t" \
  "v_readlane_b32 s28, %13, 23\n\t" \
  "v_readlane_b32 s31, %13, 25\n\t" \
  "v_readlane_b32 s89, %13, 27\n\t" \
  "v_readlane_b32 s44, %13, 24\n\t" \
  "v_readlane_b32 s71, %13, 26\n\t" \
  "s_mov_b32 s58, 4194304\n\t" \
  "s_mov_b32 s59, 0x20000\n\t" \
  "s_mov_b32 s62, 8388608\n\t" \
  "s_mov_b32 s63, 0x20000\n\t" \
  "s_mov_b32 s67, 0x20000\n\t" \
  "s_mov_b32 s68, 102080512\n\t" \
  "v_xor_b32_e32 v104, 0, %3\n\t" \
  "v_add_u32_e32 v104, 0, v104\n\t" \
  "v_xor_b32_e32 v108, 0, %4\n\t" \
  "v_add_u32_e32 v108, 0, v108\n\t" \
  "v_xor_b32_e32 v105, 64, %3\n\t" \
  "v_add_u32_e32 v105, 15360, v105\n\t" \
  "v_xor_b32_e32 v109, 64, %4\n\t" \
  "v_add_u32_e32 v109, 15360, v109\n\t" \
  "v_xor_b32_e32 v106, 0, %3\n\t" \
  "v_add_u32_e32 v106, 30720, v106\n\t" \
  "v_xor_b32_e32 v110, 0, %4\n\t" \
  "v_add_u32_e32 v110, 30720, v110\n\t" \
  "v_xor_b32_e32 v107, 64, %3\n\t" \
  "v_add_u32_e32 v107, 46080, v107\n\t" \
  "v_xor_b32_e32 v111, 64, %4\n\t" \
  "v_add_u32_e32 v111, 46080, v111\n\t" \
  "v_mov_b32_e32 v112, %5\n\t" \
  "v_xor_b32_e32 v113, 64, %5\n\t" \
  "v_xor_b32_e32 v114, 0, %6\n\t" \
  "v_xor_b32_e32 v115, 32, %6\n\t" \
  "v_xor_b32_e32 v116, 64, %6\n\t" \
  "v_xor_b32_e32 v117, 96, %6\n\t" \
  "v_xor_b32_e32 v118, 128, %6\n\t" \
  "v_xor_b32_e32 v119, 160, %6\n\t" \
  "v_xor_b32_e32 v120, 192, %6\n\t" \
  "v_xor_b32_e32 v121, 224, %6\n\t" \
  "s_and_b32 s85, s85, 0xffff\n\t" \
  "s_mov_b32 s86, MK_END_%=-MK_BEG_%=\n\t" \
  "s_mov_b32 s87, 0x20000\n\t" \
  "s_mov_b32 s45, 0x4b800000\n\t" \
  "s_mov_b32 s16, 0\n\t" \
  "s_mov_b32 s17, 4096\n\t" \
  "s_mov_b32 s18, 8192\n\t" \
  "s_mov_b32 s19, 12288\n\t" \
  "s_mov_b32 s20, 16384\n\t" \
  "s_mov_b32 s21, 20480\n\t" \
  "s_mov_b32 s22, 24576\n\t" \
  "s_mov_b32 s23, 28672\n\t" \
  "s_mov_b32 s24, 32768\n\t" \
  "v_bfe_u32 v34, %1, 4, 4\n\t" \
  "v_lshrrev_b32_e32 v35, 8, %1\n\t" \
  "v_lshlrev_b32_e32 v35, 4, v35\n\t" \
  "s_mov_b32 s26, s31\n\t" \
  "s_mov_b32 s30, 262144\n\t" \
  "s_mov_b32 s33, 4294705280\n\t" \
  "s_mov_b32 s34, 262144\n\t" \
  "s_mov_b32 s35, 0x10000\n\t" \
  "s_lshr_b32 s91, s31, 11\n\t" \
  "s_sub_u32 s91, 7, s91\n\t" \
  "s_mov_b32 s37, 0\n\t" \
  "v_readlane_b32 s92, %13, 14\n\t" \
  "v_readlane_b32 s93, %13, 15\n\t" \
  "v_readlane_b32 s94, %13, 16\n\t" \
  "v_readlane_b32 s95, %13, 17\n\t" \
  "v_mov_b32_e32 v124, 0\n\t" \
  "v_mov_b32_e32 v125, 0\n\t" \
  "v_mov_b32_e32 v126, 0\n\t" \
  "v_mov_b32_e32 v127, 0\n\t" \
  "s_add_u32 s41, s28, 786432\n\t" \
  "s_add_u32 s40, s29, 256\n\t" \
  "s_add_u32 s27, s31, 147456\n\t" \
  "s_add_u32 s29, s29, 262400\n\t" \
  "s_add_u32 s28, s28, 1048576\n\t" \
  "s_mov_b32 s66, 0x40000000\n\t" \
  "s_waitcnt vmcnt(20)\n\t" \
  "s_barrier\n\t" \
  "s_cmp_lg_u32 s44, 0\n\t" \
  "s_cbranch_scc1 MK_KVV_%=\n\t" \
  "ds_read_b128 v[156:159], v112 offset:14336\n\t" \
  "ds_read_b64_tr_b16 v[160:161], v114 offset:0\n\t" \
  "ds_read_b64_tr_b16 v[162:163], v114 offset:2048\n\t" \
  "ds_read_b64_tr_b16 v[164:165], v115 offset:0\n\t" \
  "ds_read_b64_tr_b16 v[166:167], v115 offset:2048\n\t" \
  "ds_read_b64_tr_b16 v[168:169], v116 offset:0\n\t" \
  "ds_read_b64_tr_b16 v[170:171], v116 offset:2048\n\t" \
  "ds_read_b64_tr_b16 v[172:173], v117 offset:0\n\t" \
  "ds_read_b64_tr_b16 v[174:175], v117 offset:2048\n\t" \
  "ds_read_b64_tr_b16 v[176:177], v118 offset:0\n\t" \
  "ds_read_b64_tr_b16 v[178:179], v118 offset:2048\n\t" \
  "ds_read_b64_tr_b16 v[180:181], v119 offset:0\n\t" \
  "ds_read_b64_tr_b16 v[182:183], v119 offset:2048\n\t" \
  "ds_read_b64_tr_b16 v[184:185], v120 offset:0\n\t" \
  "ds_read_b64_tr_b16 v[186:187], v120 offset:2048\n\t" \
  "ds_read_b64_tr_b16 v[188:189], v121 offset:0\n\t" \
  "ds_read_b64_tr_b16 v[190:191], v121 offset:2048\n\t" \
  "ds_read_b128 v[152:155], v112 offset:12288\n\t" \
  "ds_read_b128 v[148:151], v112 offset:10240\n\t" \
  "ds_read_b128 v[144:147], v112 offset:8192\n\t" \
  "ds_read_b128 v[140:143], v112 offset:6144\n\t" \
  "ds_read_b128 v[136:139], v112 offset:4096\n\t" \
  "ds_read_b128 v[132:135], v112 offset:2048\n\t" \
  "ds_read_b128 v[128:131], v112 offset:0\n\t" \
  "s_waitcnt lgkmcnt(0)\n\t" \
  "s_waitcnt vmcnt(12)\n\t" \
  "s_barrier\n\t" \
  "v_mfma_f32_16x16x32_bf16 a[224:227], v[160:163], v[156:159], 0\n\t" \
  "ds_read_b128 v[220:223], v113 offset:14336\n\t" \
  "v_mov_b32_e32 v68, 0x3b124925\n\t" \
  "v_lshlrev_b32_e32 v72, 16, v40\n\t" \
  "v_mfma_f32_16x16x32_bf16 a[228:231], v[164:167], v[156:159], 0\n\t" \
  "s_add_u32 m0, s31, 49152\n\t" \
  "v_and_b32_e32 v73, 0xffff0000, v40\n\t" \
  "v_mfma_f32_16x16x32_bf16 a[232:235], v[168:171], v[156:159], 0\n\t" \
  "ds_read_b64_tr_b16 v[224:225], v114 offset:16384\n\t" \
  "v_lshlrev_b32_e32 v74, 16, v41\n\t" \
  "v_and_b32_e32 v75, 0xffff0000, v41\n\t" \
  "v_mfma_f32_16x16x32_bf16 a[236:239], v[172:175], v[156:159], 0\n\t" \
  "buffer_load_dwordx4 v108, s[60:63], s41 offen offset:0 sc1 lds\n\t" \
  "v_lshlrev_b32_e32 v76, 16, v42\n\t" \
  "v_mfma_f32_16x16x32_bf16 a[240:243], v[176:179], v[156:159], 0\n\t" \
  "ds_read_b64_tr_b16 v[226:227], v114 offset:18432\n\t" \
  "v_and_b32_e32 v77, 0xffff0000, v42\n\t" \
  "v_mfma_f32_16x16x32_bf16 a[244:247], v[180:183], v[156:159], 0\n\t" \
  "buffer_load_dwordx4 v109, s[60:63], s41 offen offset:1024 sc1 lds\n\t" \
  "v_lshlrev_b32_e32 v78, 16, v43\n\t" \
  "v_and_b32_e32 v79, 0xffff0000, v43\n\t" \
  "v_mfma_f32_16x16x32_bf16 a[248:251], v[184:187], v[156:159], 0\n\t" \
  "ds_read_b64_tr_b16 v[228:229], v115 offset:16384\n\t" \
  "v_lshlrev_b32_e32 v80, 16, v44\n\t" \
  "v_mfma_f32_16x16x32_bf16 a[252:255], v[188:191], v[156:159], 0\n\t" \
  "buffer_load_dwordx4 v110, s[60:63], s41 offen offset:2048 sc1 lds\n\t" \
  "v_and_b32_e32 v81, 0xffff0000, v44\n\t" \
  "v_lshlrev_b32_e32 v82, 16, v45\n\t" \
  "v_mfma_f32_16x16x32_bf16 a[192:195], v[160:163], v[152:155], 0\n\t" \
  "ds_read_b64_tr_b16 v[230:231], v115 offset:18432\n\t" \
  "v_and_b32_e32 v83, 0xffff0000, v45\n\t" \
  "v_mfma_f32_16x16x32_bf16 a[196:199], v[164:167], v[152:155], 0\n\t" \
  "buffer_load_dwordx4 v111, s[60:63], s41 offen offset:3072 sc1 lds\n\t" \
  "v_lshlrev_b32_e32 v84, 16, v46\n\t" \
  "v_mfma_f32_16x16x32_bf16 a[200:203], v[168:171], v[152:155], 0\n\t" \
  "ds_read_b64_tr_b16 v[232:233], v116 offset:16384\n\t" \
  "v_and_b32_e32 v85, 0xffff0000, v46\n\t" \
  "v_lshlrev_b32_e32 v86, 16, v47\n\t" \
  "v_mfma_f32_16x16x32_bf16 a[204:207], v[172:175], v[152:155], 0\n\t" \
  "v_and_b32_e32 v87, 0xffff0000, v47\n\t" \
  "v_lshlrev_b32_e32 v88, 16, v48\n\t" \
  "v_mfma_f32_16x16x32_bf16 a[208:211], v[176:179], v[152:155], 0\n\t" \
  "ds_read_b64_tr_b16 v[234:235], v116 offset:18432\n\t" \
  "s_add_u32 m0, s31, 131072\n\t" \
  "v_and_b32_e32 v89, 0xffff0000, v48\n\t" \
  "v_mfma_f32_16x16x32_bf16 a[212:215], v[180:183], v[152:155], 0\n\t" \
  "v_lshlrev_b32_e32 v90, 16, v49\n\t" \
  "v_and_b32_e32 v91, 0xffff0000, v49\n\t" \
  "v_mfma_f32_16x16x32_bf16 a[216:219], v[184:187], v[152:155], 0\n\t" \
  "ds_read_b64_tr_b16 v[236:237], v117 offset:16384\n\t" \
  "buffer_load_dwordx4 v104, s[56:59], s40 offen offset:0 sc1 lds\n\t" \
  "v_mfma_f32_16x16x32_bf16 a[220:223], v[188:191], v[152:155], 0\n\t" \
  "v_lshlrev_b32_e32 v92, 16, v50\n\t" \
  "v_and_b32_e32 v93, 0xffff0000, v50\n\t" \
  "v_lshlrev_b32_e32 v94, 16, v51\n\t" \
  "v_mfma_f32_16x16x32_bf16 a[160:163], v[160:163], v[148:151], 0\n\t" \
  "ds_read_b64_tr_b16 v[238:239], v117 offset:18432\n\t" \
  "buffer_load_dwordx4 v105, s[56:59], s40 offen offset:1024 sc1 lds\n\t" \
  "v_mfma_f32_16x16x32_bf16 a[164:167], v[164:167], v[148:151], 0\n\t" \
  "v_and_b32_e32 v95, 0xffff0000, v51\n\t" \
  "v_lshlrev_b32_e32 v96, 16, v52\n\t" \
  "v_and_b32_e32 v97, 0xffff0000, v52\n\t" \
  "v_mfma_f32_16x16x32_bf16 a[168:171], v[168:171], v[148:151], 0\n\t" \
  "ds_read_b64_tr_b16 v[240:241], v118 offset:16384\n\t" \
  "buffer_load_dwordx4 v106, s[56:59], s40 offen offset:2048 sc1 lds\n\t" \
  "v_mfma_f32_16x16x32_bf16 a[172:175], v[172:175], v[148:151], 0\n\t" \
  "v_lshlrev_b32_e32 v98, 16, v53\n\t" \
  "v_and_b32_e32 v99, 0xffff0000, v53\n\t" \
  "v_mfma_f32_16x16x32_bf16 a[176:179], v[176:179], v[148:151], 0\n\t" \
  "ds_read_b64_tr_b16 v[242:243], v118 offset:18432\n\t" \
  "buffer_load_dwordx4 v107, s[56:59], s40 offen offset:3072 sc1 lds\n\t" \
  "v_lshlrev_b32_e32 v100, 16, v54\n\t" \
  "v_mfma_f32_16x16x32_bf16 a[180:183], v[180:183], v[148:151], 0\n\t" \
  "v_and_b32_e32 v101, 0xffff0000, v54\n\t" \
  "v_lshlrev_b32_e32 v102, 16, v55\n\t" \
  "v_mfma_f32_16x16x32_bf16 a[184:187], v[184:187], v[148:151], 0\n\t" \
  "ds_read_b64_tr_b16 v[244:245], v119 offset:16384\n\t" \
  "buffer_load_dword v33, off, %11, s68 sc1\n\t" \
  "v_and_b32_e32 v103, 0xffff0000, v55\n\t" \
  "v_mfma_f32_16x16x32_bf16 a[188:191], v[188:191], v[148:151], 0\n\t" \
  "global_load_dword v27, %14, off offset:0\n\t" \
  "v_add_f32_e32 v64, v72, v73\n\t" \
  "v_mfma_f32_16x16x32_bf16 a[128:131], v[160:163], v[144:147], 0\n\t" \
  "ds_read_b64_tr_b16 v[246:247], v119 offset:18432\n\t" \
  "global_load_dword v28, %14, off offset:256\n\t" \
  "v_mfma_f32_16x16x32_bf16 a[132:135], v[164:167], v[144:147], 0\n\t" \
  "global_load_dword v29, %14, off offset:512\n\t" \
  "v_mul_f32_e32 v65, v72, v72\n\t" \
  "v_fmac_f32_e32 v65, v73, v73\n\t" \
  "v_mfma_f32_16x16x32_bf16 a[136:139], v[168:171], v[144:147], 0\n\t" \
  "ds_read_b64_tr_b16 v[248:249], v120 offset:16384\n\t" \
  "global_load_dword v30, %14, off offset:768\n\t" \
  "v_mfma_f32_16x16x32_bf16 a[140:143], v[172:175], v[144:147], 0\n\t" \
  "buffer_load_dwordx4 v[36:39], %1, s[84:87], s89 offen\n\t" \
  "v_add_f32_e32 v64, v64, v74\n\t" \
  "v_fmac_f32_e32 v65, v74, v74\n\t" \
  "v_mfma_f32_16x16x32_bf16 a[144:147], v[176:179], v[144:147], 0\n\t" \
  "ds_read_b64_tr_b16 v[250:251], v120 offset:18432\n\t" \
  "v_add_f32_e32 v64, v64, v75\n\t" \
  "v_mfma_f32_16x16x32_bf16 a[148:151], v[180:183], v[144:147], 0\n\t" \
  "s_mov_b32 m0, s26\n\t" \
  "v_fmac_f32_e32 v65, v75, v75\n\t" \
  "v_mfma_f32_16x16x32_bf16 a[152:155], v[184:187], v[144:147], 0\n\t" \
  "ds_read_b64_tr_b16 v[252:253], v121 offset:16384\n\t" \
  "v_add_f32_e32 v64, v64, v76\n\t" \
  "v_fmac_f32_e32 v65, v76, v76\n\t" \
  "v_mfma_f32_16x16x32_bf16 a[156:159], v[188:191], v[144:147], 0\n\t" \
  "buffer_load_dwordx4 v108, s[60:63], s28 offen offset:0 sc1 lds\n\t" \
  "v_add_f32_e32 v64, v64, v77\n\t" \
  "v_mfma_f32_16x16x32_bf16 a[96:99], v[160:163], v[140:143], 0\n\t" \
  "ds_read_b64_tr_b16 v[254:255], v121 offset:18432\n\t" \
  "v_fmac_f32_e32 v65, v77, v77\n\t" \
  "v_add_f32_e32 v64, v64, v78\n\t" \
  "v_mfma_f32_16x16x32_bf16 a[100:103], v[164:167], v[140:143], 0\n\t" \
  "v_fmac_f32_e32 v65, v78, v78\n\t" \
  "v_add_f32_e32 v64, v64, v79\n\t" \
  "v_mfma_f32_16x16x32_bf16 a[104:107], v[168:171], v[140:143], 0\n\t" \
  "ds_read_b128 v[216:219], v113 offset:12288\n\t" \
  "v_fmac_f32_e32 v65, v79, v79\n\t" \
  "v_mfma_f32_16x16x32_bf16 a[108:111], v[172:175], v[140:143], 0\n\t" \
  "buffer_load_dwordx4 v109, s[60:63], s28 offen offset:1024 sc1 lds\n\t" \
  "v_add_f32_e32 v64, v64, v80\n\t" \
  "v_fmac_f32_e32 v65, v80, v80\n\t" \
  "v_mfma_f32_16x16x32_bf16 a[112:115], v[176:179], v[140:143], 0\n\t" \
  "ds_read_b128 v[212:215], v113 offset:10240\n\t" \
  "v_add_f32_e32 v64, v64, v81\n\t" \
  "v_mfma_f32_16x16x32_bf16 a[116:119], v[180:183], v[140:143], 0\n\t" \
  "v_fmac_f32_e32 v65, v81, v81\n\t" \
  "v_add_f32_e32 v64, v64, v82\n\t" \
  "v_fmac_f32_e32 v65, v82, v82\n\t" \
  "v_mfma_f32_16x16x32_bf16 a[120:123], v[184:187], v[140:143], 0\n\t" \
  "ds_read_b128 v[208:211], v113 offset:8192\n\t" \
  "v_add_f32_e32 v64, v64, v83\n\t" \
  "v_mfma_f32_16x16x32_bf16 a[124:127], v[188:191], v[140:143], 0\n\t" \
  "buffer_load_dwordx4 v110, s[60:63], s28 offen offset:2048 sc1 lds\n\t" \
  "v_fmac_f32_e32 v65, v83, v83\n\t" \
  "v_mfma_f32_16x16x32_bf16 a[64:67], v[160:163], v[136:139], 0\n\t" \
  "ds_read_b128 v[204:207], v113 offset:6144\n\t" \
  "v_add_f32_e32 v64, v64, v84\n\t" \
  "v_fmac_f32_e32 v65, v84, v84\n\t" \
  "v_mfma_f32_16x16x32_bf16 a[68:71], v[164:167], v[136:139], 0\n\t" \
  "v_add_f32_e32 v64, v64, v85\n\t" \
  "v_fmac_f32_e32 v65, v85, v85\n\t" \
  "v_mfma_f32_16x16x32_bf16 a[72:75], v[168:171], v[136:139], 0\n\t" \
  "ds_read_b128 v[200:203], v113 offset:4096\n\t" \
  "v_add_f32_e32 v64, v64, v86\n\t" \
  "v_fmac_f32_e32 v65, v86, v86\n\t" \
  "v_mfma_f32_16x16x32_bf16 a[76:79], v[172:175], v[136:139], 0\n\t" \
  "buffer_load_dwordx4 v111, s[60:63], s28 offen offset:3072 sc1 lds\n\t" \
  "v_add_f32_e32 v64, v64, v87\n\t" \
  "v_mfma_f32_16x16x32_bf16 a[80:83], v[176:179], v[136:139], 0\n\t" \
  "ds_read_b128 v[196:199], v113 offset:2048\n\t" \
  "v_fmac_f32_e32 v65, v87, v87\n\t" \
  "v_mfma_f32_16x16x32_bf16 a[84:87], v[180:183], v[136:139], 0\n\t" \
  "s_mov_b32 m0, s27\n\t" \
  "v_add_f32_e32 v64, v64, v88\n\t" \
  "v_fmac_f32_e32 v65, v88, v88\n\t" \
  "v_mfma_f32_16x16x32_bf16 a[88:91], v[184:187], v[136:139], 0\n\t" \
  "ds_read_b128 v[192:195], v113 offset:0\n\t" \
  "v_add_f32_e32 v64, v64, v89\n\t" \
  "v_mfma_f32_16x16x32_bf16 a[92:95], v[188:191], v[136:139], 0\n\t" \
  "buffer_load_dwordx4 v104, s[56:59], s29 offen offset:0 sc1 lds\n\t" \
  "v_fmac_f32_e32 v65, v89, v89\n\t" \
  "v_add_f32_e32 v64, v64, v90\n\t" \
  "v_mfma_f32_16x16x32_bf16 a[32:35], v[160:163], v[132:135], 0\n\t" \
  "s_add_u32 s26, s26, 0x4000\n\t" \
  "s_add_u32 s28, s28, s30\n\t" \
  "v_mfma_f32_16x16x32_bf16 a[36:39], v[164:167], v[132:135], 0\n\t" \
  "s_add_u32 s39, s35, s31\n\t" \
  "s_add_u32 s43, s35, 0x8000\n\t" \
  "v_mfma_f32_16x16x32_bf16 a[40:43], v[168:171], v[132:135], 0\n\t" \
  "s_cmp_lg_u32 s35, 0x20000\n\t" \
  "s_cselect_b32 s43, s43, 0x10000\n\t" \
  "v_fmac_f32_e32 v65, v90, v90\n\t" \
  "v_mfma_f32_16x16x32_bf16 a[44:47], v[172:175], v[132:135], 0\n\t" \
  "buffer_load_dwordx4 v105, s[56:59], s29 offen offset:1024 sc1 lds\n\t" \
  "v_add_f32_e32 v64, v64, v91\n\t" \
  "v_mfma_f32_16x16x32_bf16 a[48:51], v[176:179], v[132:135], 0\n\t" \
  "s_sub_u32 s38, s43, s35\n\t" \
  "s_mov_b32 s35, s43\n\t" \
  "v_fmac_f32_e32 v65, v91, v91\n\t" \
  "v_mfma_f32_16x16x32_bf16 a[52:55], v[180:183], v[132:135], 0\n\t" \
  "v_add_f32_e32 v64, v64, v92\n\t" \
  "v_fmac_f32_e32 v65, v92, v92\n\t" \
  "v_mfma_f32_16x16x32_bf16 a[56:59], v[184:187], v[132:135], 0\n\t" \
  "v_add_u32_e32 v112, s38, v112\n\t" \
  "v_add_u32_e32 v113, s38, v113\n\t" \
  "v_mfma_f32_16x16x32_bf16 a[60:63], v[188:191], v[132:135], 0\n\t" \
  "buffer_load_dwordx4 v106, s[56:59], s29 offen offset:2048 sc1 lds\n\t" \
  "v_xor_b32_e32 v114, 0x8000, v114\n\t" \
  "v_xor_b32_e32 v115, 0x8000, v115\n\t" \
  "v_mfma_f32_16x16x32_bf16 a[0:3], v[160:163], v[128:131], 0\n\t" \
  "v_xor_b32_e32 v116, 0x8000, v116\n\t" \
  "v_xor_b32_e32 v117, 0x8000, v117\n\t" \
  "v_add_f32_e32 v64, v64, v93\n\t" \
  "v_mfma_f32_16x16x32_bf16 a[4:7], v[164:167], v[128:131], 0\n\t" \
  "v_xor_b32_e32 v118, 0x8000, v118\n\t" \
  "v_xor_b32_e32 v119, 0x8000, v119\n\t" \
  "v_mfma_f32_16x16x32_bf16 a[8:11], v[168:171], v[128:131], 0\n\t" \
  "v_xor_b32_e32 v120, 0x8000, v120\n\t" \
  "v_xor_b32_e32 v121, 0x8000, v121\n\t" \
  "v_fmac_f32_e32 v65, v93, v93\n\t" \
  "v_mfma_f32_16x16x32_bf16 a[12:15], v[172:175], v[128:131], 0\n\t" \
  "buffer_load_dwordx4 v107, s[56:59], s29 offen offset:3072 sc1 lds\n\t" \
  "v_add_f32_e32 v64, v64, v94\n\t" \
  "v_mfma_f32_16x16x32_bf16 a[16:19], v[176:179], v[128:131], 0\n\t" \
  "s_mov_b32 s27, s39\n\t" \
  "s_add_u32 s29, s29, s33\n\t" \
  "v_mfma_f32_16x16x32_bf16 a[20:23], v[180:183], v[128:131], 0\n\t" \
  "v_fmac_f32_e32 v65, v94, v94\n\t" \
  "v_add_f32_e32 v64, v64, v95\n\t" \
  "v_fmac_f32_e32 v65, v95, v95\n\t" \
  "v_mfma_f32_16x16x32_bf16 a[24:27], v[184:187], v[128:131], 0\n\t" \
  "v_add_f32_e32 v64, v64, v96\n\t" \
  "v_fmac_f32_e32 v65, v96, v96\n\t" \
  "v_mfma_f32_16x16x32_bf16 a[28:31], v[188:191], v[128:131], 0\n\t" \
  "v_add_f32_e32 v64, v64, v97\n\t" \
  "v_fmac_f32_e32 v65, v97, v97\n\t" \
  "s_waitcnt lgkmcnt(0)\n\t" \
  "s_waitcnt vmcnt(22)\n\t" \
  "s_barrier\n\t" \
  "v_mfma_f32_16x16x32_bf16 a[224:227], v[224:227], v[220:223], a[224:227]\n\t" \
  "ds_read_b128 v[156:159], v112 offset:14336\n\t" \
  "v_add_f32_e32 v64, v64, v98\n\t" \
  "v_fmac_f32_e32 v65, v98, v98\n\t" \
  "v_mfma_f32_16x16x32_bf16 a[228:231], v[228:231], v[220:223], a[228:231]\n\t" \
  "s_mov_b32 m0, s26\n\t" \
  "v_add_f32_e32 v64, v64, v99\n\t" \
  "v_mfma_f32_16x16x32_bf16 a[232:235], v[232:235], v[220:223], a[232:235]\n\t" \
  "ds_read_b64_tr_b16 v[160:161], v114 offset:0\n\t" \
  "v_fmac_f32_e32 v65, v99, v99\n\t" \
  "v_add_f32_e32 v64, v64, v100\n\t" \
  "v_mfma_f32_16x16x32_bf16 a[236:239], v[236:239], v[220:223], a[236:239]\n\t" \
  "buffer_load_dwordx4 v108, s[60:63], s28 offen offset:0 sc1 lds\n\t" \
  "v_fmac_f32_e32 v65, v100, v100\n\t" \
  "v_mfma_f32_16x16x32_bf16 a[240:243], v[240:243], v[220:223], a[240:243]\n\t" \
  "ds_read_b64_tr_b16 v[162:163], v114 offset:2048\n\t" \
  "v_add_f32_e32 v64, v64, v101\n\t" \
  "v_mfma_f32_16x16x32_bf16 a[244:247], v[244:247], v[220:223], a[244:247]\n\t" \
  "v_fmac_f32_e32 v65, v101, v101\n\t" \
  "v_add_f32_e32 v64, v64, v102\n\t" \
  "v_fmac_f32_e32 v65, v102, v102\n\t" \
  "v_mfma_f32_16x16x32_bf16 a[248:251], v[248:251], v[220:223], a[248:251]\n\t" \
  "ds_read_b64_tr_b16 v[164:165], v115 offset:0\n\t" \
  "v_add_f32_e32 v64, v64, v103\n\t" \
  "v_mfma_f32_16x16x32_bf16 a[252:255], v[252:255], v[220:223], a[252:255]\n\t" \
  "v_fmac_f32_e32 v65, v103, v103\n\t" \
  "s_nop 1\n\t" \
  "v_add_f32_dpp v64, v64, v64 row_ror:4 row_mask:0xf bank_mask:0xf\n\t" \
  "v_mfma_f32_16x16x32_bf16 a[192:195], v[224:227], v[216:219], a[192:195]\n\t" \
  "ds_read_b64_tr_b16 v[166:167], v115 offset:2048\n\t" \
  "v_add_f32_dpp v65, v65, v65 row_ror:4 row_mask:0xf bank_mask:0xf\n\t" \
  "v_mfma_f32_16x16x32_bf16 a[196:199], v[228:231], v[216:219], a[196:199]\n\t" \
  "s_nop 1\n\t" \
  "v_add_f32_dpp v64, v64, v64 row_ror:8 row_mask:0xf bank_mask:0xf\n\t" \
  "v_mfma_f32_16x16x32_bf16 a[200:203], v[232:235], v[216:219], a[200:203]\n\t" \
  "ds_read_b64_tr_b16 v[168:169], v116 offset:0\n\t" \
  "v_add_f32_dpp v65, v65, v65 row_ror:8 row_mask:0xf bank_mask:0xf\n\t" \
  "s_nop 1\n\t" \
  "v_mfma_f32_16x16x32_bf16 a[204:207], v[236:239], v[216:219], a[204:207]\n\t" \
  "buffer_load_dwordx4 v109, s[60:63], s28 offen offset:1024 sc1 lds\n\t" \
  "v_mov_b32_e32 v62, v64\n\t" \
  "v_mfma_f32_16x16x32_bf16 a[208:211], v[240:243], v[216:219], a[208:211]\n\t" \
  "ds_read_b64_tr_b16 v[170:171], v116 offset:2048\n\t" \
  "v_mov_b32_e32 v63, v65\n\t" \
  "s_nop 1\n\t" \
  "v_mfma_f32_16x16x32_bf16 a[212:215], v[244:247], v[216:219], a[212:215]\n\t" \
  "v_permlane16_swap_b32_e32 v64, v62\n\t" \
  "v_permlane16_swap_b32_e32 v65, v63\n\t" \
  "v_mfma_f32_16x16x32_bf16 a[216:219], v[248:251], v[216:219], a[216:219]\n\t" \
  "ds_read_b64_tr_b16 v[172:173], v117 offset:0\n\t" \
  "v_add_f32_e32 v64, v64, v62\n\t" \
  "v_mfma_f32_16x16x32_bf16 a[220:223], v[252:255], v[216:219], a[220:223]\n\t" \
  "v_add_f32_e32 v65, v65, v63\n\t" \
  "v_mov_b32_e32 v62, v64\n\t" \
  "v_mov_b32_e32 v63, v65\n\t" \
  "v_mfma_f32_16x16x32_bf16 a[160:163], v[224:227], v[212:215], a[160:163]\n\t" \
  "ds_read_b64_tr_b16 v[174:175], v117 offset:2048\n\t" \
  "s_nop 1\n\t" \
  "v_mfma_f32_16x16x32_bf16 a[164:167], v[228:231], v[212:215], a[164:167]\n\t" \
  "v_permlane32_swap_b32_e32 v64, v62\n\t" \
  "v_permlane32_swap_b32_e32 v65, v63\n\t" \
  "v_add_f32_e32 v64, v64, v62\n\t" \
  "v_mfma_f32_16x16x32_bf16 a[168:171], v[232:235], v[212:215], a[168:171]\n\t" \
  "ds_read_b64_tr_b16 v[176:177], v118 offset:0\n\t" \
  "v_add_f32_e32 v65, v65, v63\n\t" \
  "v_mfma_f32_16x16x32_bf16 a[172:175], v[236:239], v[212:215], a[172:175]\n\t" \
  "buffer_load_dwordx4 v110, s[60:63], s28 offen offset:2048 sc1 lds\n\t" \
  "v_mul_f32_e32 v64, v68, v64\n\t" \
  "v_mfma_f32_16x16x32_bf16 a[176:179], v[240:243], v[212:215], a[176:179]\n\t" \
  "ds_read_b64_tr_b16 v[178:179], v118 offset:2048\n\t" \
  "v_mul_f32_e32 v65, v68, v65\n\t" \
  "v_fma_f32 v65, -v64, v64, v65\n\t" \
  "v_mfma_f32_16x16x32_bf16 a[180:183], v[244:247], v[212:215], a[180:183]\n\t" \
  "v_max_f32_e32 v65, 0, v65\n\t" \
  "v_add_f32_e32 v65, 0x3727c5ac, v65\n\t" \
  "v_mfma_f32_16x16x32_bf16 a[184:187], v[248:251], v[212:215], a[184:187]\n\t" \
  "ds_read_b64_tr_b16 v[180:181], v119 offset:0\n\t" \
  "v_rsq_f32_e32 v65, v65\n\t" \
  "s_nop 1\n\t" \
  "v_mfma_f32_16x16x32_bf16 a[188:191], v[252:255], v[212:215], a[188:191]\n\t" \
  "v_mul_f32_e32 v65, 0x3db8aa3b, v65\n\t" \
  "v_mul_f32_e64 v64, -v64, v65\n\t" \
  "v_mfma_f32_16x16x32_bf16 a[128:131], v[224:227], v[208:211], a[128:131]\n\t" \
  "ds_read_b64_tr_b16 v[182:183], v119 offset:2048\n\t" \
  "v_fma_f32 v72, v72, v65, v64\n\t" \
  "v_mfma_f32_16x16x32_bf16 a[132:135], v[228:231], v[208:211], a[132:135]\n\t" \
  "v_fma_f32 v73, v73, v65, v64\n\t" \
  "v_cvt_pk_bf16_f32 v40, v72, v73\n\t" \
  "v_fma_f32 v74, v74, v65, v64\n\t" \
  "v_mfma_f32_16x16x32_bf16 a[136:139], v[232:235], v[208:211], a[136:139]\n\t" \
  "ds_read_b64_tr_b16 v[184:185], v120 offset:0\n\t" \
  "v_fma_f32 v75, v75, v65, v64\n\t" \
  "v_mfma_f32_16x16x32_bf16 a[140:143], v[236:239], v[208:211], a[140:143]\n\t" \
  "buffer_load_dwordx4 v111, s[60:63], s28 offen offset:3072 sc1 lds\n\t" \
  "v_cvt_pk_bf16_f32 v41, v74, v75\n\t" \
  "v_mfma_f32_16x16x32_bf16 a[144:147], v[240:243], v[208:211], a[144:147]\n\t" \
  "ds_read_b64_tr_b16 v[186:187], v120 offset:2048\n\t" \
  "v_fma_f32 v76, v76, v65, v64\n\t" \
  "v_fma_f32 v77, v77, v65, v64\n\t" \
  "v_mfma_f32_16x16x32_bf16 a[148:151], v[244:247], v[208:211], a[148:151]\n\t" \
  "v_cvt_pk_bf16_f32 v44, v76, v77\n\t" \
  "v_fma_f32 v78, v78, v65, v64\n\t" \
  "v_mfma_f32_16x16x32_bf16 a[152:155], v[248:251], v[208:211], a[152:155]\n\t" \
  "ds_read_b64_tr_b16 v[188:189], v121 offset:0\n\t" \
  "v_fma_f32 v79, v79, v65, v64\n\t" \
  "v_cvt_pk_bf16_f32 v45, v78, v79\n\t" \
  "v_mfma_f32_16x16x32_bf16 a[156:159], v[252:255], v[208:211], a[156:159]\n\t" \
  "v_fma_f32 v80, v80, v65, v64\n\t" \
  "v_fma_f32 v81, v81, v65, v64\n\t" \
  "v_mfma_f32_16x16x32_bf16 a[96:99], v[224:227], v[204:207], a[96:99]\n\t" \
  "ds_read_b64_tr_b16 v[190:191], v121 offset:2048\n\t" \
  "v_cvt_pk_bf16_f32 v42, v80, v81\n\t" \
  "v_mfma_f32_16x16x32_bf16 a[100:103], v[228:231], v[204:207], a[100:103]\n\t" \
  "s_mov_b32 m0, s27\n\t" \
  "v_fma_f32 v82, v82, v65, v64\n\t" \
  "v_fma_f32 v83, v83, v65, v64\n\t" \
  "v_mfma_f32_16x16x32_bf16 a[104:107], v[232:235], v[204:207], a[104:107]\n\t" \
  "ds_read_b128 v[152:155], v112 offset:12288\n\t" \
  "v_cvt_pk_bf16_f32 v43, v82, v83\n\t" \
  "v_mfma_f32_16x16x32_bf16 a[108:111], v[236:239], v[204:207], a[108:111]\n\t" \
  "buffer_load_dwordx4 v104, s[56:59], s29 offen offset:0 sc1 lds\n\t" \
  "v_fma_f32 v84, v84, v65, v64\n\t" \
  "v_fma_f32 v85, v85, v65, v64\n\t" \
  "v_mfma_f32_16x16x32_bf16 a[112:115], v[240:243], v[204:207], a[112:115]\n\t" \
  "ds_read_b128 v[148:151], v112 offset:10240\n\t" \
  "v_cvt_pk_bf16_f32 v46, v84, v85\n\t" \
  "v_mfma_f32_16x16x32_bf16 a[116:119], v[244:247], v[204:207], a[116:119]\n\t" \
  "v_fma_f32 v86, v86, v65, v64\n\t" \
  "v_fma_f32 v87, v87, v65, v64\n\t" \
  "v_mfma_f32_16x16x32_bf16 a[120:123], v[248:251], v[204:207], a[120:123]\n\t" \
  "ds_read_b128 v[144:147], v112 offset:8192\n\t" \
  "v_cvt_pk_bf16_f32 v47, v86, v87\n\t" \
  "v_fma_f32 v88, v88, v65, v64\n\t" \
  "v_mfma_f32_16x16x32_bf16 a[124:127], v[252:255], v[204:207], a[124:127]\n\t" \
  "v_fma_f32 v89, v89, v65, v64\n\t" \
  "v_cvt_pk_bf16_f32 v48, v88, v89\n\t" \
  "v_mfma_f32_16x16x32_bf16 a[64:67], v[224:227], v[200:203], a[64:67]\n\t" \
  "ds_read_b128 v[140:143], v112 offset:6144\n\t" \
  "v_fma_f32 v90, v90, v65, v64\n\t" \
  "v_fma_f32 v91, v91, v65, v64\n\t" \
  "v_mfma_f32_16x16x32_bf16 a[68:71], v[228:231], v[200:203], a[68:71]\n\t" \
  "v_cvt_pk_bf16_f32 v49, v90, v91\n\t" \
  "v_fma_f32 v92, v92, v65, v64\n\t" \
  "v_mfma_f32_16x16x32_bf16 a[72:75], v[232:235], v[200:203], a[72:75]\n\t" \
  "ds_read_b128 v[136:139], v112 offset:4096\n\t" \
  "v_fma_f32 v93, v93, v65, v64\n\t" \
  "v_mfma_f32_16x16x32_bf16 a[76:79], v[236:239], v[200:203], a[76:79]\n\t" \
  "buffer_load_dwordx4 v105, s[56:59], s29 offen offset:1024 sc1 lds\n\t" \
  "v_cvt_pk_bf16_f32 v52, v92, v93\n\t" \
  "v_fma_f32 v94, v94, v65, v64\n\t" \
  "v_mfma_f32_16x16x32_bf16 a[80:83], v[240:243], v[200:203], a[80:83]\n\t" \
  "ds_read_b128 v[132:135], v112 offset:2048\n\t" \
  "v_fma_f32 v95, v95, v65, v64\n\t" \
  "v_mfma_f32_16x16x32_bf16 a[84:87], v[244:247], v[200:203], a[84:87]\n\t" \
  "v_cvt_pk_bf16_f32 v53, v94, v95\n\t" \
  "v_fma_f32 v96, v96, v65, v64\n\t" \
  "v_fma_f32 v97, v97, v65, v64\n\t" \
  "v_mfma_f32_16x16x32_bf16 a[88:91], v[248:251], v[200:203], a[88:91]\n\t" \
  "ds_read_b128 v[128:131], v112 offset:0\n\t" \
  "v_cvt_pk_bf16_f32 v50, v96, v97\n\t" \
  "v_mfma_f32_16x16x32_bf16 a[92:95], v[252:255], v[200:203], a[92:95]\n\t" \
  "v_fma_f32 v98, v98, v65, v64\n\t" \
  "v_fma_f32 v99, v99, v65, v64\n\t" \
  "v_mfma_f32_16x16x32_bf16 a[32:35], v[224:227], v[196:199], a[32:35]\n\t" \
  "s_xor_b32 s26, s26, 0xc000\n\t" \
  "s_add_u32 s28, s28, s30\n\t" \
  "v_cvt_pk_bf16_f32 v51, v98, v99\n\t" \
  "v_mfma_f32_16x16x32_bf16 a[36:39], v[228:231], v[196:199], a[36:39]\n\t" \
  "v_fma_f32 v100, v100, v65, v64\n\t" \
  "v_fma_f32 v101, v101, v65, v64\n\t" \
  "v_mfma_f32_16x16x32_bf16 a[40:43], v[232:235], v[196:199], a[40:43]\n\t" \
  "v_cvt_pk_bf16_f32 v54, v100, v101\n\t" \
  "v_fma_f32 v102, v102, v65, v64\n\t" \
  "v_fma_f32 v103, v103, v65, v64\n\t" \
  "v_mfma_f32_16x16x32_bf16 a[44:47], v[236:239], v[196:199], a[44:47]\n\t" \
  "buffer_load_dwordx4 v106, s[56:59], s29 offen offset:2048 sc1 lds\n\t" \
  "s_waitcnt vmcnt(16)\n\t" \
  "v_mfma_f32_16x16x32_bf16 a[48:51], v[240:243], v[196:199], a[48:51]\n\t" \
  "v_readfirstlane_b32 s90, v33\n\t" \
  "v_readlane_b32 s81, v29, 30\n\t" \
  "v_mfma_f32_16x16x32_bf16 a[52:55], v[244:247], v[196:199], a[52:55]\n\t" \
  "v_readlane_b32 s80, v29, 31\n\t" \
  "s_add_u32 s90, s90, 1\n\ts_cselect_b32 s90, 1, s90\n\t" \
  "v_cvt_pk_bf16_f32 v55, v102, v103\n\t" \
  "v_mfma_f32_16x16x32_bf16 a[56:59], v[248:251], v[196:199], a[56:59]\n\t" \
  "v_mov_b32_e32 v33, s90\n\t" \
  "v_readlane_b32 s44, v29, 2\n\t" \
  "v_mfma_f32_16x16x32_bf16 a[60:63], v[252:255], v[196:199], a[60:63]\n\t" \
  "v_readlane_b32 s68, v29, 33\n\t" \
  "v_readlane_b32 s25, v30, 9\n\t" \
  "v_mfma_f32_16x16x32_bf16 a[0:3], v[224:227], v[192:195], a[0:3]\n\t" \
  "v_readlane_b32 s42, v30, 10\n\t" \
  "buffer_store_dwordx4 v[40:43], %16, %11, 0 offen offset:0 sc1\n\t" \
  "v_mfma_f32_16x16x32_bf16 a[4:7], v[228:231], v[192:195], a[4:7]\n\t" \
  "s_mul_i32 s76, s91, 14336\n\t" \
  "s_add_u32 s76, s76, s44\n\t" \
  "v_mfma_f32_16x16x32_bf16 a[8:11], v[232:235], v[192:195], a[8:11]\n\t" \
  "s_add_u32 s72, s44, 0\n\t" \
  "s_add_u32 s73, s44, 4096\n\t" \
  "buffer_store_dwordx4 v[44:47], %16, %11, 0 offen offset:128 sc1\n\t" \
  "v_mfma_f32_16x16x32_bf16 a[12:15], v[236:239], v[192:195], a[12:15]\n\t" \
  "buffer_load_dwordx4 v107, s[56:59], s29 offen offset:3072 sc1 lds\n\t" \
  "s_add_u32 s74, s44, 8192\n\t" \
  "v_mfma_f32_16x16x32_bf16 a[16:19], v[240:243], v[192:195], a[16:19]\n\t" \
  "s_add_u32 s27, s27, 0x4000\n\t" \
  "s_add_u32 s29, s29, s34\n\t" \
  "buffer_store_dwordx4 v[48:51], %16, %11, s17 offen offset:3072 sc1\n\t" \
  "v_mfma_f32_16x16x32_bf16 a[20:23], v[244:247], v[192:195], a[20:23]\n\t" \
  "s_add_u32 s37, s37, 1\n\t" \
  "s_add_u32 s75, s44, 12288\n\t" \
  "v_mfma_f32_16x16x32_bf16 a[24:27], v[248:251], v[192:195], a[24:27]\n\t" \
  "s_add_u32 s77, s76, 4096\n\t" \
  "s_add_u32 s78, s76, 8192\n\t" \
  "buffer_store_dwordx4 v[52:55], %16, %11, s17 offen offset:3200 sc1\n\t" \
  "v_mfma_f32_16x16x32_bf16 a[28:31], v[252:255], v[192:195], a[28:31]\n\t" \
  "s_add_u32 s79, s76, 12288\n\t" \
  "MK_KL0K_%=:\n\t" \
  "s_waitcnt lgkmcnt(0)\n\t" \
  "s_waitcnt vmcnt(16)\n\t" \
  "s_barrier\n\t" \
  "v_mfma_f32_16x16x32_bf16 a[224:227], v[160:163], v[156:159], a[224:227]\n\t" \
  "ds_read_b128 v[220:223], v113 offset:14336\n\t" \
  "v_mfma_f32_16x16x32_bf16 a[228:231], v[164:167], v[156:159], a[228:231]\n\t" \
  "s_mov_b32 m0, s26\n\t" \
  "v_mfma_f32_16x16x32_bf16 a[232:235], v[168:171], v[156:159], a[232:235]\n\t" \
  "ds_read_b64_tr_b16 v[224:225], v114 offset:16384\n\t" \
  "v_mfma_f32_16x16x32_bf16 a[236:239], v[172:175], v[156:159], a[236:239]\n\t" \
  "buffer_load_dwordx4 v108, s[60:63], s28 offen offset:0 sc1 lds\n\t" \
  "v_mfma_f32_16x16x32_bf16 a[240:243], v[176:179], v[156:159], a[240:243]\n\t" \
  "ds_read_b64_tr_b16 v[226:227], v114 offset:18432\n\t" \
  "v_mfma_f32_16x16x32_bf16 a[244:247], v[180:183], v[156:159], a[244:247]\n\t" \
  "v_mfma_f32_16x16x32_bf16 a[248:251], v[184:187], v[156:159], a[248:251]\n\t" \
  "ds_read_b64_tr_b16 v[228:229], v115 offset:16384\n\t" \
  "v_mfma_f32_16x16x32_bf16 a[252:255], v[188:191], v[156:159], a[252:255]\n\t" \
  "v_mfma_f32_16x16x32_bf16 a[192:195], v[160:163], v[152:155], a[192:195]\n\t" \
  "ds_read_b64_tr_b16 v[230:231], v115 offset:18432\n\t" \
  "v_mfma_f32_16x16x32_bf16 a[196:199], v[164:167], v[152:155], a[196:199]\n\t" \
  "v_mfma_f32_16x16x32_bf16 a[200:203], v[168:171], v[152:155], a[200:203]\n\t" \
  "ds_read_b64_tr_b16 v[232:233], v116 offset:16384\n\t" \
  "v_mfma_f32_16x16x32_bf16 a[204:207], v[172:175], v[152:155], a[204:207]\n\t" \
  "buffer_load_dwordx4 v109, s[60:63], s28 offen offset:1024 sc1 lds\n\t" \
  "v_mfma_f32_16x16x32_bf16 a[208:211], v[176:179], v[152:155], a[208:211]\n\t" \
  "ds_read_b64_tr_b16 v[234:235], v116 offset:18432\n\t" \
  "v_mfma_f32_16x16x32_bf16 a[212:215], v[180:183], v[152:155], a[212:215]\n\t" \
  "v_mfma_f32_16x16x32_bf16 a[216:219], v[184:187], v[152:155], a[216:219]\n\t" \
  "ds_read_b64_tr_b16 v[236:237], v117 offset:16384\n\t" \
  "v_mfma_f32_16x16x32_bf16 a[220:223], v[188:191], v[152:155], a[220:223]\n\t" \
  "v_mfma_f32_16x16x32_bf16 a[160:163], v[160:163], v[148:151], a[160:163]\n\t" \
  "ds_read_b64_tr_b16 v[238:239], v117 offset:18432\n\t" \
  "v_mfma_f32_16x16x32_bf16 a[164:167], v[164:167], v[148:151], a[164:167]\n\t" \
  "v_mfma_f32_16x16x32_bf16 a[168:171], v[168:171], v[148:151], a[168:171]\n\t" \
  "ds_read_b64_tr_b16 v[240:241], v118 offset:16384\n\t" \
  "v_mfma_f32_16x16x32_bf16 a[172:175], v[172:175], v[148:151], a[172:175]\n\t" \
  "buffer_load_dwordx4 v110, s[60:63], s28 offen offset:2048 sc1 lds\n\t" \
  "v_mfma_f32_16x16x32_bf16 a[176:179], v[176:179], v[148:151], a[176:179]\n\t" \
  "ds_read_b64_tr_b16 v[242:243], v118 offset:18432\n\t" \
  "v_mfma_f32_16x16x32_bf16 a[180:183], v[180:183], v[148:151], a[180:183]\n\t" \
  "v_mfma_f32_16x16x32_bf16 a[184:187], v[184:187], v[148:151], a[184:187]\n\t" \
  "ds_read_b64_tr_b16 v[244:245], v119 offset:16384\n\t" \
  "v_mfma_f32_16x16x32_bf16 a[188:191], v[188:191], v[148:151], a[188:191]\n\t" \
  "v_mfma_f32_16x16x32_bf16 a[128:131], v[160:163], v[144:147], a[128:131]\n\t" \
  "ds_read_b64_tr_b16 v[246:247], v119 offset:18432\n\t" \
  "v_mfma_f32_16x16x32_bf16 a[132:135], v[164:167], v[144:147], a[132:135]\n\t" \
  "v_mfma_f32_16x16x32_bf16 a[136:139], v[168:171], v[144:147], a[136:139]\n\t" \
  "ds_read_b64_tr_b16 v[248:249], v120 offset:16384\n\t" \
  "v_mfma_f32_16x16x32_bf16 a[140:143], v[172:175], v[144:147], a[140:143]\n\t" \
  "buffer_load_dwordx4 v111, s[60:63], s28 offen offset:3072 sc1 lds\n\t" \
  "v_mfma_f32_16x16x32_bf16 a[144:147], v[176:179], v[144:147], a[144:147]\n\t" \
  "ds_read_b64_tr_b16 v[250:251], v120 offset:18432\n\t" \
  "v_mfma_f32_16x16x32_bf16 a[148:151], v[180:183], v[144:147], a[148:151]\n\t" \
  "v_mfma_f32_16x16x32_bf16 a[152:155], v[184:187], v[144:147], a[152:155]\n\t" \
  "ds_read_b64_tr_b16 v[252:253], v121 offset:16384\n\t" \
  "v_mfma_f32_16x16x32_bf16 a[156:159], v[188:191], v[144:147], a[156:159]\n\t" \
  "v_mfma_f32_16x16x32_bf16 a[96:99], v[160:163], v[140:143], a[96:99]\n\t" \
  "ds_read_b64_tr_b16 v[254:255], v121 offset:18432\n\t" \
  "v_mfma_f32_16x16x32_bf16 a[100:103], v[164:167], v[140:143], a[100:103]\n\t" \
  "s_mov_b32 m0, s27\n\t" \
  "v_mfma_f32_16x16x32_bf16 a[104:107], v[168:171], v[140:143], a[104:107]\n\t" \
  "ds_read_b128 v[216:219], v113 offset:12288\n\t" \
  "v_mfma_f32_16x16x32_bf16 a[108:111], v[172:175], v[140:143], a[108:111]\n\t" \
  "buffer_load_dwordx4 v104, s[56:59], s29 offen offset:0 sc1 lds\n\t" \
  "v_mfma_f32_16x16x32_bf16 a[112:115], v[176:179], v[140:143], a[112:115]\n\t" \
  "ds_read_b128 v[212:215], v113 offset:10240\n\t" \
  "v_mfma_f32_16x16x32_bf16 a[116:119], v[180:183], v[140:143], a[116:119]\n\t" \
  "v_mfma_f32_16x16x32_bf16 a[120:123], v[184:187], v[140:143], a[120:123]\n\t" \
  "ds_read_b128 v[208:211], v113 offset:8192\n\t" \
  "v_mfma_f32_16x16x32_bf16 a[124:127], v[188:191], v[140:143], a[124:127]\n\t" \
  "v_mfma_f32_16x16x32_bf16 a[64:67], v[160:163], v[136:139], a[64:67]\n\t" \
  "ds_read_b128 v[204:207], v113 offset:6144\n\t" \
  "v_mfma_f32_16x16x32_bf16 a[68:71], v[164:167], v[136:139], a[68:71]\n\t" \
  "v_mfma_f32_16x16x32_bf16 a[72:75], v[168:171], v[136:139], a[72:75]\n\t" \
  "ds_read_b128 v[200:203], v113 offset:4096\n\t" \
  "v_mfma_f32_16x16x32_bf16 a[76:79], v[172:175], v[136:139], a[76:79]\n\t" \
  "buffer_load_dwordx4 v105, s[56:59], s29 offen offset:1024 sc1 lds\n\t" \
  "v_mfma_f32_16x16x32_bf16 a[80:83], v[176:179], v[136:139], a[80:83]\n\t" \
  "ds_read_b128 v[196:199], v113 offset:2048\n\t" \
  "v_mfma_f32_16x16x32_bf16 a[84:87], v[180:183], v[136:139], a[84:87]\n\t" \
  "v_mfma_f32_16x16x32_bf16 a[88:91], v[184:187], v[136:139], a[88:91]\n\t" \
  "ds_read_b128 v[192:195], v113 offset:0\n\t" \
  "v_mfma_f32_16x16x32_bf16 a[92:95], v[188:191], v[136:139], a[92:95]\n\t" \
  "v_mfma_f32_16x16x32_bf16 a[32:35], v[160:163], v[132:135], a[32:35]\n\t" \
  "s_add_u32 s26, s26, 0x4000\n\t" \
  "s_add_u32 s28, s28, s30\n\t" \
  "v_mfma_f32_16x16x32_bf16 a[36:39], v[164:167], v[132:135], a[36:39]\n\t" \
  "s_add_u32 s39, s35, s31\n\t" \
  "s_add_u32 s43, s35, 0x8000\n\t" \
  "v_mfma_f32_16x16x32_bf16 a[40:43], v[168:171], v[132:135], a[40:43]\n\t" \
  "s_cmp_lg_u32 s35, 0x20000\n\t" \
  "s_cselect_b32 s43, s43, 0x10000\n\t" \
  "v_mfma_f32_16x16x32_bf16 a[44:47], v[172:175], v[132:135], a[44:47]\n\t" \
  "buffer_load_dwordx4 v106, s[56:59], s29 offen offset:2048 sc1 lds\n\t" \
  "v_mfma_f32_16x16x32_bf16 a[48:51], v[176:179], v[132:135], a[48:51]\n\t" \
  "s_sub_u32 s38, s43, s35\n\t" \
  "s_mov_b32 s35, s43\n\t" \
  "v_mfma_f32_16x16x32_bf16 a[52:55], v[180:183], v[132:135], a[52:55]\n\t" \
  "v_mfma_f32_16x16x32_bf16 a[56:59], v[184:187], v[132:135], a[56:59]\n\t" \
  "v_add_u32_e32 v112, s38, v112\n\t" \
  "v_add_u32_e32 v113, s38, v113\n\t" \
  "v_mfma_f32_16x16x32_bf16 a[60:63], v[188:191], v[132:135], a[60:63]\n\t" \
  "v_xor_b32_e32 v114, 0x8000, v114\n\t" \
  "v_xor_b32_e32 v115, 0x8000, v115\n\t" \
  "v_mfma_f32_16x16x32_bf16 a[0:3], v[160:163], v[128:131], a[0:3]\n\t" \
  "v_xor_b32_e32 v116, 0x8000, v116\n\t" \
  "v_xor_b32_e32 v117, 0x8000, v117\n\t" \
  "v_mfma_f32_16x16x32_bf16 a[4:7], v[164:167], v[128:131], a[4:7]\n\t" \
  "v_xor_b32_e32 v118, 0x8000, v118\n\t" \
  "v_xor_b32_e32 v119, 0x8000, v119\n\t" \
  "v_mfma_f32_16x16x32_bf16 a[8:11], v[168:171], v[128:131], a[8:11]\n\t" \
  "v_xor_b32_e32 v120, 0x8000, v120\n\t" \
  "v_xor_b32_e32 v121, 0x8000, v121\n\t" \
  "v_mfma_f32_16x16x32_bf16 a[12:15], v[172:175], v[128:131], a[12:15]\n\t" \
  "buffer_load_dwordx4 v107, s[56:59], s29 offen offset:3072 sc1 lds\n\t" \
  "v_mfma_f32_16x16x32_bf16 a[16:19], v[176:179], v[128:131], a[16:19]\n\t" \
  "s_mov_b32 s27, s39\n\t" \
  "s_add_u32 s29, s29, s33\n\t" \
  "v_mfma_f32_16x16x32_bf16 a[20:23], v[180:183], v[128:131], a[20:23]\n\t" \
  "s_cmp_eq_u32 s37, 13\n\t" \
  "v_mfma_f32_16x16x32_bf16 a[24:27], v[184:187], v[128:131], a[24:27]\n\t" \
  "v_mfma_f32_16x16x32_bf16 a[28:31], v[188:191], v[128:131], a[28:31]\n\t" \
  "s_cbranch_scc1 MK_KTK_%=\n\t" \
  "s_waitcnt lgkmcnt(0)\n\t" \
  "s_waitcnt vmcnt(16)\n\t" \
  "s_barrier\n\t" \
  "v_mfma_f32_16x16x32_bf16 a[224:227], v[224:227], v[220:223], a[224:227]\n\t" \
  "ds_read_b128 v[156:159], v112 offset:14336\n\t" \
  "v_mfma_f32_16x16x32_bf16 a[228:231], v[228:231], v[220:223], a[228:231]\n\t" \
  "s_mov_b32 m0, s26\n\t" \
  "v_mfma_f32_16x16x32_bf16 a[232:235], v[232:235], v[220:223], a[232:235]\n\t" \
  "ds_read_b64_tr_b16 v[160:161], v114 offset:0\n\t" \
  "v_mfma_f32_16x16x32_bf16 a[236:239], v[236:239], v[220:223], a[236:239]\n\t" \
  "buffer_load_dwordx4 v108, s[60:63], s28 offen offset:0 sc1 lds\n\t" \
  "v_mfma_f32_16x16x32_bf16 a[240:243], v[240:243], v[220:223], a[240:243]\n\t" \
  "ds_read_b64_tr_b16 v[162:163], v114 offset:2048\n\t" \
  "v_mfma_f32_16x16x32_bf16 a[244:247], v[244:247], v[220:223], a[244:247]\n\t" \
  "s_bitcmp1_b32 s25, s37\n\t" \
  "v_mfma_f32_16x16x32_bf16 a[248:251], v[248:251], v[220:223], a[248:251]\n\t" \
  "ds_read_b64_tr_b16 v[164:165], v115 offset:0\n\t" \
  "v_mfma_f32_16x16x32_bf16 a[252:255], v[252:255], v[220:223], a[252:255]\n\t" \
  "s_cbranch_scc1 MK_PLK_%=\n\t" \
  "MK_PLRK_%=:\n\t" \
  "v_mfma_f32_16x16x32_bf16 a[192:195], v[224:227], v[216:219], a[192:195]\n\t" \
  "ds_read_b64_tr_b16 v[166:167], v115 offset:2048\n\t" \
  "v_mfma_f32_16x16x32_bf16 a[196:199], v[228:231], v[216:219], a[196:199]\n\t" \
  "v_mfma_f32_16x16x32_bf16 a[200:203], v[232:235], v[216:219], a[200:203]\n\t" \
  "ds_read_b64_tr_b16 v[168:169], v116 offset:0\n\t" \
  "v_mfma_f32_16x16x32_bf16 a[204:207], v[236:239], v[216:219], a[204:207]\n\t" \
  "buffer_load_dwordx4 v109, s[60:63], s28 offen offset:1024 sc1 lds\n\t" \
  "v_mfma_f32_16x16x32_bf16 a[208:211], v[240:243], v[216:219], a[208:211]\n\t" \
  "ds_read_b64_tr_b16 v[170:171], v116 offset:2048\n\t" \
  "v_mfma_f32_16x16x32_bf16 a[212:215], v[244:247], v[216:219], a[212:215]\n\t" \
  "v_mfma_f32_16x16x32_bf16 a[216:219], v[248:251], v[216:219], a[216:219]\n\t" \
  "ds_read_b64_tr_b16 v[172:173], v117 offset:0\n\t" \
  "v_mfma_f32_16x16x32_bf16 a[220:223], v[252:255], v[216:219], a[220:223]\n\t" \
  "v_mfma_f32_16x16x32_bf16 a[160:163], v[224:227], v[212:215], a[160:163]\n\t" \
  "ds_read_b64_tr_b16 v[174:175], v117 offset:2048\n\t" \
  "v_mfma_f32_16x16x32_bf16 a[164:167], v[228:231], v[212:215], a[164:167]\n\t" \
  "v_mfma_f32_16x16x32_bf16 a[168:171], v[232:235], v[212:215], a[168:171]\n\t" \
  "ds_read_b64_tr_b16 v[176:177], v118 offset:0\n\t" \
  "v_mfma_f32_16x16x32_bf16 a[172:175], v[236:239], v[212:215], a[172:175]\n\t" \
  "buffer_load_dwordx4 v110, s[60:63], s28 offen offset:2048 sc1 lds\n\t" \
  "v_mfma_f32_16x16x32_bf16 a[176:179], v[240:243], v[212:215], a[176:179]\n\t" \
  "ds_read_b64_tr_b16 v[178:179], v118 offset:2048\n\t" \
  "v_mfma_f32_16x16x32_bf16 a[180:183], v[244:247], v[212:215], a[180:183]\n\t" \
  "v_mfma_f32_16x16x32_bf16 a[184:187], v[248:251], v[212:215], a[184:187]\n\t" \
  "ds_read_b64_tr_b16 v[180:181], v119 offset:0\n\t" \
  "v_mfma_f32_16x16x32_bf16 a[188:191], v[252:255], v[212:215], a[188:191]\n\t" \
  "v_mfma_f32_16x16x32_bf16 a[128:131], v[224:227], v[208:211], a[128:131]\n\t" \
  "ds_read_b64_tr_b16 v[182:183], v119 offset:2048\n\t" \
  "v_mfma_f32_16x16x32_bf16 a[132:135], v[228:231], v[208:211], a[132:135]\n\t" \
  "v_mfma_f32_16x16x32_bf16 a[136:139], v[232:235], v[208:211], a[136:139]\n\t" \
  "ds_read_b64_tr_b16 v[184:185], v120 offset:0\n\t" \
  "v_mfma_f32_16x16x32_bf16 a[140:143], v[236:239], v[208:211], a[140:143]\n\t" \
  "buffer_load_dwordx4 v111, s[60:63], s28 offen offset:3072 sc1 lds\n\t" \
  "v_mfma_f32_16x16x32_bf16 a[144:147], v[240:243], v[208:211], a[144:147]\n\t" \
  "ds_read_b64_tr_b16 v[186:187], v120 offset:2048\n\t" \
  "v_mfma_f32_16x16x32_bf16 a[148:151], v[244:247], v[208:211], a[148:151]\n\t" \
  "v_mfma_f32_16x16x32_bf16 a[152:155], v[248:251], v[208:211], a[152:155]\n\t" \
  "ds_read_b64_tr_b16 v[188:189], v121 offset:0\n\t" \
  "v_mfma_f32_16x16x32_bf16 a[156:159], v[252:255], v[208:211], a[156:159]\n\t" \
  "v_mfma_f32_16x16x32_bf16 a[96:99], v[224:227], v[204:207], a[96:99]\n\t" \
  "ds_read_b64_tr_b16 v[190:191], v121 offset:2048\n\t" \
  "v_mfma_f32_16x16x32_bf16 a[100:103], v[228:231], v[204:207], a[100:103]\n\t" \
  "s_mov_b32 m0, s27\n\t" \
  "v_mfma_f32_16x16x32_bf16 a[104:107], v[232:235], v[204:207], a[104:107]\n\t" \
  "ds_read_b128 v[152:155], v112 offset:12288\n\t" \
  "v_mfma_f32_16x16x32_bf16 a[108:111], v[236:239], v[204:207], a[108:111]\n\t" \
  "buffer_load_dwordx4 v104, s[56:59], s29 offen offset:0 sc1 lds\n\t" \
  "v_mfma_f32_16x16x32_bf16 a[112:115], v[240:243], v[204:207], a[112:115]\n\t" \
  "ds_read_b128 v[148:151], v112 offset:10240\n\t" \
  "v_mfma_f32_16x16x32_bf16 a[116:119], v[244:247], v[204:207], a[116:119]\n\t" \
  "v_mfma_f32_16x16x32_bf16 a[120:123], v[248:251], v[204:207], a[120:123]\n\t" \
  "ds_read_b128 v[144:147], v112 offset:8192\n\t" \
  "v_mfma_f32_16x16x32_bf16 a[124:127], v[252:255], v[204:207], a[124:127]\n\t" \
  "v_mfma_f32_16x16x32_bf16 a[64:67], v[224:227], v[200:203], a[64:67]\n\t" \
  "ds_read_b128 v[140:143], v112 offset:6144\n\t" \
  "v_mfma_f32_16x16x32_bf16 a[68:71], v[228:231], v[200:203], a[68:71]\n\t" \
  "v_mfma_f32_16x16x32_bf16 a[72:75], v[232:235], v[200:203], a[72:75]\n\t" \
  "ds_read_b128 v[136:139], v112 offset:4096\n\t" \
  "v_mfma_f32_16x16x32_bf16 a[76:79], v[236:239], v[200:203], a[76:79]\n\t" \
  "buffer_load_dwordx4 v105, s[56:59], s29 offen offset:1024 sc1 lds\n\t" \
  "v_mfma_f32_16x16x32_bf16 a[80:83], v[240:243], v[200:203], a[80:83]\n\t" \
  "ds_read_b128 v[132:135], v112 offset:2048\n\t" \
  "v_mfma_f32_16x16x32_bf16 a[84:87], v[244:247], v[200:203], a[84:87]\n\t" \
  "v_mfma_f32_16x16x32_bf16 a[88:91], v[248:251], v[200:203], a[88:91]\n\t" \
  "ds_read_b128 v[128:131], v112 offset:0\n\t" \
  "v_mfma_f32_16x16x32_bf16 a[92:95], v[252:255], v[200:203], a[92:95]\n\t" \
  "v_mfma_f32_16x16x32_bf16 a[32:35], v[224:227], v[196:199], a[32:35]\n\t" \
  "s_xor_b32 s26, s26, 0xc000\n\t" \
  "s_add_u32 s28, s28, s30\n\t" \
  "v_mfma_f32_16x16x32_bf16 a[36:39], v[228:231], v[196:199], a[36:39]\n\t" \
  "v_mfma_f32_16x16x32_bf16 a[40:43], v[232:235], v[196:199], a[40:43]\n\t" \
  "v_mfma_f32_16x16x32_bf16 a[44:47], v[236:239], v[196:199], a[44:47]\n\t" \
  "buffer_load_dwordx4 v106, s[56:59], s29 offen offset:2048 sc1 lds\n\t" \
  "v_mfma_f32_16x16x32_bf16 a[48:51], v[240:243], v[196:199], a[48:51]\n\t" \
  "v_mfma_f32_16x16x32_bf16 a[52:55], v[244:247], v[196:199], a[52:55]\n\t" \
  "v_mfma_f32_16x16x32_bf16 a[56:59], v[248:251], v[196:199], a[56:59]\n\t" \
  "v_mfma_f32_16x16x32_bf16 a[60:63], v[252:255], v[196:199], a[60:63]\n\t" \
  "v_mfma_f32_16x16x32_bf16 a[0:3], v[224:227], v[192:195], a[0:3]\n\t" \
  "v_mfma_f32_16x16x32_bf16 a[4:7], v[228:231], v[192:195], a[4:7]\n\t" \
  "v_mfma_f32_16x16x32_bf16 a[8:11], v[232:235], v[192:195], a[8:11]\n\t" \
  "v_mfma_f32_16x16x32_bf16 a[12:15], v[236:239], v[192:195], a[12:15]\n\t" \
  "buffer_load_dwordx4 v107, s[56:59], s29 offen offset:3072 sc1 lds\n\t" \
  "v_mfma_f32_16x16x32_bf16 a[16:19], v[240:243], v[192:195], a[16:19]\n\t" \
  "s_add_u32 s27, s27, 0x4000\n\t" \
  "s_add_u32 s29, s29, s34\n\t" \
  "v_mfma_f32_16x16x32_bf16 a[20:23], v[244:247], v[192:195], a[20:23]\n\t" \
  "s_add_u32 s37, s37, 1\n\t" \
  "v_mfma_f32_16x16x32_bf16 a[24:27], v[248:251], v[192:195], a[24:27]\n\t" \
  "v_mfma_f32_16x16x32_bf16 a[28:31], v[252:255], v[192:195], a[28:31]\n\t" \
  "s_branch MK_KL0K_%=\n\t" \
  "MK_PLK_%=:\n\t" \
  "s_cmp_eq_u32 s37, 10\n\t" \
  "s_cbranch_scc1 MK_PFK_%=\n\t" \
  "s_cmp_eq_u32 s37, s42\n\t" \
  "s_cbranch_scc1 MK_RLK_%=\n\t" \
  "s_cmp_lt_u32 s37, 10\n\t" \
  "s_cbranch_scc1 MK_RPK_%=\n\t" \
  "buffer_load_dwordx4 v[124:127], %1, %11, s80 offen sc1\n\t" \
  "s_branch MK_PLRK_%=\n\t" \
  "MK_PFK_%=:\n\t" \
  "buffer_store_dword v33, %12, %11, s81 offen offset:0 sc1\n\t" \
  "buffer_load_dwordx4 v[36:39], %1, s[84:87], s89 offen\n\t" \
  "s_branch MK_PLRK_%=\n\t" \
  "MK_RLK_%=:\n\t" \
  "v_readlane_b32 s41, v30, 0\n\t" \
  "v_readlane_b32 s40, v30, 1\n\t" \
  "s_nop 0\n\t" \
  "s_lshl_b32 s46, s40, 4\n\t" \
  "s_add_u32 s46, s46, s41\n\t" \
  "v_mad_u32_u24 v32, s40, v34, v35\n\t" \
  "s_cmp_eq_u32 s40, 3584\n\t" \
  "s_cselect_b64 s[64:65], s[94:95], s[92:93]\n\t" \
  "buffer_load_dwordx4 v[40:43], v32, s[64:67], s41 offen\n\t" \
  "buffer_load_dwordx4 v[44:47], v32, s[64:67], s46 offen\n\t" \
  "v_readlane_b32 s41, v30, 3\n\t" \
  "v_readlane_b32 s40, v30, 4\n\t" \
  "s_nop 0\n\t" \
  "s_lshl_b32 s46, s40, 4\n\t" \
  "s_add_u32 s46, s46, s41\n\t" \
  "v_mad_u32_u24 v32, s40, v34, v35\n\t" \
  "s_cmp_eq_u32 s40, 3584\n\t" \
  "s_cselect_b64 s[64:65], s[94:95], s[92:93]\n\t" \
  "buffer_load_dwordx4 v[48:51], v32, s[64:67], s41 offen\n\t" \
  "buffer_load_dwordx4 v[52:55], v32, s[64:67], s46 offen\n\t" \
  "v_readlane_b32 s41, v30, 6\n\t" \
  "v_readlane_b32 s40, v30, 7\n\t" \
  "s_nop 0\n\t" \
  "s_lshl_b32 s46, s40, 4\n\t" \
  "s_add_u32 s46, s46, s41\n\t" \
  "v_mad_u32_u24 v32, s40, v34, v35\n\t" \
  "s_cmp_eq_u32 s40, 3584\n\t" \
  "s_cselect_b64 s[64:65], s[94:95], s[92:93]\n\t" \
  "buffer_load_dwordx4 v[56:59], v32, s[64:67], s41 offen\n\t" \
  "buffer_load_dwordx4 v[60:63], v32, s[64:67], s46 offen\n\t" \
  "s_branch MK_PLRK_%=\n\t" \
  "MK_RPK_%=:\n\t" \
  "v_mfma_f32_16x16x32_bf16 v[64:67], v[40:43], %7, 0\n\t" \
  "v_mfma_f32_16x16x32_bf16 v[68:71], v[44:47], %7, 0\n\t" \
  "v_mfma_f32_16x16x32_bf16 v[72:75], v[40:43], %8, 0\n\t" \
  "v_mfma_f32_16x16x32_bf16 v[76:79], v[44:47], %8, 0\n\t" \
  "v_mfma_f32_16x16x32_bf16 v[80:83], v[48:51], %7, 0\n\t" \
  "v_mfma_f32_16x16x32_bf16 v[84:87], v[52:55], %7, 0\n\t" \
  "v_mfma_f32_16x16x32_bf16 v[88:91], v[48:51], %8, 0\n\t" \
  "v_mfma_f32_16x16x32_bf16 v[92:95], v[52:55], %8, 0\n\t" \
  "v_mfma_f32_16x16x32_bf16 v[96:99], v[56:59], %7, 0\n\t" \
  "v_mfma_f32_16x16x32_bf16 v[100:103], v[60:63], %7, 0\n\t" \
  "v_mfma_f32_16x16x32_bf16 v[40:43], v[56:59], %8, 0\n\t" \
  "v_mfma_f32_16x16x32_bf16 v[44:47], v[60:63], %8, 0\n\t" \
  "v_readlane_b32 s40, v30, 2\n\t" \
  "v_cvt_pk_bf16_f32 v64, v64, v65\n\t" \
  "v_cvt_pk_bf16_f32 v65, v66, v67\n\t" \
  "v_cvt_pk_bf16_f32 v66, v68, v69\n\t" \
  "v_cvt_pk_bf16_f32 v67, v70, v71\n\t" \
  "s_nop 0\n\t" \
  "buffer_store_dwordx4 v[64:67], %1, %11, s40 offen offset:0 sc1\n\t" \
  "v_cvt_pk_bf16_f32 v72, v72, v73\n\t" \
  "v_cvt_pk_bf16_f32 v73, v74, v75\n\t" \
  "v_cvt_pk_bf16_f32 v74, v76, v77\n\t" \
  "v_cvt_pk_bf16_f32 v75, v78, v79\n\t" \
  "s_nop 0\n\t" \
  "buffer_store_dwordx4 v[72:75], %1, %11, s40 offen offset:1024 sc1\n\t" \
  "v_readlane_b32 s40, v30, 5\n\t" \
  "v_cvt_pk_bf16_f32 v80, v80, v81\n\t" \
  "v_cvt_pk_bf16_f32 v81, v82, v83\n\t" \
  "v_cvt_pk_bf16_f32 v82, v84, v85\n\t" \
  "v_cvt_pk_bf16_f32 v83, v86, v87\n\t" \
  "s_nop 0\n\t" \
  "buffer_store_dwordx4 v[80:83], %1, %11, s40 offen offset:0 sc1\n\t" \
  "v_cvt_pk_bf16_f32 v88, v88, v89\n\t" \
  "v_cvt_pk_bf16_f32 v89, v90, v91\n\t" \
  "v_cvt_pk_bf16_f32 v90, v92, v93\n\t" \
  "v_cvt_pk_bf16_f32 v91, v94, v95\n\t" \
  "s_nop 0\n\t" \
  "buffer_store_dwordx4 v[88:91], %1, %11, s40 offen offset:1024 sc1\n\t" \
  "v_readlane_b32 s40, v30, 8\n\t" \
  "v_cvt_pk_bf16_f32 v96, v96, v97\n\t" \
  "v_cvt_pk_bf16_f32 v97, v98, v99\n\t" \
  "v_cvt_pk_bf16_f32 v98, v100, v101\n\t" \
  "v_cvt_pk_bf16_f32 v99, v102, v103\n\t" \
  "s_nop 0\n\t" \
  "buffer_store_dwordx4 v[96:99], %1, %11, s40 offen offset:0 sc1\n\t" \
  "v_cvt_pk_bf16_f32 v40, v40, v41\n\t" \
  "v_cvt_pk_bf16_f32 v41, v42, v43\n\t" \
  "v_cvt_pk_bf16_f32 v42, v44, v45\n\t" \
  "v_cvt_pk_bf16_f32 v43, v46, v47\n\t" \
  "s_nop 0\n\t" \
  "buffer_store_dwordx4 v[40:43], %1, %11, s40 offen offset:1024 sc1\n\t" \
  "s_branch MK_PLRK_%=\n\t" \
  "MK_KTK_%=:\n\t" \
  "s_waitcnt lgkmcnt(0)\n\t" \
  "s_waitcnt vmcnt(16)\n\t" \
  "s_barrier\n\t" \
  "v_cmp_ne_u32_e32 vcc, s90, v124\n\t" \
  "v_cmp_ne_u32_e64 s[48:49], s90, v125\n\t" \
  "v_cmp_ne_u32_e64 s[50:51], s90, v126\n\t" \
  "v_cmp_ne_u32_e64 s[52:53], s90, v127\n\t" \
  "s_or_b64 vcc, vcc, s[48:49]\n\t" \
  "s_or_b64 vcc, vcc, s[50:51]\n\t" \
  "s_or_b64 vcc, vcc, s[52:53]\n\t" \
  "s_cbranch_vccnz MK_F0KP_%=\n\t" \
  "MK_F0KR_%=:\n\t" \
  "v_mfma_f32_16x16x32_bf16 a[224:227], v[224:227], v[220:223], a[224:227]\n\t" \
  "ds_read_b128 v[156:159], v112 offset:14336\n\t" \
  "v_mfma_f32_16x16x32_bf16 a[228:231], v[228:231], v[220:223], a[228:231]\n\t" \
  "v_add_u32_e32 v34, s31, %1\n\t" \
  "s_mov_b32 m0, s26\n\t" \
  "v_mfma_f32_16x16x32_bf16 a[232:235], v[232:235], v[220:223], a[232:235]\n\t" \
  "ds_read_b64_tr_b16 v[160:161], v114 offset:0\n\t" \
  "v_mfma_f32_16x16x32_bf16 a[236:239], v[236:239], v[220:223], a[236:239]\n\t" \
  "buffer_load_dwordx4 v108, s[60:63], s28 offen offset:0 sc1 lds\n\t" \
  "v_mfma_f32_16x16x32_bf16 a[240:243], v[240:243], v[220:223], a[240:243]\n\t" \
  "ds_read_b64_tr_b16 v[162:163], v114 offset:2048\n\t" \
  "v_mfma_f32_16x16x32_bf16 a[244:247], v[244:247], v[220:223], a[244:247]\n\t" \
  "v_mfma_f32_16x16x32_bf16 a[248:251], v[248:251], v[220:223], a[248:251]\n\t" \
  "ds_read_b64_tr_b16 v[164:165], v115 offset:0\n\t" \
  "v_mfma_f32_16x16x32_bf16 a[252:255], v[252:255], v[220:223], a[252:255]\n\t" \
  "v_mfma_f32_16x16x32_bf16 a[192:195], v[224:227], v[216:219], a[192:195]\n\t" \
  "ds_read_b64_tr_b16 v[166:167], v115 offset:2048\n\t" \
  "buffer_load_dwordx4 v109, s[60:63], s28 offen offset:1024 sc1 lds\n\t" \
  "v_mfma_f32_16x16x32_bf16 a[196:199], v[228:231], v[216:219], a[196:199]\n\t" \
  "v_mfma_f32_16x16x32_bf16 a[200:203], v[232:235], v[216:219], a[200:203]\n\t" \
  "ds_read_b64_tr_b16 v[168:169], v116 offset:0\n\t" \
  "v_mfma_f32_16x16x32_bf16 a[204:207], v[236:239], v[216:219], a[204:207]\n\t" \
  "v_mfma_f32_16x16x32_bf16 a[208:211], v[240:243], v[216:219], a[208:211]\n\t" \
  "ds_read_b64_tr_b16 v[170:171], v116 offset:2048\n\t" \
  "v_mfma_f32_16x16x32_bf16 a[212:215], v[244:247], v[216:219], a[212:215]\n\t" \
  "buffer_load_dwordx4 v110, s[60:63], s28 offen offset:2048 sc1 lds\n\t" \
  "v_mfma_f32_16x16x32_bf16 a[216:219], v[248:251], v[216:219], a[216:219]\n\t" \
  "ds_read_b64_tr_b16 v[172:173], v117 offset:0\n\t" \
  "v_mfma_f32_16x16x32_bf16 a[220:223], v[252:255], v[216:219], a[220:223]\n\t" \
  "v_mfma_f32_16x16x32_bf16 a[160:163], v[224:227], v[212:215], a[160:163]\n\t" \
  "ds_read_b64_tr_b16 v[174:175], v117 offset:2048\n\t" \
  "v_mfma_f32_16x16x32_bf16 a[164:167], v[228:231], v[212:215], a[164:167]\n\t" \
  "v_mfma_f32_16x16x32_bf16 a[168:171], v[232:235], v[212:215], a[168:171]\n\t" \
  "ds_read_b64_tr_b16 v[176:177], v118 offset:0\n\t" \
  "buffer_load_dwordx4 v111, s[60:63], s28 offen offset:3072 sc1 lds\n\t" \
  "v_mfma_f32_16x16x32_bf16 a[172:175], v[236:239], v[212:215], a[172:175]\n\t" \
  "v_mfma_f32_16x16x32_bf16 a[176:179], v[240:243], v[212:215], a[176:179]\n\t" \
  "ds_read_b64_tr_b16 v[178:179], v118 offset:2048\n\t" \
  "v_mfma_f32_16x16x32_bf16 a[180:183], v[244:247], v[212:215], a[180:183]\n\t" \
  "s_add_u32 m0, s31, 98304\n\t" \
  "s_add_u32 s43, s68, 0\n\t" \
  "v_mfma_f32_16x16x32_bf16 a[184:187], v[248:251], v[212:215], a[184:187]\n\t" \
  "ds_read_b64_tr_b16 v[180:181], v119 offset:0\n\t" \
  "v_mfma_f32_16x16x32_bf16 a[188:191], v[252:255], v[212:215], a[188:191]\n\t" \
  "buffer_load_dwordx4 v34, %11, s43 offen offset:0 sc1 lds\n\t" \
  "v_mfma_f32_16x16x32_bf16 a[128:131], v[224:227], v[208:211], a[128:131]\n\t" \
  "ds_read_b64_tr_b16 v[182:183], v119 offset:2048\n\t" \
  "v_mfma_f32_16x16x32_bf16 a[132:135], v[228:231], v[208:211], a[132:135]\n\t" \
  "v_mfma_f32_16x16x32_bf16 a[136:139], v[232:235], v[208:211], a[136:139]\n\t" \
  "ds_read_b64_tr_b16 v[184:185], v120 offset:0\n\t" \
  "v_mfma_f32_16x16x32_bf16 a[140:143], v[236:239], v[208:211], a[140:143]\n\t" \
  "v_mfma_f32_16x16x32_bf16 a[144:147], v[240:243], v[208:211], a[144:147]\n\t" \
  "ds_read_b64_tr_b16 v[186:187], v120 offset:2048\n\t" \
  "buffer_load_dwordx4 v34, %11, s43 offen offset:1024 sc1 lds\n\t" \
  "v_mfma_f32_16x16x32_bf16 a[148:151], v[244:247], v[208:211], a[148:151]\n\t" \
  "v_mfma_f32_16x16x32_bf16 a[152:155], v[248:251], v[208:211], a[152:155]\n\t" \
  "ds_read_b64_tr_b16 v[188:189], v121 offset:0\n\t" \
  "v_mfma_f32_16x16x32_bf16 a[156:159], v[252:255], v[208:211], a[156:159]\n\t" \
  "v_mfma_f32_16x16x32_bf16 a[96:99], v[224:227], v[204:207], a[96:99]\n\t" \
  "ds_read_b64_tr_b16 v[190:191], v121 offset:2048\n\t" \
  "v_mfma_f32_16x16x32_bf16 a[100:103], v[228:231], v[204:207], a[100:103]\n\t" \
  "buffer_load_dwordx4 v34, %11, s43 offen offset:2048 sc1 lds\n\t" \
  "v_mfma_f32_16x16x32_bf16 a[104:107], v[232:235], v[204:207], a[104:107]\n\t" \
  "ds_read_b128 v[152:155], v112 offset:12288\n\t" \
  "v_mfma_f32_16x16x32_bf16 a[108:111], v[236:239], v[204:207], a[108:111]\n\t" \
  "v_mfma_f32_16x16x32_bf16 a[112:115], v[240:243], v[204:207], a[112:115]\n\t" \
  "ds_read_b128 v[148:151], v112 offset:10240\n\t" \
  "v_mfma_f32_16x16x32_bf16 a[116:119], v[244:247], v[204:207], a[116:119]\n\t" \
  "v_mfma_f32_16x16x32_bf16 a[120:123], v[248:251], v[204:207], a[120:123]\n\t" \
  "ds_read_b128 v[144:147], v112 offset:8192\n\t" \
  "buffer_load_dwordx4 v34, %11, s43 offen offset:3072 sc1 lds\n\t" \
  "v_mfma_f32_16x16x32_bf16 a[124:127], v[252:255], v[204:207], a[124:127]\n\t" \
  "v_mfma_f32_16x16x32_bf16 a[64:67], v[224:227], v[200:203], a[64:67]\n\t" \
  "ds_read_b128 v[140:143], v112 offset:6144\n\t" \
  "v_mfma_f32_16x16x32_bf16 a[68:71], v[228:231], v[200:203], a[68:71]\n\t" \
  "v_mfma_f32_16x16x32_bf16 a[72:75], v[232:235], v[200:203], a[72:75]\n\t" \
  "ds_read_b128 v[136:139], v112 offset:4096\n\t" \
  "v_mfma_f32_16x16x32_bf16 a[76:79], v[236:239], v[200:203], a[76:79]\n\t" \
  "buffer_load_dwordx4 v[40:43], %17, %11, s72 offen offset:0 sc1\n\t" \
  "v_mfma_f32_16x16x32_bf16 a[80:83], v[240:243], v[200:203], a[80:83]\n\t" \
  "ds_read_b128 v[132:135], v112 offset:2048\n\t" \
  "v_mfma_f32_16x16x32_bf16 a[84:87], v[244:247], v[200:203], a[84:87]\n\t" \
  "v_mfma_f32_16x16x32_bf16 a[88:91], v[248:251], v[200:203], a[88:91]\n\t" \
  "ds_read_b128 v[128:131], v112 offset:0\n\t" \
  "v_mfma_f32_16x16x32_bf16 a[92:95], v[252:255], v[200:203], a[92:95]\n\t" \
  "v_mfma_f32_16x16x32_bf16 a[32:35], v[224:227], v[196:199], a[32:35]\n\t" \
  "buffer_load_dwordx4 v[44:47], %17, %11, s76 offen offset:0 sc1\n\t" \
  "v_mfma_f32_16x16x32_bf16 a[36:39], v[228:231], v[196:199], a[36:39]\n\t" \
  "v_mfma_f32_16x16x32_bf16 a[40:43], v[232:235], v[196:199], a[40:43]\n\t" \
  "v_mfma_f32_16x16x32_bf16 a[44:47], v[236:239], v[196:199], a[44:47]\n\t" \
  "v_mfma_f32_16x16x32_bf16 a[48:51], v[240:243], v[196:199], a[48:51]\n\t" \
  "v_mfma_f32_16x16x32_bf16 a[52:55], v[244:247], v[196:199], a[52:55]\n\t" \
  "buffer_load_dwordx4 v[48:51], %17, %11, s72 offen offset:1024 sc1\n\t" \
  "v_mfma_f32_16x16x32_bf16 a[56:59], v[248:251], v[196:199], a[56:59]\n\t" \
  "v_mfma_f32_16x16x32_bf16 a[60:63], v[252:255], v[196:199], a[60:63]\n\t" \
  "v_mfma_f32_16x16x32_bf16 a[0:3], v[224:227], v[192:195], a[0:3]\n\t" \
  "v_mfma_f32_16x16x32_bf16 a[4:7], v[228:231], v[192:195], a[4:7]\n\t" \
  "v_mfma_f32_16x16x32_bf16 a[8:11], v[232:235], v[192:195], a[8:11]\n\t" \
  "buffer_load_dwordx4 v[52:55], %17, %11, s76 offen offset:1024 sc1\n\t" \
  "v_mfma_f32_16x16x32_bf16 a[12:15], v[236:239], v[192:195], a[12:15]\n\t" \
  "v_mfma_f32_16x16x32_bf16 a[16:19], v[240:243], v[192:195], a[16:19]\n\t" \
  "v_mfma_f32_16x16x32_bf16 a[20:23], v[244:247], v[192:195], a[20:23]\n\t" \
  "v_mfma_f32_16x16x32_bf16 a[24:27], v[248:251], v[192:195], a[24:27]\n\t" \
  "v_mfma_f32_16x16x32_bf16 a[28:31], v[252:255], v[192:195], a[28:31]\n\t" \
  "s_waitcnt lgkmcnt(0)\n\t" \
  "s_waitcnt vmcnt(20)\n\t" \
  "s_barrier\n\t" \
  "v_mfma_f32_16x16x32_bf16 a[224:227], v[160:163], v[156:159], a[224:227]\n\t" \
  "ds_read_b128 v[220:223], v113 offset:14336\n\t" \
  "v_mfma_f32_16x16x32_bf16 a[228:231], v[164:167], v[156:159], a[228:231]\n\t" \
  "s_add_u32 m0, s31, 114688\n\t" \
  "s_add_u32 s43, s68, 16384\n\t" \
  "v_mfma_f32_16x16x32_bf16 a[232:235], v[168:171], v[156:159], a[232:235]\n\t" \
  "ds_read_b64_tr_b16 v[224:225], v114 offset:16384\n\t" \
  "v_mfma_f32_16x16x32_bf16 a[236:239], v[172:175], v[156:159], a[236:239]\n\t" \
  "buffer_load_dwordx4 v34, %11, s43 offen offset:0 sc1 lds\n\t" \
  "v_mfma_f32_16x16x32_bf16 a[240:243], v[176:179], v[156:159], a[240:243]\n\t" \
  "ds_read_b64_tr_b16 v[226:227], v114 offset:18432\n\t" \
  "v_mfma_f32_16x16x32_bf16 a[244:247], v[180:183], v[156:159], a[244:247]\n\t" \
  "v_mfma_f32_16x16x32_bf16 a[248:251], v[184:187], v[156:159], a[248:251]\n\t" \
  "ds_read_b64_tr_b16 v[228:229], v115 offset:16384\n\t" \
  "v_mfma_f32_16x16x32_bf16 a[252:255], v[188:191], v[156:159], a[252:255]\n\t" \
  "v_mfma_f32_16x16x32_bf16 a[192:195], v[160:163], v[152:155], a[192:195]\n\t" \
  "ds_read_b64_tr_b16 v[230:231], v115 offset:18432\n\t" \
  "buffer_load_dwordx4 v34, %11, s43 offen offset:1024 sc1 lds\n\t" \
  "v_mfma_f32_16x16x32_bf16 a[196:199], v[164:167], v[152:155], a[196:199]\n\t" \
  "v_mfma_f32_16x16x32_bf16 a[200:203], v[168:171], v[152:155], a[200:203]\n\t" \
  "ds_read_b64_tr_b16 v[232:233], v116 offset:16384\n\t" \
  "v_mfma_f32_16x16x32_bf16 a[204:207], v[172:175], v[152:155], a[204:207]\n\t" \
  "v_mfma_f32_16x16x32_bf16 a[208:211], v[176:179], v[152:155], a[208:211]\n\t" \
  "ds_read_b64_tr_b16 v[234:235], v116 offset:18432\n\t" \
  "v_mfma_f32_16x16x32_bf16 a[212:215], v[180:183], v[152:155], a[212:215]\n\t" \
  "buffer_load_dwordx4 v34, %11, s43 offen offset:2048 sc1 lds\n\t" \
  "v_mfma_f32_16x16x32_bf16 a[216:219], v[184:187], v[152:155], a[216:219]\n\t" \
  "ds_read_b64_tr_b16 v[236:237], v117 offset:16384\n\t" \
  "v_mfma_f32_16x16x32_bf16 a[220:223], v[188:191], v[152:155], a[220:223]\n\t" \
  "v_mfma_f32_16x16x32_bf16 a[160:163], v[160:163], v[148:151], a[160:163]\n\t" \
  "ds_read_b64_tr_b16 v[238:239], v117 offset:18432\n\t" \
  "v_mfma_f32_16x16x32_bf16 a[164:167], v[164:167], v[148:151], a[164:167]\n\t" \
  "v_mfma_f32_16x16x32_bf16 a[168:171], v[168:171], v[148:151], a[168:171]\n\t" \
  "ds_read_b64_tr_b16 v[240:241], v118 offset:16384\n\t" \
  "buffer_load_dwordx4 v34, %11, s43 offen offset:3072 sc1 lds\n\t" \
  "v_mfma_f32_16x16x32_bf16 a[172:175], v[172:175], v[148:151], a[172:175]\n\t" \
  "v_mfma_f32_16x16x32_bf16 a[176:179], v[176:179], v[148:151], a[176:179]\n\t" \
  "ds_read_b64_tr_b16 v[242:243], v118 offset:18432\n\t" \
  "v_mfma_f32_16x16x32_bf16 a[180:183], v[180:183], v[148:151], a[180:183]\n\t" \
  "s_add_u32 m0, s31, 0\n\t" \
  "s_add_u32 s43, s68, 65536\n\t" \
  "v_mfma_f32_16x16x32_bf16 a[184:187], v[184:187], v[148:151], a[184:187]\n\t" \
  "ds_read_b64_tr_b16 v[244:245], v119 offset:16384\n\t" \
  "v_mfma_f32_16x16x32_bf16 a[188:191], v[188:191], v[148:151], a[188:191]\n\t" \
  "buffer_load_dwordx4 v34, %11, s43 offen offset:0 sc1 lds\n\t" \
  "v_mfma_f32_16x16x32_bf16 a[128:131], v[160:163], v[144:147], a[128:131]\n\t" \
  "ds_read_b64_tr_b16 v[246:247], v119 offset:18432\n\t" \
  "v_mfma_f32_16x16x32_bf16 a[132:135], v[164:167], v[144:147], a[132:135]\n\t" \
  "v_mfma_f32_16x16x32_bf16 a[136:139], v[168:171], v[144:147], a[136:139]\n\t" \
  "ds_read_b64_tr_b16 v[248:249], v120 offset:16384\n\t" \
  "v_mfma_f32_16x16x32_bf16 a[140:143], v[172:175], v[144:147], a[140:143]\n\t" \
  "v_mfma_f32_16x16x32_bf16 a[144:147], v[176:179], v[144:147], a[144:147]\n\t" \
  "ds_read_b64_tr_b16 v[250:251], v120 offset:18432\n\t" \
  "buffer_load_dwordx4 v34, %11, s43 offen offset:1024 sc1 lds\n\t" \
  "v_mfma_f32_16x16x32_bf16 a[148:151], v[180:183], v[144:147], a[148:151]\n\t" \
  "v_mfma_f32_16x16x32_bf16 a[152:155], v[184:187], v[144:147], a[152:155]\n\t" \
  "ds_read_b64_tr_b16 v[252:253], v121 offset:16384\n\t" \
  "v_mfma_f32_16x16x32_bf16 a[156:159], v[188:191], v[144:147], a[156:159]\n\t" \
  "v_mfma_f32_16x16x32_bf16 a[96:99], v[160:163], v[140:143], a[96:99]\n\t" \
  "ds_read_b64_tr_b16 v[254:255], v121 offset:18432\n\t" \
  "v_mfma_f32_16x16x32_bf16 a[100:103], v[164:167], v[140:143], a[100:103]\n\t" \
  "buffer_load_dwordx4 v34, %11, s43 offen offset:2048 sc1 lds\n\t" \
  "v_mfma_f32_16x16x32_bf16 a[104:107], v[168:171], v[140:143], a[104:107]\n\t" \
  "ds_read_b128 v[216:219], v113 offset:12288\n\t" \
  "v_mfma_f32_16x16x32_bf16 a[108:111], v[172:175], v[140:143], a[108:111]\n\t" \
  "v_mfma_f32_16x16x32_bf16 a[112:115], v[176:179], v[140:143], a[112:115]\n\t" \
  "ds_read_b128 v[212:215], v113 offset:10240\n\t" \
  "v_mfma_f32_16x16x32_bf16 a[116:119], v[180:183], v[140:143], a[116:119]\n\t" \
  "v_mfma_f32_16x16x32_bf16 a[120:123], v[184:187], v[140:143], a[120:123]\n\t" \
  "ds_read_b128 v[208:211], v113 offset:8192\n\t" \
  "buffer_load_dwordx4 v34, %11, s43 offen offset:3072 sc1 lds\n\t" \
  "v_mfma_f32_16x16x32_bf16 a[124:127], v[188:191], v[140:143], a[124:127]\n\t" \
  "v_mfma_f32_16x16x32_bf16 a[64:67], v[160:163], v[136:139], a[64:67]\n\t" \
  "ds_read_b128 v[204:207], v113 offset:6144\n\t" \
  "v_mfma_f32_16x16x32_bf16 a[68:71], v[164:167], v[136:139], a[68:71]\n\t" \
  "v_mfma_f32_16x16x32_bf16 a[72:75], v[168:171], v[136:139], a[72:75]\n\t" \
  "ds_read_b128 v[200:203], v113 offset:4096\n\t" \
  "v_mfma_f32_16x16x32_bf16 a[76:79], v[172:175], v[136:139], a[76:79]\n\t" \
  "buffer_load_dwordx4 v[56:59], %17, %11, s72 offen offset:2048 sc1\n\t" \
  "v_mfma_f32_16x16x32_bf16 a[80:83], v[176:179], v[136:139], a[80:83]\n\t" \
  "ds_read_b128 v[196:199], v113 offset:2048\n\t" \
  "v_mfma_f32_16x16x32_bf16 a[84:87], v[180:183], v[136:139], a[84:87]\n\t" \
  "v_mfma_f32_16x16x32_bf16 a[88:91], v[184:187], v[136:139], a[88:91]\n\t" \
  "ds_read_b128 v[192:195], v113 offset:0\n\t" \
  "v_mfma_f32_16x16x32_bf16 a[92:95], v[188:191], v[136:139], a[92:95]\n\t" \
  "v_mfma_f32_16x16x32_bf16 a[32:35], v[160:163], v[132:135], a[32:35]\n\t" \
  "buffer_load_dwordx4 v[60:63], %17, %11, s76 offen offset:2048 sc1\n\t" \
  "v_add_u32_e32 v112, 4294901760, v112\n\t" \
  "v_mfma_f32_16x16x32_bf16 a[36:39], v[164:167], v[132:135], a[36:39]\n\t" \
  "v_add_u32_e32 v113, 4294901760, v113\n\t" \
  "v_mfma_f32_16x16x32_bf16 a[40:43], v[168:171], v[132:135], a[40:43]\n\t" \
  "v_mfma_f32_16x16x32_bf16 a[44:47], v[172:175], v[132:135], a[44:47]\n\t" \
  "v_xor_b32_e32 v114, 0x8000, v114\n\t" \
  "v_mfma_f32_16x16x32_bf16 a[48:51], v[176:179], v[132:135], a[48:51]\n\t" \
  "v_xor_b32_e32 v115, 0x8000, v115\n\t" \
  "v_mfma_f32_16x16x32_bf16 a[52:55], v[180:183], v[132:135], a[52:55]\n\t" \
  "buffer_load_dwordx4 v[64:67], %17, %11, s72 offen offset:3072 sc1\n\t" \
  "v_mfma_f32_16x16x32_bf16 a[56:59], v[184:187], v[132:135], a[56:59]\n\t" \
  "v_xor_b32_e32 v116, 0x8000, v116\n\t" \
  "v_mfma_f32_16x16x32_bf16 a[60:63], v[188:191], v[132:135], a[60:63]\n\t" \
  "v_mfma_f32_16x16x32_bf16 a[0:3], v[160:163], v[128:131], a[0:3]\n\t" \
  "v_xor_b32_e32 v117, 0x8000, v117\n\t" \
  "v_mfma_f32_16x16x32_bf16 a[4:7], v[164:167], v[128:131], a[4:7]\n\t" \
  "v_xor_b32_e32 v118, 0x8000, v118\n\t" \
  "v_mfma_f32_16x16x32_bf16 a[8:11], v[168:171], v[128:131], a[8:11]\n\t" \
  "buffer_load_dwordx4 v[68:71], %17, %11, s76 offen offset:3072 sc1\n\t" \
  "v_mfma_f32_16x16x32_bf16 a[12:15], v[172:175], v[128:131], a[12:15]\n\t" \
  "v_xor_b32_e32 v119, 0x8000, v119\n\t" \
  "v_mfma_f32_16x16x32_bf16 a[16:19], v[176:179], v[128:131], a[16:19]\n\t" \
  "v_xor_b32_e32 v120, 0x8000, v120\n\t" \
  "v_mfma_f32_16x16x32_bf16 a[20:23], v[180:183], v[128:131], a[20:23]\n\t" \
  "v_mfma_f32_16x16x32_bf16 a[24:27], v[184:187], v[128:131], a[24:27]\n\t" \
  "v_xor_b32_e32 v121, 0x8000, v121\n\t" \
  "v_mfma_f32_16x16x32_bf16 a[28:31], v[188:191], v[128:131], a[28:31]\n\t" \
  "s_waitcnt lgkmcnt(0)\n\t" \
  "s_waitcnt vmcnt(24)\n\t" \
  "s_barrier\n\t" \
  "v_mfma_f32_16x16x32_bf16 a[224:227], v[224:227], v[220:223], a[224:227]\n\t" \
  "ds_read_b128 v[156:159], v112 offset:14336\n\t" \
  "v_mfma_f32_16x16x32_bf16 a[228:231], v[228:231], v[220:223], a[228:231]\n\t" \
  "s_add_u32 m0, s31, 131072\n\t" \
  "s_add_u32 s43, s68, 32768\n\t" \
  "v_mfma_f32_16x16x32_bf16 a[232:235], v[232:235], v[220:223], a[232:235]\n\t" \
  "ds_read_b64_tr_b16 v[160:161], v114 offset:0\n\t" \
  "v_mfma_f32_16x16x32_bf16 a[236:239], v[236:239], v[220:223], a[236:239]\n\t" \
  "buffer_load_dwordx4 v34, %11, s43 offen offset:0 sc1 lds\n\t" \
  "v_mfma_f32_16x16x32_bf16 a[240:243], v[240:243], v[220:223], a[240:243]\n\t" \
  "ds_read_b64_tr_b16 v[162:163], v114 offset:2048\n\t" \
  "v_mfma_f32_16x16x32_bf16 a[244:247], v[244:247], v[220:223], a[244:247]\n\t" \
  "v_readlane_b32 s44, v29, 35\n\t" \
  "v_mfma_f32_16x16x32_bf16 a[248:251], v[248:251], v[220:223], a[248:251]\n\t" \
  "ds_read_b64_tr_b16 v[164:165], v115 offset:0\n\t" \
  "v_mfma_f32_16x16x32_bf16 a[252:255], v[252:255], v[220:223], a[252:255]\n\t" \
  "v_readlane_b32 s69, v29, 36\n\t" \
  "v_mfma_f32_16x16x32_bf16 a[192:195], v[224:227], v[216:219], a[192:195]\n\t" \
  "ds_read_b64_tr_b16 v[166:167], v115 offset:2048\n\t" \
  "buffer_load_dwordx4 v34, %11, s43 offen offset:1024 sc1 lds\n\t" \
  "v_mfma_f32_16x16x32_bf16 a[196:199], v[228:231], v[216:219], a[196:199]\n\t" \
  "v_mfma_f32_16x16x32_bf16 a[200:203], v[232:235], v[216:219], a[200:203]\n\t" \
  "ds_read_b64_tr_b16 v[168:169], v116 offset:0\n\t" \
  "v_readlane_b32 s70, v29, 37\n\t" \
  "v_mfma_f32_16x16x32_bf16 a[204:207], v[236:239], v[216:219], a[204:207]\n\t" \
  "v_mfma_f32_16x16x32_bf16 a[208:211], v[240:243], v[216:219], a[208:211]\n\t" \
  "ds_read_b64_tr_b16 v[170:171], v116 offset:2048\n\t" \
  "v_mfma_f32_16x16x32_bf16 a[212:215], v[244:247], v[216:219], a[212:215]\n\t" \
  "buffer_load_dwordx4 v34, %11, s43 offen offset:2048 sc1 lds\n\t" \
  "v_mfma_f32_16x16x32_bf16 a[216:219], v[248:251], v[216:219], a[216:219]\n\t" \
  "ds_read_b64_tr_b16 v[172:173], v117 offset:0\n\t" \
  "s_mul_i32 s34, s69, 0\n\t" \
  "v_mfma_f32_16x16x32_bf16 a[220:223], v[252:255], v[216:219], a[220:223]\n\t" \
  "s_add_u32 s34, s34, s44\n\t" \
  "v_mfma_f32_16x16x32_bf16 a[160:163], v[224:227], v[212:215], a[160:163]\n\t" \
  "ds_read_b64_tr_b16 v[174:175], v117 offset:2048\n\t" \
  "v_mfma_f32_16x16x32_bf16 a[164:167], v[228:231], v[212:215], a[164:167]\n\t" \
  "s_mul_i32 s35, s69, 1\n\t" \
  "v_mfma_f32_16x16x32_bf16 a[168:171], v[232:235], v[212:215], a[168:171]\n\t" \
  "ds_read_b64_tr_b16 v[176:177], v118 offset:0\n\t" \
  "buffer_load_dwordx4 v34, %11, s43 offen offset:3072 sc1 lds\n\t" \
  "v_mfma_f32_16x16x32_bf16 a[172:175], v[236:239], v[212:215], a[172:175]\n\t" \
  "v_mfma_f32_16x16x32_bf16 a[176:179], v[240:243], v[212:215], a[176:179]\n\t" \
  "ds_read_b64_tr_b16 v[178:179], v118 offset:2048\n\t" \
  "s_add_u32 s35, s35, s44\n\t" \
  "v_mfma_f32_16x16x32_bf16 a[180:183], v[244:247], v[212:215], a[180:183]\n\t" \
  "s_add_u32 m0, s31, 16384\n\t" \
  "s_add_u32 s43, s68, 81920\n\t" \
  "v_mfma_f32_16x16x32_bf16 a[184:187], v[248:251], v[212:215], a[184:187]\n\t" \
  "ds_read_b64_tr_b16 v[180:181], v119 offset:0\n\t" \
  "v_mfma_f32_16x16x32_bf16 a[188:191], v[252:255], v[212:215], a[188:191]\n\t" \
  "buffer_load_dwordx4 v34, %11, s43 offen offset:0 sc1 lds\n\t" \
  "v_mfma_f32_16x16x32_bf16 a[128:131], v[224:227], v[208:211], a[128:131]\n\t" \
  "ds_read_b64_tr_b16 v[182:183], v119 offset:2048\n\t" \
  "s_mul_i32 s36, s69, 2\n\t" \
  "v_mfma_f32_16x16x32_bf16 a[132:135], v[228:231], v[208:211], a[132:135]\n\t" \
  "v_mfma_f32_16x16x32_bf16 a[136:139], v[232:235], v[208:211], a[136:139]\n\t" \
  "ds_read_b64_tr_b16 v[184:185], v120 offset:0\n\t" \
  "s_add_u32 s36, s36, s44\n\t" \
  "v_mfma_f32_16x16x32_bf16 a[140:143], v[236:239], v[208:211], a[140:143]\n\t" \
  "v_mfma_f32_16x16x32_bf16 a[144:147], v[240:243], v[208:211], a[144:147]\n\t" \
  "ds_read_b64_tr_b16 v[186:187], v120 offset:2048\n\t" \
  "buffer_load_dwordx4 v34, %11, s43 offen offset:1024 sc1 lds\n\t" \
  "v_mfma_f32_16x16x32_bf16 a[148:151], v[244:247], v[208:211], a[148:151]\n\t" \
  "s_mul_i32 s37, s69, 3\n\t" \
  "v_mfma_f32_16x16x32_bf16 a[152:155], v[248:251], v[208:211], a[152:155]\n\t" \
  "ds_read_b64_tr_b16 v[188:189], v121 offset:0\n\t" \
  "v_mfma_f32_16x16x32_bf16 a[156:159], v[252:255], v[208:211], a[156:159]\n\t" \
  "s_add_u32 s37, s37, s44\n\t" \
  "v_mfma_f32_16x16x32_bf16 a[96:99], v[224:227], v[204:207], a[96:99]\n\t" \
  "ds_read_b64_tr_b16 v[190:191], v121 offset:2048\n\t" \
  "v_mfma_f32_16x16x32_bf16 a[100:103], v[228:231], v[204:207], a[100:103]\n\t" \
  "buffer_load_dwordx4 v34, %11, s43 offen offset:2048 sc1 lds\n\t" \
  "s_mul_i32 s38, s69, 4\n\t" \
  "v_mfma_f32_16x16x32_bf16 a[104:107], v[232:235], v[204:207], a[104:107]\n\t" \
  "ds_read_b128 v[152:155], v112 offset:12288\n\t" \
  "v_mfma_f32_16x16x32_bf16 a[108:111], v[236:239], v[204:207], a[108:111]\n\t" \
  "s_add_u32 s38, s38, s44\n\t" \
  "v_mfma_f32_16x16x32_bf16 a[112:115], v[240:243], v[204:207], a[112:115]\n\t" \
  "ds_read_b128 v[148:151], v112 offset:10240\n\t" \
  "v_mfma_f32_16x16x32_bf16 a[116:119], v[244:247], v[204:207], a[116:119]\n\t" \
  "s_mul_i32 s39, s69, 5\n\t" \
  "v_mfma_f32_16x16x32_bf16 a[120:123], v[248:251], v[204:207], a[120:123]\n\t" \
  "ds_read_b128 v[144:147], v112 offset:8192\n\t" \
  "buffer_load_dwordx4 v34, %11, s43 offen offset:3072 sc1 lds\n\t" \
  "v_mfma_f32_16x16x32_bf16 a[124:127], v[252:255], v[204:207], a[124:127]\n\t" \
  "s_add_u32 s39, s39, s44\n\t" \
  "v_mfma_f32_16x16x32_bf16 a[64:67], v[224:227], v[200:203], a[64:67]\n\t" \
  "ds_read_b128 v[140:143], v112 offset:6144\n\t" \
  "v_mfma_f32_16x16x32_bf16 a[68:71], v[228:231], v[200:203], a[68:71]\n\t" \
  "s_mul_i32 s40, s69, 6\n\t" \
  "v_mfma_f32_16x16x32_bf16 a[72:75], v[232:235], v[200:203], a[72:75]\n\t" \
  "ds_read_b128 v[136:139], v112 offset:4096\n\t" \
  "v_mfma_f32_16x16x32_bf16 a[76:79], v[236:239], v[200:203], a[76:79]\n\t" \
  "buffer_load_dwordx4 v[72:75], %17, %11, s73 offen offset:0 sc1\n\t" \
  "v_mfma_f32_16x16x32_bf16 a[80:83], v[240:243], v[200:203], a[80:83]\n\t" \
  "ds_read_b128 v[132:135], v112 offset:2048\n\t" \
  "s_add_u32 s40, s40, s44\n\t" \
  "v_mfma_f32_16x16x32_bf16 a[84:87], v[244:247], v[200:203], a[84:87]\n\t" \
  "v_mfma_f32_16x16x32_bf16 a[88:91], v[248:251], v[200:203], a[88:91]\n\t" \
  "ds_read_b128 v[128:131], v112 offset:0\n\t" \
  "s_mul_i32 s41, s69, 7\n\t" \
  "v_mfma_f32_16x16x32_bf16 a[92:95], v[252:255], v[200:203], a[92:95]\n\t" \
  "v_mfma_f32_16x16x32_bf16 a[32:35], v[224:227], v[196:199], a[32:35]\n\t" \
  "buffer_load_dwordx4 v[76:79], %17, %11, s77 offen offset:0 sc1\n\t" \
  "s_add_u32 s41, s41, s44\n\t" \
  "v_mfma_f32_16x16x32_bf16 a[36:39], v[228:231], v[196:199], a[36:39]\n\t" \
  "v_mfma_f32_16x16x32_bf16 a[40:43], v[232:235], v[196:199], a[40:43]\n\t" \
  "s_mul_i32 s42, s70, 1\n\t" \
  "v_mfma_f32_16x16x32_bf16 a[44:47], v[236:239], v[196:199], a[44:47]\n\t" \
  "v_add_u32_e32 v35, s42, %1\n\t" \
  "v_mfma_f32_16x16x32_bf16 a[48:51], v[240:243], v[196:199], a[48:51]\n\t" \
  "s_mul_i32 s42, s70, 2\n\t" \
  "v_mfma_f32_16x16x32_bf16 a[52:55], v[244:247], v[196:199], a[52:55]\n\t" \
  "buffer_load_dwordx4 v[80:83], %17, %11, s73 offen offset:1024 sc1\n\t" \
  "v_mfma_f32_16x16x32_bf16 a[56:59], v[248:251], v[196:199], a[56:59]\n\t" \
  "v_add_u32_e32 v36, s42, %1\n\t" \
  "v_mfma_f32_16x16x32_bf16 a[60:63], v[252:255], v[196:199], a[60:63]\n\t" \
  "v_mfma_f32_16x16x32_bf16 a[0:3], v[224:227], v[192:195], a[0:3]\n\t" \
  "s_mul_i32 s42, s70, 3\n\t" \
  "v_mfma_f32_16x16x32_bf16 a[4:7], v[228:231], v[192:195], a[4:7]\n\t" \
  "v_add_u32_e32 v38, s42, %1\n\t" \
  "v_mfma_f32_16x16x32_bf16 a[8:11], v[232:235], v[192:195], a[8:11]\n\t" \
  "buffer_load_dwordx4 v[84:87], %17, %11, s77 offen offset:1024 sc1\n\t" \
  "v_mfma_f32_16x16x32_bf16 a[12:15], v[236:239], v[192:195], a[12:15]\n\t" \
  "v_mov_b32_e32 v37, 0xff800000\n\t" \
  "v_mfma_f32_16x16x32_bf16 a[16:19], v[240:243], v[192:195], a[16:19]\n\t" \
  "v_mfma_f32_16x16x32_bf16 a[20:23], v[244:247], v[192:195], a[20:23]\n\t" \
  "v_add_u32_e32 v32, 98304, %1\n\t" \
  "v_mfma_f32_16x16x32_bf16 a[24:27], v[248:251], v[192:195], a[24:27]\n\t" \
  "s_mov_b32 s28, 98304\n\t" \
  "v_mfma_f32_16x16x32_bf16 a[28:31], v[252:255], v[192:195], a[28:31]\n\t" \
  "s_waitcnt lgkmcnt(0)\n\t" \
  "s_waitcnt vmcnt(32)\n\t" \
  "s_barrier\n\t" \
  "v_mfma_f32_16x16x32_bf16 a[224:227], v[160:163], v[156:159], a[224:227]\n\t" \
  "ds_read_b128 v[220:223], v113 offset:14336\n\t" \
  "v_mfma_f32_16x16x32_bf16 a[228:231], v[164:167], v[156:159], a[228:231]\n\t" \
  "s_add_u32 m0, s31, 147456\n\t" \
  "s_add_u32 s43, s68, 49152\n\t" \
  "v_mfma_f32_16x16x32_bf16 a[232:235], v[168:171], v[156:159], a[232:235]\n\t" \
  "ds_read_b64_tr_b16 v[224:225], v114 offset:16384\n\t" \
  "v_mfma_f32_16x16x32_bf16 a[236:239], v[172:175], v[156:159], a[236:239]\n\t" \
  "buffer_load_dwordx4 v34, %11, s43 offen offset:0 sc1 lds\n\t" \
  "v_mfma_f32_16x16x32_bf16 a[240:243], v[176:179], v[156:159], a[240:243]\n\t" \
  "ds_read_b64_tr_b16 v[226:227], v114 offset:18432\n\t" \
  "v_mfma_f32_16x16x32_bf16 a[244:247], v[180:183], v[156:159], a[244:247]\n\t" \
  "v_mfma_f32_16x16x32_bf16 a[248:251], v[184:187], v[156:159], a[248:251]\n\t" \
  "ds_read_b64_tr_b16 v[228:229], v115 offset:16384\n\t" \
  "v_mfma_f32_16x16x32_bf16 a[252:255], v[188:191], v[156:159], a[252:255]\n\t" \
  "v_mfma_f32_16x16x32_bf16 a[192:195], v[160:163], v[152:155], a[192:195]\n\t" \
  "ds_read_b64_tr_b16 v[230:231], v115 offset:18432\n\t" \
  "buffer_load_dwordx4 v34, %11, s43 offen offset:1024 sc1 lds\n\t" \
  "v_mfma_f32_16x16x32_bf16 a[196:199], v[164:167], v[152:155], a[196:199]\n\t" \
  "v_mfma_f32_16x16x32_bf16 a[200:203], v[168:171], v[152:155], a[200:203]\n\t" \
  "ds_read_b64_tr_b16 v[232:233], v116 offset:16384\n\t" \
  "v_mfma_f32_16x16x32_bf16 a[204:207], v[172:175], v[152:155], a[204:207]\n\t" \
  "v_mfma_f32_16x16x32_bf16 a[208:211], v[176:179], v[152:155], a[208:211]\n\t" \
  "ds_read_b64_tr_b16 v[234:235], v116 offset:18432\n\t" \
  "v_mfma_f32_16x16x32_bf16 a[212:215], v[180:183], v[152:155], a[212:215]\n\t" \
  "buffer_load_dwordx4 v34, %11, s43 offen offset:2048 sc1 lds\n\t" \
  "v_mfma_f32_16x16x32_bf16 a[216:219], v[184:187], v[152:155], a[216:219]\n\t" \
  "ds_read_b64_tr_b16 v[236:237], v117 offset:16384\n\t" \
  "v_mfma_f32_16x16x32_bf16 a[220:223], v[188:191], v[152:155], a[220:223]\n\t" \
  "v_mfma_f32_16x16x32_bf16 a[160:163], v[160:163], v[148:151], a[160:163]\n\t" \
  "ds_read_b64_tr_b16 v[238:239], v117 offset:18432\n\t" \
  "v_mfma_f32_16x16x32_bf16 a[164:167], v[164:167], v[148:151], a[164:167]\n\t" \
  "v_mfma_f32_16x16x32_bf16 a[168:171], v[168:171], v[148:151], a[168:171]\n\t" \
  "ds_read_b64_tr_b16 v[240:241], v118 offset:16384\n\t" \
  "buffer_load_dwordx4 v34, %11, s43 offen offset:3072 sc1 lds\n\t" \
  "v_mfma_f32_16x16x32_bf16 a[172:175], v[172:175], v[148:151], a[172:175]\n\t" \
  "v_mfma_f32_16x16x32_bf16 a[176:179], v[176:179], v[148:151], a[176:179]\n\t" \
  "ds_read_b64_tr_b16 v[242:243], v118 offset:18432\n\t" \
  "v_mfma_f32_16x16x32_bf16 a[180:183], v[180:183], v[148:151], a[180:183]\n\t" \
  "s_add_u32 m0, s31, 32768\n\t" \
  "s_add_u32 s43, s68, 98304\n\t" \
  "v_mfma_f32_16x16x32_bf16 a[184:187], v[184:187], v[148:151], a[184:187]\n\t" \
  "ds_read_b64_tr_b16 v[244:245], v119 offset:16384\n\t" \
  "v_mfma_f32_16x16x32_bf16 a[188:191], v[188:191], v[148:151], a[188:191]\n\t" \
  "buffer_load_dwordx4 v34, %11, s43 offen offset:0 sc1 lds\n\t" \
  "v_mfma_f32_16x16x32_bf16 a[128:131], v[160:163], v[144:147], a[128:131]\n\t" \
  "ds_read_b64_tr_b16 v[246:247], v119 offset:18432\n\t" \
  "v_mfma_f32_16x16x32_bf16 a[132:135], v[164:167], v[144:147], a[132:135]\n\t" \
  "v_mfma_f32_16x16x32_bf16 a[136:139], v[168:171], v[144:147], a[136:139]\n\t" \
  "ds_read_b64_tr_b16 v[248:249], v120 offset:16384\n\t" \
  "v_mfma_f32_16x16x32_bf16 a[140:143], v[172:175], v[144:147], a[140:143]\n\t" \
  "v_mfma_f32_16x16x32_bf16 a[144:147], v[176:179], v[144:147], a[144:147]\n\t" \
  "ds_read_b64_tr_b16 v[250:251], v120 offset:18432\n\t" \
  "buffer_load_dwordx4 v34, %11, s43 offen offset:1024 sc1 lds\n\t" \
  "v_mfma_f32_16x16x32_bf16 a[148:151], v[180:183], v[144:147], a[148:151]\n\t" \
  "v_mfma_f32_16x16x32_bf16 a[152:155], v[184:187], v[144:147], a[152:155]\n\t" \
  "ds_read_b64_tr_b16 v[252:253], v121 offset:16384\n\t" \
  "v_mfma_f32_16x16x32_bf16 a[156:159], v[188:191], v[144:147], a[156:159]\n\t" \
  "v_mfma_f32_16x16x32_bf16 a[96:99], v[160:163], v[140:143], a[96:99]\n\t" \
  "ds_read_b64_tr_b16 v[254:255], v121 offset:18432\n\t" \
  "v_mfma_f32_16x16x32_bf16 a[100:103], v[164:167], v[140:143], a[100:103]\n\t" \
  "buffer_load_dwordx4 v34, %11, s43 offen offset:2048 sc1 lds\n\t" \
  "v_mfma_f32_16x16x32_bf16 a[104:107], v[168:171], v[140:143], a[104:107]\n\t" \
  "ds_read_b128 v[216:219], v113 offset:12288\n\t" \
  "v_mfma_f32_16x16x32_bf16 a[108:111], v[172:175], v[140:143], a[108:111]\n\t" \
  "v_mfma_f32_16x16x32_bf16 a[112:115], v[176:179], v[140:143], a[112:115]\n\t" \
  "ds_read_b128 v[212:215], v113 offset:10240\n\t" \
  "v_mfma_f32_16x16x32_bf16 a[116:119], v[180:183], v[140:143], a[116:119]\n\t" \
  "v_mfma_f32_16x16x32_bf16 a[120:123], v[184:187], v[140:143], a[120:123]\n\t" \
  "ds_read_b128 v[208:211], v113 offset:8192\n\t" \
  "buffer_load_dwordx4 v34, %11, s43 offen offset:3072 sc1 lds\n\t" \
  "v_mfma_f32_16x16x32_bf16 a[124:127], v[188:191], v[140:143], a[124:127]\n\t" \
  "v_mfma_f32_16x16x32_bf16 a[64:67], v[160:163], v[136:139], a[64:67]\n\t" \
  "ds_read_b128 v[204:207], v113 offset:6144\n\t" \
  "v_mfma_f32_16x16x32_bf16 a[68:71], v[164:167], v[136:139], a[68:71]\n\t" \
  "v_mfma_f32_16x16x32_bf16 a[72:75], v[168:171], v[136:139], a[72:75]\n\t" \
  "ds_read_b128 v[200:203], v113 offset:4096\n\t" \
  "v_mfma_f32_16x16x32_bf16 a[76:79], v[172:175], v[136:139], a[76:79]\n\t" \
  "buffer_load_dwordx4 v[88:91], %17, %11, s73 offen offset:2048 sc1\n\t" \
  "v_mfma_f32_16x16x32_bf16 a[80:83], v[176:179], v[136:139], a[80:83]\n\t" \
  "ds_read_b128 v[196:199], v113 offset:2048\n\t" \
  "v_mfma_f32_16x16x32_bf16 a[84:87], v[180:183], v[136:139], a[84:87]\n\t" \
  "v_mfma_f32_16x16x32_bf16 a[88:91], v[184:187], v[136:139], a[88:91]\n\t" \
  "ds_read_b128 v[192:195], v113 offset:0\n\t" \
  "v_mfma_f32_16x16x32_bf16 a[92:95], v[188:191], v[136:139], a[92:95]\n\t" \
  "v_mfma_f32_16x16x32_bf16 a[32:35], v[160:163], v[132:135], a[32:35]\n\t" \
  "buffer_load_dwordx4 v[92:95], %17, %11, s77 offen offset:2048 sc1\n\t" \
  "v_mfma_f32_16x16x32_bf16 a[36:39], v[164:167], v[132:135], a[36:39]\n\t" \
  "v_mfma_f32_16x16x32_bf16 a[40:43], v[168:171], v[132:135], a[40:43]\n\t" \
  "v_mfma_f32_16x16x32_bf16 a[44:47], v[172:175], v[132:135], a[44:47]\n\t" \
  "v_mfma_f32_16x16x32_bf16 a[48:51], v[176:179], v[132:135], a[48:51]\n\t" \
  "v_mfma_f32_16x16x32_bf16 a[52:55], v[180:183], v[132:135], a[52:55]\n\t" \
  "buffer_load_dwordx4 v[96:99], %17, %11, s73 offen offset:3072 sc1\n\t" \
  "v_mfma_f32_16x16x32_bf16 a[56:59], v[184:187], v[132:135], a[56:59]\n\t" \
  "v_mfma_f32_16x16x32_bf16 a[60:63], v[188:191], v[132:135], a[60:63]\n\t" \
  "v_mfma_f32_16x16x32_bf16 a[0:3], v[160:163], v[128:131], a[0:3]\n\t" \
  "v_mfma_f32_16x16x32_bf16 a[4:7], v[164:167], v[128:131], a[4:7]\n\t" \
  "v_mfma_f32_16x16x32_bf16 a[8:11], v[168:171], v[128:131], a[8:11]\n\t" \
  "buffer_load_dwordx4 v[100:103], %17, %11, s77 offen offset:3072 sc1\n\t" \
  "v_mfma_f32_16x16x32_bf16 a[12:15], v[172:175], v[128:131], a[12:15]\n\t" \
  "v_mfma_f32_16x16x32_bf16 a[16:19], v[176:179], v[128:131], a[16:19]\n\t" \
  "v_mfma_f32_16x16x32_bf16 a[20:23], v[180:183], v[128:131], a[20:23]\n\t" \
  "v_mfma_f32_16x16x32_bf16 a[24:27], v[184:187], v[128:131], a[24:27]\n\t" \
  "v_mfma_f32_16x16x32_bf16 a[28:31], v[188:191], v[128:131], a[28:31]\n\t" \
  "s_branch MK_KVE_%=\n\t" \
  "MK_KVV_%=:\n\t" \
  "ds_read_b64_tr_b16 v[156:157], v121 offset:0\n\t" \
  "ds_read_b64_tr_b16 v[158:159], v121 offset:2048\n\t" \
  "ds_read_b128 v[160:163], v112 offset:0\n\t" \
  "ds_read_b128 v[164:167], v112 offset:2048\n\t" \
  "ds_read_b128 v[168:171], v112 offset:4096\n\t" \
  "ds_read_b128 v[172:175], v112 offset:6144\n\t" \
  "ds_read_b128 v[176:179], v112 offset:8192\n\t" \
  "ds_read_b128 v[180:183], v112 offset:10240\n\t" \
  "ds_read_b128 v[184:187], v112 offset:12288\n\t" \
  "ds_read_b128 v[188:191], v112 offset:14336\n\t" \
  "ds_read_b64_tr_b16 v[152:153], v120 offset:0\n\t" \
  "ds_read_b64_tr_b16 v[154:155], v120 offset:2048\n\t" \
  "ds_read_b64_tr_b16 v[148:149], v119 offset:0\n\t" \
  "ds_read_b64_tr_b16 v[150:151], v119 offset:2048\n\t" \
  "ds_read_b64_tr_b16 v[144:145], v118 offset:0\n\t" \
  "ds_read_b64_tr_b16 v[146:147], v118 offset:2048\n\t" \
  "ds_read_b64_tr_b16 v[140:141], v117 offset:0\n\t" \
  "ds_read_b64_tr_b16 v[142:143], v117 offset:2048\n\t" \
  "ds_read_b64_tr_b16 v[136:137], v116 offset:0\n\t" \
  "ds_read_b64_tr_b16 v[138:139], v116 offset:2048\n\t" \
  "ds_read_b64_tr_b16 v[132:133], v115 offset:0\n\t" \
  "ds_read_b64_tr_b16 v[134:135], v115 offset:2048\n\t" \
  "ds_read_b64_tr_b16 v[128:129], v114 offset:0\n\t" \
  "ds_read_b64_tr_b16 v[130:131], v114 offset:2048\n\t" \
  "s_waitcnt lgkmcnt(0)\n\t" \
  "s_waitcnt vmcnt(12)\n\t" \
  "s_barrier\n\t" \
  "v_mfma_f32_16x16x32_bf16 a[224:227], v[160:163], v[156:159], 0\n\t" \
  "ds_read_b64_tr_b16 v[220:221], v121 offset:16384\n\t" \
  "v_mov_b32_e32 v68, 0x3b124925\n\t" \
  "v_lshlrev_b32_e32 v72, 16, v40\n\t" \
  "v_mfma_f32_16x16x32_bf16 a[228:231], v[164:167], v[156:159], 0\n\t" \
  "s_add_u32 m0, s31, 49152\n\t" \
  "v_and_b32_e32 v73, 0xffff0000, v40\n\t" \
  "v_mfma_f32_16x16x32_bf16 a[232:235], v[168:171], v[156:159], 0\n\t" \
  "ds_read_b64_tr_b16 v[222:223], v121 offset:18432\n\t" \
  "v_lshlrev_b32_e32 v74, 16, v41\n\t" \
  "v_and_b32_e32 v75, 0xffff0000, v41\n\t" \
  "v_mfma_f32_16x16x32_bf16 a[236:239], v[172:175], v[156:159], 0\n\t" \
  "buffer_load_dwordx4 v108, s[60:63], s41 offen offset:0 sc1 lds\n\t" \
  "v_lshlrev_b32_e32 v76, 16, v42\n\t" \
  "v_mfma_f32_16x16x32_bf16 a[240:243], v[176:179], v[156:159], 0\n\t" \
  "ds_read_b128 v[224:227], v113 offset:0\n\t" \
  "v_and_b32_e32 v77, 0xffff0000, v42\n\t" \
  "v_mfma_f32_16x16x32_bf16 a[244:247], v[180:183], v[156:159], 0\n\t" \
  "buffer_load_dwordx4 v109, s[60:63], s41 offen offset:1024 sc1 lds\n\t" \
  "v_lshlrev_b32_e32 v78, 16, v43\n\t" \
  "v_and_b32_e32 v79, 0xffff0000, v43\n\t" \
  "v_mfma_f32_16x16x32_bf16 a[248:251], v[184:187], v[156:159], 0\n\t" \
  "ds_read_b128 v[228:231], v113 offset:2048\n\t" \
  "v_lshlrev_b32_e32 v80, 16, v44\n\t" \
  "v_mfma_f32_16x16x32_bf16 a[252:255], v[188:191], v[156:159], 0\n\t" \
  "buffer_load_dwordx4 v110, s[60:63], s41 offen offset:2048 sc1 lds\n\t" \
  "v_and_b32_e32 v81, 0xffff0000, v44\n\t" \
  "v_lshlrev_b32_e32 v82, 16, v45\n\t" \
  "v_mfma_f32_16x16x32_bf16 a[192:195], v[160:163], v[152:155], 0\n\t" \
  "ds_read_b128 v[232:235], v113 offset:4096\n\t" \
  "v_and_b32_e32 v83, 0xffff0000, v45\n\t" \
  "v_mfma_f32_16x16x32_bf16 a[196:199], v[164:167], v[152:155], 0\n\t" \
  "buffer_load_dwordx4 v111, s[60:63], s41 offen offset:3072 sc1 lds\n\t" \
  "v_lshlrev_b32_e32 v84, 16, v46\n\t" \
  "v_mfma_f32_16x16x32_bf16 a[200:203], v[168:171], v[152:155], 0\n\t" \
  "ds_read_b128 v[236:239], v113 offset:6144\n\t" \
  "v_and_b32_e32 v85, 0xffff0000, v46\n\t" \
  "v_lshlrev_b32_e32 v86, 16, v47\n\t" \
  "v_mfma_f32_16x16x32_bf16 a[204:207], v[172:175], v[152:155], 0\n\t" \
  "v_and_b32_e32 v87, 0xffff0000, v47\n\t" \
  "v_lshlrev_b32_e32 v88, 16, v48\n\t" \
  "v_mfma_f32_16x16x32_bf16 a[208:211], v[176:179], v[152:155], 0\n\t" \
  "ds_read_b128 v[240:243], v113 offset:8192\n\t" \
  "s_add_u32 m0, s31, 131072\n\t" \
  "v_and_b32_e32 v89, 0xffff0000, v48\n\t" \
  "v_mfma_f32_16x16x32_bf16 a[212:215], v[180:183], v[152:155], 0\n\t" \
  "v_lshlrev_b32_e32 v90, 16, v49\n\t" \
  "v_and_b32_e32 v91, 0xffff0000, v49\n\t" \
  "v_mfma_f32_16x16x32_bf16 a[216:219], v[184:187], v[152:155], 0\n\t" \
  "ds_read_b128 v[244:247], v113 offset:10240\n\t" \
  "buffer_load_dwordx4 v104, s[56:59], s40 offen offset:0 sc1 lds\n\t" \
  "v_mfma_f32_16x16x32_bf16 a[220:223], v[188:191], v[152:155], 0\n\t" \
  "v_lshlrev_b32_e32 v92, 16, v50\n\t" \
  "v_and_b32_e32 v93, 0xffff0000, v50\n\t" \
  "v_lshlrev_b32_e32 v94, 16, v51\n\t" \
  "v_mfma_f32_16x16x32_bf16 a[160:163], v[160:163], v[148:151], 0\n\t" \
  "ds_read_b128 v[248:251], v113 offset:12288\n\t" \
  "buffer_load_dwordx4 v105, s[56:59], s40 offen offset:1024 sc1 lds\n\t" \
  "v_mfma_f32_16x16x32_bf16 a[164:167], v[164:167], v[148:151], 0\n\t" \
  "v_and_b32_e32 v95, 0xffff0000, v51\n\t" \
  "v_lshlrev_b32_e32 v96, 16, v52\n\t" \
  "v_and_b32_e32 v97, 0xffff0000, v52\n\t" \
  "v_mfma_f32_16x16x32_bf16 a[168:171], v[168:171], v[148:151], 0\n\t" \
  "ds_read_b128 v[252:255], v113 offset:14336\n\t" \
  "buffer_load_dwordx4 v106, s[56:59], s40 offen offset:2048 sc1 lds\n\t" \
  "v_mfma_f32_16x16x32_bf16 a[172:175], v[172:175], v[148:151], 0\n\t" \
  "v_lshlrev_b32_e32 v98, 16, v53\n\t" \
  "v_and_b32_e32 v99, 0xffff0000, v53\n\t" \
  "v_mfma_f32_16x16x32_bf16 a[176:179], v[176:179], v[148:151], 0\n\t" \
  "ds_read_b64_tr_b16 v[216:217], v120 offset:16384\n\t" \
  "buffer_load_dwordx4 v107, s[56:59], s40 offen offset:3072 sc1 lds\n\t" \
  "v_lshlrev_b32_e32 v100, 16, v54\n\t" \
  "v_mfma_f32_16x16x32_bf16 a[180:183], v[180:183], v[148:151], 0\n\t" \
  "v_and_b32_e32 v101, 0xffff0000, v54\n\t" \
  "v_lshlrev_b32_e32 v102, 16, v55\n\t" \
  "v_mfma_f32_16x16x32_bf16 a[184:187], v[184:187], v[148:151], 0\n\t" \
  "ds_read_b64_tr_b16 v[218:219], v120 offset:18432\n\t" \
  "buffer_load_dword v33, off, %11, s68 sc1\n\t" \
  "v_and_b32_e32 v103, 0xffff0000, v55\n\t" \
  "v_mfma_f32_16x16x32_bf16 a[188:191], v[188:191], v[148:151], 0\n\t" \
  "global_load_dword v27, %14, off offset:0\n\t" \
  "v_add_f32_e32 v64, v72, v73\n\t" \
  "v_mfma_f32_16x16x32_bf16 a[128:131], v[160:163], v[144:147], 0\n\t" \
  "ds_read_b64_tr_b16 v[212:213], v119 offset:16384\n\t" \
  "global_load_dword v28, %14, off offset:256\n\t" \
  "v_mfma_f32_16x16x32_bf16 a[132:135], v[164:167], v[144:147], 0\n\t" \
  "global_load_dword v29, %14, off offset:512\n\t" \
  "v_mul_f32_e32 v65, v72, v72\n\t" \
  "v_fmac_f32_e32 v65, v73, v73\n\t" \
  "v_mfma_f32_16x16x32_bf16 a[136:139], v[168:171], v[144:147], 0\n\t" \
  "ds_read_b64_tr_b16 v[214:215], v119 offset:18432\n\t" \
  "global_load_dword v30, %14, off offset:768\n\t" \
  "v_mfma_f32_16x16x32_bf16 a[140:143], v[172:175], v[144:147], 0\n\t" \
  "buffer_load_dwordx4 v[36:39], %1, s[84:87], s89 offen\n\t" \
  "v_add_f32_e32 v64, v64, v74\n\t" \
  "v_fmac_f32_e32 v65, v74, v74\n\t" \
  "v_mfma_f32_16x16x32_bf16 a[144:147], v[176:179], v[144:147], 0\n\t" \
  "ds_read_b64_tr_b16 v[208:209], v118 offset:16384\n\t" \
  "v_add_f32_e32 v64, v64, v75\n\t" \
  "v_mfma_f32_16x16x32_bf16 a[148:151], v[180:183], v[144:147], 0\n\t" \
  "s_mov_b32 m0, s26\n\t" \
  "v_fmac_f32_e32 v65, v75, v75\n\t" \
  "v_mfma_f32_16x16x32_bf16 a[152:155], v[184:187], v[144:147], 0\n\t" \
  "ds_read_b64_tr_b16 v[210:211], v118 offset:18432\n\t" \
  "v_add_f32_e32 v64, v64, v76\n\t" \
  "v_fmac_f32_e32 v65, v76, v76\n\t" \
  "v_mfma_f32_16x16x32_bf16 a[156:159], v[188:191], v[144:147], 0\n\t" \
  "buffer_load_dwordx4 v108, s[60:63], s28 offen offset:0 sc1 lds\n\t" \
  "v_add_f32_e32 v64, v64, v77\n\t" \
  "v_mfma_f32_16x16x32_bf16 a[96:99], v[160:163], v[140:143], 0\n\t" \
  "ds_read_b64_tr_b16 v[204:205], v117 offset:16384\n\t" \
  "v_fmac_f32_e32 v65, v77, v77\n\t" \
  "v_add_f32_e32 v64, v64, v78\n\t" \
  "v_mfma_f32_16x16x32_bf16 a[100:103], v[164:167], v[140:143], 0\n\t" \
  "v_fmac_f32_e32 v65, v78, v78\n\t" \
  "v_add_f32_e32 v64, v64, v79\n\t" \
  "v_mfma_f32_16x16x32_bf16 a[104:107], v[168:171], v[140:143], 0\n\t" \
  "ds_read_b64_tr_b16 v[206:207], v117 offset:18432\n\t" \
  "v_fmac_f32_e32 v65, v79, v79\n\t" \
  "v_mfma_f32_16x16x32_bf16 a[108:111], v[172:175], v[140:143], 0\n\t" \
  "buffer_load_dwordx4 v109, s[60:63], s28 offen offset:1024 sc1 lds\n\t" \
  "v_add_f32_e32 v64, v64, v80\n\t" \
  "v_fmac_f32_e32 v65, v80, v80\n\t" \
  "v_mfma_f32_16x16x32_bf16 a[112:115], v[176:179], v[140:143], 0\n\t" \
  "ds_read_b64_tr_b16 v[200:201], v116 offset:16384\n\t" \
  "v_add_f32_e32 v64, v64, v81\n\t" \
  "v_mfma_f32_16x16x32_bf16 a[116:119], v[180:183], v[140:143], 0\n\t" \
  "v_fmac_f32_e32 v65, v81, v81\n\t" \
  "v_add_f32_e32 v64, v64, v82\n\t" \
  "v_fmac_f32_e32 v65, v82, v82\n\t" \
  "v_mfma_f32_16x16x32_bf16 a[120:123], v[184:187], v[140:143], 0\n\t" \
  "ds_read_b64_tr_b16 v[202:203], v116 offset:18432\n\t" \
  "v_add_f32_e32 v64, v64, v83\n\t" \
  "v_mfma_f32_16x16x32_bf16 a[124:127], v[188:191], v[140:143], 0\n\t" \
  "buffer_load_dwordx4 v110, s[60:63], s28 offen offset:2048 sc1 lds\n\t" \
  "v_fmac_f32_e32 v65, v83, v83\n\t" \
  "v_mfma_f32_16x16x32_bf16 a[64:67], v[160:163], v[136:139], 0\n\t" \
  "ds_read_b64_tr_b16 v[196:197], v115 offset:16384\n\t" \
  "v_add_f32_e32 v64, v64, v84\n\t" \
  "v_fmac_f32_e32 v65, v84, v84\n\t" \
  "v_mfma_f32_16x16x32_bf16 a[68:71], v[164:167], v[136:139], 0\n\t" \
  "v_add_f32_e32 v64, v64, v85\n\t" \
  "v_fmac_f32_e32 v65, v85, v85\n\t" \
  "v_mfma_f32_16x16x32_bf16 a[72:75], v[168:171], v[136:139], 0\n\t" \
  "ds_read_b64_tr_b16 v[198:199], v115 offset:18432\n\t" \
  "v_add_f32_e32 v64, v64, v86\n\t" \
  "v_fmac_f32_e32 v65, v86, v86\n\t" \
  "v_mfma_f32_16x16x32_bf16 a[76:79], v[172:175], v[136:139], 0\n\t" \
  "buffer_load_dwordx4 v111, s[60:63], s28 offen offset:3072 sc1 lds\n\t" \
  "v_add_f32_e32 v64, v64, v87\n\t" \
  "v_mfma_f32_16x16x32_bf16 a[80:83], v[176:179], v[136:139], 0\n\t" \
  "ds_read_b64_tr_b16 v[192:193], v114 offset:16384\n\t" \
  "v_fmac_f32_e32 v65, v87, v87\n\t" \
  "v_mfma_f32_16x16x32_bf16 a[84:87], v[180:183], v[136:139], 0\n\t" \
  "s_mov_b32 m0, s27\n\t" \
  "v_add_f32_e32 v64, v64, v88\n\t" \
  "v_fmac_f32_e32 v65, v88, v88\n\t" \
  "v_mfma_f32_16x16x32_bf16 a[88:91], v[184:187], v[136:139], 0\n\t" \
  "ds_read_b64_tr_b16 v[194:195], v114 offset:18432\n\t" \
  "v_add_f32_e32 v64, v64, v89\n\t" \
  "v_mfma_f32_16x16x32_bf16 a[92:95], v[188:191], v[136:139], 0\n\t" \
  "buffer_load_dwordx4 v104, s[56:59], s29 offen offset:0 sc1 lds\n\t" \
  "v_fmac_f32_e32 v65, v89, v89\n\t" \
  "v_add_f32_e32 v64, v64, v90\n\t" \
  "v_mfma_f32_16x16x32_bf16 a[32:35], v[160:163], v[132:135], 0\n\t" \
  "s_add_u32 s26, s26, 0x4000\n\t" \
  "s_add_u32 s28, s28, s30\n\t" \
  "v_mfma_f32_16x16x32_bf16 a[36:39], v[164:167], v[132:135], 0\n\t" \
  "s_add_u32 s39, s35, s31\n\t" \
  "s_add_u32 s43, s35, 0x8000\n\t" \
  "v_mfma_f32_16x16x32_bf16 a[40:43], v[168:171], v[132:135], 0\n\t" \
  "s_cmp_lg_u32 s35, 0x20000\n\t" \
  "s_cselect_b32 s43, s43, 0x10000\n\t" \
  "v_fmac_f32_e32 v65, v90, v90\n\t" \
  "v_mfma_f32_16x16x32_bf16 a[44:47], v[172:175], v[132:135], 0\n\t" \
  "buffer_load_dwordx4 v105, s[56:59], s29 offen offset:1024 sc1 lds\n\t" \
  "v_add_f32_e32 v64, v64, v91\n\t" \
  "v_mfma_f32_16x16x32_bf16 a[48:51], v[176:179], v[132:135], 0\n\t" \
  "s_sub_u32 s38, s43, s35\n\t" \
  "s_mov_b32 s35, s43\n\t" \
  "v_fmac_f32_e32 v65, v91, v91\n\t" \
  "v_mfma_f32_16x16x32_bf16 a[52:55], v[180:183], v[132:135], 0\n\t" \
  "v_add_f32_e32 v64, v64, v92\n\t" \
  "v_fmac_f32_e32 v65, v92, v92\n\t" \
  "v_mfma_f32_16x16x32_bf16 a[56:59], v[184:187], v[132:135], 0\n\t" \
  "v_add_u32_e32 v112, s38, v112\n\t" \
  "v_add_u32_e32 v113, s38, v113\n\t" \
  "v_mfma_f32_16x16x32_bf16 a[60:63], v[188:191], v[132:135], 0\n\t" \
  "buffer_load_dwordx4 v106, s[56:59], s29 offen offset:2048 sc1 lds\n\t" \
  "v_xor_b32_e32 v114, 0x8000, v114\n\t" \
  "v_xor_b32_e32 v115, 0x8000, v115\n\t" \
  "v_mfma_f32_16x16x32_bf16 a[0:3], v[160:163], v[128:131], 0\n\t" \
  "v_xor_b32_e32 v116, 0x8000, v116\n\t" \
  "v_xor_b32_e32 v117, 0x8000, v117\n\t" \
  "v_add_f32_e32 v64, v64, v93\n\t" \
  "v_mfma_f32_16x16x32_bf16 a[4:7], v[164:167], v[128:131], 0\n\t" \
  "v_xor_b32_e32 v118, 0x8000, v118\n\t" \
  "v_xor_b32_e32 v119, 0x8000, v119\n\t" \
  "v_mfma_f32_16x16x32_bf16 a[8:11], v[168:171], v[128:131], 0\n\t" \
  "v_xor_b32_e32 v120, 0x8000, v120\n\t" \
  "v_xor_b32_e32 v121, 0x8000, v121\n\t" \
  "v_fmac_f32_e32 v65, v93, v93\n\t" \
  "v_mfma_f32_16x16x32_bf16 a[12:15], v[172:175], v[128:131], 0\n\t" \
  "buffer_load_dwordx4 v107, s[56:59], s29 offen offset:3072 sc1 lds\n\t" \
  "v_add_f32_e32 v64, v64, v94\n\t" \
  "v_mfma_f32_16x16x32_bf16 a[16:19], v[176:179], v[128:131], 0\n\t" \
  "s_mov_b32 s27, s39\n\t" \
  "s_add_u32 s29, s29, s33\n\t" \
  "v_mfma_f32_16x16x32_bf16 a[20:23], v[180:183], v[128:131], 0\n\t" \
  "v_fmac_f32_e32 v65, v94, v94\n\t" \
  "v_add_f32_e32 v64, v64, v95\n\t" \
  "v_fmac_f32_e32 v65, v95, v95\n\t" \
  "v_mfma_f32_16x16x32_bf16 a[24:27], v[184:187], v[128:131], 0\n\t" \
  "v_add_f32_e32 v64, v64, v96\n\t" \
  "v_fmac_f32_e32 v65, v96, v96\n\t" \
  "v_mfma_f32_16x16x32_bf16 a[28:31], v[188:191], v[128:131], 0\n\t" \
  "v_add_f32_e32 v64, v64, v97\n\t" \
  "v_fmac_f32_e32 v65, v97, v97\n\t" \
  "s_waitcnt lgkmcnt(0)\n\t" \
  "s_waitcnt vmcnt(22)\n\t" \
  "s_barrier\n\t" \
  "v_mfma_f32_16x16x32_bf16 a[224:227], v[224:227], v[220:223], a[224:227]\n\t" \
  "ds_read_b64_tr_b16 v[156:157], v121 offset:0\n\t" \
  "v_add_f32_e32 v64, v64, v98\n\t" \
  "v_fmac_f32_e32 v65, v98, v98\n\t" \
  "v_mfma_f32_16x16x32_bf16 a[228:231], v[228:231], v[220:223], a[228:231]\n\t" \
  "s_mov_b32 m0, s26\n\t" \
  "v_add_f32_e32 v64, v64, v99\n\t" \
  "v_mfma_f32_16x16x32_bf16 a[232:235], v[232:235], v[220:223], a[232:235]\n\t" \
  "ds_read_b64_tr_b16 v[158:159], v121 offset:2048\n\t" \
  "v_fmac_f32_e32 v65, v99, v99\n\t" \
  "v_add_f32_e32 v64, v64, v100\n\t" \
  "v_mfma_f32_16x16x32_bf16 a[236:239], v[236:239], v[220:223], a[236:239]\n\t" \
  "buffer_load_dwordx4 v108, s[60:63], s28 offen offset:0 sc1 lds\n\t" \
  "v_fmac_f32_e32 v65, v100, v100\n\t" \
  "v_mfma_f32_16x16x32_bf16 a[240:243], v[240:243], v[220:223], a[240:243]\n\t" \
  "ds_read_b128 v[160:163], v112 offset:0\n\t" \
  "v_add_f32_e32 v64, v64, v101\n\t" \
  "v_mfma_f32_16x16x32_bf16 a[244:247], v[244:247], v[220:223], a[244:247]\n\t" \
  "v_fmac_f32_e32 v65, v101, v101\n\t" \
  "v_add_f32_e32 v64, v64, v102\n\t" \
  "v_fmac_f32_e32 v65, v102, v102\n\t" \
  "v_mfma_f32_16x16x32_bf16 a[248:251], v[248:251], v[220:223], a[248:251]\n\t" \
  "ds_read_b128 v[164:167], v112 offset:2048\n\t" \
  "v_add_f32_e32 v64, v64, v103\n\t" \
  "v_mfma_f32_16x16x32_bf16 a[252:255], v[252:255], v[220:223], a[252:255]\n\t" \
  "v_fmac_f32_e32 v65, v103, v103\n\t" \
  "s_nop 1\n\t" \
  "v_add_f32_dpp v64, v64, v64 row_ror:4 row_mask:0xf bank_mask:0xf\n\t" \
  "v_mfma_f32_16x16x32_bf16 a[192:195], v[224:227], v[216:219], a[192:195]\n\t" \
  "ds_read_b128 v[168:171], v112 offset:4096\n\t" \
  "v_add_f32_dpp v65, v65, v65 row_ror:4 row_mask:0xf bank_mask:0xf\n\t" \
  "v_mfma_f32_16x16x32_bf16 a[196:199], v[228:231], v[216:219], a[196:199]\n\t" \
  "s_nop 1\n\t" \
  "v_add_f32_dpp v64, v64, v64 row_ror:8 row_mask:0xf bank_mask:0xf\n\t" \
  "v_mfma_f32_16x16x32_bf16 a[200:203], v[232:235], v[216:219], a[200:203]\n\t" \
  "ds_read_b128 v[172:175], v112 offset:6144\n\t" \
  "v_add_f32_dpp v65, v65, v65 row_ror:8 row_mask:0xf bank_mask:0xf\n\t" \
  "s_nop 1\n\t" \
  "v_mfma_f32_16x16x32_bf16 a[204:207], v[236:239], v[216:219], a[204:207]\n\t" \
  "buffer_load_dwordx4 v109, s[60:63], s28 offen offset:1024 sc1 lds\n\t" \
  "v_mov_b32_e32 v62, v64\n\t" \
  "v_mfma_f32_16x16x32_bf16 a[208:211], v[240:243], v[216:219], a[208:211]\n\t" \
  "ds_read_b128 v[176:179], v112 offset:8192\n\t" \
  "v_mov_b32_e32 v63, v65\n\t" \
  "s_nop 1\n\t" \
  "v_mfma_f32_16x16x32_bf16 a[212:215], v[244:247], v[216:219], a[212:215]\n\t" \
  "v_permlane16_swap_b32_e32 v64, v62\n\t" \
  "v_permlane16_swap_b32_e32 v65, v63\n\t" \
  "v_mfma_f32_16x16x32_bf16 a[216:219], v[248:251], v[216:219], a[216:219]\n\t" \
  "ds_read_b128 v[180:183], v112 offset:10240\n\t" \
  "v_add_f32_e32 v64, v64, v62\n\t" \
  "v_mfma_f32_16x16x32_bf16 a[220:223], v[252:255], v[216:219], a[220:223]\n\t" \
  "v_add_f32_e32 v65, v65, v63\n\t" \
  "v_mov_b32_e32 v62, v64\n\t" \
  "v_mov_b32_e32 v63, v65\n\t" \
  "v_mfma_f32_16x16x32_bf16 a[160:163], v[224:227], v[212:215], a[160:163]\n\t" \
  "ds_read_b128 v[184:187], v112 offset:12288\n\t" \
  "s_nop 1\n\t" \
  "v_mfma_f32_16x16x32_bf16 a[164:167], v[228:231], v[212:215], a[164:167]\n\t" \
  "v_permlane32_swap_b32_e32 v64, v62\n\t" \
  "v_permlane32_swap_b32_e32 v65, v63\n\t" \
  "v_add_f32_e32 v64, v64, v62\n\t" \
  "v_mfma_f32_16x16x32_bf16 a[168:171], v[232:235], v[212:215], a[168:171]\n\t" \
  "ds_read_b128 v[188:191], v112 offset:14336\n\t" \
  "v_add_f32_e32 v65, v65, v63\n\t" \
  "v_mfma_f32_16x16x32_bf16 a[172:175], v[236:239], v[212:215], a[172:175]\n\t" \
  "buffer_load_dwordx4 v110, s[60:63], s28 offen offset:2048 sc1 lds\n\t" \
  "v_mul_f32_e32 v64, v68, v64\n\t" \
  "v_mfma_f32_16x16x32_bf16 a[176:179], v[240:243], v[212:215], a[176:179]\n\t" \
  "ds_read_b64_tr_b16 v[152:153], v120 offset:0\n\t" \
  "v_mul_f32_e32 v65, v68, v65\n\t" \
  "v_fma_f32 v65, -v64, v64, v65\n\t" \
  "v_mfma_f32_16x16x32_bf16 a[180:183], v[244:247], v[212:215], a[180:183]\n\t" \
  "v_max_f32_e32 v65, 0, v65\n\t" \
  "v_add_f32_e32 v65, 0x3727c5ac, v65\n\t" \
  "v_mfma_f32_16x16x32_bf16 a[184:187], v[248:251], v[212:215], a[184:187]\n\t" \
  "ds_read_b64_tr_b16 v[154:155], v120 offset:2048\n\t" \
  "v_rsq_f32_e32 v65, v65\n\t" \
  "s_nop 1\n\t" \
  "v_mfma_f32_16x16x32_bf16 a[188:191], v[252:255], v[212:215], a[188:191]\n\t" \
  "v_mul_f32_e32 v65, 0x3db8aa3b, v65\n\t" \
  "v_mul_f32_e64 v64, -v64, v65\n\t" \
  "v_mfma_f32_16x16x32_bf16 a[128:131], v[224:227], v[208:211], a[128:131]\n\t" \
  "ds_read_b64_tr_b16 v[148:149], v119 offset:0\n\t" \
  "v_fma_f32 v72, v72, v65, v64\n\t" \
  "v_mfma_f32_16x16x32_bf16 a[132:135], v[228:231], v[208:211], a[132:135]\n\t" \
  "v_fma_f32 v73, v73, v65, v64\n\t" \
  "v_cvt_pk_bf16_f32 v40, v72, v73\n\t" \
  "v_fma_f32 v74, v74, v65, v64\n\t" \
  "v_mfma_f32_16x16x32_bf16 a[136:139], v[232:235], v[208:211], a[136:139]\n\t" \
  "ds_read_b64_tr_b16 v[150:151], v119 offset:2048\n\t" \
  "v_fma_f32 v75, v75, v65, v64\n\t" \
  "v_mfma_f32_16x16x32_bf16 a[140:143], v[236:239], v[208:211], a[140:143]\n\t" \
  "buffer_load_dwordx4 v111, s[60:63], s28 offen offset:3072 sc1 lds\n\t" \
  "v_cvt_pk_bf16_f32 v41, v74, v75\n\t" \
  "v_mfma_f32_16x16x32_bf16 a[144:147], v[240:243], v[208:211], a[144:147]\n\t" \
  "ds_read_b64_tr_b16 v[144:145], v118 offset:0\n\t" \
  "v_fma_f32 v76, v76, v65, v64\n\t" \
  "v_fma_f32 v77, v77, v65, v64\n\t" \
  "v_mfma_f32_16x16x32_bf16 a[148:151], v[244:247], v[208:211], a[148:151]\n\t" \
  "v_cvt_pk_bf16_f32 v44, v76, v77\n\t" \
  "v_fma_f32 v78, v78, v65, v64\n\t" \
  "v_mfma_f32_16x16x32_bf16 a[152:155], v[248:251], v[208:211], a[152:155]\n\t" \
  "ds_read_b64_tr_b16 v[146:147], v118 offset:2048\n\t" \
  "v_fma_f32 v79, v79, v65, v64\n\t" \
  "v_cvt_pk_bf16_f32 v45, v78, v79\n\t" \
  "v_mfma_f32_16x16x32_bf16 a[156:159], v[252:255], v[208:211], a[156:159]\n\t" \
  "v_fma_f32 v80, v80, v65, v64\n\t" \
  "v_fma_f32 v81, v81, v65, v64\n\t" \
  "v_mfma_f32_16x16x32_bf16 a[96:99], v[224:227], v[204:207], a[96:99]\n\t" \
  "ds_read_b64_tr_b16 v[140:141], v117 offset:0\n\t" \
  "v_cvt_pk_bf16_f32 v42, v80, v81\n\t" \
  "v_mfma_f32_16x16x32_bf16 a[100:103], v[228:231], v[204:207], a[100:103]\n\t" \
  "s_mov_b32 m0, s27\n\t" \
  "v_fma_f32 v82, v82, v65, v64\n\t" \
  "v_fma_f32 v83, v83, v65, v64\n\t" \
  "v_mfma_f32_16x16x32_bf16 a[104:107], v[232:235], v[204:207], a[104:107]\n\t" \
  "ds_read_b64_tr_b16 v[142:143], v117 offset:2048\n\t" \
  "v_cvt_pk_bf16_f32 v43, v82, v83\n\t" \
  "v_mfma_f32_16x16x32_bf16 a[108:111], v[236:239], v[204:207], a[108:111]\n\t" \
  "buffer_load_dwordx4 v104, s[56:59], s29 offen offset:0 sc1 lds\n\t" \
  "v_fma_f32 v84, v84, v65, v64\n\t" \
  "v_fma_f32 v85, v85, v65, v64\n\t" \
  "v_mfma_f32_16x16x32_bf16 a[112:115], v[240:243], v[204:207], a[112:115]\n\t" \
  "ds_read_b64_tr_b16 v[136:137], v116 offset:0\n\t" \
  "v_cvt_pk_bf16_f32 v46, v84, v85\n\t" \
  "v_mfma_f32_16x16x32_bf16 a[116:119], v[244:247], v[204:207], a[116:119]\n\t" \
  "v_fma_f32 v86, v86, v65, v64\n\t" \
  "v_fma_f32 v87, v87, v65, v64\n\t" \
  "v_mfma_f32_16x16x32_bf16 a[120:123], v[248:251], v[204:207], a[120:123]\n\t" \
  "ds_read_b64_tr_b16 v[138:139], v116 offset:2048\n\t" \
  "v_cvt_pk_bf16_f32 v47, v86, v87\n\t" \
  "v_fma_f32 v88, v88, v65, v64\n\t" \
  "v_mfma_f32_16x16x32_bf16 a[124:127], v[252:255], v[204:207], a[124:127]\n\t" \
  "v_fma_f32 v89, v89, v65, v64\n\t" \
  "v_cvt_pk_bf16_f32 v48, v88, v89\n\t" \
  "v_mfma_f32_16x16x32_bf16 a[64:67], v[224:227], v[200:203], a[64:67]\n\t" \
  "ds_read_b64_tr_b16 v[132:133], v115 offset:0\n\t" \
  "v_fma_f32 v90, v90, v65, v64\n\t" \
  "v_fma_f32 v91, v91, v65, v64\n\t" \
  "v_mfma_f32_16x16x32_bf16 a[68:71], v[228:231], v[200:203], a[68:71]\n\t" \
  "v_cvt_pk_bf16_f32 v49, v90, v91\n\t" \
  "v_fma_f32 v92, v92, v65, v64\n\t" \
  "v_mfma_f32_16x16x32_bf16 a[72:75], v[232:235], v[200:203], a[72:75]\n\t" \
  "ds_read_b64_tr_b16 v[134:135], v115 offset:2048\n\t" \
  "v_fma_f32 v93, v93, v65, v64\n\t" \
  "v_mfma_f32_16x16x32_bf16 a[76:79], v[236:239], v[200:203], a[76:79]\n\t" \
  "buffer_load_dwordx4 v105, s[56:59], s29 offen offset:1024 sc1 lds\n\t" \
  "v_cvt_pk_bf16_f32 v52, v92, v93\n\t" \
  "v_fma_f32 v94, v94, v65, v64\n\t" \
  "v_mfma_f32_16x16x32_bf16 a[80:83], v[240:243], v[200:203], a[80:83]\n\t" \
  "ds_read_b64_tr_b16 v[128:129], v114 offset:0\n\t" \
  "v_fma_f32 v95, v95, v65, v64\n\t" \
  "v_mfma_f32_16x16x32_bf16 a[84:87], v[244:247], v[200:203], a[84:87]\n\t" \
  "v_cvt_pk_bf16_f32 v53, v94, v95\n\t" \
  "v_fma_f32 v96, v96, v65, v64\n\t" \
  "v_fma_f32 v97, v97, v65, v64\n\t" \
  "v_mfma_f32_16x16x32_bf16 a[88:91], v[248:251], v[200:203], a[88:91]\n\t" \
  "ds_read_b64_tr_b16 v[130:131], v114 offset:2048\n\t" \
  "v_cvt_pk_bf16_f32 v50, v96, v97\n\t" \
  "v_mfma_f32_16x16x32_bf16 a[92:95], v[252:255], v[200:203], a[92:95]\n\t" \
  "v_fma_f32 v98, v98, v65, v64\n\t" \
  "v_fma_f32 v99, v99, v65, v64\n\t" \
  "v_mfma_f32_16x16x32_bf16 a[32:35], v[224:227], v[196:199], a[32:35]\n\t" \
  "s_xor_b32 s26, s26, 0xc000\n\t" \
  "s_add_u32 s28, s28, s30\n\t" \
  "v_cvt_pk_bf16_f32 v51, v98, v99\n\t" \
  "v_mfma_f32_16x16x32_bf16 a[36:39], v[228:231], v[196:199], a[36:39]\n\t" \
  "v_fma_f32 v100, v100, v65, v64\n\t" \
  "v_fma_f32 v101, v101, v65, v64\n\t" \
  "v_mfma_f32_16x16x32_bf16 a[40:43], v[232:235], v[196:199], a[40:43]\n\t" \
  "v_cvt_pk_bf16_f32 v54, v100, v101\n\t" \
  "v_fma_f32 v102, v102, v65, v64\n\t" \
  "v_fma_f32 v103, v103, v65, v64\n\t" \
  "v_mfma_f32_16x16x32_bf16 a[44:47], v[236:239], v[196:199], a[44:47]\n\t" \
  "buffer_load_dwordx4 v106, s[56:59], s29 offen offset:2048 sc1 lds\n\t" \
  "s_waitcnt vmcnt(16)\n\t" \
  "v_mfma_f32_16x16x32_bf16 a[48:51], v[240:243], v[196:199], a[48:51]\n\t" \
  "v_readfirstlane_b32 s90, v33\n\t" \
  "v_readlane_b32 s81, v29, 30\n\t" \
  "v_mfma_f32_16x16x32_bf16 a[52:55], v[244:247], v[196:199], a[52:55]\n\t" \
  "v_readlane_b32 s80, v29, 31\n\t" \
  "s_add_u32 s90, s90, 1\n\ts_cselect_b32 s90, 1, s90\n\t" \
  "v_cvt_pk_bf16_f32 v55, v102, v103\n\t" \
  "v_mfma_f32_16x16x32_bf16 a[56:59], v[248:251], v[196:199], a[56:59]\n\t" \
  "v_mov_b32_e32 v33, s90\n\t" \
  "v_readlane_b32 s44, v29, 2\n\t" \
  "v_mfma_f32_16x16x32_bf16 a[60:63], v[252:255], v[196:199], a[60:63]\n\t" \
  "v_readlane_b32 s68, v29, 33\n\t" \
  "v_readlane_b32 s25, v30, 9\n\t" \
  "v_mfma_f32_16x16x32_bf16 a[0:3], v[224:227], v[192:195], a[0:3]\n\t" \
  "v_readlane_b32 s42, v30, 10\n\t" \
  "buffer_store_dwordx4 v[40:43], %16, %11, 0 offen offset:0 sc1\n\t" \
  "v_mfma_f32_16x16x32_bf16 a[4:7], v[228:231], v[192:195], a[4:7]\n\t" \
  "s_mul_i32 s76, s91, 14336\n\t" \
  "s_add_u32 s76, s76, s44\n\t" \
  "v_mfma_f32_16x16x32_bf16 a[8:11], v[232:235], v[192:195], a[8:11]\n\t" \
  "s_add_u32 s72, s44, 0\n\t" \
  "s_add_u32 s73, s44, 4096\n\t" \
  "buffer_store_dwordx4 v[44:47], %16, %11, 0 offen offset:128 sc1\n\t" \
  "v_mfma_f32_16x16x32_bf16 a[12:15], v[236:239], v[192:195], a[12:15]\n\t" \
  "buffer_load_dwordx4 v107, s[56:59], s29 offen offset:3072 sc1 lds\n\t" \
  "s_add_u32 s74, s44, 8192\n\t" \
  "v_mfma_f32_16x16x32_bf16 a[16:19], v[240:243], v[192:195], a[16:19]\n\t" \
  "s_add_u32 s27, s27, 0x4000\n\t" \
  "s_add_u32 s29, s29, s34\n\t" \
  "buffer_store_dwordx4 v[48:51], %16, %11, s17 offen offset:3072 sc1\n\t" \
  "v_mfma_f32_16x16x32_bf16 a[20:23], v[244:247], v[192:195], a[20:23]\n\t" \
  "s_add_u32 s37, s37, 1\n\t" \
  "s_add_u32 s75, s44, 12288\n\t" \
  "v_mfma_f32_16x16x32_bf16 a[24:27], v[248:251], v[192:195], a[24:27]\n\t" \
  "s_add_u32 s77, s76, 4096\n\t" \
  "s_add_u32 s78, s76, 8192\n\t" \
  "buffer_store_dwordx4 v[52:55], %16, %11, s17 offen offset:3200 sc1\n\t" \
  "v_mfma_f32_16x16x32_bf16 a[28:31], v[252:255], v[192:195], a[28:31]\n\t" \
  "s_add_u32 s79, s76, 12288\n\t" \
  "MK_KL0V_%=:\n\t" \
  "s_waitcnt lgkmcnt(0)\n\t" \
  "s_waitcnt vmcnt(16)\n\t" \
  "s_barrier\n\t" \
  "v_mfma_f32_16x16x32_bf16 a[224:227], v[160:163], v[156:159], a[224:227]\n\t" \
  "ds_read_b64_tr_b16 v[220:221], v121 offset:16384\n\t" \
  "v_mfma_f32_16x16x32_bf16 a[228:231], v[164:167], v[156:159], a[228:231]\n\t" \
  "s_mov_b32 m0, s26\n\t" \
  "v_mfma_f32_16x16x32_bf16 a[232:235], v[168:171], v[156:159], a[232:235]\n\t" \
  "ds_read_b64_tr_b16 v[222:223], v121 offset:18432\n\t" \
  "v_mfma_f32_16x16x32_bf16 a[236:239], v[172:175], v[156:159], a[236:239]\n\t" \
  "buffer_load_dwordx4 v108, s[60:63], s28 offen offset:0 sc1 lds\n\t" \
  "v_mfma_f32_16x16x32_bf16 a[240:243], v[176:179], v[156:159], a[240:243]\n\t" \
  "ds_read_b128 v[224:227], v113 offset:0\n\t" \
  "v_mfma_f32_16x16x32_bf16 a[244:247], v[180:183], v[156:159], a[244:247]\n\t" \
  "v_mfma_f32_16x16x32_bf16 a[248:251], v[184:187], v[156:159], a[248:251]\n\t" \
  "ds_read_b128 v[228:231], v113 offset:2048\n\t" \
  "v_mfma_f32_16x16x32_bf16 a[252:255], v[188:191], v[156:159], a[252:255]\n\t" \
  "v_mfma_f32_16x16x32_bf16 a[192:195], v[160:163], v[152:155], a[192:195]\n\t" \
  "ds_read_b128 v[232:235], v113 offset:4096\n\t" \
  "v_mfma_f32_16x16x32_bf16 a[196:199], v[164:167], v[152:155], a[196:199]\n\t" \
  "v_mfma_f32_16x16x32_bf16 a[200:203], v[168:171], v[152:155], a[200:203]\n\t" \
  "ds_read_b128 v[236:239], v113 offset:6144\n\t" \
  "v_mfma_f32_16x16x32_bf16 a[204:207], v[172:175], v[152:155], a[204:207]\n\t" \
  "buffer_load_dwordx4 v109, s[60:63], s28 offen offset:1024 sc1 lds\n\t" \
  "v_mfma_f32_16x16x32_bf16 a[208:211], v[176:179], v[152:155], a[208:211]\n\t" \
  "ds_read_b128 v[240:243], v113 offset:8192\n\t" \
  "v_mfma_f32_16x16x32_bf16 a[212:215], v[180:183], v[152:155], a[212:215]\n\t" \
  "v_mfma_f32_16x16x32_bf16 a[216:219], v[184:187], v[152:155], a[216:219]\n\t" \
  "ds_read_b128 v[244:247], v113 offset:10240\n\t" \
  "v_mfma_f32_16x16x32_bf16 a[220:223], v[188:191], v[152:155], a[220:223]\n\t" \
  "v_mfma_f32_16x16x32_bf16 a[160:163], v[160:163], v[148:151], a[160:163]\n\t" \
  "ds_read_b128 v[248:251], v113 offset:12288\n\t" \
  "v_mfma_f32_16x16x32_bf16 a[164:167], v[164:167], v[148:151], a[164:167]\n\t" \
  "v_mfma_f32_16x16x32_bf16 a[168:171], v[168:171], v[148:151], a[168:171]\n\t" \
  "ds_read_b128 v[252:255], v113 offset:14336\n\t" \
  "v_mfma_f32_16x16x32_bf16 a[172:175], v[172:175], v[148:151], a[172:175]\n\t" \
  "buffer_load_dwordx4 v110, s[60:63], s28 offen offset:2048 sc1 lds\n\t" \
  "v_mfma_f32_16x16x32_bf16 a[176:179], v[176:179], v[148:151], a[176:179]\n\t" \
  "ds_read_b64_tr_b16 v[216:217], v120 offset:16384\n\t" \
  "v_mfma_f32_16x16x32_bf16 a[180:183], v[180:183], v[148:151], a[180:183]\n\t" \
  "v_mfma_f32_16x16x32_bf16 a[184:187], v[184:187], v[148:151], a[184:187]\n\t" \
  "ds_read_b64_tr_b16 v[218:219], v120 offset:18432\n\t" \
  "v_mfma_f32_16x16x32_bf16 a[188:191], v[188:191], v[148:151], a[188:191]\n\t" \
  "v_mfma_f32_16x16x32_bf16 a[128:131], v[160:163], v[144:147], a[128:131]\n\t" \
  "ds_read_b64_tr_b16 v[212:213], v119 offset:16384\n\t" \
  "v_mfma_f32_16x16x32_bf16 a[132:135], v[164:167], v[144:147], a[132:135]\n\t" \
  "v_mfma_f32_16x16x32_bf16 a[136:139], v[168:171], v[144:147], a[136:139]\n\t" \
  "ds_read_b64_tr_b16 v[214:215], v119 offset:18432\n\t" \
  "v_mfma_f32_16x16x32_bf16 a[140:143], v[172:175], v[144:147], a[140:143]\n\t" \
  "buffer_load_dwordx4 v111, s[60:63], s28 offen offset:3072 sc1 lds\n\t" \
  "v_mfma_f32_16x16x32_bf16 a[144:147], v[176:179], v[144:147], a[144:147]\n\t" \
  "ds_read_b64_tr_b16 v[208:209], v118 offset:16384\n\t" \
  "v_mfma_f32_16x16x32_bf16 a[148:151], v[180:183], v[144:147], a[148:151]\n\t" \
  "v_mfma_f32_16x16x32_bf16 a[152:155], v[184:187], v[144:147], a[152:155]\n\t" \
  "ds_read_b64_tr_b16 v[210:211], v118 offset:18432\n\t" \
  "v_mfma_f32_16x16x32_bf16 a[156:159], v[188:191], v[144:147], a[156:159]\n\t" \
  "v_mfma_f32_16x16x32_bf16 a[96:99], v[160:163], v[140:143], a[96:99]\n\t" \
  "ds_read_b64_tr_b16 v[204:205], v117 offset:16384\n\t" \
  "v_mfma_f32_16x16x32_bf16 a[100:103], v[164:167], v[140:143], a[100:103]\n\t" \
  "s_mov_b32 m0, s27\n\t" \
  "v_mfma_f32_16x16x32_bf16 a[104:107], v[168:171], v[140:143], a[104:107]\n\t" \
  "ds_read_b64_tr_b16 v[206:207], v117 offset:18432\n\t" \
  "v_mfma_f32_16x16x32_bf16 a[108:111], v[172:175], v[140:143], a[108:111]\n\t" \
  "buffer_load_dwordx4 v104, s[56:59], s29 offen offset:0 sc1 lds\n\t" \
  "v_mfma_f32_16x16x32_bf16 a[112:115], v[176:179], v[140:143], a[112:115]\n\t" \
  "ds_read_b64_tr_b16 v[200:201], v116 offset:16384\n\t" \
  "v_mfma_f32_16x16x32_bf16 a[116:119], v[180:183], v[140:143], a[116:119]\n\t" \
  "v_mfma_f32_16x16x32_bf16 a[120:123], v[184:187], v[140:143], a[120:123]\n\t" \
  "ds_read_b64_tr_b16 v[202:203], v116 offset:18432\n\t" \
  "v_mfma_f32_16x16x32_bf16 a[124:127], v[188:191], v[140:143], a[124:127]\n\t" \
  "v_mfma_f32_16x16x32_bf16 a[64:67], v[160:163], v[136:139], a[64:67]\n\t" \
  "ds_read_b64_tr_b16 v[196:197], v115 offset:16384\n\t" \
  "v_mfma_f32_16x16x32_bf16 a[68:71], v[164:167], v[136:139], a[68:71]\n\t" \
  "v_mfma_f32_16x16x32_bf16 a[72:75], v[168:171], v[136:139], a[72:75]\n\t" \
  "ds_read_b64_tr_b16 v[198:199], v115 offset:18432\n\t" \
  "v_mfma_f32_16x16x32_bf16 a[76:79], v[172:175], v[136:139], a[76:79]\n\t" \
  "buffer_load_dwordx4 v105, s[56:59], s29 offen offset:1024 sc1 lds\n\t" \
  "v_mfma_f32_16x16x32_bf16 a[80:83], v[176:179], v[136:139], a[80:83]\n\t" \
  "ds_read_b64_tr_b16 v[192:193], v114 offset:16384\n\t" \
  "v_mfma_f32_16x16x32_bf16 a[84:87], v[180:183], v[136:139], a[84:87]\n\t" \
  "v_mfma_f32_16x16x32_bf16 a[88:91], v[184:187], v[136:139], a[88:91]\n\t" \
  "ds_read_b64_tr_b16 v[194:195], v114 offset:18432\n\t" \
  "v_mfma_f32_16x16x32_bf16 a[92:95], v[188:191], v[136:139], a[92:95]\n\t" \
  "v_mfma_f32_16x16x32_bf16 a[32:35], v[160:163], v[132:135], a[32:35]\n\t" \
  "s_add_u32 s26, s26, 0x4000\n\t" \
  "s_add_u32 s28, s28, s30\n\t" \
  "v_mfma_f32_16x16x32_bf16 a[36:39], v[164:167], v[132:135], a[36:39]\n\t" \
  "s_add_u32 s39, s35, s31\n\t" \
  "s_add_u32 s43, s35, 0x8000\n\t" \
  "v_mfma_f32_16x16x32_bf16 a[40:43], v[168:171], v[132:135], a[40:43]\n\t" \
  "s_cmp_lg_u32 s35, 0x20000\n\t" \
  "s_cselect_b32 s43, s43, 0x10000\n\t" \
  "v_mfma_f32_16x16x32_bf16 a[44:47], v[172:175], v[132:135], a[44:47]\n\t" \
  "buffer_load_dwordx4 v106, s[56:59], s29 offen offset:2048 sc1 lds\n\t" \
  "v_mfma_f32_16x16x32_bf16 a[48:51], v[176:179], v[132:135], a[48:51]\n\t" \
  "s_sub_u32 s38, s43, s35\n\t" \
  "s_mov_b32 s35, s43\n\t" \
  "v_mfma_f32_16x16x32_bf16 a[52:55], v[180:183], v[132:135], a[52:55]\n\t" \
  "v_mfma_f32_16x16x32_bf16 a[56:59], v[184:187], v[132:135], a[56:59]\n\t" \
  "v_add_u32_e32 v112, s38, v112\n\t" \
  "v_add_u32_e32 v113, s38, v113\n\t" \
  "v_mfma_f32_16x16x32_bf16 a[60:63], v[188:191], v[132:135], a[60:63]\n\t" \
  "v_xor_b32_e32 v114, 0x8000, v114\n\t" \
  "v_xor_b32_e32 v115, 0x8000, v115\n\t" \
  "v_mfma_f32_16x16x32_bf16 a[0:3], v[160:163], v[128:131], a[0:3]\n\t" \
  "v_xor_b32_e32 v116, 0x8000, v116\n\t" \
  "v_xor_b32_e32 v117, 0x8000, v117\n\t" \
  "v_mfma_f32_16x16x32_bf16 a[4:7], v[164:167], v[128:131], a[4:7]\n\t" \
  "v_xor_b32_e32 v118, 0x8000, v118\n\t" \
  "v_xor_b32_e32 v119, 0x8000, v119\n\t" \
  "v_mfma_f32_16x16x32_bf16 a[8:11], v[168:171], v[128:131], a[8:11]\n\t" \
  "v_xor_b32_e32 v120, 0x8000, v120\n\t" \
  "v_xor_b32_e32 v121, 0x8000, v121\n\t" \
  "v_mfma_f32_16x16x32_bf16 a[12:15], v[172:175], v[128:131], a[12:15]\n\t" \
  "buffer_load_dwordx4 v107, s[56:59], s29 offen offset:3072 sc1 lds\n\t" \
  "v_mfma_f32_16x16x32_bf16 a[16:19], v[176:179], v[128:131], a[16:19]\n\t" \
  "s_mov_b32 s27, s39\n\t" \
  "s_add_u32 s29, s29, s33\n\t" \
  "v_mfma_f32_16x16x32_bf16 a[20:23], v[180:183], v[128:131], a[20:23]\n\t" \
  "s_cmp_eq_u32 s37, 13\n\t" \
  "v_mfma_f32_16x16x32_bf16 a[24:27], v[184:187], v[128:131], a[24:27]\n\t" \
  "v_mfma_f32_16x16x32_bf16 a[28:31], v[188:191], v[128:131], a[28:31]\n\t" \
  "s_cbranch_scc1 MK_KTV_%=\n\t" \
  "s_waitcnt lgkmcnt(0)\n\t" \
  "s_waitcnt vmcnt(16)\n\t" \
  "s_barrier\n\t" \
  "v_mfma_f32_16x16x32_bf16 a[224:227], v[224:227], v[220:223], a[224:227]\n\t" \
  "ds_read_b64_tr_b16 v[156:157], v121 offset:0\n\t" \
  "v_mfma_f32_16x16x32_bf16 a[228:231], v[228:231], v[220:223], a[228:231]\n\t" \
  "s_mov_b32 m0, s26\n\t" \
  "v_mfma_f32_16x16x32_bf16 a[232:235], v[232:235], v[220:223], a[232:235]\n\t" \
  "ds_read_b64_tr_b16 v[158:159], v121 offset:2048\n\t" \
  "v_mfma_f32_16x16x32_bf16 a[236:239], v[236:239], v[220:223], a[236:239]\n\t" \
  "buffer_load_dwordx4 v108, s[60:63], s28 offen offset:0 sc1 lds\n\t" \
  "v_mfma_f32_16x16x32_bf16 a[240:243], v[240:243], v[220:223], a[240:243]\n\t" \
  "ds_read_b128 v[160:163], v112 offset:0\n\t" \
  "v_mfma_f32_16x16x32_bf16 a[244:247], v[244:247], v[220:223], a[244:247]\n\t" \
  "s_bitcmp1_b32 s25, s37\n\t" \
  "v_mfma_f32_16x16x32_bf16 a[248:251], v[248:251], v[220:223], a[248:251]\n\t" \
  "ds_read_b128 v[164:167], v112 offset:2048\n\t" \
  "v_mfma_f32_16x16x32_bf16 a[252:255], v[252:255], v[220:223], a[252:255]\n\t" \
  "s_cbranch_scc1 MK_PLV_%=\n\t" \
  "MK_PLRV_%=:\n\t" \
  "v_mfma_f32_16x16x32_bf16 a[192:195], v[224:227], v[216:219], a[192:195]\n\t" \
  "ds_read_b128 v[168:171], v112 offset:4096\n\t" \
  "v_mfma_f32_16x16x32_bf16 a[196:199], v[228:231], v[216:219], a[196:199]\n\t" \
  "v_mfma_f32_16x16x32_bf16 a[200:203], v[232:235], v[216:219], a[200:203]\n\t" \
  "ds_read_b128 v[172:175], v112 offset:6144\n\t" \
  "v_mfma_f32_16x16x32_bf16 a[204:207], v[236:239], v[216:219], a[204:207]\n\t" \
  "buffer_load_dwordx4 v109, s[60:63], s28 offen offset:1024 sc1 lds\n\t" \
  "v_mfma_f32_16x16x32_bf16 a[208:211], v[240:243], v[216:219], a[208:211]\n\t" \
  "ds_read_b128 v[176:179], v112 offset:8192\n\t" \
  "v_mfma_f32_16x16x32_bf16 a[212:215], v[244:247], v[216:219], a[212:215]\n\t" \
  "v_mfma_f32_16x16x32_bf16 a[216:219], v[248:251], v[216:219], a[216:219]\n\t" \
  "ds_read_b128 v[180:183], v112 offset:10240\n\t" \
  "v_mfma_f32_16x16x32_bf16 a[220:223], v[252:255], v[216:219], a[220:223]\n\t" \
  "v_mfma_f32_16x16x32_bf16 a[160:163], v[224:227], v[212:215], a[160:163]\n\t" \
  "ds_read_b128 v[184:187], v112 offset:12288\n\t" \
  "v_mfma_f32_16x16x32_bf16 a[164:167], v[228:231], v[212:215], a[164:167]\n\t" \
  "v_mfma_f32_16x16x32_bf16 a[168:171], v[232:235], v[212:215], a[168:171]\n\t" \
  "ds_read_b128 v[188:191], v112 offset:14336\n\t" \
  "v_mfma_f32_16x16x32_bf16 a[172:175], v[236:239], v[212:215], a[172:175]\n\t" \
  "buffer_load_dwordx4 v110, s[60:63], s28 offen offset:2048 sc1 lds\n\t" \
  "v_mfma_f32_16x16x32_bf16 a[176:179], v[240:243], v[212:215], a[176:179]\n\t" \
  "ds_read_b64_tr_b16 v[152:153], v120 offset:0\n\t" \
  "v_mfma_f32_16x16x32_bf16 a[180:183], v[244:247], v[212:215], a[180:183]\n\t" \
  "v_mfma_f32_16x16x32_bf16 a[184:187], v[248:251], v[212:215], a[184:187]\n\t" \
  "ds_read_b64_tr_b16 v[154:155], v120 offset:2048\n\t" \
  "v_mfma_f32_16x16x32_bf16 a[188:191], v[252:255], v[212:215], a[188:191]\n\t" \
  "v_mfma_f32_16x16x32_bf16 a[128:131], v[224:227], v[208:211], a[128:131]\n\t" \
  "ds_read_b64_tr_b16 v[148:149], v119 offset:0\n\t" \
  "v_mfma_f32_16x16x32_bf16 a[132:135], v[228:231], v[208:211], a[132:135]\n\t" \
  "v_mfma_f32_16x16x32_bf16 a[136:139], v[232:235], v[208:211], a[136:139]\n\t" \
  "ds_read_b64_tr_b16 v[150:151], v119 offset:2048\n\t" \
  "v_mfma_f32_16x16x32_bf16 a[140:143], v[236:239], v[208:211], a[140:143]\n\t" \
  "buffer_load_dwordx4 v111, s[60:63], s28 offen offset:3072 sc1 lds\n\t" \
  "v_mfma_f32_16x16x32_bf16 a[144:147], v[240:243], v[208:211], a[144:147]\n\t" \
  "ds_read_b64_tr_b16 v[144:145], v118 offset:0\n\t" \
  "v_mfma_f32_16x16x32_bf16 a[148:151], v[244:247], v[208:211], a[148:151]\n\t" \
  "v_mfma_f32_16x16x32_bf16 a[152:155], v[248:251], v[208:211], a[152:155]\n\t" \
  "ds_read_b64_tr_b16 v[146:147], v118 offset:2048\n\t" \
  "v_mfma_f32_16x16x32_bf16 a[156:159], v[252:255], v[208:211], a[156:159]\n\t" \
  "v_mfma_f32_16x16x32_bf16 a[96:99], v[224:227], v[204:207], a[96:99]\n\t" \
  "ds_read_b64_tr_b16 v[140:141], v117 offset:0\n\t" \
  "v_mfma_f32_16x16x32_bf16 a[100:103], v[228:231], v[204:207], a[100:103]\n\t" \
  "s_mov_b32 m0, s27\n\t" \
  "v_mfma_f32_16x16x32_bf16 a[104:107], v[232:235], v[204:207], a[104:107]\n\t" \
  "ds_read_b64_tr_b16 v[142:143], v117 offset:2048\n\t" \
  "v_mfma_f32_16x16x32_bf16 a[108:111], v[236:239], v[204:207], a[108:111]\n\t" \
  "buffer_load_dwordx4 v104, s[56:59], s29 offen offset:0 sc1 lds\n\t" \
  "v_mfma_f32_16x16x32_bf16 a[112:115], v[240:243], v[204:207], a[112:115]\n\t" \
  "ds_read_b64_tr_b16 v[136:137], v116 offset:0\n\t" \
  "v_mfma_f32_16x16x32_bf16 a[116:119], v[244:247], v[204:207], a[116:119]\n\t" \
  "v_mfma_f32_16x16x32_bf16 a[120:123], v[248:251], v[204:207], a[120:123]\n\t" \
  "ds_read_b64_tr_b16 v[138:139], v116 offset:2048\n\t" \
  "v_mfma_f32_16x16x32_bf16 a[124:127], v[252:255], v[204:207], a[124:127]\n\t" \
  "v_mfma_f32_16x16x32_bf16 a[64:67], v[224:227], v[200:203], a[64:67]\n\t" \
  "ds_read_b64_tr_b16 v[132:133], v115 offset:0\n\t" \
  "v_mfma_f32_16x16x32_bf16 a[68:71], v[228:231], v[200:203], a[68:71]\n\t" \
  "v_mfma_f32_16x16x32_bf16 a[72:75], v[232:235], v[200:203], a[72:75]\n\t" \
  "ds_read_b64_tr_b16 v[134:135], v115 offset:2048\n\t" \
  "v_mfma_f32_16x16x32_bf16 a[76:79], v[236:239], v[200:203], a[76:79]\n\t" \
  "buffer_load_dwordx4 v105, s[56:59], s29 offen offset:1024 sc1 lds\n\t" \
  "v_mfma_f32_16x16x32_bf16 a[80:83], v[240:243], v[200:203], a[80:83]\n\t" \
  "ds_read_b64_tr_b16 v[128:129], v114 offset:0\n\t" \
  "v_mfma_f32_16x16x32_bf16 a[84:87], v[244:247], v[200:203], a[84:87]\n\t" \
  "v_mfma_f32_16x16x32_bf16 a[88:91], v[248:251], v[200:203], a[88:91]\n\t" \
  "ds_read_b64_tr_b16 v[130:131], v114 offset:2048\n\t" \
  "v_mfma_f32_16x16x32_bf16 a[92:95], v[252:255], v[200:203], a[92:95]\n\t" \
  "v_mfma_f32_16x16x32_bf16 a[32:35], v[224:227], v[196:199], a[32:35]\n\t" \
  "s_xor_b32 s26, s26, 0xc000\n\t" \
  "s_add_u32 s28, s28, s30\n\t" \
  "v_mfma_f32_16x16x32_bf16 a[36:39], v[228:231], v[196:199], a[36:39]\n\t" \
  "v_mfma_f32_16x16x32_bf16 a[40:43], v[232:235], v[196:199], a[40:43]\n\t" \
  "v_mfma_f32_16x16x32_bf16 a[44:47], v[236:239], v[196:199], a[44:47]\n\t" \
  "buffer_load_dwordx4 v106, s[56:59], s29 offen offset:2048 sc1 lds\n\t" \
  "v_mfma_f32_16x16x32_bf16 a[48:51], v[240:243], v[196:199], a[48:51]\n\t" \
  "v_mfma_f32_16x16x32_bf16 a[52:55], v[244:247], v[196:199], a[52:55]\n\t" \
  "v_mfma_f32_16x16x32_bf16 a[56:59], v[248:251], v[196:199], a[56:59]\n\t" \
  "v_mfma_f32_16x16x32_bf16 a[60:63], v[252:255], v[196:199], a[60:63]\n\t" \
  "v_mfma_f32_16x16x32_bf16 a[0:3], v[224:227], v[192:195], a[0:3]\n\t" \
  "v_mfma_f32_16x16x32_bf16 a[4:7], v[228:231], v[192:195], a[4:7]\n\t" \
  "v_mfma_f32_16x16x32_bf16 a[8:11], v[232:235], v[192:195], a[8:11]\n\t" \
  "v_mfma_f32_16x16x32_bf16 a[12:15], v[236:239], v[192:195], a[12:15]\n\t" \
  "buffer_load_dwordx4 v107, s[56:59], s29 offen offset:3072 sc1 lds\n\t" \
  "v_mfma_f32_16x16x32_bf16 a[16:19], v[240:243], v[192:195], a[16:19]\n\t" \
  "s_add_u32 s27, s27, 0x4000\n\t" \
  "s_add_u32 s29, s29, s34\n\t" \
  "v_mfma_f32_16x16x32_bf16 a[20:23], v[244:247], v[192:195], a[20:23]\n\t" \
  "s_add_u32 s37, s37, 1\n\t" \
  "v_mfma_f32_16x16x32_bf16 a[24:27], v[248:251], v[192:195], a[24:27]\n\t" \
  "v_mfma_f32_16x16x32_bf16 a[28:31], v[252:255], v[192:195], a[28:31]\n\t" \
  "s_branch MK_KL0V_%=\n\t" \
  "MK_PLV_%=:\n\t" \
  "s_cmp_eq_u32 s37, 10\n\t" \
  "s_cbranch_scc1 MK_PFV_%=\n\t" \
  "s_cmp_eq_u32 s37, s42\n\t" \
  "s_cbranch_scc1 MK_RLV_%=\n\t" \
  "s_cmp_lt_u32 s37, 10\n\t" \
  "s_cbranch_scc1 MK_RPV_%=\n\t" \
  "buffer_load_dwordx4 v[124:127], %1, %11, s80 offen sc1\n\t" \
  "s_branch MK_PLRV_%=\n\t" \
  "MK_PFV_%=:\n\t" \
  "buffer_store_dword v33, %12, %11, s81 offen offset:0 sc1\n\t" \
  "buffer_load_dwordx4 v[36:39], %1, s[84:87], s89 offen\n\t" \
  "s_branch MK_PLRV_%=\n\t" \
  "MK_RLV_%=:\n\t" \
  "v_readlane_b32 s41, v30, 0\n\t" \
  "v_readlane_b32 s40, v30, 1\n\t" \
  "s_nop 0\n\t" \
  "s_lshl_b32 s46, s40, 4\n\t" \
  "s_add_u32 s46, s46, s41\n\t" \
  "v_mad_u32_u24 v32, s40, v34, v35\n\t" \
  "s_cmp_eq_u32 s40, 3584\n\t" \
  "s_cselect_b64 s[64:65], s[94:95], s[92:93]\n\t" \
  "buffer_load_dwordx4 v[40:43], v32, s[64:67], s41 offen\n\t" \
  "buffer_load_dwordx4 v[44:47], v32, s[64:67], s46 offen\n\t" \
  "v_readlane_b32 s41, v30, 3\n\t" \
  "v_readlane_b32 s40, v30, 4\n\t" \
  "s_nop 0\n\t" \
  "s_lshl_b32 s46, s40, 4\n\t" \
  "s_add_u32 s46, s46, s41\n\t" \
  "v_mad_u32_u24 v32, s40, v34, v35\n\t" \
  "s_cmp_eq_u32 s40, 3584\n\t" \
  "s_cselect_b64 s[64:65], s[94:95], s[92:93]\n\t" \
  "buffer_load_dwordx4 v[48:51], v32, s[64:67], s41 offen\n\t" \
  "buffer_load_dwordx4 v[52:55], v32, s[64:67], s46 offen\n\t" \
  "v_readlane_b32 s41, v30, 6\n\t" \
  "v_readlane_b32 s40, v30, 7\n\t" \
  "s_nop 0\n\t" \
  "s_lshl_b32 s46, s40, 4\n\t" \
  "s_add_u32 s46, s46, s41\n\t" \
  "v_mad_u32_u24 v32, s40, v34, v35\n\t" \
  "s_cmp_eq_u32 s40, 3584\n\t" \
  "s_cselect_b64 s[64:65], s[94:95], s[92:93]\n\t" \
  "buffer_load_dwordx4 v[56:59], v32, s[64:67], s41 offen\n\t" \
  "buffer_load_dwordx4 v[60:63], v32, s[64:67], s46 offen\n\t" \
  "s_branch MK_PLRV_%=\n\t" \
  "MK_RPV_%=:\n\t" \
  "v_mfma_f32_16x16x32_bf16 v[64:67], v[40:43], %7, 0\n\t" \
  "v_mfma_f32_16x16x32_bf16 v[68:71], v[44:47], %7, 0\n\t" \
  "v_mfma_f32_16x16x32_bf16 v[72:75], v[40:43], %8, 0\n\t" \
  "v_mfma_f32_16x16x32_bf16 v[76:79], v[44:47], %8, 0\n\t" \
  "v_mfma_f32_16x16x32_bf16 v[80:83], v[48:51], %7, 0\n\t" \
  "v_mfma_f32_16x16x32_bf16 v[84:87], v[52:55], %7, 0\n\t" \
  "v_mfma_f32_16x16x32_bf16 v[88:91], v[48:51], %8, 0\n\t" \
  "v_mfma_f32_16x16x32_bf16 v[92:95], v[52:55], %8, 0\n\t" \
  "v_mfma_f32_16x16x32_bf16 v[96:99], v[56:59], %7, 0\n\t" \
  "v_mfma_f32_16x16x32_bf16 v[100:103], v[60:63], %7, 0\n\t" \
  "v_mfma_f32_16x16x32_bf16 v[40:43], v[56:59], %8, 0\n\t" \
  "v_mfma_f32_16x16x32_bf16 v[44:47], v[60:63], %8, 0\n\t" \
  "v_readlane_b32 s40, v30, 2\n\t" \
  "v_cvt_pk_bf16_f32 v64, v64, v65\n\t" \
  "v_cvt_pk_bf16_f32 v65, v66, v67\n\t" \
  "v_cvt_pk_bf16_f32 v66, v68, v69\n\t" \
  "v_cvt_pk_bf16_f32 v67, v70, v71\n\t" \
  "s_nop 0\n\t" \
  "buffer_store_dwordx4 v[64:67], %1, %11, s40 offen offset:0 sc1\n\t" \
  "v_cvt_pk_bf16_f32 v72, v72, v73\n\t" \
  "v_cvt_pk_bf16_f32 v73, v74, v75\n\t" \
  "v_cvt_pk_bf16_f32 v74, v76, v77\n\t" \
  "v_cvt_pk_bf16_f32 v75, v78, v79\n\t" \
  "s_nop 0\n\t" \
  "buffer_store_dwordx4 v[72:75], %1, %11, s40 offen offset:1024 sc1\n\t" \
  "v_readlane_b32 s40, v30, 5\n\t" \
  "v_cvt_pk_bf16_f32 v80, v80, v81\n\t" \
  "v_cvt_pk_bf16_f32 v81, v82, v83\n\t" \
  "v_cvt_pk_bf16_f32 v82, v84, v85\n\t" \
  "v_cvt_pk_bf16_f32 v83, v86, v87\n\t" \
  "s_nop 0\n\t" \
  "buffer_store_dwordx4 v[80:83], %1, %11, s40 offen offset:0 sc1\n\t" \
  "v_cvt_pk_bf16_f32 v88, v88, v89\n\t" \
  "v_cvt_pk_bf16_f32 v89, v90, v91\n\t" \
  "v_cvt_pk_bf16_f32 v90, v92, v93\n\t" \
  "v_cvt_pk_bf16_f32 v91, v94, v95\n\t" \
  "s_nop 0\n\t" \
  "buffer_store_dwordx4 v[88:91], %1, %11, s40 offen offset:1024 sc1\n\t" \
  "v_readlane_b32 s40, v30, 8\n\t" \
  "v_cvt_pk_bf16_f32 v96, v96, v97\n\t" \
  "v_cvt_pk_bf16_f32 v97, v98, v99\n\t" \
  "v_cvt_pk_bf16_f32 v98, v100, v101\n\t" \
  "v_cvt_pk_bf16_f32 v99, v102, v103\n\t" \
  "s_nop 0\n\t" \
  "buffer_store_dwordx4 v[96:99], %1, %11, s40 offen offset:0 sc1\n\t" \
  "v_cvt_pk_bf16_f32 v40, v40, v41\n\t" \
  "v_cvt_pk_bf16_f32 v41, v42, v43\n\t" \
  "v_cvt_pk_bf16_f32 v42, v44, v45\n\t" \
  "v_cvt_pk_bf16_f32 v43, v46, v47\n\t" \
  "s_nop 0\n\t" \
  "buffer_store_dwordx4 v[40:43], %1, %11, s40 offen offset:1024 sc1\n\t" \
  "s_branch MK_PLRV_%=\n\t" \
  "MK_KTV_%=:\n\t" \
  "s_waitcnt lgkmcnt(0)\n\t" \
  "s_waitcnt vmcnt(16)\n\t" \
  "s_barrier\n\t" \
  "v_cmp_ne_u32_e32 vcc, s90, v124\n\t" \
  "v_cmp_ne_u32_e64 s[48:49], s90, v125\n\t" \
  "v_cmp_ne_u32_e64 s[50:51], s90, v126\n\t" \
  "v_cmp_ne_u32_e64 s[52:53], s90, v127\n\t" \
  "s_or_b64 vcc, vcc, s[48:49]\n\t" \
  "s_or_b64 vcc, vcc, s[50:51]\n\t" \
  "s_or_b64 vcc, vcc, s[52:53]\n\t" \
  "s_cbranch_vccnz MK_F0VP_%=\n\t" \
  "MK_F0VR_%=:\n\t" \
  "v_mfma_f32_16x16x32_bf16 a[224:227], v[224:227], v[220:223], a[224:227]\n\t" \
  "ds_read_b64_tr_b16 v[156:157], v121 offset:0\n\t" \
  "v_mfma_f32_16x16x32_bf16 a[228:231], v[228:231], v[220:223], a[228:231]\n\t" \
  "v_add_u32_e32 v34, s31, %1\n\t" \
  "s_mov_b32 m0, s26\n\t" \
  "v_mfma_f32_16x16x32_bf16 a[232:235], v[232:235], v[220:223], a[232:235]\n\t" \
  "ds_read_b64_tr_b16 v[158:159], v121 offset:2048\n\t" \
  "v_mfma_f32_16x16x32_bf16 a[236:239], v[236:239], v[220:223], a[236:239]\n\t" \
  "buffer_load_dwordx4 v108, s[60:63], s28 offen offset:0 sc1 lds\n\t" \
  "v_mfma_f32_16x16x32_bf16 a[240:243], v[240:243], v[220:223], a[240:243]\n\t" \
  "ds_read_b128 v[160:163], v112 offset:0\n\t" \
  "v_mfma_f32_16x16x32_bf16 a[244:247], v[244:247], v[220:223], a[244:247]\n\t" \
  "v_mfma_f32_16x16x32_bf16 a[248:251], v[248:251], v[220:223], a[248:251]\n\t" \
  "ds_read_b128 v[164:167], v112 offset:2048\n\t" \
  "v_mfma_f32_16x16x32_bf16 a[252:255], v[252:255], v[220:223], a[252:255]\n\t" \
  "v_mfma_f32_16x16x32_bf16 a[192:195], v[224:227], v[216:219], a[192:195]\n\t" \
  "ds_read_b128 v[168:171], v112 offset:4096\n\t" \
  "buffer_load_dwordx4 v109, s[60:63], s28 offen offset:1024 sc1 lds\n\t" \
  "v_mfma_f32_16x16x32_bf16 a[196:199], v[228:231], v[216:219], a[196:199]\n\t" \
  "v_mfma_f32_16x16x32_bf16 a[200:203], v[232:235], v[216:219], a[200:203]\n\t" \
  "ds_read_b128 v[172:175], v112 offset:6144\n\t" \
  "v_mfma_f32_16x16x32_bf16 a[204:207], v[236:239], v[216:219], a[204:207]\n\t" \
  "v_mfma_f32_16x16x32_bf16 a[208:211], v[240:243], v[216:219], a[208:211]\n\t" \
  "ds_read_b128 v[176:179], v112 offset:8192\n\t" \
  "v_mfma_f32_16x16x32_bf16 a[212:215], v[244:247], v[216:219], a[212:215]\n\t" \
  "buffer_load_dwordx4 v110, s[60:63], s28 offen offset:2048 sc1 lds\n\t" \
  "v_mfma_f32_16x16x32_bf16 a[216:219], v[248:251], v[216:219], a[216:219]\n\t" \
  "ds_read_b128 v[180:183], v112 offset:10240\n\t" \
  "v_mfma_f32_16x16x32_bf16 a[220:223], v[252:255], v[216:219], a[220:223]\n\t" \
  "v_mfma_f32_16x16x32_bf16 a[160:163], v[224:227], v[212:215], a[160:163]\n\t" \
  "ds_read_b128 v[184:187], v112 offset:12288\n\t" \
  "v_mfma_f32_16x16x32_bf16 a[164:167], v[228:231], v[212:215], a[164:167]\n\t" \
  "v_mfma_f32_16x16x32_bf16 a[168:171], v[232:235], v[212:215], a[168:171]\n\t" \
  "ds_read_b128 v[188:191], v112 offset:14336\n\t" \
  "buffer_load_dwordx4 v111, s[60:63], s28 offen offset:3072 sc1 lds\n\t" \
  "v_mfma_f32_16x16x32_bf16 a[172:175], v[236:239], v[212:215], a[172:175]\n\t" \
  "v_mfma_f32_16x16x32_bf16 a[176:179], v[240:243], v[212:215], a[176:179]\n\t" \
  "ds_read_b64_tr_b16 v[152:153], v120 offset:0\n\t" \
  "v_mfma_f32_16x16x32_bf16 a[180:183], v[244:247], v[212:215], a[180:183]\n\t" \
  "s_add_u32 m0, s31, 98304\n\t" \
  "s_add_u32 s43, s68, 0\n\t" \
  "v_mfma_f32_16x16x32_bf16 a[184:187], v[248:251], v[212:215], a[184:187]\n\t" \
  "ds_read_b64_tr_b16 v[154:155], v120 offset:2048\n\t" \
  "v_mfma_f32_16x16x32_bf16 a[188:191], v[252:255], v[212:215], a[188:191]\n\t" \
  "buffer_load_dwordx4 v34, %11, s43 offen offset:0 sc1 lds\n\t" \
  "v_mfma_f32_16x16x32_bf16 a[128:131], v[224:227], v[208:211], a[128:131]\n\t" \
  "ds_read_b64_tr_b16 v[148:149], v119 offset:0\n\t" \
  "v_mfma_f32_16x16x32_bf16 a[132:135], v[228:231], v[208:211], a[132:135]\n\t" \
  "v_mfma_f32_16x16x32_bf16 a[136:139], v[232:235], v[208:211], a[136:139]\n\t" \
  "ds_read_b64_tr_b16 v[150:151], v119 offset:2048\n\t" \
  "v_mfma_f32_16x16x32_bf16 a[140:143], v[236:239], v[208:211], a[140:143]\n\t" \
  "v_mfma_f32_16x16x32_bf16 a[144:147], v[240:243], v[208:211], a[144:147]\n\t" \
  "ds_read_b64_tr_b16 v[144:145], v118 offset:0\n\t" \
  "buffer_load_dwordx4 v34, %11, s43 offen offset:1024 sc1 lds\n\t" \
  "v_mfma_f32_16x16x32_bf16 a[148:151], v[244:247], v[208:211], a[148:151]\n\t" \
  "v_mfma_f32_16x16x32_bf16 a[152:155], v[248:251], v[208:211], a[152:155]\n\t" \
  "ds_read_b64_tr_b16 v[146:147], v118 offset:2048\n\t" \
  "v_mfma_f32_16x16x32_bf16 a[156:159], v[252:255], v[208:211], a[156:159]\n\t" \
  "v_mfma_f32_16x16x32_bf16 a[96:99], v[224:227], v[204:207], a[96:99]\n\t" \
  "ds_read_b64_tr_b16 v[140:141], v117 offset:0\n\t" \
  "v_mfma_f32_16x16x32_bf16 a[100:103], v[228:231], v[204:207], a[100:103]\n\t" \
  "buffer_load_dwordx4 v34, %11, s43 offen offset:2048 sc1 lds\n\t" \
  "v_mfma_f32_16x16x32_bf16 a[104:107], v[232:235], v[204:207], a[104:107]\n\t" \
  "ds_read_b64_tr_b16 v[142:143], v117 offset:2048\n\t" \
  "v_mfma_f32_16x16x32_bf16 a[108:111], v[236:239], v[204:207], a[108:111]\n\t" \
  "v_mfma_f32_16x16x32_bf16 a[112:115], v[240:243], v[204:207], a[112:115]\n\t" \
  "ds_read_b64_tr_b16 v[136:137], v116 offset:0\n\t" \
  "v_mfma_f32_16x16x32_bf16 a[116:119], v[244:247], v[204:207], a[116:119]\n\t" \
  "v_mfma_f32_16x16x32_bf16 a[120:123], v[248:251], v[204:207], a[120:123]\n\t" \
  "ds_read_b64_tr_b16 v[138:139], v116 offset:2048\n\t" \
  "buffer_load_dwordx4 v34, %11, s43 offen offset:3072 sc1 lds\n\t" \
  "v_mfma_f32_16x16x32_bf16 a[124:127], v[252:255], v[204:207], a[124:127]\n\t" \
  "v_mfma_f32_16x16x32_bf16 a[64:67], v[224:227], v[200:203], a[64:67]\n\t" \
  "ds_read_b64_tr_b16 v[132:133], v115 offset:0\n\t" \
  "v_mfma_f32_16x16x32_bf16 a[68:71], v[228:231], v[200:203], a[68:71]\n\t" \
  "v_mfma_f32_16x16x32_bf16 a[72:75], v[232:235], v[200:203], a[72:75]\n\t" \
  "ds_read_b64_tr_b16 v[134:135], v115 offset:2048\n\t" \
  "v_mfma_f32_16x16x32_bf16 a[76:79], v[236:239], v[200:203], a[76:79]\n\t" \
  "buffer_load_dwordx4 v[40:43], %17, %11, s72 offen offset:0 sc1\n\t" \
  "v_mfma_f32_16x16x32_bf16 a[80:83], v[240:243], v[200:203], a[80:83]\n\t" \
  "ds_read_b64_tr_b16 v[128:129], v114 offset:0\n\t" \
  "v_mfma_f32_16x16x32_bf16 a[84:87], v[244:247], v[200:203], a[84:87]\n\t" \
  "v_mfma_f32_16x16x32_bf16 a[88:91], v[248:251], v[200:203], a[88:91]\n\t" \
  "ds_read_b64_tr_b16 v[130:131], v114 offset:2048\n\t" \
  "v_mfma_f32_16x16x32_bf16 a[92:95], v[252:255], v[200:203], a[92:95]\n\t" \
  "v_mfma_f32_16x16x32_bf16 a[32:35], v[224:227], v[196:199], a[32:35]\n\t" \
  "buffer_load_dwordx4 v[44:47], %17, %11, s76 offen offset:0 sc1\n\t" \
  "v_mfma_f32_16x16x32_bf16 a[36:39], v[228:231], v[196:199], a[36:39]\n\t" \
  "v_mfma_f32_16x16x32_bf16 a[40:43], v[232:235], v[196:199], a[40:43]\n\t" \
  "v_mfma_f32_16x16x32_bf16 a[44:47], v[236:239], v[196:199], a[44:47]\n\t" \
  "v_mfma_f32_16x16x32_bf16 a[48:51], v[240:243], v[196:199], a[48:51]\n\t" \
  "v_mfma_f32_16x16x32_bf16 a[52:55], v[244:247], v[196:199], a[52:55]\n\t" \
  "buffer_load_dwordx4 v[48:51], %17, %11, s72 offen offset:1024 sc1\n\t" \
  "v_mfma_f32_16x16x32_bf16 a[56:59], v[248:251], v[196:199], a[56:59]\n\t" \
  "v_mfma_f32_16x16x32_bf16 a[60:63], v[252:255], v[196:199], a[60:63]\n\t" \
  "v_mfma_f32_16x16x32_bf16 a[0:3], v[224:227], v[192:195], a[0:3]\n\t" \
  "v_mfma_f32_16x16x32_bf16 a[4:7], v[228:231], v[192:195], a[4:7]\n\t" \
  "v_mfma_f32_16x16x32_bf16 a[8:11], v[232:235], v[192:195], a[8:11]\n\t" \
  "buffer_load_dwordx4 v[52:55], %17, %11, s76 offen offset:1024 sc1\n\t" \
  "v_mfma_f32_16x16x32_bf16 a[12:15], v[236:239], v[192:195], a[12:15]\n\t" \
  "v_mfma_f32_16x16x32_bf16 a[16:19], v[240:243], v[192:195], a[16:19]\n\t" \
  "v_mfma_f32_16x16x32_bf16 a[20:23], v[244:247], v[192:195], a[20:23]\n\t" \
  "v_mfma_f32_16x16x32_bf16 a[24:27], v[248:251], v[192:195], a[24:27]\n\t" \
  "v_mfma_f32_16x16x32_bf16 a[28:31], v[252:255], v[192:195], a[28:31]\n\t" \
  "s_waitcnt lgkmcnt(0)\n\t" \
  "s_waitcnt vmcnt(20)\n\t" \
  "s_barrier\n\t" \
  "v_mfma_f32_16x16x32_bf16 a[224:227], v[160:163], v[156:159], a[224:227]\n\t" \
  "ds_read_b64_tr_b16 v[220:221], v121 offset:16384\n\t" \
  "v_mfma_f32_16x16x32_bf16 a[228:231], v[164:167], v[156:159], a[228:231]\n\t" \
  "s_add_u32 m0, s31, 114688\n\t" \
  "s_add_u32 s43, s68, 16384\n\t" \
  "v_mfma_f32_16x16x32_bf16 a[232:235], v[168:171], v[156:159], a[232:235]\n\t" \
  "ds_read_b64_tr_b16 v[222:223], v121 offset:18432\n\t" \
  "v_mfma_f32_16x16x32_bf16 a[236:239], v[172:175], v[156:159], a[236:239]\n\t" \
  "buffer_load_dwordx4 v34, %11, s43 offen offset:0 sc1 lds\n\t" \
  "v_mfma_f32_16x16x32_bf16 a[240:243], v[176:179], v[156:159], a[240:243]\n\t" \
  "ds_read_b128 v[224:227], v113 offset:0\n\t" \
  "v_mfma_f32_16x16x32_bf16 a[244:247], v[180:183], v[156:159], a[244:247]\n\t" \
  "v_mfma_f32_16x16x32_bf16 a[248:251], v[184:187], v[156:159], a[248:251]\n\t" \
  "ds_read_b128 v[228:231], v113 offset:2048\n\t" \
  "v_mfma_f32_16x16x32_bf16 a[252:255], v[188:191], v[156:159], a[252:255]\n\t" \
  "v_mfma_f32_16x16x32_bf16 a[192:195], v[160:163], v[152:155], a[192:195]\n\t" \
  "ds_read_b128 v[232:235], v113 offset:4096\n\t" \
  "buffer_load_dwordx4 v34, %11, s43 offen offset:1024 sc1 lds\n\t" \
  "v_mfma_f32_16x16x32_bf16 a[196:199], v[164:167], v[152:155], a[196:199]\n\t" \
  "v_mfma_f32_16x16x32_bf16 a[200:203], v[168:171], v[152:155], a[200:203]\n\t" \
  "ds_read_b128 v[236:239], v113 offset:6144\n\t" \
  "v_mfma_f32_16x16x32_bf16 a[204:207], v[172:175], v[152:155], a[204:207]\n\t" \
  "v_mfma_f32_16x16x32_bf16 a[208:211], v[176:179], v[152:155], a[208:211]\n\t" \
  "ds_read_b128 v[240:243], v113 offset:8192\n\t" \
  "v_mfma_f32_16x16x32_bf16 a[212:215], v[180:183], v[152:155], a[212:215]\n\t" \
  "buffer_load_dwordx4 v34, %11, s43 offen offset:2048 sc1 lds\n\t" \
  "v_mfma_f32_16x16x32_bf16 a[216:219], v[184:187], v[152:155], a[216:219]\n\t" \
  "ds_read_b128 v[244:247], v113 offset:10240\n\t" \
  "v_mfma_f32_16x16x32_bf16 a[220:223], v[188:191], v[152:155], a[220:223]\n\t" \
  "v_mfma_f32_16x16x32_bf16 a[160:163], v[160:163], v[148:151], a[160:163]\n\t" \
  "ds_read_b128 v[248:251], v113 offset:12288\n\t" \
  "v_mfma_f32_16x16x32_bf16 a[164:167], v[164:167], v[148:151], a[164:167]\n\t" \
  "v_mfma_f32_16x16x32_bf16 a[168:171], v[168:171], v[148:151], a[168:171]\n\t" \
  "ds_read_b128 v[252:255], v113 offset:14336\n\t" \
  "buffer_load_dwordx4 v34, %11, s43 offen offset:3072 sc1 lds\n\t" \
  "v_mfma_f32_16x16x32_bf16 a[172:175], v[172:175], v[148:151], a[172:175]\n\t" \
  "v_mfma_f32_16x16x32_bf16 a[176:179], v[176:179], v[148:151], a[176:179]\n\t" \
  "ds_read_b64_tr_b16 v[216:217], v120 offset:16384\n\t" \
  "v_mfma_f32_16x16x32_bf16 a[180:183], v[180:183], v[148:151], a[180:183]\n\t" \
  "s_add_u32 m0, s31, 0\n\t" \
  "s_add_u32 s43, s68, 65536\n\t" \
  "v_mfma_f32_16x16x32_bf16 a[184:187], v[184:187], v[148:151], a[184:187]\n\t" \
  "ds_read_b64_tr_b16 v[218:219], v120 offset:18432\n\t" \
  "v_mfma_f32_16x16x32_bf16 a[188:191], v[188:191], v[148:151], a[188:191]\n\t" \
  "buffer_load_dwordx4 v34, %11, s43 offen offset:0 sc1 lds\n\t" \
  "v_mfma_f32_16x16x32_bf16 a[128:131], v[160:163], v[144:147], a[128:131]\n\t" \
  "ds_read_b64_tr_b16 v[212:213], v119 offset:16384\n\t" \
  "v_mfma_f32_16x16x32_bf16 a[132:135], v[164:167], v[144:147], a[132:135]\n\t" \
  "v_mfma_f32_16x16x32_bf16 a[136:139], v[168:171], v[144:147], a[136:139]\n\t" \
  "ds_read_b64_tr_b16 v[214:215], v119 offset:18432\n\t" \
  "v_mfma_f32_16x16x32_bf16 a[140:143], v[172:175], v[144:147], a[140:143]\n\t" \
  "v_mfma_f32_16x16x32_bf16 a[144:147], v[176:179], v[144:147], a[144:147]\n\t" \
  "ds_read_b64_tr_b16 v[208:209], v118 offset:16384\n\t" \
  "buffer_load_dwordx4 v34, %11, s43 offen offset:1024 sc1 lds\n\t" \
  "v_mfma_f32_16x16x32_bf16 a[148:151], v[180:183], v[144:147], a[148:151]\n\t" \
  "v_mfma_f32_16x16x32_bf16 a[152:155], v[184:187], v[144:147], a[152:155]\n\t" \
  "ds_read_b64_tr_b16 v[210:211], v118 offset:18432\n\t" \
  "v_mfma_f32_16x16x32_bf16 a[156:159], v[188:191], v[144:147], a[156:159]\n\t" \
  "v_mfma_f32_16x16x32_bf16 a[96:99], v[160:163], v[140:143], a[96:99]\n\t" \
  "ds_read_b64_tr_b16 v[204:205], v117 offset:16384\n\t" \
  "v_mfma_f32_16x16x32_bf16 a[100:103], v[164:167], v[140:143], a[100:103]\n\t" \
  "buffer_load_dwordx4 v34, %11, s43 offen offset:2048 sc1 lds\n\t" \
  "v_mfma_f32_16x16x32_bf16 a[104:107], v[168:171], v[140:143], a[104:107]\n\t" \
  "ds_read_b64_tr_b16 v[206:207], v117 offset:18432\n\t" \
  "v_mfma_f32_16x16x32_bf16 a[108:111], v[172:175], v[140:143], a[108:111]\n\t" \
  "v_mfma_f32_16x16x32_bf16 a[112:115], v[176:179], v[140:143], a[112:115]\n\t" \
  "ds_read_b64_tr_b16 v[200:201], v116 offset:16384\n\t" \
  "v_mfma_f32_16x16x32_bf16 a[116:119], v[180:183], v[140:143], a[116:119]\n\t" \
  "v_mfma_f32_16x16x32_bf16 a[120:123], v[184:187], v[140:143], a[120:123]\n\t" \
  "ds_read_b64_tr_b16 v[202:203], v116 offset:18432\n\t" \
  "buffer_load_dwordx4 v34, %11, s43 offen offset:3072 sc1 lds\n\t" \
  "v_mfma_f32_16x16x32_bf16 a[124:127], v[188:191], v[140:143], a[124:127]\n\t" \
  "v_mfma_f32_16x16x32_bf16 a[64:67], v[160:163], v[136:139], a[64:67]\n\t" \
  "ds_read_b64_tr_b16 v[196:197], v115 offset:16384\n\t" \
  "v_mfma_f32_16x16x32_bf16 a[68:71], v[164:167], v[136:139], a[68:71]\n\t" \
  "v_mfma_f32_16x16x32_bf16 a[72:75], v[168:171], v[136:139], a[72:75]\n\t" \
  "ds_read_b64_tr_b16 v[198:199], v115 offset:18432\n\t" \
  "v_mfma_f32_16x16x32_bf16 a[76:79], v[172:175], v[136:139], a[76:79]\n\t" \
  "buffer_load_dwordx4 v[56:59], %17, %11, s72 offen offset:2048 sc1\n\t" \
  "v_mfma_f32_16x16x32_bf16 a[80:83], v[176:179], v[136:139], a[80:83]\n\t" \
  "ds_read_b64_tr_b16 v[192:193], v114 offset:16384\n\t" \
  "v_mfma_f32_16x16x32_bf16 a[84:87], v[180:183], v[136:139], a[84:87]\n\t" \
  "v_mfma_f32_16x16x32_bf16 a[88:91], v[184:187], v[136:139], a[88:91]\n\t" \
  "ds_read_b64_tr_b16 v[194:195], v114 offset:18432\n\t" \
  "v_mfma_f32_16x16x32_bf16 a[92:95], v[188:191], v[136:139], a[92:95]\n\t" \
  "v_mfma_f32_16x16x32_bf16 a[32:35], v[160:163], v[132:135], a[32:35]\n\t" \
  "buffer_load_dwordx4 v[60:63], %17, %11, s76 offen offset:2048 sc1\n\t" \
  "v_add_u32_e32 v112, 4294901760, v112\n\t" \
  "v_mfma_f32_16x16x32_bf16 a[36:39], v[164:167], v[132:135], a[36:39]\n\t" \
  "v_add_u32_e32 v113, 4294901760, v113\n\t" \
  "v_mfma_f32_16x16x32_bf16 a[40:43], v[168:171], v[132:135], a[40:43]\n\t" \
  "v_mfma_f32_16x16x32_bf16 a[44:47], v[172:175], v[132:135], a[44:47]\n\t" \
  "v_xor_b32_e32 v114, 0x8000, v114\n\t" \
  "v_mfma_f32_16x16x32_bf16 a[48:51], v[176:179], v[132:135], a[48:51]\n\t" \
  "v_xor_b32_e32 v115, 0x8000, v115\n\t" \
  "v_mfma_f32_16x16x32_bf16 a[52:55], v[180:183], v[132:135], a[52:55]\n\t" \
  "buffer_load_dwordx4 v[64:67], %17, %11, s72 offen offset:3072 sc1\n\t" \
  "v_mfma_f32_16x16x32_bf16 a[56:59], v[184:187], v[132:135], a[56:59]\n\t" \
  "v_xor_b32_e32 v116, 0x8000, v116\n\t" \
  "v_mfma_f32_16x16x32_bf16 a[60:63], v[188:191], v[132:135], a[60:63]\n\t" \
  "v_mfma_f32_16x16x32_bf16 a[0:3], v[160:163], v[128:131], a[0:3]\n\t" \
  "v_xor_b32_e32 v117, 0x8000, v117\n\t" \
  "v_mfma_f32_16x16x32_bf16 a[4:7], v[164:167], v[128:131], a[4:7]\n\t" \
  "v_xor_b32_e32 v118, 0x8000, v118\n\t" \
  "v_mfma_f32_16x16x32_bf16 a[8:11], v[168:171], v[128:131], a[8:11]\n\t" \
  "buffer_load_dwordx4 v[68:71], %17, %11, s76 offen offset:3072 sc1\n\t" \
  "v_mfma_f32_16x16x32_bf16 a[12:15], v[172:175], v[128:131], a[12:15]\n\t" \
  "v_xor_b32_e32 v119, 0x8000, v119\n\t" \
  "v_mfma_f32_16x16x32_bf16 a[16:19], v[176:179], v[128:131], a[16:19]\n\t" \
  "v_xor_b32_e32 v120, 0x8000, v120\n\t" \
  "v_mfma_f32_16x16x32_bf16 a[20:23], v[180:183], v[128:131], a[20:23]\n\t" \
  "v_mfma_f32_16x16x32_bf16 a[24:27], v[184:187], v[128:131], a[24:27]\n\t" \
  "v_xor_b32_e32 v121, 0x8000, v121\n\t" \
  "v_mfma_f32_16x16x32_bf16 a[28:31], v[188:191], v[128:131], a[28:31]\n\t" \
  "s_waitcnt lgkmcnt(0)\n\t" \
  "s_waitcnt vmcnt(24)\n\t" \
  "s_barrier\n\t" \
  "v_mfma_f32_16x16x32_bf16 a[224:227], v[224:227], v[220:223], a[224:227]\n\t" \
  "ds_read_b64_tr_b16 v[156:157], v121 offset:0\n\t" \
  "v_mfma_f32_16x16x32_bf16 a[228:231], v[228:231], v[220:223], a[228:231]\n\t" \
  "s_add_u32 m0, s31, 131072\n\t" \
  "s_add_u32 s43, s68, 32768\n\t" \
  "v_mfma_f32_16x16x32_bf16 a[232:235], v[232:235], v[220:223], a[232:235]\n\t" \
  "ds_read_b64_tr_b16 v[158:159], v121 offset:2048\n\t" \
  "v_mfma_f32_16x16x32_bf16 a[236:239], v[236:239], v[220:223], a[236:239]\n\t" \
  "buffer_load_dwordx4 v34, %11, s43 offen offset:0 sc1 lds\n\t" \
  "v_mfma_f32_16x16x32_bf16 a[240:243], v[240:243], v[220:223], a[240:243]\n\t" \
  "ds_read_b128 v[160:163], v112 offset:0\n\t" \
  "v_mfma_f32_16x16x32_bf16 a[244:247], v[244:247], v[220:223], a[244:247]\n\t" \
  "v_readlane_b32 s44, v29, 35\n\t" \
  "v_mfma_f32_16x16x32_bf16 a[248:251], v[248:251], v[220:223], a[248:251]\n\t" \
  "ds_read_b128 v[164:167], v112 offset:2048\n\t" \
  "v_mfma_f32_16x16x32_bf16 a[252:255], v[252:255], v[220:223], a[252:255]\n\t" \
  "v_readlane_b32 s69, v29, 36\n\t" \
  "v_mfma_f32_16x16x32_bf16 a[192:195], v[224:227], v[216:219], a[192:195]\n\t" \
  "ds_read_b128 v[168:171], v112 offset:4096\n\t" \
  "buffer_load_dwordx4 v34, %11, s43 offen offset:1024 sc1 lds\n\t" \
  "v_mfma_f32_16x16x32_bf16 a[196:199], v[228:231], v[216:219], a[196:199]\n\t" \
  "v_mfma_f32_16x16x32_bf16 a[200:203], v[232:235], v[216:219], a[200:203]\n\t" \
  "ds_read_b128 v[172:175], v112 offset:6144\n\t" \
  "v_readlane_b32 s70, v29, 37\n\t" \
  "v_mfma_f32_16x16x32_bf16 a[204:207], v[236:239], v[216:219], a[204:207]\n\t" \
  "v_mfma_f32_16x16x32_bf16 a[208:211], v[240:243], v[216:219], a[208:211]\n\t" \
  "ds_read_b128 v[176:179], v112 offset:8192\n\t" \
  "v_mfma_f32_16x16x32_bf16 a[212:215], v[244:247], v[216:219], a[212:215]\n\t" \
  "buffer_load_dwordx4 v34, %11, s43 offen offset:2048 sc1 lds\n\t" \
  "v_mfma_f32_16x16x32_bf16 a[216:219], v[248:251], v[216:219], a[216:219]\n\t" \
  "ds_read_b128 v[180:183], v112 offset:10240\n\t" \
  "s_mul_i32 s34, s69, 0\n\t" \
  "v_mfma_f32_16x16x32_bf16 a[220:223], v[252:255], v[216:219], a[220:223]\n\t" \
  "s_add_u32 s34, s34, s44\n\t" \
  "v_mfma_f32_16x16x32_bf16 a[160:163], v[224:227], v[212:215], a[160:163]\n\t" \
  "ds_read_b128 v[184:187], v112 offset:12288\n\t" \
  "v_mfma_f32_16x16x32_bf16 a[164:167], v[228:231], v[212:215], a[164:167]\n\t" \
  "s_mul_i32 s35, s69, 1\n\t" \
  "v_mfma_f32_16x16x32_bf16 a[168:171], v[232:235], v[212:215], a[168:171]\n\t" \
  "ds_read_b128 v[188:191], v112 offset:14336\n\t" \
  "buffer_load_dwordx4 v34, %11, s43 offen offset:3072 sc1 lds\n\t" \
  "v_mfma_f32_16x16x32_bf16 a[172:175], v[236:239], v[212:215], a[172:175]\n\t" \
  "v_mfma_f32_16x16x32_bf16 a[176:179], v[240:243], v[212:215], a[176:179]\n\t" \
  "ds_read_b64_tr_b16 v[152:153], v120 offset:0\n\t" \
  "s_add_u32 s35, s35, s44\n\t" \
  "v_mfma_f32_16x16x32_bf16 a[180:183], v[244:247], v[212:215], a[180:183]\n\t" \
  "s_add_u32 m0, s31, 16384\n\t" \
  "s_add_u32 s43, s68, 81920\n\t" \
  "v_mfma_f32_16x16x32_bf16 a[184:187], v[248:251], v[212:215], a[184:187]\n\t" \
  "ds_read_b64_tr_b16 v[154:155], v120 offset:2048\n\t" \
  "v_mfma_f32_16x16x32_bf16 a[188:191], v[252:255], v[212:215], a[188:191]\n\t" \
  "buffer_load_dwordx4 v34, %11, s43 offen offset:0 sc1 lds\n\t" \
  "v_mfma_f32_16x16x32_bf16 a[128:131], v[224:227], v[208:211], a[128:131]\n\t" \
  "ds_read_b64_tr_b16 v[148:149], v119 offset:0\n\t" \
  "s_mul_i32 s36, s69, 2\n\t" \
  "v_mfma_f32_16x16x32_bf16 a[132:135], v[228:231], v[208:211], a[132:135]\n\t" \
  "v_mfma_f32_16x16x32_bf16 a[136:139], v[232:235], v[208:211], a[136:139]\n\t" \
  "ds_read_b64_tr_b16 v[150:151], v119 offset:2048\n\t" \
  "s_add_u32 s36, s36, s44\n\t" \
  "v_mfma_f32_16x16x32_bf16 a[140:143], v[236:239], v[208:211], a[140:143]\n\t" \
  "v_mfma_f32_16x16x32_bf16 a[144:147], v[240:243], v[208:211], a[144:147]\n\t" \
  "ds_read_b64_tr_b16 v[144:145], v118 offset:0\n\t" \
  "buffer_load_dwordx4 v34, %11, s43 offen offset:1024 sc1 lds\n\t" \
  "v_mfma_f32_16x16x32_bf16 a[148:151], v[244:247], v[208:211], a[148:151]\n\t" \
  "s_mul_i32 s37, s69, 3\n\t" \
  "v_mfma_f32_16x16x32_bf16 a[152:155], v[248:251], v[208:211], a[152:155]\n\t" \
  "ds_read_b64_tr_b16 v[146:147], v118 offset:2048\n\t" \
  "v_mfma_f32_16x16x32_bf16 a[156:159], v[252:255], v[208:211], a[156:159]\n\t" \
  "s_add_u32 s37, s37, s44\n\t" \
  "v_mfma_f32_16x16x32_bf16 a[96:99], v[224:227], v[204:207], a[96:99]\n\t" \
  "ds_read_b64_tr_b16 v[140:141], v117 offset:0\n\t" \
  "v_mfma_f32_16x16x32_bf16 a[100:103], v[228:231], v[204:207], a[100:103]\n\t" \
  "buffer_load_dwordx4 v34, %11, s43 offen offset:2048 sc1 lds\n\t" \
  "s_mul_i32 s38, s69, 4\n\t" \
  "v_mfma_f32_16x16x32_bf16 a[104:107], v[232:235], v[204:207], a[104:107]\n\t" \
  "ds_read_b64_tr_b16 v[142:143], v117 offset:2048\n\t" \
  "v_mfma_f32_16x16x32_bf16 a[108:111], v[236:239], v[204:207], a[108:111]\n\t" \
  "s_add_u32 s38, s38, s44\n\t" \
  "v_mfma_f32_16x16x32_bf16 a[112:115], v[240:243], v[204:207], a[112:115]\n\t" \
  "ds_read_b64_tr_b16 v[136:137], v116 offset:0\n\t" \
  "v_mfma_f32_16x16x32_bf16 a[116:119], v[244:247], v[204:207], a[116:119]\n\t" \
  "s_mul_i32 s39, s69, 5\n\t" \
  "v_mfma_f32_16x16x32_bf16 a[120:123], v[248:251], v[204:207], a[120:123]\n\t" \
  "ds_read_b64_tr_b16 v[138:139], v116 offset:2048\n\t" \
  "buffer_load_dwordx4 v34, %11, s43 offen offset:3072 sc1 lds\n\t" \
  "v_mfma_f32_16x16x32_bf16 a[124:127], v[252:255], v[204:207], a[124:127]\n\t" \
  "s_add_u32 s39, s39, s44\n\t" \
  "v_mfma_f32_16x16x32_bf16 a[64:67], v[224:227], v[200:203], a[64:67]\n\t" \
  "ds_read_b64_tr_b16 v[132:133], v115 offset:0\n\t" \
  "v_mfma_f32_16x16x32_bf16 a[68:71], v[228:231], v[200:203], a[68:71]\n\t" \
  "s_mul_i32 s40, s69, 6\n\t" \
  "v_mfma_f32_16x16x32_bf16 a[72:75], v[232:235], v[200:203], a[72:75]\n\t" \
  "ds_read_b64_tr_b16 v[134:135], v115 offset:2048\n\t" \
  "v_mfma_f32_16x16x32_bf16 a[76:79], v[236:239], v[200:203], a[76:79]\n\t" \
  "buffer_load_dwordx4 v[72:75], %17, %11, s73 offen offset:0 sc1\n\t" \
  "v_mfma_f32_16x16x32_bf16 a[80:83], v[240:243], v[200:203], a[80:83]\n\t" \
  "ds_read_b64_tr_b16 v[128:129], v114 offset:0\n\t" \
  "s_add_u32 s40, s40, s44\n\t" \
  "v_mfma_f32_16x16x32_bf16 a[84:87], v[244:247], v[200:203], a[84:87]\n\t" \
  "v_mfma_f32_16x16x32_bf16 a[88:91], v[248:251], v[200:203], a[88:91]\n\t" \
  "ds_read_b64_tr_b16 v[130:131], v114 offset:2048\n\t" \
  "s_mul_i32 s41, s69, 7\n\t" \
  "v_mfma_f32_16x16x32_bf16 a[92:95], v[252:255], v[200:203], a[92:95]\n\t" \
  "v_mfma_f32_16x16x32_bf16 a[32:35], v[224:227], v[196:199], a[32:35]\n\t" \
  "buffer_load_dwordx4 v[76:79], %17, %11, s77 offen offset:0 sc1\n\t" \
  "s_add_u32 s41, s41, s44\n\t" \
  "v_mfma_f32_16x16x32_bf16 a[36:39], v[228:231], v[196:199], a[36:39]\n\t" \
  "v_mfma_f32_16x16x32_bf16 a[40:43], v[232:235], v[196:199], a[40:43]\n\t" \
  "s_mul_i32 s42, s70, 1\n\t" \
  "v_mfma_f32_16x16x32_bf16 a[44:47], v[236:239], v[196:199], a[44:47]\n\t" \
  "v_add_u32_e32 v35, s42, %1\n\t" \
  "v_mfma_f32_16x16x32_bf16 a[48:51], v[240:243], v[196:199], a[48:51]\n\t" \
  "s_mul_i32 s42, s70, 2\n\t" \
  "v_mfma_f32_16x16x32_bf16 a[52:55], v[244:247], v[196:199], a[52:55]\n\t" \
  "buffer_load_dwordx4 v[80:83], %17, %11, s73 offen offset:1024 sc1\n\t" \
  "v_mfma_f32_16x16x32_bf16 a[56:59], v[248:251], v[196:199], a[56:59]\n\t" \
  "v_add_u32_e32 v36, s42, %1\n\t" \
  "v_mfma_f32_16x16x32_bf16 a[60:63], v[252:255], v[196:199], a[60:63]\n\t" \
  "v_mfma_f32_16x16x32_bf16 a[0:3], v[224:227], v[192:195], a[0:3]\n\t" \
  "s_mul_i32 s42, s70, 3\n\t" \
  "v_mfma_f32_16x16x32_bf16 a[4:7], v[228:231], v[192:195], a[4:7]\n\t" \
  "v_add_u32_e32 v38, s42, %1\n\t" \
  "v_mfma_f32_16x16x32_bf16 a[8:11], v[232:235], v[192:195], a[8:11]\n\t" \
  "buffer_load_dwordx4 v[84:87], %17, %11, s77 offen offset:1024 sc1\n\t" \
  "v_mfma_f32_16x16x32_bf16 a[12:15], v[236:239], v[192:195], a[12:15]\n\t" \
  "v_mov_b32_e32 v37, 0xff800000\n\t" \
  "v_mfma_f32_16x16x32_bf16 a[16:19], v[240:243], v[192:195], a[16:19]\n\t" \
  "v_mfma_f32_16x16x32_bf16 a[20:23], v[244:247], v[192:195], a[20:23]\n\t" \
  "v_add_u32_e32 v32, 98304, %1\n\t" \
  "v_mfma_f32_16x16x32_bf16 a[24:27], v[248:251], v[192:195], a[24:27]\n\t" \
  "s_mov_b32 s28, 98304\n\t" \
  "v_mfma_f32_16x16x32_bf16 a[28:31], v[252:255], v[192:195], a[28:31]\n\t" \
  "s_waitcnt lgkmcnt(0)\n\t" \
  "s_waitcnt vmcnt(32)\n\t" \
  "s_barrier\n\t" \
  "v_mfma_f32_16x16x32_bf16 a[224:227], v[160:163], v[156:159], a[224:227]\n\t" \
  "ds_read_b64_tr_b16 v[220:221], v121 offset:16384\n\t" \
  "v_mfma_f32_16x16x32_bf16 a[228:231], v[164:167], v[156:159], a[228:231]\n\t" \
  "s_add_u32 m0, s31, 147456\n\t" \
  "s_add_u32 s43, s68, 49152\n\t" \
  "v_mfma_f32_16x16x32_bf16 a[232:235], v[168:171], v[156:159], a[232:235]\n\t" \
  "ds_read_b64_tr_b16 v[222:223], v121 offset:18432\n\t" \
  "v_mfma_f32_16x16x32_bf16 a[236:239], v[172:175], v[156:159], a[236:239]\n\t" \
  "buffer_load_dwordx4 v34, %11, s43 offen offset:0 sc1 lds\n\t" \
  "v_mfma_f32_16x16x32_bf16 a[240:243], v[176:179], v[156:159], a[240:243]\n\t" \
  "ds_read_b128 v[224:227], v113 offset:0\n\t" \
  "v_mfma_f32_16x16x32_bf16 a[244:247], v[180:183], v[156:159], a[244:247]\n\t" \
  "v_mfma_f32_16x16x32_bf16 a[248:251], v[184:187], v[156:159], a[248:251]\n\t" \
  "ds_read_b128 v[228:231], v113 offset:2048\n\t" \
  "v_mfma_f32_16x16x32_bf16 a[252:255], v[188:191], v[156:159], a[252:255]\n\t" \
  "v_mfma_f32_16x16x32_bf16 a[192:195], v[160:163], v[152:155], a[192:195]\n\t" \
  "ds_read_b128 v[232:235], v113 offset:4096\n\t" \
  "buffer_load_dwordx4 v34, %11, s43 offen offset:1024 sc1 lds\n\t" \
  "v_mfma_f32_16x16x32_bf16 a[196:199], v[164:167], v[152:155], a[196:199]\n\t" \
  "v_mfma_f32_16x16x32_bf16 a[200:203], v[168:171], v[152:155], a[200:203]\n\t" \
  "ds_read_b128 v[236:239], v113 offset:6144\n\t" \
  "v_mfma_f32_16x16x32_bf16 a[204:207], v[172:175], v[152:155], a[204:207]\n\t" \
  "v_mfma_f32_16x16x32_bf16 a[208:211], v[176:179], v[152:155], a[208:211]\n\t" \
  "ds_read_b128 v[240:243], v113 offset:8192\n\t" \
  "v_mfma_f32_16x16x32_bf16 a[212:215], v[180:183], v[152:155], a[212:215]\n\t" \
  "buffer_load_dwordx4 v34, %11, s43 offen offset:2048 sc1 lds\n\t" \
  "v_mfma_f32_16x16x32_bf16 a[216:219], v[184:187], v[152:155], a[216:219]\n\t" \
  "ds_read_b128 v[244:247], v113 offset:10240\n\t" \
  "v_mfma_f32_16x16x32_bf16 a[220:223], v[188:191], v[152:155], a[220:223]\n\t" \
  "v_mfma_f32_16x16x32_bf16 a[160:163], v[160:163], v[148:151], a[160:163]\n\t" \
  "ds_read_b128 v[248:251], v113 offset:12288\n\t" \
  "v_mfma_f32_16x16x32_bf16 a[164:167], v[164:167], v[148:151], a[164:167]\n\t" \
  "v_mfma_f32_16x16x32_bf16 a[168:171], v[168:171], v[148:151], a[168:171]\n\t" \
  "ds_read_b128 v[252:255], v113 offset:14336\n\t" \
  "buffer_load_dwordx4 v34, %11, s43 offen offset:3072 sc1 lds\n\t" \
  "v_mfma_f32_16x16x32_bf16 a[172:175], v[172:175], v[148:151], a[172:175]\n\t" \
  "v_mfma_f32_16x16x32_bf16 a[176:179], v[176:179], v[148:151], a[176:179]\n\t" \
  "ds_read_b64_tr_b16 v[216:217], v120 offset:16384\n\t" \
  "v_mfma_f32_16x16x32_bf16 a[180:183], v[180:183], v[148:151], a[180:183]\n\t" \
  "s_add_u32 m0, s31, 32768\n\t" \
  "s_add_u32 s43, s68, 98304\n\t" \
  "v_mfma_f32_16x16x32_bf16 a[184:187], v[184:187], v[148:151], a[184:187]\n\t" \
  "ds_read_b64_tr_b16 v[218:219], v120 offset:18432\n\t" \
  "v_mfma_f32_16x16x32_bf16 a[188:191], v[188:191], v[148:151], a[188:191]\n\t" \
  "buffer_load_dwordx4 v34, %11, s43 offen offset:0 sc1 lds\n\t" \
  "v_mfma_f32_16x16x32_bf16 a[128:131], v[160:163], v[144:147], a[128:131]\n\t" \
  "ds_read_b64_tr_b16 v[212:213], v119 offset:16384\n\t" \
  "v_mfma_f32_16x16x32_bf16 a[132:135], v[164:167], v[144:147], a[132:135]\n\t" \
  "v_mfma_f32_16x16x32_bf16 a[136:139], v[168:171], v[144:147], a[136:139]\n\t" \
  "ds_read_b64_tr_b16 v[214:215], v119 offset:18432\n\t" \
  "v_mfma_f32_16x16x32_bf16 a[140:143], v[172:175], v[144:147], a[140:143]\n\t" \
  "v_mfma_f32_16x16x32_bf16 a[144:147], v[176:179], v[144:147], a[144:147]\n\t" \
  "ds_read_b64_tr_b16 v[208:209], v118 offset:16384\n\t" \
  "buffer_load_dwordx4 v34, %11, s43 offen offset:1024 sc1 lds\n\t" \
  "v_mfma_f32_16x16x32_bf16 a[148:151], v[180:183], v[144:147], a[148:151]\n\t" \
  "v_mfma_f32_16x16x32_bf16 a[152:155], v[184:187], v[144:147], a[152:155]\n\t" \
  "ds_read_b64_tr_b16 v[210:211], v118 offset:18432\n\t" \
  "v_mfma_f32_16x16x32_bf16 a[156:159], v[188:191], v[144:147], a[156:159]\n\t" \
  "v_mfma_f32_16x16x32_bf16 a[96:99], v[160:163], v[140:143], a[96:99]\n\t" \
  "ds_read_b64_tr_b16 v[204:205], v117 offset:16384\n\t" \
  "v_mfma_f32_16x16x32_bf16 a[100:103], v[164:167], v[140:143], a[100:103]\n\t" \
  "buffer_load_dwordx4 v34, %11, s43 offen offset:2048 sc1 lds\n\t" \
  "v_mfma_f32_16x16x32_bf16 a[104:107], v[168:171], v[140:143], a[104:107]\n\t" \
  "ds_read_b64_tr_b16 v[206:207], v117 offset:18432\n\t" \
  "v_mfma_f32_16x16x32_bf16 a[108:111], v[172:175], v[140:143], a[108:111]\n\t" \
  "v_mfma_f32_16x16x32_bf16 a[112:115], v[176:179], v[140:143], a[112:115]\n\t" \
  "ds_read_b64_tr_b16 v[200:201], v116 offset:16384\n\t" \
  "v_mfma_f32_16x16x32_bf16 a[116:119], v[180:183], v[140:143], a[116:119]\n\t" \
  "v_mfma_f32_16x16x32_bf16 a[120:123], v[184:187], v[140:143], a[120:123]\n\t" \
  "ds_read_b64_tr_b16 v[202:203], v116 offset:18432\n\t" \
  "buffer_load_dwordx4 v34, %11, s43 offen offset:3072 sc1 lds\n\t" \
  "v_mfma_f32_16x16x32_bf16 a[124:127], v[188:191], v[140:143], a[124:127]\n\t" \
  "v_mfma_f32_16x16x32_bf16 a[64:67], v[160:163], v[136:139], a[64:67]\n\t" \
  "ds_read_b64_tr_b16 v[196:197], v115 offset:16384\n\t" \
  "v_mfma_f32_16x16x32_bf16 a[68:71], v[164:167], v[136:139], a[68:71]\n\t" \
  "v_mfma_f32_16x16x32_bf16 a[72:75], v[168:171], v[136:139], a[72:75]\n\t" \
  "ds_read_b64_tr_b16 v[198:199], v115 offset:18432\n\t" \
  "v_mfma_f32_16x16x32_bf16 a[76:79], v[172:175], v[136:139], a[76:79]\n\t" \
  "buffer_load_dwordx4 v[88:91], %17, %11, s73 offen offset:2048 sc1\n\t" \
  "v_mfma_f32_16x16x32_bf16 a[80:83], v[176:179], v[136:139], a[80:83]\n\t" \
  "ds_read_b64_tr_b16 v[192:193], v114 offset:16384\n\t" \
  "v_mfma_f32_16x16x32_bf16 a[84:87], v[180:183], v[136:139], a[84:87]\n\t" \
  "v_mfma_f32_16x16x32_bf16 a[88:91], v[184:187], v[136:139], a[88:91]\n\t" \
  "ds_read_b64_tr_b16 v[194:195], v114 offset:18432\n\t" \
  "v_mfma_f32_16x16x32_bf16 a[92:95], v[188:191], v[136:139], a[92:95]\n\t" \
  "v_mfma_f32_16x16x32_bf16 a[32:35], v[160:163], v[132:135], a[32:35]\n\t" \
  "buffer_load_dwordx4 v[92:95], %17, %11, s77 offen offset:2048 sc1\n\t" \
  "v_mfma_f32_16x16x32_bf16 a[36:39], v[164:167], v[132:135], a[36:39]\n\t" \
  "v_mfma_f32_16x16x32_bf16 a[40:43], v[168:171], v[132:135], a[40:43]\n\t" \
  "v_mfma_f32_16x16x32_bf16 a[44:47], v[172:175], v[132:135], a[44:47]\n\t" \
  "v_mfma_f32_16x16x32_bf16 a[48:51], v[176:179], v[132:135], a[48:51]\n\t" \
  "v_mfma_f32_16x16x32_bf16 a[52:55], v[180:183], v[132:135], a[52:55]\n\t" \
  "buffer_load_dwordx4 v[96:99], %17, %11, s73 offen offset:3072 sc1\n\t" \
  "v_mfma_f32_16x16x32_bf16 a[56:59], v[184:187], v[132:135], a[56:59]\n\t" \
  "v_mfma_f32_16x16x32_bf16 a[60:63], v[188:191], v[132:135], a[60:63]\n\t" \
  "v_mfma_f32_16x16x32_bf16 a[0:3], v[160:163], v[128:131], a[0:3]\n\t" \
  "v_mfma_f32_16x16x32_bf16 a[4:7], v[164:167], v[128:131], a[4:7]\n\t" \
  "v_mfma_f32_16x16x32_bf16 a[8:11], v[168:171], v[128:131], a[8:11]\n\t" \
  "buffer_load_dwordx4 v[100:103], %17, %11, s77 offen offset:3072 sc1\n\t" \
  "v_mfma_f32_16x16x32_bf16 a[12:15], v[172:175], v[128:131], a[12:15]\n\t" \
  "v_mfma_f32_16x16x32_bf16 a[16:19], v[176:179], v[128:131], a[16:19]\n\t" \
  "v_mfma_f32_16x16x32_bf16 a[20:23], v[180:183], v[128:131], a[20:23]\n\t" \
  "v_mfma_f32_16x16x32_bf16 a[24:27], v[184:187], v[128:131], a[24:27]\n\t" \
  "v_mfma_f32_16x16x32_bf16 a[28:31], v[188:191], v[128:131], a[28:31]\n\t" \
  "MK_KVE_%=:\n\t" \
  "s_waitcnt lgkmcnt(0)\n\t" \
  "s_waitcnt vmcnt(32)\n\t" \
  "s_barrier\n\t" \
  "v_mfma_f32_16x16x32_bf16 a[224:227], v[224:227], v[220:223], a[224:227]\n\t" \
  "v_mfma_f32_16x16x32_bf16 a[228:231], v[228:231], v[220:223], a[228:231]\n\t" \
  "s_add_u32 m0, s31, 49152\n\t" \
  "s_add_u32 s43, s68, 114688\n\t" \
  "v_mfma_f32_16x16x32_bf16 a[232:235], v[232:235], v[220:223], a[232:235]\n\t" \
  "v_mfma_f32_16x16x32_bf16 a[236:239], v[236:239], v[220:223], a[236:239]\n\t" \
  "buffer_load_dwordx4 v34, %11, s43 offen offset:0 sc1 lds\n\t" \
  "v_mfma_f32_16x16x32_bf16 a[240:243], v[240:243], v[220:223], a[240:243]\n\t" \
  "buffer_load_dwordx4 v34, %11, s43 offen offset:1024 sc1 lds\n\t" \
  "v_mfma_f32_16x16x32_bf16 a[244:247], v[244:247], v[220:223], a[244:247]\n\t" \
  "buffer_load_dwordx4 v34, %11, s43 offen offset:2048 sc1 lds\n\t" \
  "v_mfma_f32_16x16x32_bf16 a[248:251], v[248:251], v[220:223], a[248:251]\n\t" \
  "buffer_load_dwordx4 v34, %11, s43 offen offset:3072 sc1 lds\n\t" \
  "v_mfma_f32_16x16x32_bf16 a[252:255], v[252:255], v[220:223], a[252:255]\n\t" \
  "s_add_u32 m0, s31, 65536\n\t" \
  "s_add_u32 s43, s68, 131072\n\t" \
  "v_mfma_f32_16x16x32_bf16 a[192:195], v[224:227], v[216:219], a[192:195]\n\t" \
  "v_mfma_f32_16x16x32_bf16 a[196:199], v[228:231], v[216:219], a[196:199]\n\t" \
  "buffer_load_dwordx4 v34, %11, s43 offen offset:0 sc1 lds\n\t" \
  "v_mfma_f32_16x16x32_bf16 a[200:203], v[232:235], v[216:219], a[200:203]\n\t" \
  "buffer_load_dwordx4 v34, %11, s43 offen offset:1024 sc1 lds\n\t" \
  "v_accvgpr_read_b32 v104, a224\n\t" \
  "v_accvgpr_read_b32 v105, a225\n\t" \
  "v_mfma_f32_16x16x32_bf16 a[204:207], v[236:239], v[216:219], a[204:207]\n\t" \
  "buffer_load_dwordx4 v34, %11, s43 offen offset:2048 sc1 lds\n\t" \
  "v_accvgpr_read_b32 v106, a226\n\t" \
  "v_mfma_f32_16x16x32_bf16 a[208:211], v[240:243], v[216:219], a[208:211]\n\t" \
  "buffer_load_dwordx4 v34, %11, s43 offen offset:3072 sc1 lds\n\t" \
  "v_accvgpr_read_b32 v107, a227\n\t" \
  "v_mfma_f32_16x16x32_bf16 a[212:215], v[244:247], v[216:219], a[212:215]\n\t" \
  "s_add_u32 m0, s31, 81920\n\t" \
  "s_add_u32 s43, s68, 147456\n\t" \
  "v_mfma_f32_16x16x32_bf16 a[216:219], v[248:251], v[216:219], a[216:219]\n\t" \
  "v_accvgpr_read_b32 v108, a228\n\t" \
  "v_accvgpr_read_b32 v109, a229\n\t" \
  "v_mfma_f32_16x16x32_bf16 a[220:223], v[252:255], v[216:219], a[220:223]\n\t" \
  "buffer_load_dwordx4 v34, %11, s43 offen offset:0 sc1 lds\n\t" \
  "v_accvgpr_read_b32 v110, a230\n\t" \
  "v_mfma_f32_16x16x32_bf16 a[160:163], v[224:227], v[212:215], a[160:163]\n\t" \
  "buffer_load_dwordx4 v34, %11, s43 offen offset:1024 sc1 lds\n\t" \
  "v_accvgpr_read_b32 v111, a231\n\t" \
  "v_mfma_f32_16x16x32_bf16 a[164:167], v[228:231], v[212:215], a[164:167]\n\t" \
  "buffer_load_dwordx4 v34, %11, s43 offen offset:2048 sc1 lds\n\t" \
  "v_cvt_pk_bf16_f32 v112, v104, v105\n\t" \
  "v_mfma_f32_16x16x32_bf16 a[168:171], v[232:235], v[212:215], a[168:171]\n\t" \
  "buffer_load_dwordx4 v34, %11, s43 offen offset:3072 sc1 lds\n\t" \
  "v_cvt_pk_bf16_f32 v113, v106, v107\n\t" \
  "v_mfma_f32_16x16x32_bf16 a[172:175], v[236:239], v[212:215], a[172:175]\n\t" \
  "v_cvt_pk_bf16_f32 v114, v108, v109\n\t" \
  "v_cvt_pk_bf16_f32 v115, v110, v111\n\t" \
  "v_mfma_f32_16x16x32_bf16 a[176:179], v[240:243], v[212:215], a[176:179]\n\t" \
  "buffer_store_dwordx4 v[112:115], %1, %11, s41 offen sc1\n\t" \
  "v_accvgpr_read_b32 v116, a232\n\t" \
  "v_accvgpr_read_b32 v117, a233\n\t" \
  "v_mfma_f32_16x16x32_bf16 a[180:183], v[244:247], v[212:215], a[180:183]\n\t" \
  "v_accvgpr_read_b32 v118, a234\n\t" \
  "v_accvgpr_read_b32 v119, a235\n\t" \
  "v_mfma_f32_16x16x32_bf16 a[184:187], v[248:251], v[212:215], a[184:187]\n\t" \
  "v_accvgpr_read_b32 v120, a236\n\t" \
  "v_accvgpr_read_b32 v121, a237\n\t" \
  "v_mfma_f32_16x16x32_bf16 a[188:191], v[252:255], v[212:215], a[188:191]\n\t" \
  "v_accvgpr_read_b32 v122, a238\n\t" \
  "v_accvgpr_read_b32 v123, a239\n\t" \
  "v_mfma_f32_16x16x32_bf16 a[128:131], v[224:227], v[208:211], a[128:131]\n\t" \
  "v_cvt_pk_bf16_f32 v124, v116, v117\n\t" \
  "v_cvt_pk_bf16_f32 v125, v118, v119\n\t" \
  "v_mfma_f32_16x16x32_bf16 a[132:135], v[228:231], v[208:211], a[132:135]\n\t" \
  "v_cvt_pk_bf16_f32 v126, v120, v121\n\t" \
  "v_cvt_pk_bf16_f32 v127, v122, v123\n\t" \
  "v_mfma_f32_16x16x32_bf16 a[136:139], v[232:235], v[208:211], a[136:139]\n\t" \
  "buffer_store_dwordx4 v[124:127], v35, %11, s41 offen sc1\n\t" \
  "v_accvgpr_read_b32 v104, a240\n\t" \
  "v_mfma_f32_16x16x32_bf16 a[140:143], v[236:239], v[208:211], a[140:143]\n\t" \
  "v_accvgpr_read_b32 v105, a241\n\t" \
  "v_accvgpr_read_b32 v106, a242\n\t" \
  "v_mfma_f32_16x16x32_bf16 a[144:147], v[240:243], v[208:211], a[144:147]\n\t" \
  "v_accvgpr_read_b32 v107, a243\n\t" \
  "v_accvgpr_read_b32 v108, a244\n\t" \
  "v_mfma_f32_16x16x32_bf16 a[148:151], v[244:247], v[208:211], a[148:151]\n\t" \
  "v_accvgpr_read_b32 v109, a245\n\t" \
  "v_accvgpr_read_b32 v110, a246\n\t" \
  "v_mfma_f32_16x16x32_bf16 a[152:155], v[248:251], v[208:211], a[152:155]\n\t" \
  "v_accvgpr_read_b32 v111, a247\n\t" \
  "v_cvt_pk_bf16_f32 v112, v104, v105\n\t" \
  "v_mfma_f32_16x16x32_bf16 a[156:159], v[252:255], v[208:211], a[156:159]\n\t" \
  "v_cvt_pk_bf16_f32 v113, v106, v107\n\t" \
  "v_cvt_pk_bf16_f32 v114, v108, v109\n\t" \
  "v_cvt_pk_bf16_f32 v115, v110, v111\n\t" \
  "v_mfma_f32_16x16x32_bf16 a[96:99], v[224:227], v[204:207], a[96:99]\n\t" \
  "buffer_store_dwordx4 v[112:115], v36, %11, s41 offen sc1\n\t" \
  "v_accvgpr_read_b32 v116, a248\n\t" \
  "v_mfma_f32_16x16x32_bf16 a[100:103], v[228:231], v[204:207], a[100:103]\n\t" \
  "v_accvgpr_read_b32 v117, a249\n\t" \
  "v_accvgpr_read_b32 v118, a250\n\t" \
  "v_mfma_f32_16x16x32_bf16 a[104:107], v[232:235], v[204:207], a[104:107]\n\t" \
  "v_accvgpr_read_b32 v119, a251\n\t" \
  "v_accvgpr_read_b32 v120, a252\n\t" \
  "v_mfma_f32_16x16x32_bf16 a[108:111], v[236:239], v[204:207], a[108:111]\n\t" \
  "v_accvgpr_read_b32 v121, a253\n\t" \
  "v_accvgpr_read_b32 v122, a254\n\t" \
  "v_mfma_f32_16x16x32_bf16 a[112:115], v[240:243], v[204:207], a[112:115]\n\t" \
  "v_accvgpr_read_b32 v123, a255\n\t" \
  "v_cvt_pk_bf16_f32 v124, v116, v117\n\t" \
  "v_mfma_f32_16x16x32_bf16 a[116:119], v[244:247], v[204:207], a[116:119]\n\t" \
  "v_cvt_pk_bf16_f32 v125, v118, v119\n\t" \
  "v_cvt_pk_bf16_f32 v126, v120, v121\n\t" \
  "v_mfma_f32_16x16x32_bf16 a[120:123], v[248:251], v[204:207], a[120:123]\n\t" \
  "v_cvt_pk_bf16_f32 v127, v122, v123\n\t" \
  "buffer_store_dwordx4 v[124:127], v38, %11, s41 offen sc1\n\t" \
  "v_mfma_f32_16x16x32_bf16 a[124:127], v[252:255], v[204:207], a[124:127]\n\t" \
  "v_accvgpr_read_b32 v104, a192\n\t" \
  "v_accvgpr_read_b32 v105, a193\n\t" \
  "v_mfma_f32_16x16x32_bf16 a[64:67], v[224:227], v[200:203], a[64:67]\n\t" \
  "v_accvgpr_read_b32 v106, a194\n\t" \
  "v_accvgpr_read_b32 v107, a195\n\t" \
  "v_mfma_f32_16x16x32_bf16 a[68:71], v[228:231], v[200:203], a[68:71]\n\t" \
  "v_accvgpr_read_b32 v108, a196\n\t" \
  "v_accvgpr_read_b32 v109, a197\n\t" \
  "v_mfma_f32_16x16x32_bf16 a[72:75], v[232:235], v[200:203], a[72:75]\n\t" \
  "v_accvgpr_read_b32 v110, a198\n\t" \
  "v_accvgpr_read_b32 v111, a199\n\t" \
  "v_cvt_pk_bf16_f32 v112, v104, v105\n\t" \
  "v_mfma_f32_16x16x32_bf16 a[76:79], v[236:239], v[200:203], a[76:79]\n\t" \
  "v_cvt_pk_bf16_f32 v113, v106, v107\n\t" \
  "v_cvt_pk_bf16_f32 v114, v108, v109\n\t" \
  "v_mfma_f32_16x16x32_bf16 a[80:83], v[240:243], v[200:203], a[80:83]\n\t" \
  "v_cvt_pk_bf16_f32 v115, v110, v111\n\t" \
  "buffer_store_dwordx4 v[112:115], %1, %11, s40 offen sc1\n\t" \
  "v_mfma_f32_16x16x32_bf16 a[84:87], v[244:247], v[200:203], a[84:87]\n\t" \
  "v_accvgpr_read_b32 v116, a200\n\t" \
  "v_accvgpr_read_b32 v117, a201\n\t" \
  "v_mfma_f32_16x16x32_bf16 a[88:91], v[248:251], v[200:203], a[88:91]\n\t" \
  "v_accvgpr_read_b32 v118, a202\n\t" \
  "v_accvgpr_read_b32 v119, a203\n\t" \
  "v_mfma_f32_16x16x32_bf16 a[92:95], v[252:255], v[200:203], a[92:95]\n\t" \
  "v_accvgpr_read_b32 v120, a204\n\t" \
  "v_accvgpr_read_b32 v121, a205\n\t" \
  "v_mfma_f32_16x16x32_bf16 a[32:35], v[224:227], v[196:199], a[32:35]\n\t" \
  "v_accvgpr_read_b32 v122, a206\n\t" \
  "v_accvgpr_read_b32 v123, a207\n\t" \
  "v_mfma_f32_16x16x32_bf16 a[36:39], v[228:231], v[196:199], a[36:39]\n\t" \
  "v_cvt_pk_bf16_f32 v124, v116, v117\n\t" \
  "v_cvt_pk_bf16_f32 v125, v118, v119\n\t" \
  "v_mfma_f32_16x16x32_bf16 a[40:43], v[232:235], v[196:199], a[40:43]\n\t" \
  "v_cvt_pk_bf16_f32 v126, v120, v121\n\t" \
  "v_cvt_pk_bf16_f32 v127, v122, v123\n\t" \
  "v_mfma_f32_16x16x32_bf16 a[44:47], v[236:239], v[196:199], a[44:47]\n\t" \
  "buffer_store_dwordx4 v[124:127], v35, %11, s40 offen sc1\n\t" \
  "v_accvgpr_read_b32 v104, a208\n\t" \
  "v_mfma_f32_16x16x32_bf16 a[48:51], v[240:243], v[196:199], a[48:51]\n\t" \
  "v_accvgpr_read_b32 v105, a209\n\t" \
  "v_accvgpr_read_b32 v106, a210\n\t" \
  "v_mfma_f32_16x16x32_bf16 a[52:55], v[244:247], v[196:199], a[52:55]\n\t" \
  "v_accvgpr_read_b32 v107, a211\n\t" \
  "v_accvgpr_read_b32 v108, a212\n\t" \
  "v_accvgpr_read_b32 v109, a213\n\t" \
  "v_mfma_f32_16x16x32_bf16 a[56:59], v[248:251], v[196:199], a[56:59]\n\t" \
  "v_accvgpr_read_b32 v110, a214\n\t" \
  "v_accvgpr_read_b32 v111, a215\n\t" \
  "v_mfma_f32_16x16x32_bf16 a[60:63], v[252:255], v[196:199], a[60:63]\n\t" \
  "v_cvt_pk_bf16_f32 v112, v104, v105\n\t" \
  "v_cvt_pk_bf16_f32 v113, v106, v107\n\t" \
  "v_mfma_f32_16x16x32_bf16 a[0:3], v[224:227], v[192:195], a[0:3]\n\t" \
  "v_cvt_pk_bf16_f32 v114, v108, v109\n\t" \
  "v_cvt_pk_bf16_f32 v115, v110, v111\n\t" \
  "v_mfma_f32_16x16x32_bf16 a[4:7], v[228:231], v[192:195], a[4:7]\n\t" \
  "buffer_store_dwordx4 v[112:115], v36, %11, s40 offen sc1\n\t" \
  "v_accvgpr_read_b32 v116, a216\n\t" \
  "v_mfma_f32_16x16x32_bf16 a[8:11], v[232:235], v[192:195], a[8:11]\n\t" \
  "v_accvgpr_read_b32 v117, a217\n\t" \
  "v_accvgpr_read_b32 v118, a218\n\t" \
  "v_mfma_f32_16x16x32_bf16 a[12:15], v[236:239], v[192:195], a[12:15]\n\t" \
  "v_accvgpr_read_b32 v119, a219\n\t" \
  "v_accvgpr_read_b32 v120, a220\n\t" \
  "v_mfma_f32_16x16x32_bf16 a[16:19], v[240:243], v[192:195], a[16:19]\n\t" \
  "v_accvgpr_read_b32 v121, a221\n\t" \
  "v_accvgpr_read_b32 v122, a222\n\t" \
  "v_mfma_f32_16x16x32_bf16 a[20:23], v[244:247], v[192:195], a[20:23]\n\t" \
  "v_accvgpr_read_b32 v123, a223\n\t" \
  "v_cvt_pk_bf16_f32 v124, v116, v117\n\t" \
  "v_mfma_f32_16x16x32_bf16 a[24:27], v[248:251], v[192:195], a[24:27]\n\t" \
  "v_cvt_pk_bf16_f32 v125, v118, v119\n\t" \
  "v_cvt_pk_bf16_f32 v126, v120, v121\n\t" \
  "v_mfma_f32_16x16x32_bf16 a[28:31], v[252:255], v[192:195], a[28:31]\n\t" \
  "v_cvt_pk_bf16_f32 v127, v122, v123\n\t" \
  "buffer_store_dwordx4 v[124:127], v38, %11, s40 offen sc1\n\t" \
  "ds_read_b128 a[192:195], v32 offset:0\n\t" \
  "ds_read_b128 a[196:199], v32 offset:1024\n\t" \
  "ds_read_b128 a[200:203], v32 offset:2048\n\t" \
  "ds_read_b128 a[204:207], v32 offset:3072\n\t" \
  "ds_read_b128 a[208:211], v32 offset:4096\n\t" \
  "ds_read_b128 a[212:215], v32 offset:5120\n\t" \
  "ds_read_b128 a[216:219], v32 offset:6144\n\t" \
  "ds_read_b128 a[220:223], v32 offset:7168\n\t" \
  "ds_read_b128 a[224:227], v32 offset:8192\n\t" \
  "ds_read_b128 a[228:231], v32 offset:9216\n\t" \
  "ds_read_b128 a[232:235], v32 offset:10240\n\t" \
  "ds_read_b128 a[236:239], v32 offset:11264\n\t" \
  "s_waitcnt vmcnt(28)\n\t" \
  "s_barrier\n\t" \
  "s_waitcnt lgkmcnt(4)\n\t" \
  "v_mfma_f32_16x16x32_bf16 v[128:131], a[192:195], v[40:43], 0\n\t" \
  "s_add_u32 s30, s28, 0x8000\n\t" \
  "ds_read_b128 a[240:243], v32 offset:12288\n\t" \
  "v_mfma_f32_16x16x32_bf16 v[136:139], a[192:195], v[44:47], 0\n\t" \
  "s_cmp_lg_u32 s28, 0x20000\n\t" \
  "s_cselect_b32 s30, s30, 0\n\t" \
  "v_mfma_f32_16x16x32_bf16 v[132:135], a[196:199], v[40:43], 0\n\t" \
  "ds_read_b128 a[244:247], v32 offset:13312\n\t" \
  "v_accvgpr_read_b32 v104, a0\n\t" \
  "v_mfma_f32_16x16x32_bf16 v[140:143], a[196:199], v[44:47], 0\n\t" \
  "v_accvgpr_read_b32 v105, a1\n\t" \
  "v_accvgpr_read_b32 v106, a2\n\t" \
  "v_mfma_f32_16x16x32_bf16 v[144:147], a[200:203], v[40:43], 0\n\t" \
  "ds_read_b128 a[248:251], v32 offset:14336\n\t" \
  "v_accvgpr_read_b32 v107, a3\n\t" \
  "v_mfma_f32_16x16x32_bf16 v[152:155], a[200:203], v[44:47], 0\n\t" \
  "v_accvgpr_read_b32 v108, a4\n\t" \
  "v_mfma_f32_16x16x32_bf16 v[148:151], a[204:207], v[40:43], 0\n\t" \
  "v_add_u32_e32 v33, s30, %1\n\t" \
  "ds_read_b128 a[252:255], v32 offset:15360\n\t" \
  "v_mfma_f32_16x16x32_bf16 v[156:159], a[204:207], v[44:47], 0\n\t" \
  "v_accvgpr_read_b32 v109, a5\n\t" \
  "v_accvgpr_read_b32 v110, a6\n\t" \
  "v_mfma_f32_16x16x32_bf16 v[160:163], a[208:211], v[40:43], 0\n\t" \
  "ds_read_b128 a[192:195], v32 offset:16384\n\t" \
  "v_accvgpr_read_b32 v111, a7\n\t" \
  "v_mfma_f32_16x16x32_bf16 v[168:171], a[208:211], v[44:47], 0\n\t" \
  "v_cvt_pk_bf16_f32 v112, v104, v105\n\t" \
  "v_mfma_f32_16x16x32_bf16 v[164:167], a[212:215], v[40:43], 0\n\t" \
  "ds_read_b128 a[196:199], v32 offset:17408\n\t" \
  "v_cvt_pk_bf16_f32 v113, v106, v107\n\t" \
  "v_mfma_f32_16x16x32_bf16 v[172:175], a[212:215], v[44:47], 0\n\t" \
  "v_cvt_pk_bf16_f32 v114, v108, v109\n\t" \
  "v_mfma_f32_16x16x32_bf16 v[176:179], a[216:219], v[40:43], 0\n\t" \
  "ds_read_b128 a[200:203], v32 offset:18432\n\t" \
  "v_cvt_pk_bf16_f32 v115, v110, v111\n\t" \
  "v_mfma_f32_16x16x32_bf16 v[184:187], a[216:219], v[44:47], 0\n\t" \
  "buffer_store_dwordx4 v[112:115], %1, %11, s34 offen sc1\n\t" \
  "v_mfma_f32_16x16x32_bf16 v[180:183], a[220:223], v[40:43], 0\n\t" \
  "ds_read_b128 a[204:207], v32 offset:19456\n\t" \
  "v_accvgpr_read_b32 v116, a8\n\t" \
  "v_mfma_f32_16x16x32_bf16 v[188:191], a[220:223], v[44:47], 0\n\t" \
  "v_accvgpr_read_b32 v117, a9\n\t" \
  "s_waitcnt lgkmcnt(4)\n\t" \
  "v_mfma_f32_16x16x32_bf16 v[192:195], a[224:227], v[40:43], 0\n\t" \
  "ds_read_b128 a[208:211], v32 offset:20480\n\t" \
  "v_accvgpr_read_b32 v118, a10\n\t" \
  "v_mfma_f32_16x16x32_bf16 v[200:203], a[224:227], v[44:47], 0\n\t" \
  "v_accvgpr_read_b32 v119, a11\n\t" \
  "v_mfma_f32_16x16x32_bf16 v[196:199], a[228:231], v[40:43], 0\n\t" \
  "ds_read_b128 a[212:215], v32 offset:21504\n\t" \
  "v_accvgpr_read_b32 v120, a12\n\t" \
  "v_mfma_f32_16x16x32_bf16 v[204:207], a[228:231], v[44:47], 0\n\t" \
  "v_accvgpr_read_b32 v121, a13\n\t" \
  "v_mfma_f32_16x16x32_bf16 v[208:211], a[232:235], v[40:43], 0\n\t" \
  "ds_read_b128 a[216:219], v32 offset:22528\n\t" \
  "v_accvgpr_read_b32 v122, a14\n\t" \
  "v_mfma_f32_16x16x32_bf16 v[216:219], a[232:235], v[44:47], 0\n\t" \
  "v_accvgpr_read_b32 v123, a15\n\t" \
  "v_mfma_f32_16x16x32_bf16 v[212:215], a[236:239], v[40:43], 0\n\t" \
  "ds_read_b128 a[220:223], v32 offset:23552\n\t" \
  "v_cvt_pk_bf16_f32 v124, v116, v117\n\t" \
  "v_mfma_f32_16x16x32_bf16 v[220:223], a[236:239], v[44:47], 0\n\t" \
  "v_cvt_pk_bf16_f32 v125, v118, v119\n\t" \
  "v_cvt_pk_bf16_f32 v126, v120, v121\n\t" \
  "v_mfma_f32_16x16x32_bf16 v[224:227], a[240:243], v[40:43], 0\n\t" \
  "ds_read_b128 a[224:227], v32 offset:24576\n\t" \
  "v_cvt_pk_bf16_f32 v127, v122, v123\n\t" \
  "v_mfma_f32_16x16x32_bf16 v[232:235], a[240:243], v[44:47], 0\n\t" \
  "buffer_store_dwordx4 v[124:127], v35, %11, s34 offen sc1\n\t" \
  "v_mfma_f32_16x16x32_bf16 v[228:231], a[244:247], v[40:43], 0\n\t" \
  "ds_read_b128 a[228:231], v32 offset:25600\n\t" \
  "v_accvgpr_read_b32 v104, a16\n\t" \
  "v_mfma_f32_16x16x32_bf16 v[236:239], a[244:247], v[44:47], 0\n\t" \
  "v_accvgpr_read_b32 v105, a17\n\t" \
  "v_mfma_f32_16x16x32_bf16 v[240:243], a[248:251], v[40:43], 0\n\t" \
  "ds_read_b128 a[232:235], v32 offset:26624\n\t" \
  "v_accvgpr_read_b32 v106, a18\n\t" \
  "v_mfma_f32_16x16x32_bf16 v[248:251], a[248:251], v[44:47], 0\n\t" \
  "v_accvgpr_read_b32 v107, a19\n\t" \
  "v_mfma_f32_16x16x32_bf16 v[244:247], a[252:255], v[40:43], 0\n\t" \
  "ds_read_b128 a[236:239], v32 offset:27648\n\t" \
  "v_accvgpr_read_b32 v108, a20\n\t" \
  "v_mfma_f32_16x16x32_bf16 v[252:255], a[252:255], v[44:47], 0\n\t" \
  "v_accvgpr_read_b32 v109, a21\n\t" \
  "s_waitcnt lgkmcnt(4)\n\t" \
  "v_mfma_f32_16x16x32_bf16 v[128:131], a[192:195], v[48:51], v[128:131]\n\t" \
  "ds_read_b128 a[240:243], v32 offset:28672\n\t" \
  "v_accvgpr_read_b32 v110, a22\n\t" \
  "v_mfma_f32_16x16x32_bf16 v[136:139], a[192:195], v[52:55], v[136:139]\n\t" \
  "v_accvgpr_read_b32 v111, a23\n\t" \
  "v_mfma_f32_16x16x32_bf16 v[132:135], a[196:199], v[48:51], v[132:135]\n\t" \
  "ds_read_b128 a[244:247], v32 offset:29696\n\t" \
  "v_cvt_pk_bf16_f32 v112, v104, v105\n\t" \
  "v_mfma_f32_16x16x32_bf16 v[140:143], a[196:199], v[52:55], v[140:143]\n\t" \
  "v_cvt_pk_bf16_f32 v113, v106, v107\n\t" \
  "v_mfma_f32_16x16x32_bf16 v[144:147], a[200:203], v[48:51], v[144:147]\n\t" \
  "ds_read_b128 a[248:251], v32 offset:30720\n\t" \
  "v_cvt_pk_bf16_f32 v114, v108, v109\n\t" \
  "v_mfma_f32_16x16x32_bf16 v[152:155], a[200:203], v[52:55], v[152:155]\n\t" \
  "v_cvt_pk_bf16_f32 v115, v110, v111\n\t" \
  "v_mfma_f32_16x16x32_bf16 v[148:151], a[204:207], v[48:51], v[148:151]\n\t" \
  "ds_read_b128 a[252:255], v32 offset:31744\n\t" \
  "buffer_store_dwordx4 v[112:115], v36, %11, s34 offen sc1\n\t" \
  "v_mfma_f32_16x16x32_bf16 v[156:159], a[204:207], v[52:55], v[156:159]\n\t" \
  "v_accvgpr_read_b32 v116, a24\n\t" \
  "v_mfma_f32_16x16x32_bf16 v[160:163], a[208:211], v[48:51], v[160:163]\n\t" \
  "ds_read_b128 a[192:195], v33 offset:0\n\t" \
  "v_accvgpr_read_b32 v117, a25\n\t" \
  "v_mfma_f32_16x16x32_bf16 v[168:171], a[208:211], v[52:55], v[168:171]\n\t" \
  "v_accvgpr_read_b32 v118, a26\n\t" \
  "v_mfma_f32_16x16x32_bf16 v[164:167], a[212:215], v[48:51], v[164:167]\n\t" \
  "ds_read_b128 a[196:199], v33 offset:1024\n\t" \
  "v_accvgpr_read_b32 v119, a27\n\t" \
  "v_mfma_f32_16x16x32_bf16 v[172:175], a[212:215], v[52:55], v[172:175]\n\t" \
  "v_accvgpr_read_b32 v120, a28\n\t" \
  "v_accvgpr_read_b32 v121, a29\n\t" \
  "v_mfma_f32_16x16x32_bf16 v[176:179], a[216:219], v[48:51], v[176:179]\n\t" \
  "ds_read_b128 a[200:203], v33 offset:2048\n\t" \
  "v_accvgpr_read_b32 v122, a30\n\t" \
  "v_mfma_f32_16x16x32_bf16 v[184:187], a[216:219], v[52:55], v[184:187]\n\t" \
  "v_accvgpr_read_b32 v123, a31\n\t" \
  "v_mfma_f32_16x16x32_bf16 v[180:183], a[220:223], v[48:51], v[180:183]\n\t" \
  "ds_read_b128 a[204:207], v33 offset:3072\n\t" \
  "v_cvt_pk_bf16_f32 v124, v116, v117\n\t" \
  "v_mfma_f32_16x16x32_bf16 v[188:191], a[220:223], v[52:55], v[188:191]\n\t" \
  "v_cvt_pk_bf16_f32 v125, v118, v119\n\t" \
  "s_waitcnt lgkmcnt(4)\n\t" \
  "v_mfma_f32_16x16x32_bf16 v[192:195], a[224:227], v[48:51], v[192:195]\n\t" \
  "ds_read_b128 a[208:211], v33 offset:4096\n\t" \
  "s_mov_b32 s29, s28\n\t" \
  "v_mfma_f32_16x16x32_bf16 v[200:203], a[224:227], v[52:55], v[200:203]\n\t" \
  "v_cvt_pk_bf16_f32 v126, v120, v121\n\t" \
  "v_cvt_pk_bf16_f32 v127, v122, v123\n\t" \
  "v_mfma_f32_16x16x32_bf16 v[196:199], a[228:231], v[48:51], v[196:199]\n\t" \
  "ds_read_b128 a[212:215], v33 offset:5120\n\t" \
  "s_mov_b32 s28, s30\n\t" \
  "v_mfma_f32_16x16x32_bf16 v[204:207], a[228:231], v[52:55], v[204:207]\n\t" \
  "buffer_store_dwordx4 v[124:127], v38, %11, s34 offen sc1\n\t" \
  "v_accvgpr_read_b32 v104, a32\n\t" \
  "v_mfma_f32_16x16x32_bf16 v[208:211], a[232:235], v[48:51], v[208:211]\n\t" \
  "ds_read_b128 a[216:219], v33 offset:6144\n\t" \
  "v_accvgpr_read_b32 v105, a33\n\t" \
  "v_mfma_f32_16x16x32_bf16 v[216:219], a[232:235], v[52:55], v[216:219]\n\t" \
  "v_accvgpr_read_b32 v106, a34\n\t" \
  "v_mfma_f32_16x16x32_bf16 v[212:215], a[236:239], v[48:51], v[212:215]\n\t" \
  "ds_read_b128 a[220:223], v33 offset:7168\n\t" \
  "v_accvgpr_read_b32 v107, a35\n\t" \
  "v_mfma_f32_16x16x32_bf16 v[220:223], a[236:239], v[52:55], v[220:223]\n\t" \
  "v_accvgpr_read_b32 v108, a36\n\t" \
  "v_mfma_f32_16x16x32_bf16 v[224:227], a[240:243], v[48:51], v[224:227]\n\t" \
  "ds_read_b128 a[224:227], v33 offset:8192\n\t" \
  "v_accvgpr_read_b32 v109, a37\n\t" \
  "v_mfma_f32_16x16x32_bf16 v[232:235], a[240:243], v[52:55], v[232:235]\n\t" \
  "v_accvgpr_read_b32 v110, a38\n\t" \
  "v_mfma_f32_16x16x32_bf16 v[228:231], a[244:247], v[48:51], v[228:231]\n\t" \
  "ds_read_b128 a[228:231], v33 offset:9216\n\t" \
  "v_accvgpr_read_b32 v111, a39\n\t" \
  "v_mfma_f32_16x16x32_bf16 v[236:239], a[244:247], v[52:55], v[236:239]\n\t" \
  "v_cvt_pk_bf16_f32 v112, v104, v105\n\t" \
  "v_mfma_f32_16x16x32_bf16 v[240:243], a[248:251], v[48:51], v[240:243]\n\t" \
  "ds_read_b128 a[232:235], v33 offset:10240\n\t" \
  "v_cvt_pk_bf16_f32 v113, v106, v107\n\t" \
  "v_mfma_f32_16x16x32_bf16 v[248:251], a[248:251], v[52:55], v[248:251]\n\t" \
  "v_cvt_pk_bf16_f32 v114, v108, v109\n\t" \
  "v_mfma_f32_16x16x32_bf16 v[244:247], a[252:255], v[48:51], v[244:247]\n\t" \
  "ds_read_b128 a[236:239], v33 offset:11264\n\t" \
  "v_cvt_pk_bf16_f32 v115, v110, v111\n\t" \
  "v_mfma_f32_16x16x32_bf16 v[252:255], a[252:255], v[52:55], v[252:255]\n\t" \
  "buffer_store_dwordx4 v[112:115], %1, %11, s35 offen sc1\n\t" \
  "s_barrier\n\t" \
  "s_waitcnt lgkmcnt(4)\n\t" \
  "v_mfma_f32_16x16x32_bf16 v[128:131], a[192:195], v[56:59], v[128:131]\n\t" \
  "s_add_u32 s30, s28, 0x8000\n\t" \
  "ds_read_b128 a[240:243], v33 offset:12288\n\t" \
  "v_readlane_b32 s26, v27, 5\n\t" \
  "v_readlane_b32 s27, v28, 5\n\t" \
  "v_mfma_f32_16x16x32_bf16 v[136:139], a[192:195], v[60:63], v[136:139]\n\t" \
  "s_cmp_lg_u32 s28, 0x20000\n\t" \
  "s_cselect_b32 s30, s30, 0\n\t" \
  "v_mfma_f32_16x16x32_bf16 v[132:135], a[196:199], v[56:59], v[132:135]\n\t" \
  "ds_read_b128 a[244:247], v33 offset:13312\n\t" \
  "v_accvgpr_read_b32 v116, a40\n\t" \
  "v_mfma_f32_16x16x32_bf16 v[140:143], a[196:199], v[60:63], v[140:143]\n\t" \
  "s_add_u32 m0, s31, 98304\n\t" \
  "v_accvgpr_read_b32 v117, a41\n\t" \
  "v_mfma_f32_16x16x32_bf16 v[144:147], a[200:203], v[56:59], v[144:147]\n\t" \
  "ds_read_b128 a[248:251], v33 offset:14336\n\t" \
  "buffer_load_dwordx4 v34, %11, s26 offen offset:0 sc1 lds\n\t" \
  "v_mfma_f32_16x16x32_bf16 v[152:155], a[200:203], v[60:63], v[152:155]\n\t" \
  "v_accvgpr_read_b32 v118, a42\n\t" \
  "v_accvgpr_read_b32 v119, a43\n\t" \
  "v_mfma_f32_16x16x32_bf16 v[148:151], a[204:207], v[56:59], v[148:151]\n\t" \
  "v_add_u32_e32 v32, s30, %1\n\t" \
  "ds_read_b128 a[252:255], v33 offset:15360\n\t" \
  "buffer_load_dwordx4 v34, %11, s26 offen offset:1024 sc1 lds\n\t" \
  "v_mfma_f32_16x16x32_bf16 v[156:159], a[204:207], v[60:63], v[156:159]\n\t" \
  "v_accvgpr_read_b32 v120, a44\n\t" \
  "v_accvgpr_read_b32 v121, a45\n\t" \
  "v_mfma_f32_16x16x32_bf16 v[160:163], a[208:211], v[56:59], v[160:163]\n\t" \
  "ds_read_b128 a[192:195], v33 offset:16384\n\t" \
  "buffer_load_dwordx4 v34, %11, s26 offen offset:2048 sc1 lds\n\t" \
  "v_mfma_f32_16x16x32_bf16 v[168:171], a[208:211], v[60:63], v[168:171]\n\t" \
  "v_accvgpr_read_b32 v122, a46\n\t" \
  "v_accvgpr_read_b32 v123, a47\n\t" \
  "v_mfma_f32_16x16x32_bf16 v[164:167], a[212:215], v[56:59], v[164:167]\n\t" \
  "ds_read_b128 a[196:199], v33 offset:17408\n\t" \
  "buffer_load_dwordx4 v34, %11, s26 offen offset:3072 sc1 lds\n\t" \
  "v_mfma_f32_16x16x32_bf16 v[172:175], a[212:215], v[60:63], v[172:175]\n\t" \
  "s_add_u32 m0, m0, 0x4000\n\t" \
  "v_cvt_pk_bf16_f32 v124, v116, v117\n\t" \
  "v_mfma_f32_16x16x32_bf16 v[176:179], a[216:219], v[56:59], v[176:179]\n\t" \
  "ds_read_b128 a[200:203], v33 offset:18432\n\t" \
  "buffer_load_dwordx4 v34, %11, s27 offen offset:0 sc1 lds\n\t" \
  "v_mfma_f32_16x16x32_bf16 v[184:187], a[216:219], v[60:63], v[184:187]\n\t" \
  "v_cvt_pk_bf16_f32 v125, v118, v119\n\t" \
  "v_cvt_pk_bf16_f32 v126, v120, v121\n\t" \
  "v_mfma_f32_16x16x32_bf16 v[180:183], a[220:223], v[56:59], v[180:183]\n\t" \
  "ds_read_b128 a[204:207], v33 offset:19456\n\t" \
  "buffer_load_dwordx4 v34, %11, s27 offen offset:1024 sc1 lds\n\t" \
  "v_mfma_f32_16x16x32_bf16 v[188:191], a[220:223], v[60:63], v[188:191]\n\t" \
  "v_cvt_pk_bf16_f32 v127, v122, v123\n\t" \
  "s_waitcnt lgkmcnt(4)\n\t" \
  "v_mfma_f32_16x16x32_bf16 v[192:195], a[224:227], v[56:59], v[192:195]\n\t" \
  "ds_read_b128 a[208:211], v33 offset:20480\n\t" \
  "buffer_load_dwordx4 v34, %11, s27 offen offset:2048 sc1 lds\n\t" \
  "v_mfma_f32_16x16x32_bf16 v[200:203], a[224:227], v[60:63], v[200:203]\n\t" \
  "v_accvgpr_read_b32 v104, a48\n\t" \
  "v_accvgpr_read_b32 v105, a49\n\t" \
  "v_mfma_f32_16x16x32_bf16 v[196:199], a[228:231], v[56:59], v[196:199]\n\t" \
  "ds_read_b128 a[212:215], v33 offset:21504\n\t" \
  "buffer_load_dwordx4 v34, %11, s27 offen offset:3072 sc1 lds\n\t" \
  "v_mfma_f32_16x16x32_bf16 v[204:207], a[228:231], v[60:63], v[204:207]\n\t" \
  "v_accvgpr_read_b32 v106, a50\n\t" \
  "v_accvgpr_read_b32 v107, a51\n\t" \
  "v_mfma_f32_16x16x32_bf16 v[208:211], a[232:235], v[56:59], v[208:211]\n\t" \
  "ds_read_b128 a[216:219], v33 offset:22528\n\t" \
  "buffer_load_dwordx4 a[0:3], %17, %11, s74 offen offset:0 sc1\n\t" \
  "v_mfma_f32_16x16x32_bf16 v[216:219], a[232:235], v[60:63], v[216:219]\n\t" \
  "buffer_load_dwordx4 a[4:7], %17, %11, s78 offen offset:0 sc1\n\t" \
  "v_accvgpr_read_b32 v108, a52\n\t" \
  "v_mfma_f32_16x16x32_bf16 v[212:215], a[236:239], v[56:59], v[212:215]\n\t" \
  "ds_read_b128 a[220:223], v33 offset:23552\n\t" \
  "buffer_load_dwordx4 a[8:11], %17, %11, s74 offen offset:1024 sc1\n\t" \
  "v_mfma_f32_16x16x32_bf16 v[220:223], a[236:239], v[60:63], v[220:223]\n\t" \
  "buffer_load_dwordx4 a[12:15], %17, %11, s78 offen offset:1024 sc1\n\t" \
  "v_accvgpr_read_b32 v109, a53\n\t" \
  "v_mfma_f32_16x16x32_bf16 v[224:227], a[240:243], v[56:59], v[224:227]\n\t" \
  "ds_read_b128 a[224:227], v33 offset:24576\n\t" \
  "buffer_load_dwordx4 a[16:19], %17, %11, s74 offen offset:2048 sc1\n\t" \
  "v_mfma_f32_16x16x32_bf16 v[232:235], a[240:243], v[60:63], v[232:235]\n\t" \
  "buffer_load_dwordx4 a[20:23], %17, %11, s78 offen offset:2048 sc1\n\t" \
  "v_accvgpr_read_b32 v110, a54\n\t" \
  "v_mfma_f32_16x16x32_bf16 v[228:231], a[244:247], v[56:59], v[228:231]\n\t" \
  "ds_read_b128 a[228:231], v33 offset:25600\n\t" \
  "v_accvgpr_read_b32 v111, a55\n\t" \
  "v_mfma_f32_16x16x32_bf16 v[236:239], a[244:247], v[60:63], v[236:239]\n\t" \
  "v_cvt_pk_bf16_f32 v112, v104, v105\n\t" \
  "v_cvt_pk_bf16_f32 v113, v106, v107\n\t" \
  "v_cvt_pk_bf16_f32 v114, v108, v109\n\t" \
  "v_cvt_pk_bf16_f32 v115, v110, v111\n\t" \
  "v_mfma_f32_16x16x32_bf16 v[240:243], a[248:251], v[56:59], v[240:243]\n\t" \
  "ds_read_b128 a[232:235], v33 offset:26624\n\t" \
  "buffer_store_dwordx4 v[124:127], v35, %11, s35 offen sc1\n\t" \
  "v_mfma_f32_16x16x32_bf16 v[248:251], a[248:251], v[60:63], v[248:251]\n\t" \
  "buffer_store_dwordx4 v[112:115], v36, %11, s35 offen sc1\n\t" \
  "v_accvgpr_read_b32 v116, a56\n\t" \
  "v_mfma_f32_16x16x32_bf16 v[244:247], a[252:255], v[56:59], v[244:247]\n\t" \
  "ds_read_b128 a[236:239], v33 offset:27648\n\t" \
  "v_accvgpr_read_b32 v117, a57\n\t" \
  "v_mfma_f32_16x16x32_bf16 v[252:255], a[252:255], v[60:63], v[252:255]\n\t" \
  "v_accvgpr_read_b32 v118, a58\n\t" \
  "s_waitcnt lgkmcnt(4)\n\t" \
  "v_mfma_f32_16x16x32_bf16 v[128:131], a[192:195], v[64:67], v[128:131]\n\t" \
  "ds_read_b128 a[240:243], v33 offset:28672\n\t" \
  "v_accvgpr_read_b32 v119, a59\n\t" \
  "v_mfma_f32_16x16x32_bf16 v[136:139], a[192:195], v[68:71], v[136:139]\n\t" \
  "v_accvgpr_read_b32 v120, a60\n\t" \
  "v_mfma_f32_16x16x32_bf16 v[132:135], a[196:199], v[64:67], v[132:135]\n\t" \
  "ds_read_b128 a[244:247], v33 offset:29696\n\t" \
  "v_accvgpr_read_b32 v121, a61\n\t" \
  "v_mfma_f32_16x16x32_bf16 v[140:143], a[196:199], v[68:71], v[140:143]\n\t" \
  "v_accvgpr_read_b32 v122, a62\n\t" \
  "v_accvgpr_read_b32 v123, a63\n\t" \
  "v_mfma_f32_16x16x32_bf16 v[144:147], a[200:203], v[64:67], v[144:147]\n\t" \
  "ds_read_b128 a[248:251], v33 offset:30720\n\t" \
  "v_cvt_pk_bf16_f32 v124, v116, v117\n\t" \
  "v_mfma_f32_16x16x32_bf16 v[152:155], a[200:203], v[68:71], v[152:155]\n\t" \
  "v_cvt_pk_bf16_f32 v125, v118, v119\n\t" \
  "v_mfma_f32_16x16x32_bf16 v[148:151], a[204:207], v[64:67], v[148:151]\n\t" \
  "ds_read_b128 a[252:255], v33 offset:31744\n\t" \
  "v_cvt_pk_bf16_f32 v126, v120, v121\n\t" \
  "v_mfma_f32_16x16x32_bf16 v[156:159], a[204:207], v[68:71], v[156:159]\n\t" \
  "v_cvt_pk_bf16_f32 v127, v122, v123\n\t" \
  "v_mfma_f32_16x16x32_bf16 v[160:163], a[208:211], v[64:67], v[160:163]\n\t" \
  "ds_read_b128 a[192:195], v32 offset:0\n\t" \
  "buffer_store_dwordx4 v[124:127], v38, %11, s35 offen sc1\n\t" \
  "v_mfma_f32_16x16x32_bf16 v[168:171], a[208:211], v[68:71], v[168:171]\n\t" \
  "v_accvgpr_read_b32 v104, a128\n\t" \
  "v_mfma_f32_16x16x32_bf16 v[164:167], a[212:215], v[64:67], v[164:167]\n\t" \
  "ds_read_b128 a[196:199], v32 offset:1024\n\t" \
  "v_accvgpr_read_b32 v105, a129\n\t" \
  "v_mfma_f32_16x16x32_bf16 v[172:175], a[212:215], v[68:71], v[172:175]\n\t" \
  "v_accvgpr_read_b32 v106, a130\n\t" \
  "v_accvgpr_read_b32 v107, a131\n\t" \
  "v_mfma_f32_16x16x32_bf16 v[176:179], a[216:219], v[64:67], v[176:179]\n\t" \
  "ds_read_b128 a[200:203], v32 offset:2048\n\t" \
  "v_accvgpr_read_b32 v108, a132\n\t" \
  "v_mfma_f32_16x16x32_bf16 v[184:187], a[216:219], v[68:71], v[184:187]\n\t" \
  "v_accvgpr_read_b32 v109, a133\n\t" \
  "v_mfma_f32_16x16x32_bf16 v[180:183], a[220:223], v[64:67], v[180:183]\n\t" \
  "ds_read_b128 a[204:207], v32 offset:3072\n\t" \
  "v_accvgpr_read_b32 v110, a134\n\t" \
  "v_mfma_f32_16x16x32_bf16 v[188:191], a[220:223], v[68:71], v[188:191]\n\t" \
  "v_accvgpr_read_b32 v111, a135\n\t" \
  "s_waitcnt lgkmcnt(4)\n\t" \
  "v_mfma_f32_16x16x32_bf16 v[192:195], a[224:227], v[64:67], v[192:195]\n\t" \
  "ds_read_b128 a[208:211], v32 offset:4096\n\t" \
  "s_mov_b32 s29, s28\n\t" \
  "v_mfma_f32_16x16x32_bf16 v[200:203], a[224:227], v[68:71], v[200:203]\n\t" \
  "v_cvt_pk_bf16_f32 v112, v104, v105\n\t" \
  "v_cvt_pk_bf16_f32 v113, v106, v107\n\t" \
  "v_mfma_f32_16x16x32_bf16 v[196:199], a[228:231], v[64:67], v[196:199]\n\t" \
  "ds_read_b128 a[212:215], v32 offset:5120\n\t" \
  "s_mov_b32 s28, s30\n\t" \
  "v_mfma_f32_16x16x32_bf16 v[204:207], a[228:231], v[68:71], v[204:207]\n\t" \
  "v_cvt_pk_bf16_f32 v114, v108, v109\n\t" \
  "v_cvt_pk_bf16_f32 v115, v110, v111\n\t" \
  "v_mfma_f32_16x16x32_bf16 v[208:211], a[232:235], v[64:67], v[208:211]\n\t" \
  "ds_read_b128 a[216:219], v32 offset:6144\n\t" \
  "buffer_store_dwordx4 v[112:115], %1, %11, s38 offen sc1\n\t" \
  "v_mfma_f32_16x16x32_bf16 v[216:219], a[232:235], v[68:71], v[216:219]\n\t" \
  "v_accvgpr_read_b32 v116, a136\n\t" \
  "v_accvgpr_read_b32 v117, a137\n\t" \
  "v_mfma_f32_16x16x32_bf16 v[212:215], a[236:239], v[64:67], v[212:215]\n\t" \
  "ds_read_b128 a[220:223], v32 offset:7168\n\t" \
  "v_accvgpr_read_b32 v118, a138\n\t" \
  "v_mfma_f32_16x16x32_bf16 v[220:223], a[236:239], v[68:71], v[220:223]\n\t" \
  "v_accvgpr_read_b32 v119, a139\n\t" \
  "v_mfma_f32_16x16x32_bf16 v[224:227], a[240:243], v[64:67], v[224:227]\n\t" \
  "ds_read_b128 a[224:227], v32 offset:8192\n\t" \
  "v_accvgpr_read_b32 v120, a140\n\t" \
  "v_mfma_f32_16x16x32_bf16 v[232:235], a[240:243], v[68:71], v[232:235]\n\t" \
  "v_accvgpr_read_b32 v121, a141\n\t" \
  "v_accvgpr_read_b32 v122, a142\n\t" \
  "v_mfma_f32_16x16x32_bf16 v[228:231], a[244:247], v[64:67], v[228:231]\n\t" \
  "ds_read_b128 a[228:231], v32 offset:9216\n\t" \
  "v_accvgpr_read_b32 v123, a143\n\t" \
  "v_mfma_f32_16x16x32_bf16 v[236:239], a[244:247], v[68:71], v[236:239]\n\t" \
  "v_cvt_pk_bf16_f32 v124, v116, v117\n\t" \
  "v_mfma_f32_16x16x32_bf16 v[240:243], a[248:251], v[64:67], v[240:243]\n\t" \
  "ds_read_b128 a[232:235], v32 offset:10240\n\t" \
  "v_cvt_pk_bf16_f32 v125, v118, v119\n\t" \
  "v_mfma_f32_16x16x32_bf16 v[248:251], a[248:251], v[68:71], v[248:251]\n\t" \
  "v_cvt_pk_bf16_f32 v126, v120, v121\n\t" \
  "v_mfma_f32_16x16x32_bf16 v[244:247], a[252:255], v[64:67], v[244:247]\n\t" \
  "ds_read_b128 a[236:239], v32 offset:11264\n\t" \
  "v_cvt_pk_bf16_f32 v127, v122, v123\n\t" \
  "v_mfma_f32_16x16x32_bf16 v[252:255], a[252:255], v[68:71], v[252:255]\n\t" \
  "buffer_store_dwordx4 v[124:127], v35, %11, s38 offen sc1\n\t" \
  "s_waitcnt vmcnt(40)\n\t" \
  "s_barrier\n\t" \
  "s_waitcnt lgkmcnt(4)\n\t" \
  "v_mfma_f32_16x16x32_bf16 v[128:131], a[192:195], v[72:75], v[128:131]\n\t" \
  "s_add_u32 s30, s28, 0x8000\n\t" \
  "ds_read_b128 a[240:243], v32 offset:12288\n\t" \
  "v_readlane_b32 s26, v27, 6\n\t" \
  "v_readlane_b32 s27, v28, 6\n\t" \
  "v_mfma_f32_16x16x32_bf16 v[136:139], a[192:195], v[76:79], v[136:139]\n\t" \
  "s_cmp_lg_u32 s28, 0x20000\n\t" \
  "s_cselect_b32 s30, s30, 0\n\t" \
  "v_mfma_f32_16x16x32_bf16 v[132:135], a[196:199], v[72:75], v[132:135]\n\t" \
  "ds_read_b128 a[244:247], v32 offset:13312\n\t" \
  "v_accvgpr_read_b32 v104, a144\n\t" \
  "v_mfma_f32_16x16x32_bf16 v[140:143], a[196:199], v[76:79], v[140:143]\n\t" \
  "s_add_u32 m0, s31, 131072\n\t" \
  "v_accvgpr_read_b32 v105, a145\n\t" \
  "v_mfma_f32_16x16x32_bf16 v[144:147], a[200:203], v[72:75], v[144:147]\n\t" \
  "ds_read_b128 a[248:251], v32 offset:14336\n\t" \
  "buffer_load_dwordx4 v34, %11, s26 offen offset:0 sc1 lds\n\t" \
  "v_mfma_f32_16x16x32_bf16 v[152:155], a[200:203], v[76:79], v[152:155]\n\t" \
  "v_accvgpr_read_b32 v106, a146\n\t" \
  "v_accvgpr_read_b32 v107, a147\n\t" \
  "v_mfma_f32_16x16x32_bf16 v[148:151], a[204:207], v[72:75], v[148:151]\n\t" \
  "v_add_u32_e32 v33, s30, %1\n\t" \
  "ds_read_b128 a[252:255], v32 offset:15360\n\t" \
  "buffer_load_dwordx4 v34, %11, s26 offen offset:1024 sc1 lds\n\t" \
  "v_mfma_f32_16x16x32_bf16 v[156:159], a[204:207], v[76:79], v[156:159]\n\t" \
  "v_accvgpr_read_b32 v108, a148\n\t" \
  "v_accvgpr_read_b32 v109, a149\n\t" \
  "v_mfma_f32_16x16x32_bf16 v[160:163], a[208:211], v[72:75], v[160:163]\n\t" \
  "ds_read_b128 a[192:195], v32 offset:16384\n\t" \
  "buffer_load_dwordx4 v34, %11, s26 offen offset:2048 sc1 lds\n\t" \
  "v_mfma_f32_16x16x32_bf16 v[168:171], a[208:211], v[76:79], v[168:171]\n\t" \
  "v_accvgpr_read_b32 v110, a150\n\t" \
  "v_accvgpr_read_b32 v111, a151\n\t" \
  "v_mfma_f32_16x16x32_bf16 v[164:167], a[212:215], v[72:75], v[164:167]\n\t" \
  "ds_read_b128 a[196:199], v32 offset:17408\n\t" \
  "buffer_load_dwordx4 v34, %11, s26 offen offset:3072 sc1 lds\n\t" \
  "v_mfma_f32_16x16x32_bf16 v[172:175], a[212:215], v[76:79], v[172:175]\n\t" \
  "s_add_u32 m0, m0, 0x4000\n\t" \
  "v_cvt_pk_bf16_f32 v112, v104, v105\n\t" \
  "v_mfma_f32_16x16x32_bf16 v[176:179], a[216:219], v[72:75], v[176:179]\n\t" \
  "ds_read_b128 a[200:203], v32 offset:18432\n\t" \
  "buffer_load_dwordx4 v34, %11, s27 offen offset:0 sc1 lds\n\t" \
  "v_mfma_f32_16x16x32_bf16 v[184:187], a[216:219], v[76:79], v[184:187]\n\t" \
  "v_cvt_pk_bf16_f32 v113, v106, v107\n\t" \
  "v_cvt_pk_bf16_f32 v114, v108, v109\n\t" \
  "v_mfma_f32_16x16x32_bf16 v[180:183], a[220:223], v[72:75], v[180:183]\n\t" \
  "ds_read_b128 a[204:207], v32 offset:19456\n\t" \
  "buffer_load_dwordx4 v34, %11, s27 offen offset:1024 sc1 lds\n\t" \
  "v_mfma_f32_16x16x32_bf16 v[188:191], a[220:223], v[76:79], v[188:191]\n\t" \
  "v_cvt_pk_bf16_f32 v115, v110, v111\n\t" \
  "s_waitcnt lgkmcnt(4)\n\t" \
  "v_mfma_f32_16x16x32_bf16 v[192:195], a[224:227], v[72:75], v[192:195]\n\t" \
  "ds_read_b128 a[208:211], v32 offset:20480\n\t" \
  "buffer_load_dwordx4 v34, %11, s27 offen offset:2048 sc1 lds\n\t" \
  "v_mfma_f32_16x16x32_bf16 v[200:203], a[224:227], v[76:79], v[200:203]\n\t" \
  "v_accvgpr_read_b32 v116, a152\n\t" \
  "v_accvgpr_read_b32 v117, a153\n\t" \
  "v_mfma_f32_16x16x32_bf16 v[196:199], a[228:231], v[72:75], v[196:199]\n\t" \
  "ds_read_b128 a[212:215], v32 offset:21504\n\t" \
  "buffer_load_dwordx4 v34, %11, s27 offen offset:3072 sc1 lds\n\t" \
  "v_mfma_f32_16x16x32_bf16 v[204:207], a[228:231], v[76:79], v[204:207]\n\t" \
  "v_accvgpr_read_b32 v118, a154\n\t" \
  "v_accvgpr_read_b32 v119, a155\n\t" \
  "v_mfma_f32_16x16x32_bf16 v[208:211], a[232:235], v[72:75], v[208:211]\n\t" \
  "ds_read_b128 a[216:219], v32 offset:22528\n\t" \
  "buffer_load_dwordx4 a[24:27], %17, %11, s74 offen offset:3072 sc1\n\t" \
  "v_mfma_f32_16x16x32_bf16 v[216:219], a[232:235], v[76:79], v[216:219]\n\t" \
  "buffer_load_dwordx4 a[28:31], %17, %11, s78 offen offset:3072 sc1\n\t" \
  "v_accvgpr_read_b32 v120, a156\n\t" \
  "v_mfma_f32_16x16x32_bf16 v[212:215], a[236:239], v[72:75], v[212:215]\n\t" \
  "ds_read_b128 a[220:223], v32 offset:23552\n\t" \
  "buffer_load_dwordx4 a[32:35], %17, %11, s75 offen offset:0 sc1\n\t" \
  "v_mfma_f32_16x16x32_bf16 v[220:223], a[236:239], v[76:79], v[220:223]\n\t" \
  "buffer_load_dwordx4 a[36:39], %17, %11, s79 offen offset:0 sc1\n\t" \
  "v_accvgpr_read_b32 v121, a157\n\t" \
  "v_mfma_f32_16x16x32_bf16 v[224:227], a[240:243], v[72:75], v[224:227]\n\t" \
  "ds_read_b128 a[224:227], v32 offset:24576\n\t" \
  "buffer_load_dwordx4 a[40:43], %17, %11, s75 offen offset:1024 sc1\n\t" \
  "v_mfma_f32_16x16x32_bf16 v[232:235], a[240:243], v[76:79], v[232:235]\n\t" \
  "buffer_load_dwordx4 a[44:47], %17, %11, s79 offen offset:1024 sc1\n\t" \
  "v_accvgpr_read_b32 v122, a158\n\t" \
  "v_mfma_f32_16x16x32_bf16 v[228:231], a[244:247], v[72:75], v[228:231]\n\t" \
  "ds_read_b128 a[228:231], v32 offset:25600\n\t" \
  "v_accvgpr_read_b32 v123, a159\n\t" \
  "v_mfma_f32_16x16x32_bf16 v[236:239], a[244:247], v[76:79], v[236:239]\n\t" \
  "v_cvt_pk_bf16_f32 v124, v116, v117\n\t" \
  "v_cvt_pk_bf16_f32 v125, v118, v119\n\t" \
  "v_cvt_pk_bf16_f32 v126, v120, v121\n\t" \
  "v_cvt_pk_bf16_f32 v127, v122, v123\n\t" \
  "v_mfma_f32_16x16x32_bf16 v[240:243], a[248:251], v[72:75], v[240:243]\n\t" \
  "ds_read_b128 a[232:235], v32 offset:26624\n\t" \
  "buffer_store_dwordx4 v[112:115], v36, %11, s38 offen sc1\n\t" \
  "v_mfma_f32_16x16x32_bf16 v[248:251], a[248:251], v[76:79], v[248:251]\n\t" \
  "buffer_store_dwordx4 v[124:127], v38, %11, s38 offen sc1\n\t" \
  "v_accvgpr_read_b32 v104, a160\n\t" \
  "v_mfma_f32_16x16x32_bf16 v[244:247], a[252:255], v[72:75], v[244:247]\n\t" \
  "ds_read_b128 a[236:239], v32 offset:27648\n\t" \
  "v_accvgpr_read_b32 v105, a161\n\t" \
  "v_mfma_f32_16x16x32_bf16 v[252:255], a[252:255], v[76:79], v[252:255]\n\t" \
  "v_accvgpr_read_b32 v106, a162\n\t" \
  "s_waitcnt lgkmcnt(4)\n\t" \
  "v_mfma_f32_16x16x32_bf16 v[128:131], a[192:195], v[80:83], v[128:131]\n\t" \
  "ds_read_b128 a[240:243], v32 offset:28672\n\t" \
  "v_accvgpr_read_b32 v107, a163\n\t" \
  "v_mfma_f32_16x16x32_bf16 v[136:139], a[192:195], v[84:87], v[136:139]\n\t" \
  "v_accvgpr_read_b32 v108, a164\n\t" \
  "v_mfma_f32_16x16x32_bf16 v[132:135], a[196:199], v[80:83], v[132:135]\n\t" \
  "ds_read_b128 a[244:247], v32 offset:29696\n\t" \
  "v_accvgpr_read_b32 v109, a165\n\t" \
  "v_mfma_f32_16x16x32_bf16 v[140:143], a[196:199], v[84:87], v[140:143]\n\t" \
  "v_accvgpr_read_b32 v110, a166\n\t" \
  "v_accvgpr_read_b32 v111, a167\n\t" \
  "v_mfma_f32_16x16x32_bf16 v[144:147], a[200:203], v[80:83], v[144:147]\n\t" \
  "ds_read_b128 a[248:251], v32 offset:30720\n\t" \
  "v_cvt_pk_bf16_f32 v112, v104, v105\n\t" \
  "v_mfma_f32_16x16x32_bf16 v[152:155], a[200:203], v[84:87], v[152:155]\n\t" \
  "v_cvt_pk_bf16_f32 v113, v106, v107\n\t" \
  "v_mfma_f32_16x16x32_bf16 v[148:151], a[204:207], v[80:83], v[148:151]\n\t" \
  "ds_read_b128 a[252:255], v32 offset:31744\n\t" \
  "v_cvt_pk_bf16_f32 v114, v108, v109\n\t" \
  "v_mfma_f32_16x16x32_bf16 v[156:159], a[204:207], v[84:87], v[156:159]\n\t" \
  "v_cvt_pk_bf16_f32 v115, v110, v111\n\t" \
  "v_mfma_f32_16x16x32_bf16 v[160:163], a[208:211], v[80:83], v[160:163]\n\t" \
  "ds_read_b128 a[192:195], v33 offset:0\n\t" \
  "buffer_store_dwordx4 v[112:115], %1, %11, s39 offen sc1\n\t" \
  "v_mfma_f32_16x16x32_bf16 v[168:171], a[208:211], v[84:87], v[168:171]\n\t" \
  "v_accvgpr_read_b32 v116, a168\n\t" \
  "v_mfma_f32_16x16x32_bf16 v[164:167], a[212:215], v[80:83], v[164:167]\n\t" \
  "ds_read_b128 a[196:199], v33 offset:1024\n\t" \
  "v_accvgpr_read_b32 v117, a169\n\t" \
  "v_mfma_f32_16x16x32_bf16 v[172:175], a[212:215], v[84:87], v[172:175]\n\t" \
  "v_accvgpr_read_b32 v118, a170\n\t" \
  "v_accvgpr_read_b32 v119, a171\n\t" \
  "v_mfma_f32_16x16x32_bf16 v[176:179], a[216:219], v[80:83], v[176:179]\n\t" \
  "ds_read_b128 a[200:203], v33 offset:2048\n\t" \
  "v_accvgpr_read_b32 v120, a172\n\t" \
  "v_mfma_f32_16x16x32_bf16 v[184:187], a[216:219], v[84:87], v[184:187]\n\t" \
  "v_accvgpr_read_b32 v121, a173\n\t" \
  "v_mfma_f32_16x16x32_bf16 v[180:183], a[220:223], v[80:83], v[180:183]\n\t" \
  "ds_read_b128 a[204:207], v33 offset:3072\n\t" \
  "v_accvgpr_read_b32 v122, a174\n\t" \
  "v_mfma_f32_16x16x32_bf16 v[188:191], a[220:223], v[84:87], v[188:191]\n\t" \
  "v_accvgpr_read_b32 v123, a175\n\t" \
  "s_waitcnt lgkmcnt(4)\n\t" \
  "v_mfma_f32_16x16x32_bf16 v[192:195], a[224:227], v[80:83], v[192:195]\n\t" \
  "ds_read_b128 a[208:211], v33 offset:4096\n\t" \
  "s_mov_b32 s29, s28\n\t" \
  "v_mfma_f32_16x16x32_bf16 v[200:203], a[224:227], v[84:87], v[200:203]\n\t" \
  "v_cvt_pk_bf16_f32 v124, v116, v117\n\t" \
  "v_cvt_pk_bf16_f32 v125, v118, v119\n\t" \
  "v_mfma_f32_16x16x32_bf16 v[196:199], a[228:231], v[80:83], v[196:199]\n\t" \
  "ds_read_b128 a[212:215], v33 offset:5120\n\t" \
  "s_mov_b32 s28, s30\n\t" \
  "v_mfma_f32_16x16x32_bf16 v[204:207], a[228:231], v[84:87], v[204:207]\n\t" \
  "v_cvt_pk_bf16_f32 v126, v120, v121\n\t" \
  "v_cvt_pk_bf16_f32 v127, v122, v123\n\t" \
  "v_mfma_f32_16x16x32_bf16 v[208:211], a[232:235], v[80:83], v[208:211]\n\t" \
  "ds_read_b128 a[216:219], v33 offset:6144\n\t" \
  "buffer_store_dwordx4 v[124:127], v35, %11, s39 offen sc1\n\t" \
  "v_mfma_f32_16x16x32_bf16 v[216:219], a[232:235], v[84:87], v[216:219]\n\t" \
  "v_accvgpr_read_b32 v104, a176\n\t" \
  "v_accvgpr_read_b32 v105, a177\n\t" \
  "v_mfma_f32_16x16x32_bf16 v[212:215], a[236:239], v[80:83], v[212:215]\n\t" \
  "ds_read_b128 a[220:223], v33 offset:7168\n\t" \
  "v_accvgpr_read_b32 v106, a178\n\t" \
  "v_mfma_f32_16x16x32_bf16 v[220:223], a[236:239], v[84:87], v[220:223]\n\t" \
  "v_accvgpr_read_b32 v107, a179\n\t" \
  "v_mfma_f32_16x16x32_bf16 v[224:227], a[240:243], v[80:83], v[224:227]\n\t" \
  "ds_read_b128 a[224:227], v33 offset:8192\n\t" \
  "v_accvgpr_read_b32 v108, a180\n\t" \
  "v_mfma_f32_16x16x32_bf16 v[232:235], a[240:243], v[84:87], v[232:235]\n\t" \
  "v_accvgpr_read_b32 v109, a181\n\t" \
  "v_accvgpr_read_b32 v110, a182\n\t" \
  "v_mfma_f32_16x16x32_bf16 v[228:231], a[244:247], v[80:83], v[228:231]\n\t" \
  "ds_read_b128 a[228:231], v33 offset:9216\n\t" \
  "v_accvgpr_read_b32 v111, a183\n\t" \
  "v_mfma_f32_16x16x32_bf16 v[236:239], a[244:247], v[84:87], v[236:239]\n\t" \
  "v_cvt_pk_bf16_f32 v112, v104, v105\n\t" \
  "v_mfma_f32_16x16x32_bf16 v[240:243], a[248:251], v[80:83], v[240:243]\n\t" \
  "ds_read_b128 a[232:235], v33 offset:10240\n\t" \
  "v_cvt_pk_bf16_f32 v113, v106, v107\n\t" \
  "v_mfma_f32_16x16x32_bf16 v[248:251], a[248:251], v[84:87], v[248:251]\n\t" \
  "v_cvt_pk_bf16_f32 v114, v108, v109\n\t" \
  "v_mfma_f32_16x16x32_bf16 v[244:247], a[252:255], v[80:83], v[244:247]\n\t" \
  "ds_read_b128 a[236:239], v33 offset:11264\n\t" \
  "v_cvt_pk_bf16_f32 v115, v110, v111\n\t" \
  "v_mfma_f32_16x16x32_bf16 v[252:255], a[252:255], v[84:87], v[252:255]\n\t" \
  "buffer_store_dwordx4 v[112:115], v36, %11, s39 offen sc1\n\t" \
  "s_waitcnt vmcnt(51)\n\t" \
  "s_barrier\n\t" \
  "s_waitcnt lgkmcnt(4)\n\t" \
  "v_mfma_f32_16x16x32_bf16 v[128:131], a[192:195], v[88:91], v[128:131]\n\t" \
  "s_add_u32 s30, s28, 0x8000\n\t" \
  "ds_read_b128 a[240:243], v33 offset:12288\n\t" \
  "v_readlane_b32 s26, v27, 7\n\t" \
  "v_readlane_b32 s27, v28, 7\n\t" \
  "v_mfma_f32_16x16x32_bf16 v[136:139], a[192:195], v[92:95], v[136:139]\n\t" \
  "s_cmp_lg_u32 s28, 0x20000\n\t" \
  "s_cselect_b32 s30, s30, 0\n\t" \
  "v_mfma_f32_16x16x32_bf16 v[132:135], a[196:199], v[88:91], v[132:135]\n\t" \
  "ds_read_b128 a[244:247], v33 offset:13312\n\t" \
  "v_accvgpr_read_b32 v116, a184\n\t" \
  "v_mfma_f32_16x16x32_bf16 v[140:143], a[196:199], v[92:95], v[140:143]\n\t" \
  "s_add_u32 m0, s31, 0\n\t" \
  "v_accvgpr_read_b32 v117, a185\n\t" \
  "v_mfma_f32_16x16x32_bf16 v[144:147], a[200:203], v[88:91], v[144:147]\n\t" \
  "ds_read_b128 a[248:251], v33 offset:14336\n\t" \
  "buffer_load_dwordx4 v34, %11, s26 offen offset:0 sc1 lds\n\t" \
  "v_mfma_f32_16x16x32_bf16 v[152:155], a[200:203], v[92:95], v[152:155]\n\t" \
  "v_accvgpr_read_b32 v118, a186\n\t" \
  "v_accvgpr_read_b32 v119, a187\n\t" \
  "v_mfma_f32_16x16x32_bf16 v[148:151], a[204:207], v[88:91], v[148:151]\n\t" \
  "v_add_u32_e32 v32, s30, %1\n\t" \
  "ds_read_b128 a[252:255], v33 offset:15360\n\t" \
  "buffer_load_dwordx4 v34, %11, s26 offen offset:1024 sc1 lds\n\t" \
  "v_mfma_f32_16x16x32_bf16 v[156:159], a[204:207], v[92:95], v[156:159]\n\t" \
  "v_accvgpr_read_b32 v120, a188\n\t" \
  "v_accvgpr_read_b32 v121, a189\n\t" \
  "v_mfma_f32_16x16x32_bf16 v[160:163], a[208:211], v[88:91], v[160:163]\n\t" \
  "ds_read_b128 a[192:195], v33 offset:16384\n\t" \
  "buffer_load_dwordx4 v34, %11, s26 offen offset:2048 sc1 lds\n\t" \
  "v_mfma_f32_16x16x32_bf16 v[168:171], a[208:211], v[92:95], v[168:171]\n\t" \
  "v_accvgpr_read_b32 v122, a190\n\t" \
  "v_accvgpr_read_b32 v123, a191\n\t" \
  "v_mfma_f32_16x16x32_bf16 v[164:167], a[212:215], v[88:91], v[164:167]\n\t" \
  "ds_read_b128 a[196:199], v33 offset:17408\n\t" \
  "buffer_load_dwordx4 v34, %11, s26 offen offset:3072 sc1 lds\n\t" \
  "v_mfma_f32_16x16x32_bf16 v[172:175], a[212:215], v[92:95], v[172:175]\n\t" \
  "s_add_u32 m0, m0, 0x4000\n\t" \
  "v_cvt_pk_bf16_f32 v124, v116, v117\n\t" \
  "v_mfma_f32_16x16x32_bf16 v[176:179], a[216:219], v[88:91], v[176:179]\n\t" \
  "ds_read_b128 a[200:203], v33 offset:18432\n\t" \
  "buffer_load_dwordx4 v34, %11, s27 offen offset:0 sc1 lds\n\t" \
  "v_mfma_f32_16x16x32_bf16 v[184:187], a[216:219], v[92:95], v[184:187]\n\t" \
  "v_cvt_pk_bf16_f32 v125, v118, v119\n\t" \
  "v_cvt_pk_bf16_f32 v126, v120, v121\n\t" \
  "v_mfma_f32_16x16x32_bf16 v[180:183], a[220:223], v[88:91], v[180:183]\n\t" \
  "ds_read_b128 a[204:207], v33 offset:19456\n\t" \
  "buffer_load_dwordx4 v34, %11, s27 offen offset:1024 sc1 lds\n\t" \
  "v_mfma_f32_16x16x32_bf16 v[188:191], a[220:223], v[92:95], v[188:191]\n\t" \
  "v_cvt_pk_bf16_f32 v127, v122, v123\n\t" \
  "s_waitcnt lgkmcnt(4)\n\t" \
  "v_mfma_f32_16x16x32_bf16 v[192:195], a[224:227], v[88:91], v[192:195]\n\t" \
  "ds_read_b128 a[208:211], v33 offset:20480\n\t" \
  "buffer_load_dwordx4 v34, %11, s27 offen offset:2048 sc1 lds\n\t" \
  "v_mfma_f32_16x16x32_bf16 v[200:203], a[224:227], v[92:95], v[200:203]\n\t" \
  "v_accvgpr_read_b32 v104, a64\n\t" \
  "v_accvgpr_read_b32 v105, a65\n\t" \
  "v_mfma_f32_16x16x32_bf16 v[196:199], a[228:231], v[88:91], v[196:199]\n\t" \
  "ds_read_b128 a[212:215], v33 offset:21504\n\t" \
  "buffer_load_dwordx4 v34, %11, s27 offen offset:3072 sc1 lds\n\t" \
  "v_mfma_f32_16x16x32_bf16 v[204:207], a[228:231], v[92:95], v[204:207]\n\t" \
  "v_accvgpr_read_b32 v106, a66\n\t" \
  "v_accvgpr_read_b32 v107, a67\n\t" \
  "v_mfma_f32_16x16x32_bf16 v[208:211], a[232:235], v[88:91], v[208:211]\n\t" \
  "ds_read_b128 a[216:219], v33 offset:22528\n\t" \
  "v_accvgpr_read_b32 v108, a68\n\t" \
  "v_mfma_f32_16x16x32_bf16 v[216:219], a[232:235], v[92:95], v[216:219]\n\t" \
  "v_accvgpr_read_b32 v109, a69\n\t" \
  "v_accvgpr_read_b32 v110, a70\n\t" \
  "v_mfma_f32_16x16x32_bf16 v[212:215], a[236:239], v[88:91], v[212:215]\n\t" \
  "ds_read_b128 a[220:223], v33 offset:23552\n\t" \
  "v_accvgpr_read_b32 v111, a71\n\t" \
  "v_mfma_f32_16x16x32_bf16 v[220:223], a[236:239], v[92:95], v[220:223]\n\t" \
  "v_cvt_pk_bf16_f32 v112, v104, v105\n\t" \
  "v_cvt_pk_bf16_f32 v113, v106, v107\n\t" \
  "v_mfma_f32_16x16x32_bf16 v[224:227], a[240:243], v[88:91], v[224:227]\n\t" \
  "ds_read_b128 a[224:227], v33 offset:24576\n\t" \
  "v_cvt_pk_bf16_f32 v114, v108, v109\n\t" \
  "v_mfma_f32_16x16x32_bf16 v[232:235], a[240:243], v[92:95], v[232:235]\n\t" \
  "v_cvt_pk_bf16_f32 v115, v110, v111\n\t" \
  "v_mfma_f32_16x16x32_bf16 v[228:231], a[244:247], v[88:91], v[228:231]\n\t" \
  "ds_read_b128 a[228:231], v33 offset:25600\n\t" \
  "v_mfma_f32_16x16x32_bf16 v[236:239], a[244:247], v[92:95], v[236:239]\n\t" \
  "v_mfma_f32_16x16x32_bf16 v[240:243], a[248:251], v[88:91], v[240:243]\n\t" \
  "ds_read_b128 a[232:235], v33 offset:26624\n\t" \
  "buffer_store_dwordx4 v[124:127], v38, %11, s39 offen sc1\n\t" \
  "v_mfma_f32_16x16x32_bf16 v[248:251], a[248:251], v[92:95], v[248:251]\n\t" \
  "buffer_store_dwordx4 v[112:115], %1, %11, s36 offen sc1\n\t" \
  "v_accvgpr_read_b32 v116, a72\n\t" \
  "v_mfma_f32_16x16x32_bf16 v[244:247], a[252:255], v[88:91], v[244:247]\n\t" \
  "ds_read_b128 a[236:239], v33 offset:27648\n\t" \
  "v_accvgpr_read_b32 v117, a73\n\t" \
  "v_mfma_f32_16x16x32_bf16 v[252:255], a[252:255], v[92:95], v[252:255]\n\t" \
  "v_accvgpr_read_b32 v118, a74\n\t" \
  "s_waitcnt lgkmcnt(4)\n\t" \
  "v_mfma_f32_16x16x32_bf16 v[128:131], a[192:195], v[96:99], v[128:131]\n\t" \
  "ds_read_b128 a[240:243], v33 offset:28672\n\t" \
  "v_accvgpr_read_b32 v119, a75\n\t" \
  "v_mfma_f32_16x16x32_bf16 v[136:139], a[192:195], v[100:103], v[136:139]\n\t" \
  "v_accvgpr_read_b32 v120, a76\n\t" \
  "v_mfma_f32_16x16x32_bf16 v[132:135], a[196:199], v[96:99], v[132:135]\n\t" \
  "ds_read_b128 a[244:247], v33 offset:29696\n\t" \
  "v_accvgpr_read_b32 v121, a77\n\t" \
  "v_mfma_f32_16x16x32_bf16 v[140:143], a[196:199], v[100:103], v[140:143]\n\t" \
  "v_accvgpr_read_b32 v122, a78\n\t" \
  "v_accvgpr_read_b32 v123, a79\n\t" \
  "v_mfma_f32_16x16x32_bf16 v[144:147], a[200:203], v[96:99], v[144:147]\n\t" \
  "ds_read_b128 a[248:251], v33 offset:30720\n\t" \
  "v_cvt_pk_bf16_f32 v124, v116, v117\n\t" \
  "v_mfma_f32_16x16x32_bf16 v[152:155], a[200:203], v[100:103], v[152:155]\n\t" \
  "v_cvt_pk_bf16_f32 v125, v118, v119\n\t" \
  "v_mfma_f32_16x16x32_bf16 v[148:151], a[204:207], v[96:99], v[148:151]\n\t" \
  "ds_read_b128 a[252:255], v33 offset:31744\n\t" \
  "v_cvt_pk_bf16_f32 v126, v120, v121\n\t" \
  "v_mfma_f32_16x16x32_bf16 v[156:159], a[204:207], v[100:103], v[156:159]\n\t" \
  "v_cvt_pk_bf16_f32 v127, v122, v123\n\t" \
  "v_mfma_f32_16x16x32_bf16 v[160:163], a[208:211], v[96:99], v[160:163]\n\t" \
  "ds_read_b128 a[192:195], v32 offset:0\n\t" \
  "buffer_store_dwordx4 v[124:127], v35, %11, s36 offen sc1\n\t" \
  "v_mfma_f32_16x16x32_bf16 v[168:171], a[208:211], v[100:103], v[168:171]\n\t" \
  "v_accvgpr_read_b32 v104, a80\n\t" \
  "v_mfma_f32_16x16x32_bf16 v[164:167], a[212:215], v[96:99], v[164:167]\n\t" \
  "ds_read_b128 a[196:199], v32 offset:1024\n\t" \
  "v_accvgpr_read_b32 v105, a81\n\t" \
  "v_mfma_f32_16x16x32_bf16 v[172:175], a[212:215], v[100:103], v[172:175]\n\t" \
  "v_accvgpr_read_b32 v106, a82\n\t" \
  "v_accvgpr_read_b32 v107, a83\n\t" \
  "v_mfma_f32_16x16x32_bf16 v[176:179], a[216:219], v[96:99], v[176:179]\n\t" \
  "ds_read_b128 a[200:203], v32 offset:2048\n\t" \
  "v_accvgpr_read_b32 v108, a84\n\t" \
  "v_mfma_f32_16x16x32_bf16 v[184:187], a[216:219], v[100:103], v[184:187]\n\t" \
  "v_accvgpr_read_b32 v109, a85\n\t" \
  "v_mfma_f32_16x16x32_bf16 v[180:183], a[220:223], v[96:99], v[180:183]\n\t" \
  "ds_read_b128 a[204:207], v32 offset:3072\n\t" \
  "v_accvgpr_read_b32 v110, a86\n\t" \
  "v_mfma_f32_16x16x32_bf16 v[188:191], a[220:223], v[100:103], v[188:191]\n\t" \
  "v_accvgpr_read_b32 v111, a87\n\t" \
  "s_waitcnt lgkmcnt(4)\n\t" \
  "v_mfma_f32_16x16x32_bf16 v[192:195], a[224:227], v[96:99], v[192:195]\n\t" \
  "ds_read_b128 a[208:211], v32 offset:4096\n\t" \
  "s_mov_b32 s29, s28\n\t" \
  "v_mfma_f32_16x16x32_bf16 v[200:203], a[224:227], v[100:103], v[200:203]\n\t" \
  "v_cvt_pk_bf16_f32 v112, v104, v105\n\t" \
  "v_cvt_pk_bf16_f32 v113, v106, v107\n\t" \
  "v_mfma_f32_16x16x32_bf16 v[196:199], a[228:231], v[96:99], v[196:199]\n\t" \
  "ds_read_b128 a[212:215], v32 offset:5120\n\t" \
  "s_mov_b32 s28, s30\n\t" \
  "v_mfma_f32_16x16x32_bf16 v[204:207], a[228:231], v[100:103], v[204:207]\n\t" \
  "v_cvt_pk_bf16_f32 v114, v108, v109\n\t" \
  "v_cvt_pk_bf16_f32 v115, v110, v111\n\t" \
  "v_mfma_f32_16x16x32_bf16 v[208:211], a[232:235], v[96:99], v[208:211]\n\t" \
  "ds_read_b128 a[216:219], v32 offset:6144\n\t" \
  "buffer_store_dwordx4 v[112:115], v36, %11, s36 offen sc1\n\t" \
  "v_mfma_f32_16x16x32_bf16 v[216:219], a[232:235], v[100:103], v[216:219]\n\t" \
  "v_accvgpr_read_b32 v116, a88\n\t" \
  "v_accvgpr_read_b32 v117, a89\n\t" \
  "v_mfma_f32_16x16x32_bf16 v[212:215], a[236:239], v[96:99], v[212:215]\n\t" \
  "ds_read_b128 a[220:223], v32 offset:7168\n\t" \
  "v_accvgpr_read_b32 v118, a90\n\t" \
  "v_mfma_f32_16x16x32_bf16 v[220:223], a[236:239], v[100:103], v[220:223]\n\t" \
  "v_accvgpr_read_b32 v119, a91\n\t" \
  "v_mfma_f32_16x16x32_bf16 v[224:227], a[240:243], v[96:99], v[224:227]\n\t" \
  "ds_read_b128 a[224:227], v32 offset:8192\n\t" \
  "v_accvgpr_read_b32 v120, a92\n\t" \
  "v_mfma_f32_16x16x32_bf16 v[232:235], a[240:243], v[100:103], v[232:235]\n\t" \
  "v_accvgpr_read_b32 v121, a93\n\t" \
  "v_accvgpr_read_b32 v122, a94\n\t" \
  "v_mfma_f32_16x16x32_bf16 v[228:231], a[244:247], v[96:99], v[228:231]\n\t" \
  "ds_read_b128 a[228:231], v32 offset:9216\n\t" \
  "v_accvgpr_read_b32 v123, a95\n\t" \
  "v_mfma_f32_16x16x32_bf16 v[236:239], a[244:247], v[100:103], v[236:239]\n\t" \
  "v_cvt_pk_bf16_f32 v124, v116, v117\n\t" \
  "v_mfma_f32_16x16x32_bf16 v[240:243], a[248:251], v[96:99], v[240:243]\n\t" \
  "ds_read_b128 a[232:235], v32 offset:10240\n\t" \
  "v_cvt_pk_bf16_f32 v125, v118, v119\n\t" \
  "v_mfma_f32_16x16x32_bf16 v[248:251], a[248:251], v[100:103], v[248:251]\n\t" \
  "v_cvt_pk_bf16_f32 v126, v120, v121\n\t" \
  "v_mfma_f32_16x16x32_bf16 v[244:247], a[252:255], v[96:99], v[244:247]\n\t" \
  "ds_read_b128 a[236:239], v32 offset:11264\n\t" \
  "v_cvt_pk_bf16_f32 v127, v122, v123\n\t" \
  "v_mfma_f32_16x16x32_bf16 v[252:255], a[252:255], v[100:103], v[252:255]\n\t" \
  "buffer_store_dwordx4 v[124:127], v38, %11, s36 offen sc1\n\t" \
  "s_waitcnt vmcnt(43)\n\t" \
  "s_barrier\n\t" \
  "s_waitcnt lgkmcnt(4)\n\t" \
  "s_waitcnt vmcnt(41)\n\t" \
  "v_mfma_f32_16x16x32_bf16 v[128:131], a[192:195], a[0:3], v[128:131]\n\t" \
  "s_add_u32 s30, s28, 0x8000\n\t" \
  "ds_read_b128 a[240:243], v32 offset:12288\n\t" \
  "v_readlane_b32 s26, v27, 8\n\t" \
  "v_readlane_b32 s27, v28, 8\n\t" \
  "v_mfma_f32_16x16x32_bf16 v[136:139], a[192:195], a[4:7], v[136:139]\n\t" \
  "s_cmp_lg_u32 s28, 0x20000\n\t" \
  "s_cselect_b32 s30, s30, 0\n\t" \
  "v_mfma_f32_16x16x32_bf16 v[132:135], a[196:199], a[0:3], v[132:135]\n\t" \
  "ds_read_b128 a[244:247], v32 offset:13312\n\t" \
  "v_accvgpr_read_b32 v104, a96\n\t" \
  "v_mfma_f32_16x16x32_bf16 v[140:143], a[196:199], a[4:7], v[140:143]\n\t" \
  "s_add_u32 m0, s31, 32768\n\t" \
  "v_accvgpr_read_b32 v105, a97\n\t" \
  "v_mfma_f32_16x16x32_bf16 v[144:147], a[200:203], a[0:3], v[144:147]\n\t" \
  "ds_read_b128 a[248:251], v32 offset:14336\n\t" \
  "buffer_load_dwordx4 v34, %11, s26 offen offset:0 sc1 lds\n\t" \
  "v_mfma_f32_16x16x32_bf16 v[152:155], a[200:203], a[4:7], v[152:155]\n\t" \
  "v_accvgpr_read_b32 v106, a98\n\t" \
  "v_accvgpr_read_b32 v107, a99\n\t" \
  "v_mfma_f32_16x16x32_bf16 v[148:151], a[204:207], a[0:3], v[148:151]\n\t" \
  "v_add_u32_e32 v33, s30, %1\n\t" \
  "ds_read_b128 a[252:255], v32 offset:15360\n\t" \
  "buffer_load_dwordx4 v34, %11, s26 offen offset:1024 sc1 lds\n\t" \
  "v_mfma_f32_16x16x32_bf16 v[156:159], a[204:207], a[4:7], v[156:159]\n\t" \
  "v_accvgpr_read_b32 v108, a100\n\t" \
  "v_accvgpr_read_b32 v109, a101\n\t" \
  "v_mfma_f32_16x16x32_bf16 v[160:163], a[208:211], a[0:3], v[160:163]\n\t" \
  "ds_read_b128 a[192:195], v32 offset:16384\n\t" \
  "buffer_load_dwordx4 v34, %11, s26 offen offset:2048 sc1 lds\n\t" \
  "v_mfma_f32_16x16x32_bf16 v[168:171], a[208:211], a[4:7], v[168:171]\n\t" \
  "v_accvgpr_read_b32 v110, a102\n\t" \
  "v_accvgpr_read_b32 v111, a103\n\t" \
  "v_mfma_f32_16x16x32_bf16 v[164:167], a[212:215], a[0:3], v[164:167]\n\t" \
  "ds_read_b128 a[196:199], v32 offset:17408\n\t" \
  "buffer_load_dwordx4 v34, %11, s26 offen offset:3072 sc1 lds\n\t" \
  "v_mfma_f32_16x16x32_bf16 v[172:175], a[212:215], a[4:7], v[172:175]\n\t" \
  "s_add_u32 m0, m0, 0x4000\n\t" \
  "v_cvt_pk_bf16_f32 v112, v104, v105\n\t" \
  "v_mfma_f32_16x16x32_bf16 v[176:179], a[216:219], a[0:3], v[176:179]\n\t" \
  "ds_read_b128 a[200:203], v32 offset:18432\n\t" \
  "buffer_load_dwordx4 v34, %11, s27 offen offset:0 sc1 lds\n\t" \
  "v_mfma_f32_16x16x32_bf16 v[184:187], a[216:219], a[4:7], v[184:187]\n\t" \
  "v_cvt_pk_bf16_f32 v113, v106, v107\n\t" \
  "v_cvt_pk_bf16_f32 v114, v108, v109\n\t" \
  "v_mfma_f32_16x16x32_bf16 v[180:183], a[220:223], a[0:3], v[180:183]\n\t" \
  "ds_read_b128 a[204:207], v32 offset:19456\n\t" \
  "buffer_load_dwordx4 v34, %11, s27 offen offset:1024 sc1 lds\n\t" \
  "v_mfma_f32_16x16x32_bf16 v[188:191], a[220:223], a[4:7], v[188:191]\n\t" \
  "v_cvt_pk_bf16_f32 v115, v110, v111\n\t" \
  "s_waitcnt lgkmcnt(4)\n\t" \
  "v_mfma_f32_16x16x32_bf16 v[192:195], a[224:227], a[0:3], v[192:195]\n\t" \
  "ds_read_b128 a[208:211], v32 offset:20480\n\t" \
  "buffer_load_dwordx4 v34, %11, s27 offen offset:2048 sc1 lds\n\t" \
  "v_mfma_f32_16x16x32_bf16 v[200:203], a[224:227], a[4:7], v[200:203]\n\t" \
  "v_accvgpr_read_b32 v116, a104\n\t" \
  "v_accvgpr_read_b32 v117, a105\n\t" \
  "v_mfma_f32_16x16x32_bf16 v[196:199], a[228:231], a[0:3], v[196:199]\n\t" \
  "ds_read_b128 a[212:215], v32 offset:21504\n\t" \
  "buffer_load_dwordx4 v34, %11, s27 offen offset:3072 sc1 lds\n\t" \
  "v_mfma_f32_16x16x32_bf16 v[204:207], a[228:231], a[4:7], v[204:207]\n\t" \
  "v_accvgpr_read_b32 v118, a106\n\t" \
  "v_accvgpr_read_b32 v119, a107\n\t" \
  "v_mfma_f32_16x16x32_bf16 v[208:211], a[232:235], a[0:3], v[208:211]\n\t" \
  "ds_read_b128 a[216:219], v32 offset:22528\n\t" \
  "v_accvgpr_read_b32 v120, a108\n\t" \
  "v_mfma_f32_16x16x32_bf16 v[216:219], a[232:235], a[4:7], v[216:219]\n\t" \
  "v_accvgpr_read_b32 v121, a109\n\t" \
  "v_accvgpr_read_b32 v122, a110\n\t" \
  "v_mfma_f32_16x16x32_bf16 v[212:215], a[236:239], a[0:3], v[212:215]\n\t" \
  "ds_read_b128 a[220:223], v32 offset:23552\n\t" \
  "v_accvgpr_read_b32 v123, a111\n\t" \
  "v_mfma_f32_16x16x32_bf16 v[220:223], a[236:239], a[4:7], v[220:223]\n\t" \
  "v_cvt_pk_bf16_f32 v124, v116, v117\n\t" \
  "v_cvt_pk_bf16_f32 v125, v118, v119\n\t" \
  "v_mfma_f32_16x16x32_bf16 v[224:227], a[240:243], a[0:3], v[224:227]\n\t" \
  "ds_read_b128 a[224:227], v32 offset:24576\n\t" \
  "v_cvt_pk_bf16_f32 v126, v120, v121\n\t" \
  "v_mfma_f32_16x16x32_bf16 v[232:235], a[240:243], a[4:7], v[232:235]\n\t" \
  "v_cvt_pk_bf16_f32 v127, v122, v123\n\t" \
  "v_mfma_f32_16x16x32_bf16 v[228:231], a[244:247], a[0:3], v[228:231]\n\t" \
  "ds_read_b128 a[228:231], v32 offset:25600\n\t" \
  "v_mfma_f32_16x16x32_bf16 v[236:239], a[244:247], a[4:7], v[236:239]\n\t" \
  "v_mfma_f32_16x16x32_bf16 v[240:243], a[248:251], a[0:3], v[240:243]\n\t" \
  "ds_read_b128 a[232:235], v32 offset:26624\n\t" \
  "buffer_store_dwordx4 v[112:115], %1, %11, s37 offen sc1\n\t" \
  "v_mfma_f32_16x16x32_bf16 v[248:251], a[248:251], a[4:7], v[248:251]\n\t" \
  "buffer_store_dwordx4 v[124:127], v35, %11, s37 offen sc1\n\t" \
  "v_mfma_f32_16x16x32_bf16 v[244:247], a[252:255], a[0:3], v[244:247]\n\t" \
  "ds_read_b128 a[236:239], v32 offset:27648\n\t" \
  "v_accvgpr_read_b32 v104, a112\n\t" \
  "v_readlane_b32 s72, v29, 20\n\t" \
  "v_mfma_f32_16x16x32_bf16 v[252:255], a[252:255], a[4:7], v[252:255]\n\t" \
  "v_accvgpr_read_b32 v105, a113\n\t" \
  "s_waitcnt lgkmcnt(4)\n\t" \
  "s_waitcnt vmcnt(49)\n\t" \
  "v_mfma_f32_16x16x32_bf16 v[128:131], a[192:195], a[8:11], v[128:131]\n\t" \
  "ds_read_b128 a[240:243], v32 offset:28672\n\t" \
  "v_mfma_f32_16x16x32_bf16 v[136:139], a[192:195], a[12:15], v[136:139]\n\t" \
  "v_accvgpr_read_b32 v106, a114\n\t" \
  "v_mfma_f32_16x16x32_bf16 v[132:135], a[196:199], a[8:11], v[132:135]\n\t" \
  "ds_read_b128 a[244:247], v32 offset:29696\n\t" \
  "v_accvgpr_read_b32 v107, a115\n\t" \
  "v_mfma_f32_16x16x32_bf16 v[140:143], a[196:199], a[12:15], v[140:143]\n\t" \
  "v_accvgpr_read_b32 v108, a116\n\t" \
  "v_mfma_f32_16x16x32_bf16 v[144:147], a[200:203], a[8:11], v[144:147]\n\t" \
  "ds_read_b128 a[248:251], v32 offset:30720\n\t" \
  "v_mfma_f32_16x16x32_bf16 v[152:155], a[200:203], a[12:15], v[152:155]\n\t" \
  "v_accvgpr_read_b32 v109, a117\n\t" \
  "v_mfma_f32_16x16x32_bf16 v[148:151], a[204:207], a[8:11], v[148:151]\n\t" \
  "ds_read_b128 a[252:255], v32 offset:31744\n\t" \
  "v_accvgpr_read_b32 v110, a118\n\t" \
  "v_mfma_f32_16x16x32_bf16 v[156:159], a[204:207], a[12:15], v[156:159]\n\t" \
  "v_accvgpr_read_b32 v111, a119\n\t" \
  "v_mfma_f32_16x16x32_bf16 v[160:163], a[208:211], a[8:11], v[160:163]\n\t" \
  "ds_read_b128 a[192:195], v33 offset:0\n\t" \
  "v_cvt_pk_bf16_f32 v112, v104, v105\n\t" \
  "s_add_u32 s73, s72, 4096\n\t" \
  "v_mfma_f32_16x16x32_bf16 v[168:171], a[208:211], a[12:15], v[168:171]\n\t" \
  "v_mfma_f32_16x16x32_bf16 v[164:167], a[212:215], a[8:11], v[164:167]\n\t" \
  "ds_read_b128 a[196:199], v33 offset:1024\n\t" \
  "v_cvt_pk_bf16_f32 v113, v106, v107\n\t" \
  "v_mfma_f32_16x16x32_bf16 v[172:175], a[212:215], a[12:15], v[172:175]\n\t" \
  "v_cvt_pk_bf16_f32 v114, v108, v109\n\t" \
  "v_mfma_f32_16x16x32_bf16 v[176:179], a[216:219], a[8:11], v[176:179]\n\t" \
  "ds_read_b128 a[200:203], v33 offset:2048\n\t" \
  "v_cvt_pk_bf16_f32 v115, v110, v111\n\t" \
  "v_mfma_f32_16x16x32_bf16 v[184:187], a[216:219], a[12:15], v[184:187]\n\t" \
  "v_mfma_f32_16x16x32_bf16 v[180:183], a[220:223], a[8:11], v[180:183]\n\t" \
  "ds_read_b128 a[204:207], v33 offset:3072\n\t" \
  "buffer_store_dwordx4 v[112:115], v36, %11, s37 offen sc1\n\t" \
  "v_mfma_f32_16x16x32_bf16 v[188:191], a[220:223], a[12:15], v[188:191]\n\t" \
  "v_accvgpr_read_b32 v116, a120\n\t" \
  "s_waitcnt lgkmcnt(4)\n\t" \
  "v_mfma_f32_16x16x32_bf16 v[192:195], a[224:227], a[8:11], v[192:195]\n\t" \
  "ds_read_b128 a[208:211], v33 offset:4096\n\t" \
  "s_mov_b32 s29, s28\n\t" \
  "s_add_u32 s74, s72, 8192\n\t" \
  "v_mfma_f32_16x16x32_bf16 v[200:203], a[224:227], a[12:15], v[200:203]\n\t" \
  "v_accvgpr_read_b32 v117, a121\n\t" \
  "v_accvgpr_read_b32 v118, a122\n\t" \
  "v_mfma_f32_16x16x32_bf16 v[196:199], a[228:231], a[8:11], v[196:199]\n\t" \
  "ds_read_b128 a[212:215], v33 offset:5120\n\t" \
  "s_mov_b32 s28, s30\n\t" \
  "v_mfma_f32_16x16x32_bf16 v[204:207], a[228:231], a[12:15], v[204:207]\n\t" \
  "v_accvgpr_read_b32 v119, a123\n\t" \
  "v_mfma_f32_16x16x32_bf16 v[208:211], a[232:235], a[8:11], v[208:211]\n\t" \
  "ds_read_b128 a[216:219], v33 offset:6144\n\t" \
  "v_accvgpr_read_b32 v120, a124\n\t" \
  "v_mfma_f32_16x16x32_bf16 v[216:219], a[232:235], a[12:15], v[216:219]\n\t" \
  "v_accvgpr_read_b32 v121, a125\n\t" \
  "v_mfma_f32_16x16x32_bf16 v[212:215], a[236:239], a[8:11], v[212:215]\n\t" \
  "ds_read_b128 a[220:223], v33 offset:7168\n\t" \
  "v_mfma_f32_16x16x32_bf16 v[220:223], a[236:239], a[12:15], v[220:223]\n\t" \
  "v_accvgpr_read_b32 v122, a126\n\t" \
  "v_mfma_f32_16x16x32_bf16 v[224:227], a[240:243], a[8:11], v[224:227]\n\t" \
  "ds_read_b128 a[224:227], v33 offset:8192\n\t" \
  "v_accvgpr_read_b32 v123, a127\n\t" \
  "s_add_u32 s75, s72, 12288\n\t" \
  "v_mfma_f32_16x16x32_bf16 v[232:235], a[240:243], a[12:15], v[232:235]\n\t" \
  "v_cvt_pk_bf16_f32 v124, v116, v117\n\t" \
  "v_mfma_f32_16x16x32_bf16 v[228:231], a[244:247], a[8:11], v[228:231]\n\t" \
  "ds_read_b128 a[228:231], v33 offset:9216\n\t" \
  "v_cvt_pk_bf16_f32 v125, v118, v119\n\t" \
  "v_mfma_f32_16x16x32_bf16 v[236:239], a[244:247], a[12:15], v[236:239]\n\t" \
  "v_mfma_f32_16x16x32_bf16 v[240:243], a[248:251], a[8:11], v[240:243]\n\t" \
  "ds_read_b128 a[232:235], v33 offset:10240\n\t" \
  "v_cvt_pk_bf16_f32 v126, v120, v121\n\t" \
  "v_mfma_f32_16x16x32_bf16 v[248:251], a[248:251], a[12:15], v[248:251]\n\t" \
  "v_cvt_pk_bf16_f32 v127, v122, v123\n\t" \
  "v_mfma_f32_16x16x32_bf16 v[244:247], a[252:255], a[8:11], v[244:247]\n\t" \
  "ds_read_b128 a[236:239], v33 offset:11264\n\t" \
  "buffer_store_dwordx4 v[124:127], v38, %11, s37 offen sc1\n\t" \
  "v_mfma_f32_16x16x32_bf16 v[252:255], a[252:255], a[12:15], v[252:255]\n\t" \
  "s_waitcnt vmcnt(36)\n\t" \
  "s_barrier\n\t" \
  "s_waitcnt lgkmcnt(4)\n\t" \
  "v_mfma_f32_16x16x32_bf16 v[128:131], a[192:195], a[16:19], v[128:131]\n\t" \
  "s_add_u32 s30, s28, 0x8000\n\t" \
  "ds_read_b128 a[240:243], v33 offset:12288\n\t" \
  "v_mfma_f32_16x16x32_bf16 v[136:139], a[192:195], a[20:23], v[136:139]\n\t" \
  "s_cmp_lg_u32 s28, 0x20000\n\t" \
  "s_cselect_b32 s30, s30, 0\n\t" \
  "v_readlane_b32 s26, v27, 9\n\t" \
  "v_readlane_b32 s27, v28, 9\n\t" \
  "v_mfma_f32_16x16x32_bf16 v[132:135], a[196:199], a[16:19], v[132:135]\n\t" \
  "ds_read_b128 a[244:247], v33 offset:13312\n\t" \
  "v_readlane_b32 s81, v29, 40\n\t" \
  "v_accvgpr_write_b32 a48, 0\n\t" \
  "v_mfma_f32_16x16x32_bf16 v[140:143], a[196:199], a[20:23], v[140:143]\n\t" \
  "v_readlane_b32 s47, v29, 19\n\t" \
  "v_accvgpr_write_b32 a49, 0\n\t" \
  "s_waitcnt vmcnt(34)\n\t" \
  "v_mfma_f32_16x16x32_bf16 v[128:131], a[200:203], a[24:27], v[128:131]\n\t" \
  "ds_read_b128 a[248:251], v33 offset:14336\n\t" \
  "s_add_u32 m0, s31, 65536\n\t" \
  "v_mfma_f32_16x16x32_bf16 v[136:139], a[200:203], a[28:31], v[136:139]\n\t" \
  "buffer_load_dwordx4 v34, %11, s26 offen offset:0 sc1 lds\n\t" \
  "v_readlane_b32 s80, v29, 41\n\t" \
  "v_mfma_f32_16x16x32_bf16 v[132:135], a[204:207], a[24:27], v[132:135]\n\t" \
  "v_add_u32_e32 v32, s30, %1\n\t" \
  "ds_read_b128 a[252:255], v33 offset:15360\n\t" \
  "v_mfma_f32_16x16x32_bf16 v[140:143], a[204:207], a[28:31], v[140:143]\n\t" \
  "v_readlane_b32 s40, v29, 22\n\t" \
  "v_readlane_b32 s68, v29, 18\n\t" \
  "s_waitcnt vmcnt(33)\n\t" \
  "v_mfma_f32_16x16x32_bf16 v[128:131], a[208:211], a[32:35], v[128:131]\n\t" \
  "ds_read_b128 a[192:195], v33 offset:16384\n\t" \
  "v_readlane_b32 s41, v29, 7\n\t" \
  "v_mfma_f32_16x16x32_bf16 v[136:139], a[208:211], a[36:39], v[136:139]\n\t" \
  "v_accvgpr_write_b32 a50, 0\n\t" \
  "v_accvgpr_write_b32 a51, 0\n\t" \
  "v_mfma_f32_16x16x32_bf16 v[132:135], a[212:215], a[32:35], v[132:135]\n\t" \
  "ds_read_b128 a[196:199], v33 offset:17408\n\t" \
  "v_readlane_b32 s34, v29, 23\n\t" \
  "v_accvgpr_write_b32 a52, 0\n\t" \
  "v_mfma_f32_16x16x32_bf16 v[140:143], a[212:215], a[36:39], v[140:143]\n\t" \
  "v_readlane_b32 s64, %13, 4\n\t" \
  "v_accvgpr_write_b32 a53, 0\n\t" \
  "s_waitcnt vmcnt(31)\n\t" \
  "v_mfma_f32_16x16x32_bf16 v[128:131], a[216:219], a[40:43], v[128:131]\n\t" \
  "ds_read_b128 a[200:203], v33 offset:18432\n\t" \
  "buffer_load_dwordx4 v34, %11, s26 offen offset:1024 sc1 lds\n\t" \
  "v_mfma_f32_16x16x32_bf16 v[136:139], a[216:219], a[44:47], v[136:139]\n\t" \
  "v_readlane_b32 s65, %13, 5\n\t" \
  "v_accvgpr_write_b32 a54, 0\n\t" \
  "v_mfma_f32_16x16x32_bf16 v[132:135], a[220:223], a[40:43], v[132:135]\n\t" \
  "ds_read_b128 a[204:207], v33 offset:19456\n\t" \
  "v_mov_b32_e32 v38, s90\n\t" \
  "v_mfma_f32_16x16x32_bf16 v[140:143], a[220:223], a[44:47], v[140:143]\n\t" \
  "s_mov_b32 s66, 3584\n\t" \
  "s_waitcnt lgkmcnt(4)\n\t" \
  "v_mfma_f32_16x16x32_bf16 v[144:147], a[224:227], a[16:19], v[144:147]\n\t" \
  "ds_read_b128 a[208:211], v33 offset:20480\n\t" \
  "v_lshrrev_b32_e32 v39, 8, %1\n\t" \
  "v_mfma_f32_16x16x32_bf16 v[152:155], a[224:227], a[20:23], v[152:155]\n\t" \
  "v_accvgpr_write_b32 a55, 0\n\t" \
  "v_accvgpr_write_b32 a56, 0\n\t" \
  "v_mfma_f32_16x16x32_bf16 v[148:151], a[228:231], a[16:19], v[148:151]\n\t" \
  "ds_read_b128 a[212:215], v33 offset:21504\n\t" \
  "v_lshlrev_b32_e32 v39, 3, v39\n\t" \
  "v_mfma_f32_16x16x32_bf16 v[156:159], a[228:231], a[20:23], v[156:159]\n\t" \
  "buffer_load_dwordx4 v34, %11, s26 offen offset:2048 sc1 lds\n\t" \
  "v_bfe_u32 v125, %1, 2, 6\n\t" \
  "v_accvgpr_write_b32 a57, 0\n\t" \
  "v_mfma_f32_16x16x32_bf16 v[144:147], a[232:235], a[24:27], v[144:147]\n\t" \
  "ds_read_b128 a[216:219], v33 offset:22528\n\t" \
  "v_accvgpr_write_b32 a58, 0\n\t" \
  "v_mfma_f32_16x16x32_bf16 v[152:155], a[232:235], a[28:31], v[152:155]\n\t" \
  "v_cvt_pk_bf16_f32 v128, v128, v129\n\t" \
  "v_cvt_pk_bf16_f32 v129, v130, v131\n\t" \
  "v_mfma_f32_16x16x32_bf16 v[148:151], a[236:239], a[24:27], v[148:151]\n\t" \
  "ds_read_b128 a[220:223], v33 offset:23552\n\t" \
  "v_cvt_pk_bf16_f32 v130, v132, v133\n\t" \
  "v_mfma_f32_16x16x32_bf16 v[156:159], a[236:239], a[28:31], v[156:159]\n\t" \
  "v_cvt_pk_bf16_f32 v131, v134, v135\n\t" \
  "buffer_store_dwordx4 v[128:131], %1, %11, s72 offen offset:0 sc1\n\t" \
  "v_mfma_f32_16x16x32_bf16 v[144:147], a[240:243], a[32:35], v[144:147]\n\t" \
  "ds_read_b128 a[224:227], v33 offset:24576\n\t" \
  "v_cvt_pk_bf16_f32 v132, v136, v137\n\t" \
  "v_mfma_f32_16x16x32_bf16 v[152:155], a[240:243], a[36:39], v[152:155]\n\t" \
  "v_cvt_pk_bf16_f32 v133, v138, v139\n\t" \
  "v_cvt_pk_bf16_f32 v134, v140, v141\n\t" \
  "v_mfma_f32_16x16x32_bf16 v[148:151], a[244:247], a[32:35], v[148:151]\n\t" \
  "ds_read_b128 a[228:231], v33 offset:25600\n\t" \
  "buffer_load_dwordx4 v34, %11, s26 offen offset:3072 sc1 lds\n\t" \
  "v_mfma_f32_16x16x32_bf16 v[156:159], a[244:247], a[36:39], v[156:159]\n\t" \
  "v_cvt_pk_bf16_f32 v135, v142, v143\n\t" \
  "buffer_store_dwordx4 v[132:135], %1, %11, s72 offen offset:1024 sc1\n\t" \
  "v_mfma_f32_16x16x32_bf16 v[144:147], a[248:251], a[40:43], v[144:147]\n\t" \
  "ds_read_b128 a[232:235], v33 offset:26624\n\t" \
  "v_accvgpr_write_b32 a59, 0\n\t" \
  "v_accvgpr_write_b32 a60, 0\n\t" \
  "v_mfma_f32_16x16x32_bf16 v[152:155], a[248:251], a[44:47], v[152:155]\n\t" \
  "v_accvgpr_write_b32 a61, 0\n\t" \
  "v_accvgpr_write_b32 a62, 0\n\t" \
  "v_mfma_f32_16x16x32_bf16 v[148:151], a[252:255], a[40:43], v[148:151]\n\t" \
  "ds_read_b128 a[236:239], v33 offset:27648\n\t" \
  "v_accvgpr_write_b32 a63, 0\n\t" \
  "v_mfma_f32_16x16x32_bf16 v[156:159], a[252:255], a[44:47], v[156:159]\n\t" \
  "v_accvgpr_write_b32 a64, 0\n\t" \
  "s_waitcnt lgkmcnt(4)\n\t" \
  "v_mfma_f32_16x16x32_bf16 v[160:163], a[192:195], a[16:19], v[160:163]\n\t" \
  "ds_read_b128 a[240:243], v33 offset:28672\n\t" \
  "s_add_u32 m0, m0, 0x4000\n\t" \
  "v_mfma_f32_16x16x32_bf16 v[168:171], a[192:195], a[20:23], v[168:171]\n\t" \
  "buffer_load_dwordx4 v34, %11, s27 offen offset:0 sc1 lds\n\t" \
  "v_accvgpr_write_b32 a65, 0\n\t" \
  "v_mfma_f32_16x16x32_bf16 v[164:167], a[196:199], a[16:19], v[164:167]\n\t" \
  "ds_read_b128 a[244:247], v33 offset:29696\n\t" \
  "v_accvgpr_write_b32 a66, 0\n\t" \
  "v_mfma_f32_16x16x32_bf16 v[172:175], a[196:199], a[20:23], v[172:175]\n\t" \
  "v_accvgpr_write_b32 a67, 0\n\t" \
  "v_accvgpr_write_b32 a68, 0\n\t" \
  "v_mfma_f32_16x16x32_bf16 v[160:163], a[200:203], a[24:27], v[160:163]\n\t" \
  "ds_read_b128 a[248:251], v33 offset:30720\n\t" \
  "v_accvgpr_write_b32 a69, 0\n\t" \
  "v_mfma_f32_16x16x32_bf16 v[168:171], a[200:203], a[28:31], v[168:171]\n\t" \
  "v_cvt_pk_bf16_f32 v136, v144, v145\n\t" \
  "v_cvt_pk_bf16_f32 v137, v146, v147\n\t" \
  "v_accvgpr_write_b32 a70, 0\n\t" \
  "v_mfma_f32_16x16x32_bf16 v[164:167], a[204:207], a[24:27], v[164:167]\n\t" \
  "ds_read_b128 a[252:255], v33 offset:31744\n\t" \
  "v_cvt_pk_bf16_f32 v138, v148, v149\n\t" \
  "v_mfma_f32_16x16x32_bf16 v[172:175], a[204:207], a[28:31], v[172:175]\n\t" \
  "v_cvt_pk_bf16_f32 v139, v150, v151\n\t" \
  "buffer_store_dwordx4 v[136:139], %1, %11, s72 offen offset:2048 sc1\n\t" \
  "v_mfma_f32_16x16x32_bf16 v[160:163], a[208:211], a[32:35], v[160:163]\n\t" \
  "ds_read_b128 a[192:195], v32 offset:0\n\t" \
  "buffer_load_dwordx4 v34, %11, s27 offen offset:1024 sc1 lds\n\t" \
  "v_mfma_f32_16x16x32_bf16 v[168:171], a[208:211], a[36:39], v[168:171]\n\t" \
  "v_cvt_pk_bf16_f32 v140, v152, v153\n\t" \
  "v_cvt_pk_bf16_f32 v141, v154, v155\n\t" \
  "v_mfma_f32_16x16x32_bf16 v[164:167], a[212:215], a[32:35], v[164:167]\n\t" \
  "ds_read_b128 a[196:199], v32 offset:1024\n\t" \
  "v_cvt_pk_bf16_f32 v142, v156, v157\n\t" \
  "v_mfma_f32_16x16x32_bf16 v[172:175], a[212:215], a[36:39], v[172:175]\n\t" \
  "v_cvt_pk_bf16_f32 v143, v158, v159\n\t" \
  "buffer_store_dwordx4 v[140:143], %1, %11, s72 offen offset:3072 sc1\n\t" \
  "v_mfma_f32_16x16x32_bf16 v[160:163], a[216:219], a[40:43], v[160:163]\n\t" \
  "ds_read_b128 a[200:203], v32 offset:2048\n\t" \
  "v_accvgpr_write_b32 a71, 0\n\t" \
  "v_mfma_f32_16x16x32_bf16 v[168:171], a[216:219], a[44:47], v[168:171]\n\t" \
  "v_accvgpr_write_b32 a72, 0\n\t" \
  "v_accvgpr_write_b32 a73, 0\n\t" \
  "v_mfma_f32_16x16x32_bf16 v[164:167], a[220:223], a[40:43], v[164:167]\n\t" \
  "ds_read_b128 a[204:207], v32 offset:3072\n\t" \
  "v_accvgpr_write_b32 a74, 0\n\t" \
  "v_accvgpr_write_b32 a75, 0\n\t" \
  "v_mfma_f32_16x16x32_bf16 v[172:175], a[220:223], a[44:47], v[172:175]\n\t" \
  "buffer_load_dwordx4 v34, %11, s27 offen offset:2048 sc1 lds\n\t" \
  "s_waitcnt lgkmcnt(4)\n\t" \
  "v_mfma_f32_16x16x32_bf16 v[176:179], a[224:227], a[16:19], v[176:179]\n\t" \
  "ds_read_b128 a[208:211], v32 offset:4096\n\t" \
  "s_mov_b32 s29, s28\n\t" \
  "v_mfma_f32_16x16x32_bf16 v[184:187], a[224:227], a[20:23], v[184:187]\n\t" \
  "v_accvgpr_write_b32 a76, 0\n\t" \
  "v_accvgpr_write_b32 a77, 0\n\t" \
  "v_mfma_f32_16x16x32_bf16 v[180:183], a[228:231], a[16:19], v[180:183]\n\t" \
  "ds_read_b128 a[212:215], v32 offset:5120\n\t" \
  "s_mov_b32 s28, s30\n\t" \
  "v_mfma_f32_16x16x32_bf16 v[188:191], a[228:231], a[20:23], v[188:191]\n\t" \
  "v_accvgpr_write_b32 a78, 0\n\t" \
  "v_accvgpr_write_b32 a79, 0\n\t" \
  "v_mfma_f32_16x16x32_bf16 v[176:179], a[232:235], a[24:27], v[176:179]\n\t" \
  "ds_read_b128 a[216:219], v32 offset:6144\n\t" \
  "v_accvgpr_write_b32 a80, 0\n\t" \
  "v_mfma_f32_16x16x32_bf16 v[184:187], a[232:235], a[28:31], v[184:187]\n\t" \
  "v_cvt_pk_bf16_f32 v144, v160, v161\n\t" \
  "v_cvt_pk_bf16_f32 v145, v162, v163\n\t" \
  "v_mfma_f32_16x16x32_bf16 v[180:183], a[236:239], a[24:27], v[180:183]\n\t" \
  "ds_read_b128 a[220:223], v32 offset:7168\n\t" \
  "buffer_load_dwordx4 v34, %11, s27 offen offset:3072 sc1 lds\n\t" \
  "v_mfma_f32_16x16x32_bf16 v[188:191], a[236:239], a[28:31], v[188:191]\n\t" \
  "v_cvt_pk_bf16_f32 v146, v164, v165\n\t" \
  "v_cvt_pk_bf16_f32 v147, v166, v167\n\t" \
  "v_accvgpr_write_b32 a81, 0\n\t" \
  "v_mfma_f32_16x16x32_bf16 v[176:179], a[240:243], a[32:35], v[176:179]\n\t" \
  "ds_read_b128 a[224:227], v32 offset:8192\n\t" \
  "buffer_store_dwordx4 v[144:147], %1, %11, s73 offen offset:0 sc1\n\t" \
  "v_mfma_f32_16x16x32_bf16 v[184:187], a[240:243], a[36:39], v[184:187]\n\t" \
  "v_cvt_pk_bf16_f32 v148, v168, v169\n\t" \
  "v_cvt_pk_bf16_f32 v149, v170, v171\n\t" \
  "v_mfma_f32_16x16x32_bf16 v[180:183], a[244:247], a[32:35], v[180:183]\n\t" \
  "ds_read_b128 a[228:231], v32 offset:9216\n\t" \
  "v_cvt_pk_bf16_f32 v150, v172, v173\n\t" \
  "v_mfma_f32_16x16x32_bf16 v[188:191], a[244:247], a[36:39], v[188:191]\n\t" \
  "v_cvt_pk_bf16_f32 v151, v174, v175\n\t" \
  "buffer_store_dwordx4 v[148:151], %1, %11, s73 offen offset:1024 sc1\n\t" \
  "v_mfma_f32_16x16x32_bf16 v[176:179], a[248:251], a[40:43], v[176:179]\n\t" \
  "ds_read_b128 a[232:235], v32 offset:10240\n\t" \
  "v_accvgpr_write_b32 a82, 0\n\t" \
  "v_mfma_f32_16x16x32_bf16 v[184:187], a[248:251], a[44:47], v[184:187]\n\t" \
  "v_accvgpr_write_b32 a83, 0\n\t" \
  "v_accvgpr_write_b32 a84, 0\n\t" \
  "v_mfma_f32_16x16x32_bf16 v[180:183], a[252:255], a[40:43], v[180:183]\n\t" \
  "ds_read_b128 a[236:239], v32 offset:11264\n\t" \
  "v_accvgpr_write_b32 a85, 0\n\t" \
  "v_mfma_f32_16x16x32_bf16 v[188:191], a[252:255], a[44:47], v[188:191]\n\t" \
  "v_accvgpr_write_b32 a86, 0\n\t" \
  "v_accvgpr_write_b32 a87, 0\n\t" \
  "s_waitcnt vmcnt(14)\n\t" \
  "s_barrier\n\t" \
  "s_waitcnt lgkmcnt(4)\n\t" \
  "v_mfma_f32_16x16x32_bf16 v[192:195], a[192:195], a[16:19], v[192:195]\n\t" \
  "s_add_u32 s30, s28, 0x8000\n\t" \
  "ds_read_b128 a[240:243], v32 offset:12288\n\t" \
  "buffer_store_dword v38, %12, %11, s81 offen offset:0 sc1\n\t" \
  "v_mfma_f32_16x16x32_bf16 v[200:203], a[192:195], a[20:23], v[200:203]\n\t" \
  "s_cmp_lg_u32 s28, 0x20000\n\t" \
  "s_cselect_b32 s30, s30, 0\n\t" \
  "v_readlane_b32 s26, v27, 10\n\t" \
  "v_readlane_b32 s27, v28, 10\n\t" \
  "v_mfma_f32_16x16x32_bf16 v[196:199], a[196:199], a[16:19], v[196:199]\n\t" \
  "ds_read_b128 a[244:247], v32 offset:13312\n\t" \
  "v_accvgpr_write_b32 a88, 0\n\t" \
  "v_mfma_f32_16x16x32_bf16 v[204:207], a[196:199], a[20:23], v[204:207]\n\t" \
  "v_accvgpr_write_b32 a89, 0\n\t" \
  "v_accvgpr_write_b32 a90, 0\n\t" \
  "v_mfma_f32_16x16x32_bf16 v[192:195], a[200:203], a[24:27], v[192:195]\n\t" \
  "ds_read_b128 a[248:251], v32 offset:14336\n\t" \
  "s_add_u32 m0, s31, 98304\n\t" \
  "v_mfma_f32_16x16x32_bf16 v[200:203], a[200:203], a[28:31], v[200:203]\n\t" \
  "buffer_load_dwordx4 v34, %11, s26 offen offset:0 sc1 lds\n\t" \
  "v_cvt_pk_bf16_f32 v152, v176, v177\n\t" \
  "v_mfma_f32_16x16x32_bf16 v[196:199], a[204:207], a[24:27], v[196:199]\n\t" \
  "v_add_u32_e32 v33, s30, %1\n\t" \
  "ds_read_b128 a[252:255], v32 offset:15360\n\t" \
  "v_mfma_f32_16x16x32_bf16 v[204:207], a[204:207], a[28:31], v[204:207]\n\t" \
  "v_cvt_pk_bf16_f32 v153, v178, v179\n\t" \
  "v_cvt_pk_bf16_f32 v154, v180, v181\n\t" \
  "v_mfma_f32_16x16x32_bf16 v[192:195], a[208:211], a[32:35], v[192:195]\n\t" \
  "ds_read_b128 a[192:195], v32 offset:16384\n\t" \
  "v_cvt_pk_bf16_f32 v155, v182, v183\n\t" \
  "v_mfma_f32_16x16x32_bf16 v[200:203], a[208:211], a[36:39], v[200:203]\n\t" \
  "buffer_store_dwordx4 v[152:155], %1, %11, s73 offen offset:2048 sc1\n\t" \
  "v_cvt_pk_bf16_f32 v156, v184, v185\n\t" \
  "v_mfma_f32_16x16x32_bf16 v[196:199], a[212:215], a[32:35], v[196:199]\n\t" \
  "ds_read_b128 a[196:199], v32 offset:17408\n\t" \
  "v_cvt_pk_bf16_f32 v157, v186, v187\n\t" \
  "v_mfma_f32_16x16x32_bf16 v[204:207], a[212:215], a[36:39], v[204:207]\n\t" \
  "v_cvt_pk_bf16_f32 v158, v188, v189\n\t" \
  "v_cvt_pk_bf16_f32 v159, v190, v191\n\t" \
  "v_mfma_f32_16x16x32_bf16 v[192:195], a[216:219], a[40:43], v[192:195]\n\t" \
  "ds_read_b128 a[200:203], v32 offset:18432\n\t" \
  "buffer_load_dwordx4 v34, %11, s26 offen offset:1024 sc1 lds\n\t" \
  "v_mfma_f32_16x16x32_bf16 v[200:203], a[216:219], a[44:47], v[200:203]\n\t" \
  "buffer_store_dwordx4 v[156:159], %1, %11, s73 offen offset:3072 sc1\n\t" \
  "v_accvgpr_write_b32 a91, 0\n\t" \
  "v_mfma_f32_16x16x32_bf16 v[196:199], a[220:223], a[40:43], v[196:199]\n\t" \
  "ds_read_b128 a[204:207], v32 offset:19456\n\t" \
  "v_accvgpr_write_b32 a92, 0\n\t" \
  "v_mfma_f32_16x16x32_bf16 v[204:207], a[220:223], a[44:47], v[204:207]\n\t" \
  "v_accvgpr_write_b32 a93, 0\n\t" \
  "s_waitcnt lgkmcnt(4)\n\t" \
  "v_mfma_f32_16x16x32_bf16 v[208:211], a[224:227], a[16:19], v[208:211]\n\t" \
  "ds_read_b128 a[208:211], v32 offset:20480\n\t" \
  "v_accvgpr_write_b32 a94, 0\n\t" \
  "v_mfma_f32_16x16x32_bf16 v[216:219], a[224:227], a[20:23], v[216:219]\n\t" \
  "v_accvgpr_write_b32 a95, 0\n\t" \
  "v_accvgpr_write_b32 a96, 0\n\t" \
  "v_mfma_f32_16x16x32_bf16 v[212:215], a[228:231], a[16:19], v[212:215]\n\t" \
  "ds_read_b128 a[212:215], v32 offset:21504\n\t" \
  "v_accvgpr_write_b32 a97, 0\n\t" \
  "v_mfma_f32_16x16x32_bf16 v[220:223], a[228:231], a[20:23], v[220:223]\n\t" \
  "buffer_load_dwordx4 v34, %11, s26 offen offset:2048 sc1 lds\n\t" \
  "v_accvgpr_write_b32 a98, 0\n\t" \
  "v_mfma_f32_16x16x32_bf16 v[208:211], a[232:235], a[24:27], v[208:211]\n\t" \
  "ds_read_b128 a[216:219], v32 offset:22528\n\t" \
  "v_accvgpr_write_b32 a99, 0\n\t" \
  "v_mfma_f32_16x16x32_bf16 v[216:219], a[232:235], a[28:31], v[216:219]\n\t" \
  "v_cvt_pk_bf16_f32 v160, v192, v193\n\t" \
  "v_cvt_pk_bf16_f32 v161, v194, v195\n\t" \
  "v_mfma_f32_16x16x32_bf16 v[212:215], a[236:239], a[24:27], v[212:215]\n\t" \
  "ds_read_b128 a[220:223], v32 offset:23552\n\t" \
  "v_cvt_pk_bf16_f32 v162, v196, v197\n\t" \
  "v_mfma_f32_16x16x32_bf16 v[220:223], a[236:239], a[28:31], v[220:223]\n\t" \
  "v_cvt_pk_bf16_f32 v163, v198, v199\n\t" \
  "buffer_store_dwordx4 v[160:163], %1, %11, s74 offen offset:0 sc1\n\t" \
  "v_mfma_f32_16x16x32_bf16 v[208:211], a[240:243], a[32:35], v[208:211]\n\t" \
  "ds_read_b128 a[224:227], v32 offset:24576\n\t" \
  "v_cvt_pk_bf16_f32 v164, v200, v201\n\t" \
  "v_mfma_f32_16x16x32_bf16 v[216:219], a[240:243], a[36:39], v[216:219]\n\t" \
  "v_cvt_pk_bf16_f32 v165, v202, v203\n\t" \
  "v_cvt_pk_bf16_f32 v166, v204, v205\n\t" \
  "v_mfma_f32_16x16x32_bf16 v[212:215], a[244:247], a[32:35], v[212:215]\n\t" \
  "ds_read_b128 a[228:231], v32 offset:25600\n\t" \
  "buffer_load_dwordx4 v34, %11, s26 offen offset:3072 sc1 lds\n\t" \
  "v_mfma_f32_16x16x32_bf16 v[220:223], a[244:247], a[36:39], v[220:223]\n\t" \
  "v_cvt_pk_bf16_f32 v167, v206, v207\n\t" \
  "buffer_store_dwordx4 v[164:167], %1, %11, s74 offen offset:1024 sc1\n\t" \
  "v_mfma_f32_16x16x32_bf16 v[208:211], a[248:251], a[40:43], v[208:211]\n\t" \
  "ds_read_b128 a[232:235], v32 offset:26624\n\t" \
  "v_accvgpr_write_b32 a100, 0\n\t" \
  "v_mfma_f32_16x16x32_bf16 v[216:219], a[248:251], a[44:47], v[216:219]\n\t" \
  "v_accvgpr_write_b32 a101, 0\n\t" \
  "v_accvgpr_write_b32 a102, 0\n\t" \
  "v_mfma_f32_16x16x32_bf16 v[212:215], a[252:255], a[40:43], v[212:215]\n\t" \
  "ds_read_b128 a[236:239], v32 offset:27648\n\t" \
  "v_accvgpr_write_b32 a103, 0\n\t" \
  "buffer_load_dwordx2 v[104:105], v39, s[64:67], s47 offen offset:0\n\t" \
  "v_mfma_f32_16x16x32_bf16 v[220:223], a[252:255], a[44:47], v[220:223]\n\t" \
  "v_accvgpr_write_b32 a104, 0\n\t" \
  "s_waitcnt lgkmcnt(4)\n\t" \
  "v_mfma_f32_16x16x32_bf16 v[224:227], a[192:195], a[16:19], v[224:227]\n\t" \
  "ds_read_b128 a[240:243], v32 offset:28672\n\t" \
  "v_accvgpr_write_b32 a105, 0\n\t" \
  "v_mfma_f32_16x16x32_bf16 v[232:235], a[192:195], a[20:23], v[232:235]\n\t" \
  "v_accvgpr_write_b32 a106, 0\n\t" \
  "v_accvgpr_write_b32 a107, 0\n\t" \
  "v_mfma_f32_16x16x32_bf16 v[228:231], a[196:199], a[16:19], v[228:231]\n\t" \
  "ds_read_b128 a[244:247], v32 offset:29696\n\t" \
  "buffer_load_dwordx2 v[106:107], v39, s[64:67], s47 offen offset:32\n\t" \
  "v_mfma_f32_16x16x32_bf16 v[236:239], a[196:199], a[20:23], v[236:239]\n\t" \
  "v_accvgpr_write_b32 a108, 0\n\t" \
  "v_accvgpr_write_b32 a109, 0\n\t" \
  "v_mfma_f32_16x16x32_bf16 v[224:227], a[200:203], a[24:27], v[224:227]\n\t" \
  "ds_read_b128 a[248:251], v32 offset:30720\n\t" \
  "v_accvgpr_write_b32 a110, 0\n\t" \
  "v_mfma_f32_16x16x32_bf16 v[232:235], a[200:203], a[28:31], v[232:235]\n\t" \
  "v_cvt_pk_bf16_f32 v168, v208, v209\n\t" \
  "v_cvt_pk_bf16_f32 v169, v210, v211\n\t" \
  "v_mfma_f32_16x16x32_bf16 v[228:231], a[204:207], a[24:27], v[228:231]\n\t" \
  "ds_read_b128 a[252:255], v32 offset:31744\n\t" \
  "v_cvt_pk_bf16_f32 v170, v212, v213\n\t" \
  "buffer_load_dwordx2 v[108:109], v39, s[64:67], s47 offen offset:64\n\t" \
  "v_mfma_f32_16x16x32_bf16 v[236:239], a[204:207], a[28:31], v[236:239]\n\t" \
  "v_cvt_pk_bf16_f32 v171, v214, v215\n\t" \
  "buffer_store_dwordx4 v[168:171], %1, %11, s74 offen offset:2048 sc1\n\t" \
  "v_mfma_f32_16x16x32_bf16 v[224:227], a[208:211], a[32:35], v[224:227]\n\t" \
  "ds_read_b128 a[192:195], v33 offset:0\n\t" \
  "v_cvt_pk_bf16_f32 v172, v216, v217\n\t" \
  "v_mfma_f32_16x16x32_bf16 v[232:235], a[208:211], a[36:39], v[232:235]\n\t" \
  "v_cvt_pk_bf16_f32 v173, v218, v219\n\t" \
  "v_cvt_pk_bf16_f32 v174, v220, v221\n\t" \
  "v_mfma_f32_16x16x32_bf16 v[228:231], a[212:215], a[32:35], v[228:231]\n\t" \
  "ds_read_b128 a[196:199], v33 offset:1024\n\t" \
  "v_cvt_pk_bf16_f32 v175, v222, v223\n\t" \
  "buffer_load_dwordx2 v[110:111], v39, s[64:67], s47 offen offset:96\n\t" \
  "v_mfma_f32_16x16x32_bf16 v[236:239], a[212:215], a[36:39], v[236:239]\n\t" \
  "buffer_store_dwordx4 v[172:175], %1, %11, s74 offen offset:3072 sc1\n\t" \
  "v_accvgpr_write_b32 a111, 0\n\t" \
  "v_mfma_f32_16x16x32_bf16 v[224:227], a[216:219], a[40:43], v[224:227]\n\t" \
  "ds_read_b128 a[200:203], v33 offset:2048\n\t" \
  "v_accvgpr_write_b32 a112, 0\n\t" \
  "v_mfma_f32_16x16x32_bf16 v[232:235], a[216:219], a[44:47], v[232:235]\n\t" \
  "v_accvgpr_write_b32 a113, 0\n\t" \
  "v_accvgpr_write_b32 a114, 0\n\t" \
  "v_mfma_f32_16x16x32_bf16 v[228:231], a[220:223], a[40:43], v[228:231]\n\t" \
  "ds_read_b128 a[204:207], v33 offset:3072\n\t" \
  "v_accvgpr_write_b32 a115, 0\n\t" \
  "buffer_load_dwordx2 v[112:113], v39, s[64:67], s47 offen offset:128\n\t" \
  "v_mfma_f32_16x16x32_bf16 v[236:239], a[220:223], a[44:47], v[236:239]\n\t" \
  "v_accvgpr_write_b32 a116, 0\n\t" \
  "s_waitcnt lgkmcnt(4)\n\t" \
  "v_mfma_f32_16x16x32_bf16 v[240:243], a[224:227], a[16:19], v[240:243]\n\t" \
  "ds_read_b128 a[208:211], v33 offset:4096\n\t" \
  "s_mov_b32 s29, s28\n\t" \
  "v_mfma_f32_16x16x32_bf16 v[248:251], a[224:227], a[20:23], v[248:251]\n\t" \
  "v_accvgpr_write_b32 a117, 0\n\t" \
  "v_accvgpr_write_b32 a118, 0\n\t" \
  "v_mfma_f32_16x16x32_bf16 v[244:247], a[228:231], a[16:19], v[244:247]\n\t" \
  "ds_read_b128 a[212:215], v33 offset:5120\n\t" \
  "s_mov_b32 s28, s30\n\t" \
  "buffer_load_dwordx2 v[114:115], v39, s[64:67], s47 offen offset:160\n\t" \
  "v_mfma_f32_16x16x32_bf16 v[252:255], a[228:231], a[20:23], v[252:255]\n\t" \
  "v_accvgpr_write_b32 a119, 0\n\t" \
  "v_accvgpr_write_b32 a120, 0\n\t" \
  "v_mfma_f32_16x16x32_bf16 v[240:243], a[232:235], a[24:27], v[240:243]\n\t" \
  "ds_read_b128 a[216:219], v33 offset:6144\n\t" \
  "v_accvgpr_write_b32 a121, 0\n\t" \
  "v_mfma_f32_16x16x32_bf16 v[248:251], a[232:235], a[28:31], v[248:251]\n\t" \
  "v_cvt_pk_bf16_f32 v176, v224, v225\n\t" \
  "v_cvt_pk_bf16_f32 v177, v226, v227\n\t" \
  "v_mfma_f32_16x16x32_bf16 v[244:247], a[236:239], a[24:27], v[244:247]\n\t" \
  "ds_read_b128 a[220:223], v33 offset:7168\n\t" \
  "v_cvt_pk_bf16_f32 v178, v228, v229\n\t" \
  "buffer_load_dwordx2 v[116:117], v39, s[64:67], s47 offen offset:192\n\t" \
  "v_mfma_f32_16x16x32_bf16 v[252:255], a[236:239], a[28:31], v[252:255]\n\t" \
  "v_cvt_pk_bf16_f32 v179, v230, v231\n\t" \
  "buffer_store_dwordx4 v[176:179], %1, %11, s75 offen offset:0 sc1\n\t" \
  "v_mfma_f32_16x16x32_bf16 v[240:243], a[240:243], a[32:35], v[240:243]\n\t" \
  "ds_read_b128 a[224:227], v33 offset:8192\n\t" \
  "v_cvt_pk_bf16_f32 v180, v232, v233\n\t" \
  "v_mfma_f32_16x16x32_bf16 v[248:251], a[240:243], a[36:39], v[248:251]\n\t" \
  "v_cvt_pk_bf16_f32 v181, v234, v235\n\t" \
  "v_cvt_pk_bf16_f32 v182, v236, v237\n\t" \
  "v_mfma_f32_16x16x32_bf16 v[244:247], a[244:247], a[32:35], v[244:247]\n\t" \
  "ds_read_b128 a[228:231], v33 offset:9216\n\t" \
  "v_cvt_pk_bf16_f32 v183, v238, v239\n\t" \
  "buffer_load_dwordx2 v[118:119], v39, s[64:67], s47 offen offset:224\n\t" \
  "v_mfma_f32_16x16x32_bf16 v[252:255], a[244:247], a[36:39], v[252:255]\n\t" \
  "buffer_store_dwordx4 v[180:183], %1, %11, s75 offen offset:1024 sc1\n\t" \
  "v_accvgpr_write_b32 a122, 0\n\t" \
  "v_mfma_f32_16x16x32_bf16 v[240:243], a[248:251], a[40:43], v[240:243]\n\t" \
  "ds_read_b128 a[232:235], v33 offset:10240\n\t" \
  "v_accvgpr_write_b32 a123, 0\n\t" \
  "v_mfma_f32_16x16x32_bf16 v[248:251], a[248:251], a[44:47], v[248:251]\n\t" \
  "v_accvgpr_write_b32 a124, 0\n\t" \
  "v_accvgpr_write_b32 a125, 0\n\t" \
  "v_mfma_f32_16x16x32_bf16 v[244:247], a[252:255], a[40:43], v[244:247]\n\t" \
  "ds_read_b128 a[236:239], v33 offset:11264\n\t" \
  "v_accvgpr_write_b32 a126, 0\n\t" \
  "v_mfma_f32_16x16x32_bf16 v[252:255], a[252:255], a[44:47], v[252:255]\n\t" \
  "v_accvgpr_write_b32 a127, 0\n\t" \
  "s_barrier\n\t" \
  "s_waitcnt lgkmcnt(4)\n\t" \
  "v_mfma_f32_16x16x32_bf16 v[192:195], a[192:195], v[40:43], 0\n\t" \
  "s_add_u32 s30, s28, 0x8000\n\t" \
  "ds_read_b128 a[240:243], v33 offset:12288\n\t" \
  "v_mfma_f32_16x16x32_bf16 v[200:203], a[192:195], v[44:47], 0\n\t" \
  "s_cmp_lg_u32 s28, 0x20000\n\t" \
  "s_cselect_b32 s30, s30, 0\n\t" \
  "v_mfma_f32_16x16x32_bf16 v[196:199], a[196:199], v[40:43], 0\n\t" \
  "ds_read_b128 a[244:247], v33 offset:13312\n\t" \
  "s_mov_b32 s69, 0x41317218\n\t" \
  "v_mfma_f32_16x16x32_bf16 v[204:207], a[196:199], v[44:47], 0\n\t" \
  "s_mul_i32 s70, s91, 188416\n\t" \
  "v_mfma_f32_16x16x32_bf16 v[192:195], a[200:203], v[48:51], v[192:195]\n\t" \
  "ds_read_b128 a[248:251], v33 offset:14336\n\t" \
  "s_add_u32 s70, s70, s40\n\t" \
  "v_mfma_f32_16x16x32_bf16 v[200:203], a[200:203], v[52:55], v[200:203]\n\t" \
  "v_cvt_pk_bf16_f32 v184, v240, v241\n\t" \
  "v_cvt_pk_bf16_f32 v185, v242, v243\n\t" \
  "v_mfma_f32_16x16x32_bf16 v[196:199], a[204:207], v[48:51], v[196:199]\n\t" \
  "v_add_u32_e32 v32, s30, %1\n\t" \
  "ds_read_b128 a[252:255], v33 offset:15360\n\t" \
  "v_mfma_f32_16x16x32_bf16 v[204:207], a[204:207], v[52:55], v[204:207]\n\t" \
  "v_cvt_pk_bf16_f32 v186, v244, v245\n\t" \
  "v_cvt_pk_bf16_f32 v187, v246, v247\n\t" \
  "v_mfma_f32_16x16x32_bf16 v[192:195], a[208:211], v[56:59], v[192:195]\n\t" \
  "ds_read_b128 a[192:195], v33 offset:16384\n\t" \
  "buffer_store_dwordx4 v[184:187], %1, %11, s75 offen offset:2048 sc1\n\t" \
  "v_mfma_f32_16x16x32_bf16 v[200:203], a[208:211], v[60:63], v[200:203]\n\t" \
  "v_cvt_pk_bf16_f32 v188, v248, v249\n\t" \
  "v_cvt_pk_bf16_f32 v189, v250, v251\n\t" \
  "v_mfma_f32_16x16x32_bf16 v[196:199], a[212:215], v[56:59], v[196:199]\n\t" \
  "ds_read_b128 a[196:199], v33 offset:17408\n\t" \
  "v_cvt_pk_bf16_f32 v190, v252, v253\n\t" \
  "v_mfma_f32_16x16x32_bf16 v[204:207], a[212:215], v[60:63], v[204:207]\n\t" \
  "v_cvt_pk_bf16_f32 v191, v254, v255\n\t" \
  "buffer_store_dwordx4 v[188:191], %1, %11, s75 offen offset:3072 sc1\n\t" \
  "v_mfma_f32_16x16x32_bf16 v[192:195], a[216:219], v[64:67], v[192:195]\n\t" \
  "ds_read_b128 a[200:203], v33 offset:18432\n\t" \
  "v_mfma_f32_16x16x32_bf16 v[200:203], a[216:219], v[68:71], v[200:203]\n\t" \
  "v_mfma_f32_16x16x32_bf16 v[196:199], a[220:223], v[64:67], v[196:199]\n\t" \
  "ds_read_b128 a[204:207], v33 offset:19456\n\t" \
  "v_mfma_f32_16x16x32_bf16 v[204:207], a[220:223], v[68:71], v[204:207]\n\t" \
  "s_waitcnt lgkmcnt(4)\n\t" \
  "v_mfma_f32_16x16x32_bf16 v[192:195], a[224:227], v[72:75], v[192:195]\n\t" \
  "ds_read_b128 a[208:211], v33 offset:20480\n\t" \
  "v_accvgpr_write_b32 a128, v128\n\t" \
  "v_mfma_f32_16x16x32_bf16 v[200:203], a[224:227], v[76:79], v[200:203]\n\t" \
  "v_accvgpr_write_b32 a129, v129\n\t" \
  "v_accvgpr_write_b32 a130, v130\n\t" \
  "v_mfma_f32_16x16x32_bf16 v[196:199], a[228:231], v[72:75], v[196:199]\n\t" \
  "ds_read_b128 a[212:215], v33 offset:21504\n\t" \
  "v_accvgpr_write_b32 a131, v131\n\t" \
  "v_mfma_f32_16x16x32_bf16 v[204:207], a[228:231], v[76:79], v[204:207]\n\t" \
  "v_accvgpr_write_b32 a132, v132\n\t" \
  "v_accvgpr_write_b32 a133, v133\n\t" \
  "v_mfma_f32_16x16x32_bf16 v[192:195], a[232:235], v[80:83], v[192:195]\n\t" \
  "ds_read_b128 a[216:219], v33 offset:22528\n\t" \
  "v_accvgpr_write_b32 a134, v134\n\t" \
  "v_mfma_f32_16x16x32_bf16 v[200:203], a[232:235], v[84:87], v[200:203]\n\t" \
  "v_accvgpr_write_b32 a135, v135\n\t" \
  "v_accvgpr_write_b32 a136, v136\n\t" \
  "v_mfma_f32_16x16x32_bf16 v[196:199], a[236:239], v[80:83], v[196:199]\n\t" \
  "ds_read_b128 a[220:223], v33 offset:23552\n\t" \
  "v_accvgpr_write_b32 a137, v137\n\t" \
  "v_mfma_f32_16x16x32_bf16 v[204:207], a[236:239], v[84:87], v[204:207]\n\t" \
  "v_accvgpr_write_b32 a138, v138\n\t" \
  "v_accvgpr_write_b32 a139, v139\n\t" \
  "v_mfma_f32_16x16x32_bf16 v[192:195], a[240:243], v[88:91], v[192:195]\n\t" \
  "ds_read_b128 a[224:227], v33 offset:24576\n\t" \
  "buffer_load_dword v126, v125, %11, s80 offen sc1\n\t" \
  "v_mfma_f32_16x16x32_bf16 v[200:203], a[240:243], v[92:95], v[200:203]\n\t" \
  "v_accvgpr_write_b32 a140, v140\n\t" \
  "v_accvgpr_write_b32 a141, v141\n\t" \
  "v_mfma_f32_16x16x32_bf16 v[196:199], a[244:247], v[88:91], v[196:199]\n\t" \
  "ds_read_b128 a[228:231], v33 offset:25600\n\t" \
  "v_accvgpr_write_b32 a142, v142\n\t" \
  "v_mfma_f32_16x16x32_bf16 v[204:207], a[244:247], v[92:95], v[204:207]\n\t" \
  "v_accvgpr_write_b32 a143, v143\n\t" \
  "v_accvgpr_write_b32 a144, v144\n\t" \
  "v_mfma_f32_16x16x32_bf16 v[192:195], a[248:251], v[96:99], v[192:195]\n\t" \
  "ds_read_b128 a[232:235], v33 offset:26624\n\t" \
  "v_accvgpr_write_b32 a145, v145\n\t" \
  "v_mfma_f32_16x16x32_bf16 v[200:203], a[248:251], v[100:103], v[200:203]\n\t" \
  "v_accvgpr_write_b32 a146, v146\n\t" \
  "v_accvgpr_write_b32 a147, v147\n\t" \
  "v_mfma_f32_16x16x32_bf16 v[196:199], a[252:255], v[96:99], v[196:199]\n\t" \
  "ds_read_b128 a[236:239], v33 offset:27648\n\t" \
  "v_accvgpr_write_b32 a148, v148\n\t" \
  "v_mfma_f32_16x16x32_bf16 v[204:207], a[252:255], v[100:103], v[204:207]\n\t" \
  "v_accvgpr_write_b32 a149, v149\n\t" \
  "s_waitcnt lgkmcnt(4)\n\t" \
  "v_mfma_f32_16x16x32_bf16 v[192:195], a[192:195], a[0:3], v[192:195]\n\t" \
  "ds_read_b128 a[240:243], v33 offset:28672\n\t" \
  "v_mfma_f32_16x16x32_bf16 v[200:203], a[192:195], a[4:7], v[200:203]\n\t" \
  "v_accvgpr_write_b32 a150, v150\n\t" \
  "v_accvgpr_write_b32 a151, v151\n\t" \
  "v_mfma_f32_16x16x32_bf16 v[196:199], a[196:199], a[0:3], v[196:199]\n\t" \
  "ds_read_b128 a[244:247], v33 offset:29696\n\t" \
  "v_accvgpr_write_b32 a152, v152\n\t" \
  "v_mfma_f32_16x16x32_bf16 v[204:207], a[196:199], a[4:7], v[204:207]\n\t" \
  "v_accvgpr_write_b32 a153, v153\n\t" \
  "v_accvgpr_write_b32 a154, v154\n\t" \
  "v_mfma_f32_16x16x32_bf16 v[192:195], a[200:203], a[8:11], v[192:195]\n\t" \
  "ds_read_b128 a[248:251], v33 offset:30720\n\t" \
  "v_accvgpr_write_b32 a155, v155\n\t" \
  "v_mfma_f32_16x16x32_bf16 v[200:203], a[200:203], a[12:15], v[200:203]\n\t" \
  "v_accvgpr_write_b32 a156, v156\n\t" \
  "v_accvgpr_write_b32 a157, v157\n\t" \
  "v_mfma_f32_16x16x32_bf16 v[196:199], a[204:207], a[8:11], v[196:199]\n\t" \
  "ds_read_b128 a[252:255], v33 offset:31744\n\t" \
  "v_accvgpr_write_b32 a158, v158\n\t" \
  "v_mfma_f32_16x16x32_bf16 v[204:207], a[204:207], a[12:15], v[204:207]\n\t" \
  "v_accvgpr_write_b32 a159, v159\n\t" \
  "v_accvgpr_write_b32 a160, v160\n\t" \
  "v_mfma_f32_16x16x32_bf16 v[192:195], a[208:211], a[16:19], v[192:195]\n\t" \
  "ds_read_b128 a[192:195], v32 offset:0\n\t" \
  "v_accvgpr_write_b32 a161, v161\n\t" \
  "v_mfma_f32_16x16x32_bf16 v[200:203], a[208:211], a[20:23], v[200:203]\n\t" \
  "v_accvgpr_write_b32 a162, v162\n\t" \
  "v_accvgpr_write_b32 a163, v163\n\t" \
  "v_mfma_f32_16x16x32_bf16 v[196:199], a[212:215], a[16:19], v[196:199]\n\t" \
  "ds_read_b128 a[196:199], v32 offset:1024\n\t" \
  "v_accvgpr_write_b32 a164, v164\n\t" \
  "v_mfma_f32_16x16x32_bf16 v[204:207], a[212:215], a[20:23], v[204:207]\n\t" \
  "v_accvgpr_write_b32 a165, v165\n\t" \
  "v_accvgpr_write_b32 a166, v166\n\t" \
  "v_mfma_f32_16x16x32_bf16 v[192:195], a[216:219], a[24:27], v[192:195]\n\t" \
  "ds_read_b128 a[200:203], v32 offset:2048\n\t" \
  "v_accvgpr_write_b32 a167, v167\n\t" \
  "v_mfma_f32_16x16x32_bf16 v[200:203], a[216:219], a[28:31], v[200:203]\n\t" \
  "v_accvgpr_write_b32 a168, v168\n\t" \
  "v_accvgpr_write_b32 a169, v169\n\t" \
  "v_mfma_f32_16x16x32_bf16 v[196:199], a[220:223], a[24:27], v[196:199]\n\t" \
  "ds_read_b128 a[204:207], v32 offset:3072\n\t" \
  "v_accvgpr_write_b32 a170, v170\n\t" \
  "v_mfma_f32_16x16x32_bf16 v[204:207], a[220:223], a[28:31], v[204:207]\n\t" \
  "s_waitcnt lgkmcnt(4)\n\t" \
  "v_mfma_f32_16x16x32_bf16 v[192:195], a[224:227], a[32:35], v[192:195]\n\t" \
  "ds_read_b128 a[208:211], v32 offset:4096\n\t" \
  "s_mov_b32 s29, s28\n\t" \
  "v_mfma_f32_16x16x32_bf16 v[200:203], a[224:227], a[36:39], v[200:203]\n\t" \
  "v_accvgpr_write_b32 a171, v171\n\t" \
  "v_accvgpr_write_b32 a172, v172\n\t" \
  "v_mfma_f32_16x16x32_bf16 v[196:199], a[228:231], a[32:35], v[196:199]\n\t" \
  "ds_read_b128 a[212:215], v32 offset:5120\n\t" \
  "s_mov_b32 s28, s30\n\t" \
  "v_mfma_f32_16x16x32_bf16 v[204:207], a[228:231], a[36:39], v[204:207]\n\t" \
  "v_accvgpr_write_b32 a173, v173\n\t" \
  "v_accvgpr_write_b32 a174, v174\n\t" \
  "v_mfma_f32_16x16x32_bf16 v[192:195], a[232:235], a[40:43], v[192:195]\n\t" \
  "ds_read_b128 a[216:219], v32 offset:6144\n\t" \
  "v_accvgpr_write_b32 a175, v175\n\t" \
  "v_mfma_f32_16x16x32_bf16 v[200:203], a[232:235], a[44:47], v[200:203]\n\t" \
  "v_accvgpr_write_b32 a176, v176\n\t" \
  "v_accvgpr_write_b32 a177, v177\n\t" \
  "v_mfma_f32_16x16x32_bf16 v[196:199], a[236:239], a[40:43], v[196:199]\n\t" \
  "ds_read_b128 a[220:223], v32 offset:7168\n\t" \
  "v_accvgpr_write_b32 a178, v178\n\t" \
  "v_mfma_f32_16x16x32_bf16 v[204:207], a[236:239], a[44:47], v[204:207]\n\t" \
  "v_accvgpr_write_b32 a179, v179\n\t" \
  "v_accvgpr_write_b32 a180, v180\n\t" \
  "v_mfma_f32_16x16x32_bf16 v[208:211], a[240:243], v[40:43], 0\n\t" \
  "ds_read_b128 a[224:227], v32 offset:8192\n\t" \
  "v_accvgpr_write_b32 a181, v181\n\t" \
  "v_mfma_f32_16x16x32_bf16 v[216:219], a[240:243], v[44:47], 0\n\t" \
  "v_accvgpr_write_b32 a182, v182\n\t" \
  "v_accvgpr_write_b32 a183, v183\n\t" \
  "v_mfma_f32_16x16x32_bf16 v[212:215], a[244:247], v[40:43], 0\n\t" \
  "ds_read_b128 a[228:231], v32 offset:9216\n\t" \
  "v_accvgpr_write_b32 a184, v184\n\t" \
  "v_mfma_f32_16x16x32_bf16 v[220:223], a[244:247], v[44:47], 0\n\t" \
  "v_accvgpr_write_b32 a185, v185\n\t" \
  "v_accvgpr_write_b32 a186, v186\n\t" \
  "v_mfma_f32_16x16x32_bf16 v[208:211], a[248:251], v[48:51], v[208:211]\n\t" \
  "ds_read_b128 a[232:235], v32 offset:10240\n\t" \
  "v_accvgpr_write_b32 a187, v187\n\t" \
  "v_mfma_f32_16x16x32_bf16 v[216:219], a[248:251], v[52:55], v[216:219]\n\t" \
  "v_accvgpr_write_b32 a188, v188\n\t" \
  "v_accvgpr_write_b32 a189, v189\n\t" \
  "v_mfma_f32_16x16x32_bf16 v[212:215], a[252:255], v[48:51], v[212:215]\n\t" \
  "ds_read_b128 a[236:239], v32 offset:11264\n\t" \
  "v_accvgpr_write_b32 a190, v190\n\t" \
  "v_mfma_f32_16x16x32_bf16 v[220:223], a[252:255], v[52:55], v[220:223]\n\t" \
  "v_accvgpr_write_b32 a191, v191\n\t" \
  "s_waitcnt vmcnt(0)\n\t" \
  "s_barrier\n\t" \
  "v_cmp_ne_u32_e32 vcc, s90, v126\n\t" \
  "s_cbranch_vccnz MK_F1P_%=\n\t" \
  "MK_F1R_%=:\n\t" \
  "s_waitcnt lgkmcnt(4)\n\t" \
  "v_mfma_f32_16x16x32_bf16 v[208:211], a[192:195], v[56:59], v[208:211]\n\t" \
  "s_add_u32 s30, s28, 0x8000\n\t" \
  "ds_read_b128 a[240:243], v32 offset:12288\n\t" \
  "buffer_store_dword v38, %12, %11, s34 offen offset:0 sc1\n\t" \
  "v_readlane_b32 s26, v27, 11\n\t" \
  "v_readlane_b32 s27, v28, 11\n\t" \
  "v_mfma_f32_16x16x32_bf16 v[216:219], a[192:195], v[60:63], v[216:219]\n\t" \
  "s_cmp_lg_u32 s28, 0x20000\n\t" \
  "s_cselect_b32 s30, s30, 0\n\t" \
  "v_lshlrev_b32_e32 v120, 16, v104\n\t" \
  "v_mfma_f32_16x16x32_bf16 v[212:215], a[196:199], v[56:59], v[212:215]\n\t" \
  "ds_read_b128 a[244:247], v32 offset:13312\n\t" \
  "v_and_b32_e32 v121, 0xffff0000, v104\n\t" \
  "v_mfma_f32_16x16x32_bf16 v[220:223], a[196:199], v[60:63], v[220:223]\n\t" \
  "s_add_u32 m0, s31, 131072\n\t" \
  "v_lshlrev_b32_e32 v122, 16, v105\n\t" \
  "v_mfma_f32_16x16x32_bf16 v[208:211], a[200:203], v[64:67], v[208:211]\n\t" \
  "ds_read_b128 a[248:251], v32 offset:14336\n\t" \
  "buffer_load_dwordx4 v34, %11, s26 offen offset:0 sc1 lds\n\t" \
  "v_and_b32_e32 v123, 0xffff0000, v105\n\t" \
  "v_mfma_f32_16x16x32_bf16 v[216:219], a[200:203], v[68:71], v[216:219]\n\t" \
  "v_fma_f32 v192, v192, s69, v120\n\t" \
  "v_fma_f32 v193, v193, s69, v121\n\t" \
  "v_mfma_f32_16x16x32_bf16 v[212:215], a[204:207], v[64:67], v[212:215]\n\t" \
  "v_add_u32_e32 v33, s30, %1\n\t" \
  "ds_read_b128 a[252:255], v32 offset:15360\n\t" \
  "v_mfma_f32_16x16x32_bf16 v[220:223], a[204:207], v[68:71], v[220:223]\n\t" \
  "v_fma_f32 v194, v194, s69, v122\n\t" \
  "v_fma_f32 v195, v195, s69, v123\n\t" \
  "v_max_f32_e32 v192, 0, v192\n\t" \
  "v_mfma_f32_16x16x32_bf16 v[208:211], a[208:211], v[72:75], v[208:211]\n\t" \
  "ds_read_b128 a[192:195], v32 offset:16384\n\t" \
  "buffer_load_dwordx4 v34, %11, s26 offen offset:1024 sc1 lds\n\t" \
  "v_mfma_f32_16x16x32_bf16 v[216:219], a[208:211], v[76:79], v[216:219]\n\t" \
  "v_max_f32_e32 v193, 0, v193\n\t" \
  "v_max_f32_e32 v194, 0, v194\n\t" \
  "v_mfma_f32_16x16x32_bf16 v[212:215], a[212:215], v[72:75], v[212:215]\n\t" \
  "ds_read_b128 a[196:199], v32 offset:17408\n\t" \
  "v_max_f32_e32 v195, 0, v195\n\t" \
  "v_mfma_f32_16x16x32_bf16 v[220:223], a[212:215], v[76:79], v[220:223]\n\t" \
  "v_mul_f32_e32 v192, v192, v192\n\t" \
  "v_mul_f32_e32 v193, v193, v193\n\t" \
  "v_mul_f32_e32 v194, v194, v194\n\t" \
  "v_mfma_f32_16x16x32_bf16 v[208:211], a[216:219], v[80:83], v[208:211]\n\t" \
  "ds_read_b128 a[200:203], v32 offset:18432\n\t" \
  "buffer_load_dwordx4 v34, %11, s26 offen offset:2048 sc1 lds\n\t" \
  "v_mfma_f32_16x16x32_bf16 v[216:219], a[216:219], v[84:87], v[216:219]\n\t" \
  "v_mul_f32_e32 v195, v195, v195\n\t" \
  "v_cvt_pk_bf16_f32 v192, v192, v193\n\t" \
  "v_mfma_f32_16x16x32_bf16 v[212:215], a[220:223], v[80:83], v[212:215]\n\t" \
  "ds_read_b128 a[204:207], v32 offset:19456\n\t" \
  "v_cvt_pk_bf16_f32 v193, v194, v195\n\t" \
  "v_lshlrev_b32_e32 v120, 16, v106\n\t" \
  "v_mfma_f32_16x16x32_bf16 v[220:223], a[220:223], v[84:87], v[220:223]\n\t" \
  "v_and_b32_e32 v121, 0xffff0000, v106\n\t" \
  "s_waitcnt lgkmcnt(4)\n\t" \
  "v_mfma_f32_16x16x32_bf16 v[208:211], a[224:227], v[88:91], v[208:211]\n\t" \
  "ds_read_b128 a[208:211], v32 offset:20480\n\t" \
  "buffer_load_dwordx4 v34, %11, s26 offen offset:3072 sc1 lds\n\t" \
  "v_mfma_f32_16x16x32_bf16 v[216:219], a[224:227], v[92:95], v[216:219]\n\t" \
  "v_lshlrev_b32_e32 v122, 16, v107\n\t" \
  "v_and_b32_e32 v123, 0xffff0000, v107\n\t" \
  "v_mfma_f32_16x16x32_bf16 v[212:215], a[228:231], v[88:91], v[212:215]\n\t" \
  "ds_read_b128 a[212:215], v32 offset:21504\n\t" \
  "v_fma_f32 v196, v196, s69, v120\n\t" \
  "v_fma_f32 v197, v197, s69, v121\n\t" \
  "v_mfma_f32_16x16x32_bf16 v[220:223], a[228:231], v[92:95], v[220:223]\n\t" \
  "v_readlane_b32 s26, v27, 12\n\t" \
  "v_readlane_b32 s27, v28, 12\n\t" \
  "v_mfma_f32_16x16x32_bf16 v[208:211], a[232:235], v[96:99], v[208:211]\n\t" \
  "ds_read_b128 a[216:219], v32 offset:22528\n\t" \
  "v_fma_f32 v198, v198, s69, v122\n\t" \
  "v_mfma_f32_16x16x32_bf16 v[216:219], a[232:235], v[100:103], v[216:219]\n\t" \
  "v_fma_f32 v199, v199, s69, v123\n\t" \
  "v_max_f32_e32 v196, 0, v196\n\t" \
  "v_max_f32_e32 v197, 0, v197\n\t" \
  "v_mfma_f32_16x16x32_bf16 v[212:215], a[236:239], v[96:99], v[212:215]\n\t" \
  "ds_read_b128 a[220:223], v32 offset:23552\n\t" \
  "s_add_u32 m0, s31, 0\n\t" \
  "v_mfma_f32_16x16x32_bf16 v[220:223], a[236:239], v[100:103], v[220:223]\n\t" \
  "buffer_load_dwordx4 v34, %11, s26 offen offset:0 sc1 lds\n\t" \
  "v_max_f32_e32 v198, 0, v198\n\t" \
  "v_mfma_f32_16x16x32_bf16 v[208:211], a[240:243], a[0:3], v[208:211]\n\t" \
  "ds_read_b128 a[224:227], v32 offset:24576\n\t" \
  "v_max_f32_e32 v199, 0, v199\n\t" \
  "v_mfma_f32_16x16x32_bf16 v[216:219], a[240:243], a[4:7], v[216:219]\n\t" \
  "v_mul_f32_e32 v196, v196, v196\n\t" \
  "v_mul_f32_e32 v197, v197, v197\n\t" \
  "v_mul_f32_e32 v198, v198, v198\n\t" \
  "v_mfma_f32_16x16x32_bf16 v[212:215], a[244:247], a[0:3], v[212:215]\n\t" \
  "ds_read_b128 a[228:231], v32 offset:25600\n\t" \
  "v_mul_f32_e32 v199, v199, v199\n\t" \
  "v_mfma_f32_16x16x32_bf16 v[220:223], a[244:247], a[4:7], v[220:223]\n\t" \
  "v_cvt_pk_bf16_f32 v194, v196, v197\n\t" \
  "v_cvt_pk_bf16_f32 v195, v198, v199\n\t" \
  "v_mfma_f32_16x16x32_bf16 v[208:211], a[248:251], a[8:11], v[208:211]\n\t" \
  "ds_read_b128 a[232:235], v32 offset:26624\n\t" \
  "buffer_load_dwordx4 v34, %11, s26 offen offset:1024 sc1 lds\n\t" \
  "v_lshlrev_b32_e32 v120, 16, v104\n\t" \
  "v_mfma_f32_16x16x32_bf16 v[216:219], a[248:251], a[12:15], v[216:219]\n\t" \
  "v_and_b32_e32 v121, 0xffff0000, v104\n\t" \
  "v_lshlrev_b32_e32 v122, 16, v105\n\t" \
  "v_mfma_f32_16x16x32_bf16 v[212:215], a[252:255], a[8:11], v[212:215]\n\t" \
  "ds_read_b128 a[236:239], v32 offset:27648\n\t" \
  "v_and_b32_e32 v123, 0xffff0000, v105\n\t" \
  "v_mfma_f32_16x16x32_bf16 v[220:223], a[252:255], a[12:15], v[220:223]\n\t" \
  "v_fma_f32 v200, v200, s69, v120\n\t" \
  "s_waitcnt lgkmcnt(4)\n\t" \
  "v_mfma_f32_16x16x32_bf16 v[208:211], a[192:195], a[16:19], v[208:211]\n\t" \
  "ds_read_b128 a[240:243], v32 offset:28672\n\t" \
  "v_fma_f32 v201, v201, s69, v121\n\t" \
  "v_fma_f32 v202, v202, s69, v122\n\t" \
  "v_mfma_f32_16x16x32_bf16 v[216:219], a[192:195], a[20:23], v[216:219]\n\t" \
  "buffer_load_dwordx4 v34, %11, s26 offen offset:2048 sc1 lds\n\t" \
  "v_fma_f32 v203, v203, s69, v123\n\t" \
  "v_mfma_f32_16x16x32_bf16 v[212:215], a[196:199], a[16:19], v[212:215]\n\t" \
  "ds_read_b128 a[244:247], v32 offset:29696\n\t" \
  "v_max_f32_e32 v200, 0, v200\n\t" \
  "v_mfma_f32_16x16x32_bf16 v[220:223], a[196:199], a[20:23], v[220:223]\n\t" \
  "v_max_f32_e32 v201, 0, v201\n\t" \
  "v_max_f32_e32 v202, 0, v202\n\t" \
  "v_max_f32_e32 v203, 0, v203\n\t" \
  "v_mfma_f32_16x16x32_bf16 v[208:211], a[200:203], a[24:27], v[208:211]\n\t" \
  "ds_read_b128 a[248:251], v32 offset:30720\n\t" \
  "v_mul_f32_e32 v200, v200, v200\n\t" \
  "v_mfma_f32_16x16x32_bf16 v[216:219], a[200:203], a[28:31], v[216:219]\n\t" \
  "v_mul_f32_e32 v201, v201, v201\n\t" \
  "v_mul_f32_e32 v202, v202, v202\n\t" \
  "v_mfma_f32_16x16x32_bf16 v[212:215], a[204:207], a[24:27], v[212:215]\n\t" \
  "ds_read_b128 a[252:255], v32 offset:31744\n\t" \
  "buffer_load_dwordx4 v34, %11, s26 offen offset:3072 sc1 lds\n\t" \
  "v_mfma_f32_16x16x32_bf16 v[220:223], a[204:207], a[28:31], v[220:223]\n\t" \
  "v_mul_f32_e32 v203, v203, v203\n\t" \
  "v_cvt_pk_bf16_f32 v196, v200, v201\n\t" \
  "v_cvt_pk_bf16_f32 v197, v202, v203\n\t" \
  "v_mfma_f32_16x16x32_bf16 v[208:211], a[208:211], a[32:35], v[208:211]\n\t" \
  "ds_read_b128 a[192:195], v33 offset:0\n\t" \
  "v_lshlrev_b32_e32 v120, 16, v106\n\t" \
  "v_mfma_f32_16x16x32_bf16 v[216:219], a[208:211], a[36:39], v[216:219]\n\t" \
  "v_and_b32_e32 v121, 0xffff0000, v106\n\t" \
  "v_lshlrev_b32_e32 v122, 16, v107\n\t" \
  "v_mfma_f32_16x16x32_bf16 v[212:215], a[212:215], a[32:35], v[212:215]\n\t" \
  "ds_read_b128 a[196:199], v33 offset:1024\n\t" \
  "s_add_u32 m0, m0, 0x4000\n\t" \
  "v_and_b32_e32 v123, 0xffff0000, v107\n\t" \
  "v_mfma_f32_16x16x32_bf16 v[220:223], a[212:215], a[36:39], v[220:223]\n\t" \
  "buffer_load_dwordx4 v34, %11, s27 offen offset:0 sc1 lds\n\t" \
  "v_fma_f32 v204, v204, s69, v120\n\t" \
  "v_mfma_f32_16x16x32_bf16 v[208:211], a[216:219], a[40:43], v[208:211]\n\t" \
  "ds_read_b128 a[200:203], v33 offset:2048\n\t" \
  "v_fma_f32 v205, v205, s69, v121\n\t" \
  "v_mfma_f32_16x16x32_bf16 v[216:219], a[216:219], a[44:47], v[216:219]\n\t" \
  "v_fma_f32 v206, v206, s69, v122\n\t" \
  "v_fma_f32 v207, v207, s69, v123\n\t" \
  "v_mfma_f32_16x16x32_bf16 v[212:215], a[220:223], a[40:43], v[212:215]\n\t" \
  "ds_read_b128 a[204:207], v33 offset:3072\n\t" \
  "v_max_f32_e32 v204, 0, v204\n\t" \
  "v_max_f32_e32 v205, 0, v205\n\t" \
  "v_mfma_f32_16x16x32_bf16 v[220:223], a[220:223], a[44:47], v[220:223]\n\t" \
  "v_max_f32_e32 v206, 0, v206\n\t" \
  "s_waitcnt lgkmcnt(4)\n\t" \
  "v_mfma_f32_16x16x32_bf16 v[224:227], a[224:227], v[40:43], 0\n\t" \
  "ds_read_b128 a[208:211], v33 offset:4096\n\t" \
  "s_mov_b32 s29, s28\n\t" \
  "buffer_load_dwordx4 v34, %11, s27 offen offset:1024 sc1 lds\n\t" \
  "v_mfma_f32_16x16x32_bf16 v[232:235], a[224:227], v[44:47], 0\n\t" \
  "v_max_f32_e32 v207, 0, v207\n\t" \
  "v_mul_f32_e32 v204, v204, v204\n\t" \
  "v_mfma_f32_16x16x32_bf16 v[228:231], a[228:231], v[40:43], 0\n\t" \
  "ds_read_b128 a[212:215], v33 offset:5120\n\t" \
  "s_mov_b32 s28, s30\n\t" \
  "v_mul_f32_e32 v205, v205, v205\n\t" \
  "v_mfma_f32_16x16x32_bf16 v[236:239], a[228:231], v[44:47], 0\n\t" \
  "v_mul_f32_e32 v206, v206, v206\n\t" \
  "v_mul_f32_e32 v207, v207, v207\n\t" \
  "v_mfma_f32_16x16x32_bf16 v[224:227], a[232:235], v[48:51], v[224:227]\n\t" \
  "ds_read_b128 a[216:219], v33 offset:6144\n\t" \
  "v_cvt_pk_bf16_f32 v198, v204, v205\n\t" \
  "v_mfma_f32_16x16x32_bf16 v[232:235], a[232:235], v[52:55], v[232:235]\n\t" \
  "buffer_load_dwordx4 v34, %11, s27 offen offset:2048 sc1 lds\n\t" \
  "v_cvt_pk_bf16_f32 v199, v206, v207\n\t" \
  "v_mfma_f32_16x16x32_bf16 v[228:231], a[236:239], v[48:51], v[228:231]\n\t" \
  "ds_read_b128 a[220:223], v33 offset:7168\n\t" \
  "v_lshlrev_b32_e32 v120, 16, v108\n\t" \
  "v_mfma_f32_16x16x32_bf16 v[236:239], a[236:239], v[52:55], v[236:239]\n\t" \
  "v_and_b32_e32 v121, 0xffff0000, v108\n\t" \
  "v_lshlrev_b32_e32 v122, 16, v109\n\t" \
  "v_mfma_f32_16x16x32_bf16 v[224:227], a[240:243], v[56:59], v[224:227]\n\t" \
  "ds_read_b128 a[224:227], v33 offset:8192\n\t" \
  "v_and_b32_e32 v123, 0xffff0000, v109\n\t" \
  "v_mfma_f32_16x16x32_bf16 v[232:235], a[240:243], v[60:63], v[232:235]\n\t" \
  "v_fma_f32 v208, v208, s69, v120\n\t" \
  "v_fma_f32 v209, v209, s69, v121\n\t" \
  "v_mfma_f32_16x16x32_bf16 v[228:231], a[244:247], v[56:59], v[228:231]\n\t" \
  "ds_read_b128 a[228:231], v33 offset:9216\n\t" \
  "buffer_load_dwordx4 v34, %11, s27 offen offset:3072 sc1 lds\n\t" \
  "v_mfma_f32_16x16x32_bf16 v[236:239], a[244:247], v[60:63], v[236:239]\n\t" \
  "v_fma_f32 v210, v210, s69, v122\n\t" \
  "v_fma_f32 v211, v211, s69, v123\n\t" \
  "v_mfma_f32_16x16x32_bf16 v[224:227], a[248:251], v[64:67], v[224:227]\n\t" \
  "ds_read_b128 a[232:235], v33 offset:10240\n\t" \
  "v_max_f32_e32 v208, 0, v208\n\t" \
  "v_mfma_f32_16x16x32_bf16 v[232:235], a[248:251], v[68:71], v[232:235]\n\t" \
  "v_max_f32_e32 v209, 0, v209\n\t" \
  "v_max_f32_e32 v210, 0, v210\n\t" \
  "v_mfma_f32_16x16x32_bf16 v[228:231], a[252:255], v[64:67], v[228:231]\n\t" \
  "ds_read_b128 a[236:239], v33 offset:11264\n\t" \
  "v_max_f32_e32 v211, 0, v211\n\t" \
  "v_mfma_f32_16x16x32_bf16 v[236:239], a[252:255], v[68:71], v[236:239]\n\t" \
  "v_mul_f32_e32 v208, v208, v208\n\t" \
  "v_mul_f32_e32 v209, v209, v209\n\t" \
  "s_barrier\n\t" \
  "s_waitcnt lgkmcnt(4)\n\t" \
  "v_mfma_f32_16x16x32_bf16 v[224:227], a[192:195], v[72:75], v[224:227]\n\t" \
  "s_add_u32 s30, s28, 0x8000\n\t" \
  "ds_read_b128 a[240:243], v33 offset:12288\n\t" \
  "v_mul_f32_e32 v210, v210, v210\n\t" \
  "v_mfma_f32_16x16x32_bf16 v[232:235], a[192:195], v[76:79], v[232:235]\n\t" \
  "s_cmp_lg_u32 s28, 0x20000\n\t" \
  "s_cselect_b32 s30, s30, 0\n\t" \
  "v_readlane_b32 s26, v27, 13\n\t" \
  "v_readlane_b32 s27, v28, 13\n\t" \
  "v_mfma_f32_16x16x32_bf16 v[228:231], a[196:199], v[72:75], v[228:231]\n\t" \
  "ds_read_b128 a[244:247], v33 offset:13312\n\t" \
  "v_mul_f32_e32 v211, v211, v211\n\t" \
  "v_readlane_b32 s34, v29, 0\n\t" \
  "v_mfma_f32_16x16x32_bf16 v[236:239], a[196:199], v[76:79], v[236:239]\n\t" \
  "v_cvt_pk_bf16_f32 v208, v208, v209\n\t" \
  "v_cvt_pk_bf16_f32 v209, v210, v211\n\t" \
  "v_mfma_f32_16x16x32_bf16 v[224:227], a[200:203], v[80:83], v[224:227]\n\t" \
  "ds_read_b128 a[248:251], v33 offset:14336\n\t" \
  "s_add_u32 m0, s31, 32768\n\t" \
  "v_mfma_f32_16x16x32_bf16 v[232:235], a[200:203], v[84:87], v[232:235]\n\t" \
  "buffer_load_dwordx4 v34, %11, s26 offen offset:0 sc1 lds\n\t" \
  "v_lshlrev_b32_e32 v120, 16, v110\n\t" \
  "v_readlane_b32 s88, v29, 1\n\t" \
  "v_mfma_f32_16x16x32_bf16 v[228:231], a[204:207], v[80:83], v[228:231]\n\t" \
  "v_add_u32_e32 v32, s30, %1\n\t" \
  "ds_read_b128 a[252:255], v33 offset:15360\n\t" \
  "v_mfma_f32_16x16x32_bf16 v[236:239], a[204:207], v[84:87], v[236:239]\n\t" \
  "v_and_b32_e32 v121, 0xffff0000, v110\n\t" \
  "v_lshlrev_b32_e32 v122, 16, v111\n\t" \
  "v_mfma_f32_16x16x32_bf16 v[224:227], a[208:211], v[88:91], v[224:227]\n\t" \
  "ds_read_b128 a[192:195], v33 offset:16384\n\t" \
  "v_and_b32_e32 v123, 0xffff0000, v111\n\t" \
  "v_readlane_b32 s38, v29, 4\n\t" \
  "v_mfma_f32_16x16x32_bf16 v[232:235], a[208:211], v[92:95], v[232:235]\n\t" \
  "v_fma_f32 v212, v212, s69, v120\n\t" \
  "v_fma_f32 v213, v213, s69, v121\n\t" \
  "v_mfma_f32_16x16x32_bf16 v[228:231], a[212:215], v[88:91], v[228:231]\n\t" \
  "ds_read_b128 a[196:199], v33 offset:17408\n\t" \
  "v_fma_f32 v214, v214, s69, v122\n\t" \
  "v_mfma_f32_16x16x32_bf16 v[236:239], a[212:215], v[92:95], v[236:239]\n\t" \
  "v_fma_f32 v215, v215, s69, v123\n\t" \
  "v_max_f32_e32 v212, 0, v212\n\t" \
  "v_readlane_b32 s39, v29, 5\n\t" \
  "v_mfma_f32_16x16x32_bf16 v[224:227], a[216:219], v[96:99], v[224:227]\n\t" \
  "ds_read_b128 a[200:203], v33 offset:18432\n\t" \
  "buffer_load_dwordx4 v34, %11, s26 offen offset:1024 sc1 lds\n\t" \
  "v_max_f32_e32 v213, 0, v213\n\t" \
  "v_mfma_f32_16x16x32_bf16 v[232:235], a[216:219], v[100:103], v[232:235]\n\t" \
  "v_max_f32_e32 v214, 0, v214\n\t" \
  "v_max_f32_e32 v215, 0, v215\n\t" \
  "v_mfma_f32_16x16x32_bf16 v[228:231], a[220:223], v[96:99], v[228:231]\n\t" \
  "ds_read_b128 a[204:207], v33 offset:19456\n\t" \
  "v_mul_f32_e32 v212, v212, v212\n\t" \
  "v_mfma_f32_16x16x32_bf16 v[236:239], a[220:223], v[100:103], v[236:239]\n\t" \
  "v_mul_f32_e32 v213, v213, v213\n\t" \
  "v_readlane_b32 s42, v29, 21\n\t" \
  "s_waitcnt lgkmcnt(4)\n\t" \
  "v_mfma_f32_16x16x32_bf16 v[224:227], a[224:227], a[0:3], v[224:227]\n\t" \
  "ds_read_b128 a[208:211], v33 offset:20480\n\t" \
  "v_mul_f32_e32 v214, v214, v214\n\t" \
  "v_mfma_f32_16x16x32_bf16 v[232:235], a[224:227], a[4:7], v[232:235]\n\t" \
  "v_mul_f32_e32 v215, v215, v215\n\t" \
  "v_cvt_pk_bf16_f32 v210, v212, v213\n\t" \
  "v_mfma_f32_16x16x32_bf16 v[228:231], a[228:231], a[0:3], v[228:231]\n\t" \
  "ds_read_b128 a[212:215], v33 offset:21504\n\t" \
  "v_cvt_pk_bf16_f32 v211, v214, v215\n\t" \
  "v_readlane_b32 s62, v29, 17\n\t" \
  "v_mfma_f32_16x16x32_bf16 v[236:239], a[228:231], a[4:7], v[236:239]\n\t" \
  "buffer_load_dwordx4 v34, %11, s26 offen offset:2048 sc1 lds\n\t" \
  "v_lshlrev_b32_e32 v120, 16, v108\n\t" \
  "v_mfma_f32_16x16x32_bf16 v[224:227], a[232:235], a[8:11], v[224:227]\n\t" \
  "ds_read_b128 a[216:219], v33 offset:22528\n\t" \
  "v_and_b32_e32 v121, 0xffff0000, v108\n\t" \
  "v_mfma_f32_16x16x32_bf16 v[232:235], a[232:235], a[12:15], v[232:235]\n\t" \
  "v_lshlrev_b32_e32 v122, 16, v109\n\t" \
  "v_and_b32_e32 v123, 0xffff0000, v109\n\t" \
  "v_readlane_b32 s71, v29, 42\n\t" \
  "v_mfma_f32_16x16x32_bf16 v[228:231], a[236:239], a[8:11], v[228:231]\n\t" \
  "ds_read_b128 a[220:223], v33 offset:23552\n\t" \
  "v_fma_f32 v216, v216, s69, v120\n\t" \
  "v_mfma_f32_16x16x32_bf16 v[236:239], a[236:239], a[12:15], v[236:239]\n\t" \
  "v_fma_f32 v217, v217, s69, v121\n\t" \
  "v_fma_f32 v218, v218, s69, v122\n\t" \
  "v_fma_f32 v219, v219, s69, v123\n\t" \
  "v_mfma_f32_16x16x32_bf16 v[224:227], a[240:243], a[16:19], v[224:227]\n\t" \
  "ds_read_b128 a[224:227], v33 offset:24576\n\t" \
  "v_max_f32_e32 v216, 0, v216\n\t" \
  "v_mfma_f32_16x16x32_bf16 v[232:235], a[240:243], a[20:23], v[232:235]\n\t" \
  "v_max_f32_e32 v217, 0, v217\n\t" \
  "v_max_f32_e32 v218, 0, v218\n\t" \
  "v_readlane_b32 s80, v29, 43\n\t" \
  "v_mfma_f32_16x16x32_bf16 v[228:231], a[244:247], a[16:19], v[228:231]\n\t" \
  "ds_read_b128 a[228:231], v33 offset:25600\n\t" \
  "buffer_load_dwordx4 v34, %11, s26 offen offset:3072 sc1 lds\n\t" \
  "v_mfma_f32_16x16x32_bf16 v[236:239], a[244:247], a[20:23], v[236:239]\n\t" \
  "v_max_f32_e32 v219, 0, v219\n\t" \
  "v_mul_f32_e32 v216, v216, v216\n\t" \
  "v_mfma_f32_16x16x32_bf16 v[224:227], a[248:251], a[24:27], v[224:227]\n\t" \
  "ds_read_b128 a[232:235], v33 offset:26624\n\t" \
  "v_mul_f32_e32 v217, v217, v217\n\t" \
  "v_readlane_b32 s56, %13, 12\n\t" \
  "v_mfma_f32_16x16x32_bf16 v[232:235], a[248:251], a[28:31], v[232:235]\n\t" \
  "v_mul_f32_e32 v218, v218, v218\n\t" \
  "v_mul_f32_e32 v219, v219, v219\n\t" \
  "v_mfma_f32_16x16x32_bf16 v[228:231], a[252:255], a[24:27], v[228:231]\n\t" \
  "ds_read_b128 a[236:239], v33 offset:27648\n\t" \
  "buffer_load_dword v127, off, %11, s68 offset:4 sc1\n\t" \
  "v_mfma_f32_16x16x32_bf16 v[236:239], a[252:255], a[28:31], v[236:239]\n\t" \
  "v_cvt_pk_bf16_f32 v212, v216, v217\n\t" \
  "v_readlane_b32 s57, %13, 13\n\t" \
  "s_waitcnt lgkmcnt(4)\n\t" \
  "v_mfma_f32_16x16x32_bf16 v[224:227], a[192:195], a[32:35], v[224:227]\n\t" \
  "ds_read_b128 a[240:243], v33 offset:28672\n\t" \
  "s_add_u32 m0, m0, 0x4000\n\t" \
  "v_mfma_f32_16x16x32_bf16 v[232:235], a[192:195], a[36:39], v[232:235]\n\t" \
  "buffer_load_dwordx4 v34, %11, s27 offen offset:0 sc1 lds\n\t" \
  "v_cvt_pk_bf16_f32 v213, v218, v219\n\t" \
  "v_mfma_f32_16x16x32_bf16 v[228:231], a[196:199], a[32:35], v[228:231]\n\t" \
  "ds_read_b128 a[244:247], v33 offset:29696\n\t" \
  "v_lshlrev_b32_e32 v120, 16, v110\n\t" \
  "v_and_b32_e32 v121, 0xffff0000, v110\n\t" \
  "v_mfma_f32_16x16x32_bf16 v[236:239], a[196:199], a[36:39], v[236:239]\n\t" \
  "v_lshlrev_b32_e32 v122, 16, v111\n\t" \
  "v_and_b32_e32 v123, 0xffff0000, v111\n\t" \
  "s_mov_b32 s25, 16\n\t" \
  "v_mfma_f32_16x16x32_bf16 v[224:227], a[200:203], a[40:43], v[224:227]\n\t" \
  "ds_read_b128 a[248:251], v33 offset:30720\n\t" \
  "v_fma_f32 v220, v220, s69, v120\n\t" \
  "v_mfma_f32_16x16x32_bf16 v[232:235], a[200:203], a[44:47], v[232:235]\n\t" \
  "v_fma_f32 v221, v221, s69, v121\n\t" \
  "v_fma_f32 v222, v222, s69, v122\n\t" \
  "v_mfma_f32_16x16x32_bf16 v[228:231], a[204:207], a[40:43], v[228:231]\n\t" \
  "ds_read_b128 a[252:255], v33 offset:31744\n\t" \
  "v_fma_f32 v223, v223, s69, v123\n\t" \
  "s_mov_b32 s58, 33792\n\t" \
  "v_mfma_f32_16x16x32_bf16 v[236:239], a[204:207], a[44:47], v[236:239]\n\t" \
  "v_max_f32_e32 v220, 0, v220\n\t" \
  "v_max_f32_e32 v221, 0, v221\n\t" \
  "v_mfma_f32_16x16x32_bf16 v[240:243], a[208:211], v[40:43], 0\n\t" \
  "ds_read_b128 a[192:195], v32 offset:0\n\t" \
  "buffer_load_dwordx4 v34, %11, s27 offen offset:1024 sc1 lds\n\t" \
  "v_mfma_f32_16x16x32_bf16 v[248:251], a[208:211], v[44:47], 0\n\t" \
  "v_max_f32_e32 v222, 0, v222\n\t" \
  "v_max_f32_e32 v223, 0, v223\n\t" \
  "s_add_u32 s56, s56, s42\n\ts_addc_u32 s57, s57, 0\n\t" \
  "v_mfma_f32_16x16x32_bf16 v[244:247], a[212:215], v[40:43], 0\n\t" \
  "ds_read_b128 a[196:199], v32 offset:1024\n\t" \
  "v_mul_f32_e32 v220, v220, v220\n\t" \
  "v_mfma_f32_16x16x32_bf16 v[252:255], a[212:215], v[44:47], 0\n\t" \
  "v_mul_f32_e32 v221, v221, v221\n\t" \
  "v_mul_f32_e32 v222, v222, v222\n\t" \
  "v_mfma_f32_16x16x32_bf16 v[240:243], a[216:219], v[48:51], v[240:243]\n\t" \
  "ds_read_b128 a[200:203], v32 offset:2048\n\t" \
  "v_mul_f32_e32 v223, v223, v223\n\t" \
  "s_mov_b32 s60, s56\n\t" \
  "v_mfma_f32_16x16x32_bf16 v[248:251], a[216:219], v[52:55], v[248:251]\n\t" \
  "v_cvt_pk_bf16_f32 v214, v220, v221\n\t" \
  "v_cvt_pk_bf16_f32 v215, v222, v223\n\t" \
  "v_mfma_f32_16x16x32_bf16 v[244:247], a[220:223], v[48:51], v[244:247]\n\t" \
  "ds_read_b128 a[204:207], v32 offset:3072\n\t" \
  "v_lshlrev_b32_e32 v120, 16, v112\n\t" \
  "v_and_b32_e32 v121, 0xffff0000, v112\n\t" \
  "v_mfma_f32_16x16x32_bf16 v[252:255], a[220:223], v[52:55], v[252:255]\n\t" \
  "buffer_load_dwordx4 v34, %11, s27 offen offset:2048 sc1 lds\n\t" \
  "s_waitcnt lgkmcnt(4)\n\t" \
  "v_mfma_f32_16x16x32_bf16 v[240:243], a[224:227], v[56:59], v[240:243]\n\t" \
  "ds_read_b128 a[208:211], v32 offset:4096\n\t" \
  "s_mov_b32 s29, s28\n\t" \
  "s_mov_b32 s61, s57\n\t" \
  "v_mfma_f32_16x16x32_bf16 v[248:251], a[224:227], v[60:63], v[248:251]\n\t" \
  "v_lshlrev_b32_e32 v122, 16, v113\n\t" \
  "v_and_b32_e32 v123, 0xffff0000, v113\n\t" \
  "v_mfma_f32_16x16x32_bf16 v[244:247], a[228:231], v[56:59], v[244:247]\n\t" \
  "ds_read_b128 a[212:215], v32 offset:5120\n\t" \
  "s_mov_b32 s28, s30\n\t" \
  "v_fma_f32 v224, v224, s69, v120\n\t" \
  "v_mfma_f32_16x16x32_bf16 v[252:255], a[228:231], v[60:63], v[252:255]\n\t" \
  "v_fma_f32 v225, v225, s69, v121\n\t" \
  "v_fma_f32 v226, v226, s69, v122\n\t" \
  "v_mfma_f32_16x16x32_bf16 v[240:243], a[232:235], v[64:67], v[240:243]\n\t" \
  "ds_read_b128 a[216:219], v32 offset:6144\n\t" \
  "v_fma_f32 v227, v227, s69, v123\n\t" \
  "v_mov_b32_e32 v36, 0\n\t" \
  "v_mfma_f32_16x16x32_bf16 v[248:251], a[232:235], v[68:71], v[248:251]\n\t" \
  "v_max_f32_e32 v224, 0, v224\n\t" \
  "v_max_f32_e32 v225, 0, v225\n\t" \
  "v_mfma_f32_16x16x32_bf16 v[244:247], a[236:239], v[64:67], v[244:247]\n\t" \
  "ds_read_b128 a[220:223], v32 offset:7168\n\t" \
  "buffer_load_dwordx4 v34, %11, s27 offen offset:3072 sc1 lds\n\t" \
  "v_mfma_f32_16x16x32_bf16 v[252:255], a[236:239], v[68:71], v[252:255]\n\t" \
  "v_max_f32_e32 v226, 0, v226\n\t" \
  "v_max_f32_e32 v227, 0, v227\n\t" \
  "v_mul_f32_e32 v224, v224, v224\n\t" \
  "v_mfma_f32_16x16x32_bf16 v[240:243], a[240:243], v[72:75], v[240:243]\n\t" \
  "ds_read_b128 a[224:227], v32 offset:8192\n\t" \
  "v_mul_f32_e32 v225, v225, v225\n\t" \
  "s_mov_b32 s36, 0x60000000\n\t" \
  "v_mfma_f32_16x16x32_bf16 v[248:251], a[240:243], v[76:79], v[248:251]\n\t" \
  "v_mul_f32_e32 v226, v226, v226\n\t" \
  "v_mul_f32_e32 v227, v227, v227\n\t" \
  "v_mfma_f32_16x16x32_bf16 v[244:247], a[244:247], v[72:75], v[244:247]\n\t" \
  "ds_read_b128 a[228:231], v32 offset:9216\n\t" \
  "v_cvt_pk_bf16_f32 v224, v224, v225\n\t" \
  "v_mfma_f32_16x16x32_bf16 v[252:255], a[244:247], v[76:79], v[252:255]\n\t" \
  "v_cvt_pk_bf16_f32 v225, v226, v227\n\t" \
  "v_lshlrev_b32_e32 v120, 16, v114\n\t" \
  "v_and_b32_e32 v121, 0xffff0000, v114\n\t" \
  "v_mfma_f32_16x16x32_bf16 v[240:243], a[248:251], v[80:83], v[240:243]\n\t" \
  "ds_read_b128 a[232:235], v32 offset:10240\n\t" \
  "v_lshlrev_b32_e32 v122, 16, v115\n\t" \
  "s_mov_b32 s37, 0x60000000\n\t" \
  "v_mfma_f32_16x16x32_bf16 v[248:251], a[248:251], v[84:87], v[248:251]\n\t" \
  "v_and_b32_e32 v123, 0xffff0000, v115\n\t" \
  "v_fma_f32 v228, v228, s69, v120\n\t" \
  "v_mfma_f32_16x16x32_bf16 v[244:247], a[252:255], v[80:83], v[244:247]\n\t" \
  "ds_read_b128 a[236:239], v32 offset:11264\n\t" \
  "v_fma_f32 v229, v229, s69, v121\n\t" \
  "v_mfma_f32_16x16x32_bf16 v[252:255], a[252:255], v[84:87], v[252:255]\n\t" \
  "v_fma_f32 v230, v230, s69, v122\n\t" \
  "v_fma_f32 v231, v231, s69, v123\n\t" \
  "s_waitcnt vmcnt(4)\n\t" \
  "s_barrier\n\t" \
  "s_bitcmp1_b32 s41, 0\n\t" \
  "s_cbranch_scc1 MK_QL_%=\n\t" \
  "MK_QLR_%=:\n\t" \
  "s_waitcnt lgkmcnt(4)\n\t" \
  "v_mfma_f32_16x16x32_bf16 v[240:243], a[192:195], v[88:91], v[240:243]\n\t" \
  "s_add_u32 s30, s28, 0x8000\n\t" \
  "ds_read_b128 a[240:243], v32 offset:12288\n\t" \
  "v_readlane_b32 s26, v27, 14\n\t" \
  "v_readlane_b32 s27, v28, 14\n\t" \
  "v_mfma_f32_16x16x32_bf16 v[248:251], a[192:195], v[92:95], v[248:251]\n\t" \
  "s_cmp_lg_u32 s28, 0x20000\n\t" \
  "s_cselect_b32 s30, s30, 0\n\t" \
  "v_max_f32_e32 v228, 0, v228\n\t" \
  "v_mfma_f32_16x16x32_bf16 v[244:247], a[196:199], v[88:91], v[244:247]\n\t" \
  "ds_read_b128 a[244:247], v32 offset:13312\n\t" \
  "v_max_f32_e32 v229, 0, v229\n\t" \
  "v_mfma_f32_16x16x32_bf16 v[252:255], a[196:199], v[92:95], v[252:255]\n\t" \
  "s_add_u32 m0, s31, 65536\n\t" \
  "v_max_f32_e32 v230, 0, v230\n\t" \
  "v_max_f32_e32 v231, 0, v231\n\t" \
  "v_mfma_f32_16x16x32_bf16 v[240:243], a[200:203], v[96:99], v[240:243]\n\t" \
  "ds_read_b128 a[248:251], v32 offset:14336\n\t" \
  "buffer_load_dwordx4 v34, %11, s26 offen offset:0 sc1 lds\n\t" \
  "v_mfma_f32_16x16x32_bf16 v[248:251], a[200:203], v[100:103], v[248:251]\n\t" \
  "v_mul_f32_e32 v228, v228, v228\n\t" \
  "v_mul_f32_e32 v229, v229, v229\n\t" \
  "v_mul_f32_e32 v230, v230, v230\n\t" \
  "v_mfma_f32_16x16x32_bf16 v[244:247], a[204:207], v[96:99], v[244:247]\n\t" \
  "v_add_u32_e32 v33, s30, %1\n\t" \
  "ds_read_b128 a[252:255], v32 offset:15360\n\t" \
  "v_mfma_f32_16x16x32_bf16 v[252:255], a[204:207], v[100:103], v[252:255]\n\t" \
  "buffer_load_dwordx4 v34, %11, s26 offen offset:1024 sc1 lds\n\t" \
  "v_mul_f32_e32 v231, v231, v231\n\t" \
  "v_cvt_pk_bf16_f32 v226, v228, v229\n\t" \
  "v_mfma_f32_16x16x32_bf16 v[240:243], a[208:211], a[0:3], v[240:243]\n\t" \
  "ds_read_b128 a[192:195], v33 offset:0\n\t" \
  "v_cvt_pk_bf16_f32 v227, v230, v231\n\t" \
  "v_mfma_f32_16x16x32_bf16 v[248:251], a[208:211], a[4:7], v[248:251]\n\t" \
  "v_lshlrev_b32_e32 v120, 16, v112\n\t" \
  "v_and_b32_e32 v121, 0xffff0000, v112\n\t" \
  "v_lshlrev_b32_e32 v122, 16, v113\n\t" \
  "v_mfma_f32_16x16x32_bf16 v[244:247], a[212:215], a[0:3], v[244:247]\n\t" \
  "ds_read_b128 a[196:199], v33 offset:1024\n\t" \
  "buffer_load_dwordx4 v34, %11, s26 offen offset:2048 sc1 lds\n\t" \
  "v_mfma_f32_16x16x32_bf16 v[252:255], a[212:215], a[4:7], v[252:255]\n\t" \
  "v_and_b32_e32 v123, 0xffff0000, v113\n\t" \
  "v_fma_f32 v232, v232, s69, v120\n\t" \
  "v_fma_f32 v233, v233, s69, v121\n\t" \
  "v_mfma_f32_16x16x32_bf16 v[240:243], a[216:219], a[8:11], v[240:243]\n\t" \
  "ds_read_b128 a[200:203], v33 offset:2048\n\t" \
  "v_fma_f32 v234, v234, s69, v122\n\t" \
  "v_mfma_f32_16x16x32_bf16 v[248:251], a[216:219], a[12:15], v[248:251]\n\t" \
  "buffer_load_dwordx4 v34, %11, s26 offen offset:3072 sc1 lds\n\t" \
  "v_fma_f32 v235, v235, s69, v123\n\t" \
  "v_max_f32_e32 v232, 0, v232\n\t" \
  "v_mfma_f32_16x16x32_bf16 v[244:247], a[220:223], a[8:11], v[244:247]\n\t" \
  "ds_read_b128 a[204:207], v33 offset:3072\n\t" \
  "v_max_f32_e32 v233, 0, v233\n\t" \
  "v_mfma_f32_16x16x32_bf16 v[252:255], a[220:223], a[12:15], v[252:255]\n\t" \
  "s_add_u32 m0, m0, 0x4000\n\t" \
  "v_max_f32_e32 v234, 0, v234\n\t" \
  "s_waitcnt lgkmcnt(4)\n\t" \
  "v_mfma_f32_16x16x32_bf16 v[240:243], a[224:227], a[16:19], v[240:243]\n\t" \
  "ds_read_b128 a[208:211], v33 offset:4096\n\t" \
  "buffer_load_dwordx4 v34, %11, s27 offen offset:0 sc1 lds\n\t" \
  "v_mfma_f32_16x16x32_bf16 v[248:251], a[224:227], a[20:23], v[248:251]\n\t" \
  "v_max_f32_e32 v235, 0, v235\n\t" \
  "v_mul_f32_e32 v232, v232, v232\n\t" \
  "v_mul_f32_e32 v233, v233, v233\n\t" \
  "v_mfma_f32_16x16x32_bf16 v[244:247], a[228:231], a[16:19], v[244:247]\n\t" \
  "ds_read_b128 a[212:215], v33 offset:5120\n\t" \
  "v_mul_f32_e32 v234, v234, v234\n\t" \
  "v_mfma_f32_16x16x32_bf16 v[252:255], a[228:231], a[20:23], v[252:255]\n\t" \
  "buffer_load_dwordx4 v34, %11, s27 offen offset:1024 sc1 lds\n\t" \
  "v_mul_f32_e32 v235, v235, v235\n\t" \
  "v_cvt_pk_bf16_f32 v228, v232, v233\n\t" \
  "v_mfma_f32_16x16x32_bf16 v[240:243], a[232:235], a[24:27], v[240:243]\n\t" \
  "ds_read_b128 a[216:219], v33 offset:6144\n\t" \
  "v_cvt_pk_bf16_f32 v229, v234, v235\n\t" \
  "v_mfma_f32_16x16x32_bf16 v[248:251], a[232:235], a[28:31], v[248:251]\n\t" \
  "v_lshlrev_b32_e32 v120, 16, v114\n\t" \
  "v_and_b32_e32 v121, 0xffff0000, v114\n\t" \
  "v_lshlrev_b32_e32 v122, 16, v115\n\t" \
  "v_mfma_f32_16x16x32_bf16 v[244:247], a[236:239], a[24:27], v[244:247]\n\t" \
  "ds_read_b128 a[220:223], v33 offset:7168\n\t" \
  "buffer_load_dwordx4 v34, %11, s27 offen offset:2048 sc1 lds\n\t" \
  "v_mfma_f32_16x16x32_bf16 v[252:255], a[236:239], a[28:31], v[252:255]\n\t" \
  "v_and_b32_e32 v123, 0xffff0000, v115\n\t" \
  "v_fma_f32 v236, v236, s69, v120\n\t" \
  "v_fma_f32 v237, v237, s69, v121\n\t" \
  "v_mfma_f32_16x16x32_bf16 v[240:243], a[240:243], a[32:35], v[240:243]\n\t" \
  "ds_read_b128 a[224:227], v33 offset:8192\n\t" \
  "v_fma_f32 v238, v238, s69, v122\n\t" \
  "v_mfma_f32_16x16x32_bf16 v[248:251], a[240:243], a[36:39], v[248:251]\n\t" \
  "buffer_load_dwordx4 v34, %11, s27 offen offset:3072 sc1 lds\n\t" \
  "v_fma_f32 v239, v239, s69, v123\n\t" \
  "v_max_f32_e32 v236, 0, v236\n\t" \
  "v_mfma_f32_16x16x32_bf16 v[244:247], a[244:247], a[32:35], v[244:247]\n\t" \
  "ds_read_b128 a[228:231], v33 offset:9216\n\t" \
  "s_mov_b32 s29, s28\n\t" \
  "v_mfma_f32_16x16x32_bf16 v[252:255], a[244:247], a[36:39], v[252:255]\n\t" \
  "s_mov_b32 s28, s30\n\t" \
  "v_max_f32_e32 v237, 0, v237\n\t" \
  "v_max_f32_e32 v238, 0, v238\n\t" \
  "v_mfma_f32_16x16x32_bf16 v[240:243], a[248:251], a[40:43], v[240:243]\n\t" \
  "ds_read_b128 a[232:235], v33 offset:10240\n\t" \
  "v_max_f32_e32 v239, 0, v239\n\t" \
  "v_readlane_b32 s26, v27, 15\n\t" \
  "v_mfma_f32_16x16x32_bf16 v[248:251], a[248:251], a[44:47], v[248:251]\n\t" \
  "v_mul_f32_e32 v236, v236, v236\n\t" \
  "v_mul_f32_e32 v237, v237, v237\n\t" \
  "v_mul_f32_e32 v238, v238, v238\n\t" \
  "v_readlane_b32 s27, v28, 15\n\t" \
  "v_mfma_f32_16x16x32_bf16 v[244:247], a[252:255], a[40:43], v[244:247]\n\t" \
  "ds_read_b128 a[236:239], v33 offset:11264\n\t" \
  "v_mul_f32_e32 v239, v239, v239\n\t" \
  "v_mov_b64_e32 v[124:125], 0\n\t" \
  "v_mfma_f32_16x16x32_bf16 v[252:255], a[252:255], a[44:47], v[252:255]\n\t" \
  "v_cvt_pk_bf16_f32 v230, v236, v237\n\t" \
  "v_cvt_pk_bf16_f32 v231, v238, v239\n\t" \
  "v_mov_b64_e32 v[126:127], 0\n\t" \
  "v_mov_b64_e32 v[40:41], v[192:193]\n\t" \
  "v_mov_b64_e32 v[42:43], v[194:195]\n\t" \
  "v_mov_b64_e32 v[44:45], v[196:197]\n\t" \
  "v_mov_b64_e32 v[46:47], v[198:199]\n\t" \
  "v_mov_b64_e32 v[48:49], v[208:209]\n\t" \
  "v_mov_b64_e32 v[50:51], v[210:211]\n\t" \
  "v_mov_b64_e32 v[52:53], v[212:213]\n\t" \
  "v_mov_b64_e32 v[54:55], v[214:215]\n\t" \
  "v_mov_b64_e32 v[56:57], v[224:225]\n\t" \
  "v_mov_b64_e32 v[58:59], v[226:227]\n\t" \
  "v_mov_b64_e32 v[60:61], v[228:229]\n\t" \
  "v_mov_b64_e32 v[62:63], v[230:231]\n\t" \
  "v_lshlrev_b32_e32 v120, 16, v116\n\t" \
  "v_and_b32_e32 v121, 0xffff0000, v116\n\t" \
  "v_lshlrev_b32_e32 v122, 16, v117\n\t" \
  "v_and_b32_e32 v123, 0xffff0000, v117\n\t" \
  "v_fma_f32 v240, v240, s69, v120\n\t" \
  "v_fma_f32 v241, v241, s69, v121\n\t" \
  "v_fma_f32 v242, v242, s69, v122\n\t" \
  "v_fma_f32 v243, v243, s69, v123\n\t" \
  "v_max_f32_e32 v240, 0, v240\n\t" \
  "v_max_f32_e32 v241, 0, v241\n\t" \
  "v_max_f32_e32 v242, 0, v242\n\t" \
  "v_max_f32_e32 v243, 0, v243\n\t" \
  "v_pk_mul_f32 v[240:241], v[240:241], v[240:241]\n\t" \
  "v_pk_mul_f32 v[242:243], v[242:243], v[242:243]\n\t" \
  "v_cvt_pk_bf16_f32 v64, v240, v241\n\t" \
  "v_cvt_pk_bf16_f32 v65, v242, v243\n\t" \
  "v_lshlrev_b32_e32 v120, 16, v118\n\t" \
  "v_and_b32_e32 v121, 0xffff0000, v118\n\t" \
  "v_lshlrev_b32_e32 v122, 16, v119\n\t" \
  "v_and_b32_e32 v123, 0xffff0000, v119\n\t" \
  "v_fma_f32 v244, v244, s69, v120\n\t" \
  "v_fma_f32 v245, v245, s69, v121\n\t" \
  "v_fma_f32 v246, v246, s69, v122\n\t" \
  "v_fma_f32 v247, v247, s69, v123\n\t" \
  "v_max_f32_e32 v244, 0, v244\n\t" \
  "v_max_f32_e32 v245, 0, v245\n\t" \
  "v_max_f32_e32 v246, 0, v246\n\t" \
  "v_max_f32_e32 v247, 0, v247\n\t" \
  "v_pk_mul_f32 v[244:245], v[244:245], v[244:245]\n\t" \
  "v_pk_mul_f32 v[246:247], v[246:247], v[246:247]\n\t" \
  "v_cvt_pk_bf16_f32 v66, v244, v245\n\t" \
  "v_cvt_pk_bf16_f32 v67, v246, v247\n\t" \
  "v_lshlrev_b32_e32 v120, 16, v116\n\t" \
  "v_and_b32_e32 v121, 0xffff0000, v116\n\t" \
  "v_lshlrev_b32_e32 v122, 16, v117\n\t" \
  "v_and_b32_e32 v123, 0xffff0000, v117\n\t" \
  "v_fma_f32 v248, v248, s69, v120\n\t" \
  "v_fma_f32 v249, v249, s69, v121\n\t" \
  "v_fma_f32 v250, v250, s69, v122\n\t" \
  "v_fma_f32 v251, v251, s69, v123\n\t" \
  "v_max_f32_e32 v248, 0, v248\n\t" \
  "v_max_f32_e32 v249, 0, v249\n\t" \
  "v_max_f32_e32 v250, 0, v250\n\t" \
  "v_max_f32_e32 v251, 0, v251\n\t" \
  "v_pk_mul_f32 v[248:249], v[248:249], v[248:249]\n\t" \
  "v_pk_mul_f32 v[250:251], v[250:251], v[250:251]\n\t" \
  "v_cvt_pk_bf16_f32 v68, v248, v249\n\t" \
  "v_cvt_pk_bf16_f32 v69, v250, v251\n\t" \
  "v_lshlrev_b32_e32 v120, 16, v118\n\t" \
  "v_and_b32_e32 v121, 0xffff0000, v118\n\t" \
  "v_lshlrev_b32_e32 v122, 16, v119\n\t" \
  "v_and_b32_e32 v123, 0xffff0000, v119\n\t" \
  "v_fma_f32 v252, v252, s69, v120\n\t" \
  "v_fma_f32 v253, v253, s69, v121\n\t" \
  "v_fma_f32 v254, v254, s69, v122\n\t" \
  "v_fma_f32 v255, v255, s69, v123\n\t" \
  "v_max_f32_e32 v252, 0, v252\n\t" \
  "v_max_f32_e32 v253, 0, v253\n\t" \
  "v_max_f32_e32 v254, 0, v254\n\t" \
  "v_max_f32_e32 v255, 0, v255\n\t" \
  "v_pk_mul_f32 v[252:253], v[252:253], v[252:253]\n\t" \
  "v_pk_mul_f32 v[254:255], v[254:255], v[254:255]\n\t" \
  "v_cvt_pk_bf16_f32 v70, v252, v253\n\t" \
  "v_cvt_pk_bf16_f32 v71, v254, v255\n\t" \
  "s_waitcnt vmcnt(8)\n\t" \
  "s_barrier\n\t" \
  "v_mov_b32_e32 v240, 0\n\t" \
  "v_mov_b32_e32 v241, 0\n\t" \
  "v_mov_b32_e32 v242, 0\n\t" \
  "v_mov_b32_e32 v243, 0\n\t" \
  "v_mov_b32_e32 v244, 0\n\t" \
  "v_mov_b32_e32 v245, 0\n\t" \
  "v_mov_b32_e32 v246, 0\n\t" \
  "v_mov_b32_e32 v247, 0\n\t" \
  "v_mov_b32_e32 v250, 0\n\t" \
  "v_mov_b32_e32 v251, 0\n\t" \
  "v_add_u32_e32 v35, s38, %2\n\t" \
  "v_cmp_gt_i32_e64 s[48:49], 0, v35\n\t" \
  "v_cmp_gt_i32_e64 s[50:51], 1, v35\n\t" \
  "v_cmp_gt_i32_e64 s[52:53], 2, v35\n\t" \
  "v_cmp_gt_i32_e64 s[54:55], 3, v35\n\t" \
  "v_cndmask_b32_e64 v192, v240, v37, s[48:49]\n\t" \
  "v_cndmask_b32_e64 v193, v241, v37, s[50:51]\n\t" \
  "v_cndmask_b32_e64 v194, v242, v37, s[52:53]\n\t" \
  "v_cndmask_b32_e64 v195, v243, v37, s[54:55]\n\t" \
  "s_sub_u32 s38, s38, 32\n\t" \
  "s_waitcnt lgkmcnt(4)\n\t" \
  "v_mfma_f32_16x16x32_bf16 v[192:195], a[192:195], v[128:131], v[192:195]\n\t" \
  "s_add_u32 s30, s28, 0x8000\n\t" \
  "ds_read_b128 a[240:243], v33 offset:12288\n\t" \
  "v_cmp_gt_i32_e64 s[48:49], 16, v35\n\t" \
  "v_mfma_f32_16x16x32_bf16 v[196:199], a[192:195], v[132:135], v[244:247]\n\t" \
  "s_cmp_lg_u32 s28, 0x20000\n\t" \
  "s_cselect_b32 s30, s30, 0\n\t" \
  "v_cmp_gt_i32_e64 s[50:51], 17, v35\n\t" \
  "v_mfma_f32_16x16x32_bf16 v[192:195], a[196:199], v[136:139], v[192:195]\n\t" \
  "ds_read_b128 a[244:247], v33 offset:13312\n\t" \
  "s_add_u32 m0, s29, s31\n\t" \
  "v_accvgpr_write_b32 a0, 0\n\t" \
  "v_mfma_f32_16x16x32_bf16 v[196:199], a[196:199], v[140:143], v[196:199]\n\t" \
  "v_cmp_gt_i32_e64 s[52:53], 18, v35\n\t" \
  "v_accvgpr_write_b32 a1, 0\n\t" \
  "v_accvgpr_write_b32 a2, 0\n\t" \
  "v_mfma_f32_16x16x32_bf16 v[192:195], a[200:203], v[144:147], v[192:195]\n\t" \
  "ds_read_b128 a[248:251], v33 offset:14336\n\t" \
  "buffer_load_dwordx4 v34, %11, s26 offen offset:0 sc1 lds\n\t" \
  "v_accvgpr_write_b32 a3, 0\n\t" \
  "v_mfma_f32_16x16x32_bf16 v[196:199], a[200:203], v[148:151], v[196:199]\n\t" \
  "v_cmp_gt_i32_e64 s[54:55], 19, v35\n\t" \
  "v_accvgpr_write_b32 a4, 0\n\t" \
  "v_accvgpr_write_b32 a5, 0\n\t" \
  "v_mfma_f32_16x16x32_bf16 v[192:195], a[204:207], v[152:155], v[192:195]\n\t" \
  "v_add_u32_e32 v32, s30, %1\n\t" \
  "ds_read_b128 a[252:255], v33 offset:15360\n\t" \
  "v_accvgpr_write_b32 a6, 0\n\t" \
  "v_mfma_f32_16x16x32_bf16 v[196:199], a[204:207], v[156:159], v[196:199]\n\t" \
  "buffer_load_dwordx4 v34, %11, s26 offen offset:1024 sc1 lds\n\t" \
  "v_cndmask_b32_e64 v200, v240, v37, s[48:49]\n\t" \
  "v_accvgpr_write_b32 a7, 0\n\t" \
  "v_mfma_f32_16x16x32_bf16 v[192:195], a[208:211], v[160:163], v[192:195]\n\t" \
  "ds_read_b128 a[192:195], v32 offset:0\n\t" \
  "v_cndmask_b32_e64 v201, v241, v37, s[50:51]\n\t" \
  "v_accvgpr_write_b32 a8, 0\n\t" \
  "v_mfma_f32_16x16x32_bf16 v[196:199], a[208:211], v[164:167], v[196:199]\n\t" \
  "v_accvgpr_write_b32 a9, 0\n\t" \
  "v_accvgpr_write_b32 a10, 0\n\t" \
  "v_mfma_f32_16x16x32_bf16 v[192:195], a[212:215], v[168:171], v[192:195]\n\t" \
  "ds_read_b128 a[196:199], v32 offset:1024\n\t" \
  "buffer_load_dwordx4 v34, %11, s26 offen offset:2048 sc1 lds\n\t" \
  "v_cndmask_b32_e64 v202, v242, v37, s[52:53]\n\t" \
  "v_mfma_f32_16x16x32_bf16 v[196:199], a[212:215], v[172:175], v[196:199]\n\t" \
  "v_accvgpr_write_b32 a11, 0\n\t" \
  "v_accvgpr_write_b32 a12, 0\n\t" \
  "v_accvgpr_write_b32 a13, 0\n\t" \
  "v_mfma_f32_16x16x32_bf16 v[192:195], a[216:219], v[176:179], v[192:195]\n\t" \
  "ds_read_b128 a[200:203], v32 offset:2048\n\t" \
  "v_cndmask_b32_e64 v203, v243, v37, s[54:55]\n\t" \
  "v_accvgpr_write_b32 a14, 0\n\t" \
  "v_mfma_f32_16x16x32_bf16 v[196:199], a[216:219], v[180:183], v[196:199]\n\t" \
  "buffer_load_dwordx4 v34, %11, s26 offen offset:3072 sc1 lds\n\t" \
  "v_accvgpr_write_b32 a15, 0\n\t" \
  "v_accvgpr_write_b32 a16, 0\n\t" \
  "v_mfma_f32_16x16x32_bf16 v[192:195], a[220:223], v[184:187], v[192:195]\n\t" \
  "ds_read_b128 a[204:207], v32 offset:3072\n\t" \
  "s_add_u32 m0, m0, 0x4000\n\t" \
  "v_accvgpr_write_b32 a17, 0\n\t" \
  "v_mfma_f32_16x16x32_bf16 v[196:199], a[220:223], v[188:191], v[196:199]\n\t" \
  "v_accvgpr_write_b32 a18, 0\n\t" \
  "v_accvgpr_write_b32 a19, 0\n\t" \
  "s_waitcnt lgkmcnt(4)\n\t" \
  "v_mfma_f32_16x16x32_bf16 v[200:203], a[224:227], v[128:131], v[200:203]\n\t" \
  "ds_read_b128 a[208:211], v32 offset:4096\n\t" \
  "buffer_load_dwordx4 v34, %11, s27 offen offset:0 sc1 lds\n\t" \
  "v_accvgpr_write_b32 a20, 0\n\t" \
  "v_mfma_f32_16x16x32_bf16 v[204:207], a[224:227], v[132:135], v[244:247]\n\t" \
  "v_accvgpr_write_b32 a21, 0\n\t" \
  "v_accvgpr_write_b32 a22, 0\n\t" \
  "v_mfma_f32_16x16x32_bf16 v[200:203], a[228:231], v[136:139], v[200:203]\n\t" \
  "ds_read_b128 a[212:215], v32 offset:5120\n\t" \
  "v_accvgpr_write_b32 a23, 0\n\t" \
  "v_accvgpr_write_b32 a24, 0\n\t" \
  "v_mfma_f32_16x16x32_bf16 v[204:207], a[228:231], v[140:143], v[204:207]\n\t" \
  "buffer_load_dwordx4 v34, %11, s27 offen offset:1024 sc1 lds\n\t" \
  "v_accvgpr_write_b32 a25, 0\n\t" \
  "v_accvgpr_write_b32 a26, 0\n\t" \
  "v_mfma_f32_16x16x32_bf16 v[200:203], a[232:235], v[144:147], v[200:203]\n\t" \
  "ds_read_b128 a[216:219], v32 offset:6144\n\t" \
  "v_accvgpr_write_b32 a27, 0\n\t" \
  "v_accvgpr_write_b32 a28, 0\n\t" \
  "v_mfma_f32_16x16x32_bf16 v[204:207], a[232:235], v[148:151], v[204:207]\n\t" \
  "v_accvgpr_write_b32 a29, 0\n\t" \
  "v_accvgpr_write_b32 a30, 0\n\t" \
  "v_accvgpr_write_b32 a31, 0\n\t" \
  "v_mfma_f32_16x16x32_bf16 v[200:203], a[236:239], v[152:155], v[200:203]\n\t" \
  "ds_read_b128 a[220:223], v32 offset:7168\n\t" \
  "buffer_load_dwordx4 v34, %11, s27 offen offset:2048 sc1 lds\n\t" \
  "v_accvgpr_write_b32 a32, 0\n\t" \
  "v_mfma_f32_16x16x32_bf16 v[204:207], a[236:239], v[156:159], v[204:207]\n\t" \
  "v_accvgpr_write_b32 a33, 0\n\t" \
  "v_accvgpr_write_b32 a34, 0\n\t" \
  "v_accvgpr_write_b32 a35, 0\n\t" \
  "v_mfma_f32_16x16x32_bf16 v[200:203], a[240:243], v[160:163], v[200:203]\n\t" \
  "ds_read_b128 a[224:227], v32 offset:8192\n\t" \
  "v_accvgpr_write_b32 a36, 0\n\t" \
  "v_mfma_f32_16x16x32_bf16 v[204:207], a[240:243], v[164:167], v[204:207]\n\t" \
  "buffer_load_dwordx4 v34, %11, s27 offen offset:3072 sc1 lds\n\t" \
  "v_accvgpr_write_b32 a37, 0\n\t" \
  "v_accvgpr_write_b32 a38, 0\n\t" \
  "v_mfma_f32_16x16x32_bf16 v[200:203], a[244:247], v[168:171], v[200:203]\n\t" \
  "ds_read_b128 a[228:231], v32 offset:9216\n\t" \
  "v_readlane_b32 s26, v27, s25\n\t" \
  "v_accvgpr_write_b32 a39, 0\n\t" \
  "v_mfma_f32_16x16x32_bf16 v[204:207], a[244:247], v[172:175], v[204:207]\n\t" \
  "v_readlane_b32 s27, v28, s25\n\t" \
  "v_accvgpr_write_b32 a40, 0\n\t" \
  "v_accvgpr_write_b32 a41, 0\n\t" \
  "v_mfma_f32_16x16x32_bf16 v[200:203], a[248:251], v[176:179], v[200:203]\n\t" \
  "ds_read_b128 a[232:235], v32 offset:10240\n\t" \
  "s_add_u32 s25, s25, 1\n\t" \
  "v_accvgpr_write_b32 a42, 0\n\t" \
  "v_mfma_f32_16x16x32_bf16 v[204:207], a[248:251], v[180:183], v[204:207]\n\t" \
  "s_mov_b32 s29, s28\n\t" \
  "v_accvgpr_write_b32 a43, 0\n\t" \
  "v_accvgpr_write_b32 a44, 0\n\t" \
  "v_mfma_f32_16x16x32_bf16 v[200:203], a[252:255], v[184:187], v[200:203]\n\t" \
  "ds_read_b128 a[236:239], v32 offset:11264\n\t" \
  "s_mov_b32 s28, s30\n\t" \
  "v_accvgpr_write_b32 a45, 0\n\t" \
  "v_mfma_f32_16x16x32_bf16 v[204:207], a[252:255], v[188:191], v[204:207]\n\t" \
  "v_accvgpr_write_b32 a46, 0\n\t" \
  "v_accvgpr_write_b32 a47, 0\n\t" \
  "MK_AT1_%=:\n\t" \
  "s_nop 7\n\t" \
  "v_max3_f32 v38, v192, v193, v194\n\t" \
  "v_max3_f32 v38, v38, v195, v200\n\t" \
  "v_max3_f32 v38, v38, v201, v202\n\t" \
  "v_max_f32_e32 v38, v38, v203\n\t" \
  "v_mov_b32_e32 v252, v38\n\t" \
  "s_nop 1\n\t" \
  "v_permlane16_swap_b32_e32 v38, v252\n\t" \
  "s_nop 0\n\t" \
  "v_max_f32_e32 v38, v38, v252\n\t" \
  "v_mov_b32_e32 v252, v38\n\t" \
  "s_nop 1\n\t" \
  "v_permlane32_swap_b32_e32 v38, v252\n\t" \
  "s_nop 0\n\t" \
  "v_max_f32_e32 v38, v38, v252\n\t" \
  "v_sub_f32_e32 v192, v192, v38\n\t" \
  "v_sub_f32_e32 v193, v193, v38\n\t" \
  "v_sub_f32_e32 v194, v194, v38\n\t" \
  "v_sub_f32_e32 v195, v195, v38\n\t" \
  "v_sub_f32_e32 v200, v200, v38\n\t" \
  "v_sub_f32_e32 v201, v201, v38\n\t" \
  "v_sub_f32_e32 v202, v202, v38\n\t" \
  "v_sub_f32_e32 v203, v203, v38\n\t" \
  "v_sub_f32_e32 v240, 0, v38\n\t" \
  "v_sub_f32_e32 v241, 0, v38\n\t" \
  "v_sub_f32_e32 v242, 0, v38\n\t" \
  "v_sub_f32_e32 v243, 0, v38\n\t" \
  "v_max3_f32 v39, v196, v197, v198\n\t" \
  "v_max3_f32 v39, v39, v199, v204\n\t" \
  "v_max3_f32 v39, v39, v205, v206\n\t" \
  "v_max_f32_e32 v39, v39, v207\n\t" \
  "v_mov_b32_e32 v252, v39\n\t" \
  "s_nop 1\n\t" \
  "v_permlane16_swap_b32_e32 v39, v252\n\t" \
  "s_nop 0\n\t" \
  "v_max_f32_e32 v39, v39, v252\n\t" \
  "v_mov_b32_e32 v252, v39\n\t" \
  "s_nop 1\n\t" \
  "v_permlane32_swap_b32_e32 v39, v252\n\t" \
  "s_nop 0\n\t" \
  "v_max_f32_e32 v39, v39, v252\n\t" \
  "v_sub_f32_e32 v196, v196, v39\n\t" \
  "v_sub_f32_e32 v197, v197, v39\n\t" \
  "v_sub_f32_e32 v198, v198, v39\n\t" \
  "v_sub_f32_e32 v199, v199, v39\n\t" \
  "v_sub_f32_e32 v204, v204, v39\n\t" \
  "v_sub_f32_e32 v205, v205, v39\n\t" \
  "v_sub_f32_e32 v206, v206, v39\n\t" \
  "v_sub_f32_e32 v207, v207, v39\n\t" \
  "v_sub_f32_e32 v244, 0, v39\n\t" \
  "v_sub_f32_e32 v245, 0, v39\n\t" \
  "v_sub_f32_e32 v246, 0, v39\n\t" \
  "v_sub_f32_e32 v247, 0, v39\n\t" \
  "v_exp_f32_e32 v252, v192\n\t" \
  "v_exp_f32_e32 v253, v193\n\t" \
  "v_add_f32_e32 v248, v250, v252\n\t" \
  "v_add_f32_e32 v248, v248, v253\n\t" \
  "v_cvt_pk_bf16_f32 v224, v252, v253\n\t" \
  "v_exp_f32_e32 v254, v194\n\t" \
  "v_exp_f32_e32 v255, v195\n\t" \
  "v_add_f32_e32 v248, v248, v254\n\t" \
  "v_add_f32_e32 v248, v248, v255\n\t" \
  "v_cvt_pk_bf16_f32 v225, v254, v255\n\t" \
  "v_exp_f32_e32 v252, v196\n\t" \
  "v_exp_f32_e32 v253, v197\n\t" \
  "v_add_f32_e32 v249, v251, v252\n\t" \
  "v_add_f32_e32 v249, v249, v253\n\t" \
  "v_cvt_pk_bf16_f32 v228, v252, v253\n\t" \
  "v_exp_f32_e32 v254, v198\n\t" \
  "v_exp_f32_e32 v255, v199\n\t" \
  "v_add_f32_e32 v249, v249, v254\n\t" \
  "v_add_f32_e32 v249, v249, v255\n\t" \
  "v_cvt_pk_bf16_f32 v229, v254, v255\n\t" \
  "v_mov_b32_e32 v39, s90\n\t" \
  "s_and_b32 s43, s41, 1\n\t" \
  "s_cselect_b32 s43, 2, 6\n\t" \
  "s_sub_i32 s33, s34, s43\n\t" \
  "s_cmp_lt_i32 s33, 0\n\t" \
  "s_cbranch_scc1 MK_T3_%=\n\t" \
  "s_add_u32 s43, s33, 1\n\t" \
  "s_lshl_b32 s43, s43, 5\n\t" \
  "s_sub_u32 s38, s38, s43\n\t" \
  "s_cmp_eq_u32 s33, 0\n\t" \
  "s_cbranch_scc1 MK_FEND_%=\n\t" \
  "MK_FAST_%=:\n\t" \
  "s_waitcnt vmcnt(16)\n\t" \
  "s_barrier\n\t" \
  "s_waitcnt lgkmcnt(4)\n\t" \
  "v_mfma_f32_16x16x32_bf16 v[208:211], a[192:195], v[128:131], v[240:243]\n\t" \
  "s_add_u32 s30, s28, 0x8000\n\t" \
  "ds_read_b128 a[240:243], v32 offset:12288\n\t" \
  "v_mfma_f32_16x16x32_bf16 v[212:215], a[192:195], v[132:135], v[244:247]\n\t" \
  "s_cmp_lg_u32 s28, 0x20000\n\t" \
  "s_cselect_b32 s30, s30, 0\n\t" \
  "v_mfma_f32_16x16x32_bf16 v[208:211], a[196:199], v[136:139], v[208:211]\n\t" \
  "ds_read_b128 a[244:247], v32 offset:13312\n\t" \
  "s_add_u32 m0, s29, s31\n\t" \
  "v_mfma_f32_16x16x32_bf16 v[212:215], a[196:199], v[140:143], v[212:215]\n\t" \
  "v_exp_f32_e32 v252, v200\n\t" \
  "v_mfma_f32_16x16x32_bf16 v[208:211], a[200:203], v[144:147], v[208:211]\n\t" \
  "ds_read_b128 a[248:251], v32 offset:14336\n\t" \
  "buffer_load_dwordx4 v34, %11, s26 offen offset:0 sc1 lds\n\t" \
  "v_mfma_f32_16x16x32_bf16 v[212:215], a[200:203], v[148:151], v[212:215]\n\t" \
  "v_exp_f32_e32 v253, v201\n\t" \
  "v_mfma_f32_16x16x32_bf16 v[208:211], a[204:207], v[152:155], v[208:211]\n\t" \
  "v_add_u32_e32 v33, s30, %1\n\t" \
  "ds_read_b128 a[252:255], v32 offset:15360\n\t" \
  "v_mfma_f32_16x16x32_bf16 v[212:215], a[204:207], v[156:159], v[212:215]\n\t" \
  "v_exp_f32_e32 v254, v202\n\t" \
  "v_mfma_f32_16x16x32_bf16 v[208:211], a[208:211], v[160:163], v[208:211]\n\t" \
  "ds_read_b128 a[192:195], v32 offset:16384\n\t" \
  "v_add_f32_e32 v248, v248, v252\n\t" \
  "v_mfma_f32_16x16x32_bf16 v[212:215], a[208:211], v[164:167], v[212:215]\n\t" \
  "v_add_f32_e32 v248, v248, v253\n\t" \
  "v_cvt_pk_bf16_f32 v226, v252, v253\n\t" \
  "v_mfma_f32_16x16x32_bf16 v[208:211], a[212:215], v[168:171], v[208:211]\n\t" \
  "ds_read_b128 a[196:199], v32 offset:17408\n\t" \
  "buffer_load_dwordx4 v34, %11, s26 offen offset:1024 sc1 lds\n\t" \
  "v_mfma_f32_16x16x32_bf16 v[212:215], a[212:215], v[172:175], v[212:215]\n\t" \
  "v_exp_f32_e32 v255, v203\n\t" \
  "v_mfma_f32_16x16x32_bf16 v[208:211], a[216:219], v[176:179], v[208:211]\n\t" \
  "ds_read_b128 a[200:203], v32 offset:18432\n\t" \
  "v_add_f32_e32 v248, v248, v254\n\t" \
  "v_mfma_f32_16x16x32_bf16 v[212:215], a[216:219], v[180:183], v[212:215]\n\t" \
  "v_exp_f32_e32 v252, v204\n\t" \
  "v_mfma_f32_16x16x32_bf16 v[208:211], a[220:223], v[184:187], v[208:211]\n\t" \
  "ds_read_b128 a[204:207], v32 offset:19456\n\t" \
  "v_add_f32_e32 v248, v248, v255\n\t" \
  "v_mfma_f32_16x16x32_bf16 v[212:215], a[220:223], v[188:191], v[212:215]\n\t" \
  "v_cvt_pk_bf16_f32 v227, v254, v255\n\t" \
  "s_waitcnt lgkmcnt(4)\n\t" \
  "v_mfma_f32_16x16x32_bf16 v[216:219], a[224:227], v[128:131], v[240:243]\n\t" \
  "ds_read_b128 a[208:211], v32 offset:20480\n\t" \
  "buffer_load_dwordx4 v34, %11, s26 offen offset:2048 sc1 lds\n\t" \
  "v_mfma_f32_16x16x32_bf16 v[220:223], a[224:227], v[132:135], v[244:247]\n\t" \
  "v_exp_f32_e32 v253, v205\n\t" \
  "v_mfma_f32_16x16x32_bf16 v[216:219], a[228:231], v[136:139], v[216:219]\n\t" \
  "ds_read_b128 a[212:215], v32 offset:21504\n\t" \
  "v_add_f32_e32 v249, v249, v252\n\t" \
  "v_mfma_f32_16x16x32_bf16 v[220:223], a[228:231], v[140:143], v[220:223]\n\t" \
  "v_add_f32_e32 v249, v249, v253\n\t" \
  "v_cvt_pk_bf16_f32 v230, v252, v253\n\t" \
  "v_mfma_f32_16x16x32_bf16 v[216:219], a[232:235], v[144:147], v[216:219]\n\t" \
  "ds_read_b128 a[216:219], v32 offset:22528\n\t" \
  "v_mfma_f32_16x16x32_bf16 v[220:223], a[232:235], v[148:151], v[220:223]\n\t" \
  "v_exp_f32_e32 v254, v206\n\t" \
  "v_mfma_f32_16x16x32_bf16 v[216:219], a[236:239], v[152:155], v[216:219]\n\t" \
  "ds_read_b128 a[220:223], v32 offset:23552\n\t" \
  "buffer_load_dwordx4 v34, %11, s26 offen offset:3072 sc1 lds\n\t" \
  "v_mfma_f32_16x16x32_bf16 v[220:223], a[236:239], v[156:159], v[220:223]\n\t" \
  "v_exp_f32_e32 v255, v207\n\t" \
  "v_mfma_f32_16x16x32_bf16 v[216:219], a[240:243], v[160:163], v[216:219]\n\t" \
  "ds_read_b128 a[224:227], v32 offset:24576\n\t" \
  "v_add_f32_e32 v249, v249, v254\n\t" \
  "v_mfma_f32_16x16x32_bf16 v[220:223], a[240:243], v[164:167], v[220:223]\n\t" \
  "v_add_f32_e32 v249, v249, v255\n\t" \
  "v_cvt_pk_bf16_f32 v231, v254, v255\n\t" \
  "v_mfma_f32_16x16x32_bf16 v[216:219], a[244:247], v[168:171], v[216:219]\n\t" \
  "ds_read_b128 a[228:231], v32 offset:25600\n\t" \
  "s_add_u32 m0, m0, 0x4000\n\t" \
  "v_mfma_f32_16x16x32_bf16 v[220:223], a[244:247], v[172:175], v[220:223]\n\t" \
  "v_max_f32_e32 v38, v248, v249\n\t" \
  "v_mfma_f32_16x16x32_bf16 v[216:219], a[248:251], v[176:179], v[216:219]\n\t" \
  "ds_read_b128 a[232:235], v32 offset:26624\n\t" \
  "buffer_load_dwordx4 v34, %11, s27 offen offset:0 sc1 lds\n\t" \
  "v_cmp_nge_f32_e32 vcc, s45, v38\n\t" \
  "v_mfma_f32_16x16x32_bf16 v[220:223], a[248:251], v[180:183], v[220:223]\n\t" \
  "v_mfma_f32_16x16x32_bf16 v[216:219], a[252:255], v[184:187], v[216:219]\n\t" \
  "ds_read_b128 a[236:239], v32 offset:27648\n\t" \
  "v_mfma_f32_16x16x32_bf16 v[220:223], a[252:255], v[188:191], v[220:223]\n\t" \
  "s_cbranch_vccnz MK_T0_0_%=\n\t" \
  "MK_R0_0_%=:\n\t" \
  "s_waitcnt lgkmcnt(4)\n\t" \
  "v_mfma_f32_16x16x32_bf16 a[0:3], a[192:195], v[224:227], a[0:3]\n\t" \
  "ds_read_b128 a[240:243], v32 offset:28672\n\t" \
  "buffer_store_dword v39, %12, %11, s36 offen offset:0 sc1\n\t" \
  "v_mfma_f32_16x16x32_bf16 a[4:7], a[192:195], v[228:231], a[4:7]\n\t" \
  "v_exp_f32_e32 v252, v208\n\t" \
  "v_mfma_f32_16x16x32_bf16 a[8:11], a[196:199], v[224:227], a[8:11]\n\t" \
  "ds_read_b128 a[244:247], v32 offset:29696\n\t" \
  "buffer_load_dwordx4 v34, %11, s27 offen offset:1024 sc1 lds\n\t" \
  "v_mfma_f32_16x16x32_bf16 a[12:15], a[196:199], v[228:231], a[12:15]\n\t" \
  "v_exp_f32_e32 v253, v209\n\t" \
  "v_mfma_f32_16x16x32_bf16 a[16:19], a[200:203], v[224:227], a[16:19]\n\t" \
  "ds_read_b128 a[248:251], v32 offset:30720\n\t" \
  "v_add_f32_e32 v250, v248, v252\n\t" \
  "v_mfma_f32_16x16x32_bf16 a[20:23], a[200:203], v[228:231], a[20:23]\n\t" \
  "v_add_f32_e32 v250, v250, v253\n\t" \
  "v_cvt_pk_bf16_f32 v232, v252, v253\n\t" \
  "v_mfma_f32_16x16x32_bf16 a[24:27], a[204:207], v[224:227], a[24:27]\n\t" \
  "ds_read_b128 a[252:255], v32 offset:31744\n\t" \
  "v_mfma_f32_16x16x32_bf16 a[28:31], a[204:207], v[228:231], a[28:31]\n\t" \
  "v_exp_f32_e32 v254, v210\n\t" \
  "v_mfma_f32_16x16x32_bf16 a[32:35], a[208:211], v[224:227], a[32:35]\n\t" \
  "ds_read_b128 a[192:195], v33 offset:0\n\t" \
  "buffer_load_dwordx4 v34, %11, s27 offen offset:2048 sc1 lds\n\t" \
  "v_mfma_f32_16x16x32_bf16 a[36:39], a[208:211], v[228:231], a[36:39]\n\t" \
  "v_add_f32_e32 v250, v250, v254\n\t" \
  "v_mfma_f32_16x16x32_bf16 a[40:43], a[212:215], v[224:227], a[40:43]\n\t" \
  "ds_read_b128 a[196:199], v33 offset:1024\n\t" \
  "s_mov_b32 s36, s37\n\t" \
  "v_mfma_f32_16x16x32_bf16 a[44:47], a[212:215], v[228:231], a[44:47]\n\t" \
  "v_exp_f32_e32 v255, v211\n\t" \
  "v_mfma_f32_16x16x32_bf16 a[48:51], a[216:219], v[224:227], a[48:51]\n\t" \
  "ds_read_b128 a[200:203], v33 offset:2048\n\t" \
  "v_add_f32_e32 v250, v250, v255\n\t" \
  "v_mfma_f32_16x16x32_bf16 a[52:55], a[216:219], v[228:231], a[52:55]\n\t" \
  "v_cvt_pk_bf16_f32 v233, v254, v255\n\t" \
  "v_mfma_f32_16x16x32_bf16 a[56:59], a[220:223], v[224:227], a[56:59]\n\t" \
  "ds_read_b128 a[204:207], v33 offset:3072\n\t" \
  "buffer_load_dwordx4 v34, %11, s27 offen offset:3072 sc1 lds\n\t" \
  "v_mfma_f32_16x16x32_bf16 a[60:63], a[220:223], v[228:231], a[60:63]\n\t" \
  "s_waitcnt lgkmcnt(4)\n\t" \
  "v_mfma_f32_16x16x32_bf16 a[64:67], a[224:227], v[224:227], a[64:67]\n\t" \
  "ds_read_b128 a[208:211], v33 offset:4096\n\t" \
  "v_readlane_b32 s26, v27, s25\n\t" \
  "v_mfma_f32_16x16x32_bf16 a[68:71], a[224:227], v[228:231], a[68:71]\n\t" \
  "v_exp_f32_e32 v252, v212\n\t" \
  "v_mfma_f32_16x16x32_bf16 a[72:75], a[228:231], v[224:227], a[72:75]\n\t" \
  "ds_read_b128 a[212:215], v33 offset:5120\n\t" \
  "v_readlane_b32 s27, v28, s25\n\t" \
  "v_mfma_f32_16x16x32_bf16 a[76:79], a[228:231], v[228:231], a[76:79]\n\t" \
  "v_exp_f32_e32 v253, v213\n\t" \
  "v_mfma_f32_16x16x32_bf16 a[80:83], a[232:235], v[224:227], a[80:83]\n\t" \
  "ds_read_b128 a[216:219], v33 offset:6144\n\t" \
  "s_add_u32 s25, s25, 1\n\t" \
  "v_mfma_f32_16x16x32_bf16 a[84:87], a[232:235], v[228:231], a[84:87]\n\t" \
  "v_add_f32_e32 v251, v249, v252\n\t" \
  "v_add_f32_e32 v251, v251, v253\n\t" \
  "v_mfma_f32_16x16x32_bf16 a[88:91], a[236:239], v[224:227], a[88:91]\n\t" \
  "ds_read_b128 a[220:223], v33 offset:7168\n\t" \
  "s_mov_b32 s29, s28\n\t" \
  "v_mfma_f32_16x16x32_bf16 a[92:95], a[236:239], v[228:231], a[92:95]\n\t" \
  "v_exp_f32_e32 v254, v214\n\t" \
  "v_mfma_f32_16x16x32_bf16 a[96:99], a[240:243], v[224:227], a[96:99]\n\t" \
  "ds_read_b128 a[224:227], v33 offset:8192\n\t" \
  "s_mov_b32 s28, s30\n\t" \
  "v_mfma_f32_16x16x32_bf16 a[100:103], a[240:243], v[228:231], a[100:103]\n\t" \
  "v_cvt_pk_bf16_f32 v236, v252, v253\n\t" \
  "v_add_f32_e32 v251, v251, v254\n\t" \
  "v_mfma_f32_16x16x32_bf16 a[104:107], a[244:247], v[224:227], a[104:107]\n\t" \
  "ds_read_b128 a[228:231], v33 offset:9216\n\t" \
  "s_mov_b32 s37, 0x60000000\n\t" \
  "v_mfma_f32_16x16x32_bf16 a[108:111], a[244:247], v[228:231], a[108:111]\n\t" \
  "v_exp_f32_e32 v255, v215\n\t" \
  "v_mfma_f32_16x16x32_bf16 a[112:115], a[248:251], v[224:227], a[112:115]\n\t" \
  "ds_read_b128 a[232:235], v33 offset:10240\n\t" \
  "v_add_f32_e32 v251, v251, v255\n\t" \
  "v_mfma_f32_16x16x32_bf16 a[116:119], a[248:251], v[228:231], a[116:119]\n\t" \
  "v_cvt_pk_bf16_f32 v237, v254, v255\n\t" \
  "v_mfma_f32_16x16x32_bf16 a[120:123], a[252:255], v[224:227], a[120:123]\n\t" \
  "ds_read_b128 a[236:239], v33 offset:11264\n\t" \
  "v_mfma_f32_16x16x32_bf16 a[124:127], a[252:255], v[228:231], a[124:127]\n\t" \
  "s_waitcnt vmcnt(16)\n\t" \
  "s_barrier\n\t" \
  "s_waitcnt lgkmcnt(4)\n\t" \
  "v_mfma_f32_16x16x32_bf16 v[192:195], a[192:195], v[128:131], v[240:243]\n\t" \
  "s_add_u32 s30, s28, 0x8000\n\t" \
  "ds_read_b128 a[240:243], v33 offset:12288\n\t" \
  "v_mfma_f32_16x16x32_bf16 v[196:199], a[192:195], v[132:135], v[244:247]\n\t" \
  "s_cmp_lg_u32 s28, 0x20000\n\t" \
  "s_cselect_b32 s30, s30, 0\n\t" \
  "v_mfma_f32_16x16x32_bf16 v[192:195], a[196:199], v[136:139], v[192:195]\n\t" \
  "ds_read_b128 a[244:247], v33 offset:13312\n\t" \
  "s_add_u32 m0, s29, s31\n\t" \
  "v_mfma_f32_16x16x32_bf16 v[196:199], a[196:199], v[140:143], v[196:199]\n\t" \
  "v_exp_f32_e32 v252, v216\n\t" \
  "v_mfma_f32_16x16x32_bf16 v[192:195], a[200:203], v[144:147], v[192:195]\n\t" \
  "ds_read_b128 a[248:251], v33 offset:14336\n\t" \
  "buffer_load_dwordx4 v34, %11, s26 offen offset:0 sc1 lds\n\t" \
  "v_mfma_f32_16x16x32_bf16 v[196:199], a[200:203], v[148:151], v[196:199]\n\t" \
  "v_exp_f32_e32 v253, v217\n\t" \
  "v_mfma_f32_16x16x32_bf16 v[192:195], a[204:207], v[152:155], v[192:195]\n\t" \
  "v_add_u32_e32 v32, s30, %1\n\t" \
  "ds_read_b128 a[252:255], v33 offset:15360\n\t" \
  "v_mfma_f32_16x16x32_bf16 v[196:199], a[204:207], v[156:159], v[196:199]\n\t" \
  "v_exp_f32_e32 v254, v218\n\t" \
  "v_mfma_f32_16x16x32_bf16 v[192:195], a[208:211], v[160:163], v[192:195]\n\t" \
  "ds_read_b128 a[192:195], v33 offset:16384\n\t" \
  "v_add_f32_e32 v250, v250, v252\n\t" \
  "v_mfma_f32_16x16x32_bf16 v[196:199], a[208:211], v[164:167], v[196:199]\n\t" \
  "v_add_f32_e32 v250, v250, v253\n\t" \
  "v_cvt_pk_bf16_f32 v234, v252, v253\n\t" \
  "v_mfma_f32_16x16x32_bf16 v[192:195], a[212:215], v[168:171], v[192:195]\n\t" \
  "ds_read_b128 a[196:199], v33 offset:17408\n\t" \
  "buffer_load_dwordx4 v34, %11, s26 offen offset:1024 sc1 lds\n\t" \
  "v_mfma_f32_16x16x32_bf16 v[196:199], a[212:215], v[172:175], v[196:199]\n\t" \
  "v_exp_f32_e32 v255, v219\n\t" \
  "v_mfma_f32_16x16x32_bf16 v[192:195], a[216:219], v[176:179], v[192:195]\n\t" \
  "ds_read_b128 a[200:203], v33 offset:18432\n\t" \
  "v_add_f32_e32 v250, v250, v254\n\t" \
  "v_mfma_f32_16x16x32_bf16 v[196:199], a[216:219], v[180:183], v[196:199]\n\t" \
  "v_exp_f32_e32 v252, v220\n\t" \
  "v_mfma_f32_16x16x32_bf16 v[192:195], a[220:223], v[184:187], v[192:195]\n\t" \
  "ds_read_b128 a[204:207], v33 offset:19456\n\t" \
  "v_add_f32_e32 v250, v250, v255\n\t" \
  "v_mfma_f32_16x16x32_bf16 v[196:199], a[220:223], v[188:191], v[196:199]\n\t" \
  "v_cvt_pk_bf16_f32 v235, v254, v255\n\t" \
  "s_waitcnt lgkmcnt(4)\n\t" \
  "v_mfma_f32_16x16x32_bf16 v[200:203], a[224:227], v[128:131], v[240:243]\n\t" \
  "ds_read_b128 a[208:211], v33 offset:20480\n\t" \
  "buffer_load_dwordx4 v34, %11, s26 offen offset:2048 sc1 lds\n\t" \
  "v_mfma_f32_16x16x32_bf16 v[204:207], a[224:227], v[132:135], v[244:247]\n\t" \
  "v_exp_f32_e32 v253, v221\n\t" \
  "v_mfma_f32_16x16x32_bf16 v[200:203], a[228:231], v[136:139], v[200:203]\n\t" \
  "ds_read_b128 a[212:215], v33 offset:21504\n\t" \
  "v_add_f32_e32 v251, v251, v252\n\t" \
  "v_mfma_f32_16x16x32_bf16 v[204:207], a[228:231], v[140:143], v[204:207]\n\t" \
  "v_add_f32_e32 v251, v251, v253\n\t" \
  "v_cvt_pk_bf16_f32 v238, v252, v253\n\t" \
  "v_mfma_f32_16x16x32_bf16 v[200:203], a[232:235], v[144:147], v[200:203]\n\t" \
  "ds_read_b128 a[216:219], v33 offset:22528\n\t" \
  "v_mfma_f32_16x16x32_bf16 v[204:207], a[232:235], v[148:151], v[204:207]\n\t" \
  "v_exp_f32_e32 v254, v222\n\t" \
  "v_mfma_f32_16x16x32_bf16 v[200:203], a[236:239], v[152:155], v[200:203]\n\t" \
  "ds_read_b128 a[220:223], v33 offset:23552\n\t" \
  "buffer_load_dwordx4 v34, %11, s26 offen offset:3072 sc1 lds\n\t" \
  "v_mfma_f32_16x16x32_bf16 v[204:207], a[236:239], v[156:159], v[204:207]\n\t" \
  "v_exp_f32_e32 v255, v223\n\t" \
  "v_mfma_f32_16x16x32_bf16 v[200:203], a[240:243], v[160:163], v[200:203]\n\t" \
  "ds_read_b128 a[224:227], v33 offset:24576\n\t" \
  "v_add_f32_e32 v251, v251, v254\n\t" \
  "v_mfma_f32_16x16x32_bf16 v[204:207], a[240:243], v[164:167], v[204:207]\n\t" \
  "v_add_f32_e32 v251, v251, v255\n\t" \
  "v_cvt_pk_bf16_f32 v239, v254, v255\n\t" \
  "v_mfma_f32_16x16x32_bf16 v[200:203], a[244:247], v[168:171], v[200:203]\n\t" \
  "ds_read_b128 a[228:231], v33 offset:25600\n\t" \
  "s_add_u32 m0, m0, 0x4000\n\t" \
  "v_mfma_f32_16x16x32_bf16 v[204:207], a[244:247], v[172:175], v[204:207]\n\t" \
  "v_max_f32_e32 v38, v250, v251\n\t" \
  "v_mfma_f32_16x16x32_bf16 v[200:203], a[248:251], v[176:179], v[200:203]\n\t" \
  "ds_read_b128 a[232:235], v33 offset:26624\n\t" \
  "buffer_load_dwordx4 v34, %11, s27 offen offset:0 sc1 lds\n\t" \
  "v_cmp_nge_f32_e32 vcc, s45, v38\n\t" \
  "v_mfma_f32_16x16x32_bf16 v[204:207], a[248:251], v[180:183], v[204:207]\n\t" \
  "v_mfma_f32_16x16x32_bf16 v[200:203], a[252:255], v[184:187], v[200:203]\n\t" \
  "ds_read_b128 a[236:239], v33 offset:27648\n\t" \
  "v_mfma_f32_16x16x32_bf16 v[204:207], a[252:255], v[188:191], v[204:207]\n\t" \
  "s_cbranch_vccnz MK_T1_0_%=\n\t" \
  "MK_R1_0_%=:\n\t" \
  "s_waitcnt lgkmcnt(4)\n\t" \
  "v_mfma_f32_16x16x32_bf16 a[0:3], a[192:195], v[232:235], a[0:3]\n\t" \
  "ds_read_b128 a[240:243], v33 offset:28672\n\t" \
  "v_mfma_f32_16x16x32_bf16 a[4:7], a[192:195], v[236:239], a[4:7]\n\t" \
  "v_exp_f32_e32 v252, v192\n\t" \
  "v_mfma_f32_16x16x32_bf16 a[8:11], a[196:199], v[232:235], a[8:11]\n\t" \
  "ds_read_b128 a[244:247], v33 offset:29696\n\t" \
  "buffer_load_dwordx4 v34, %11, s27 offen offset:1024 sc1 lds\n\t" \
  "v_mfma_f32_16x16x32_bf16 a[12:15], a[196:199], v[236:239], a[12:15]\n\t" \
  "v_exp_f32_e32 v253, v193\n\t" \
  "v_mfma_f32_16x16x32_bf16 a[16:19], a[200:203], v[232:235], a[16:19]\n\t" \
  "ds_read_b128 a[248:251], v33 offset:30720\n\t" \
  "v_add_f32_e32 v248, v250, v252\n\t" \
  "v_mfma_f32_16x16x32_bf16 a[20:23], a[200:203], v[236:239], a[20:23]\n\t" \
  "v_add_f32_e32 v248, v248, v253\n\t" \
  "v_cvt_pk_bf16_f32 v224, v252, v253\n\t" \
  "v_mfma_f32_16x16x32_bf16 a[24:27], a[204:207], v[232:235], a[24:27]\n\t" \
  "ds_read_b128 a[252:255], v33 offset:31744\n\t" \
  "v_mfma_f32_16x16x32_bf16 a[28:31], a[204:207], v[236:239], a[28:31]\n\t" \
  "v_exp_f32_e32 v254, v194\n\t" \
  "v_mfma_f32_16x16x32_bf16 a[32:35], a[208:211], v[232:235], a[32:35]\n\t" \
  "ds_read_b128 a[192:195], v32 offset:0\n\t" \
  "buffer_load_dwordx4 v34, %11, s27 offen offset:2048 sc1 lds\n\t" \
  "v_mfma_f32_16x16x32_bf16 a[36:39], a[208:211], v[236:239], a[36:39]\n\t" \
  "v_add_f32_e32 v248, v248, v254\n\t" \
  "v_mfma_f32_16x16x32_bf16 a[40:43], a[212:215], v[232:235], a[40:43]\n\t" \
  "ds_read_b128 a[196:199], v32 offset:1024\n\t" \
  "v_mfma_f32_16x16x32_bf16 a[44:47], a[212:215], v[236:239], a[44:47]\n\t" \
  "v_exp_f32_e32 v255, v195\n\t" \
  "v_mfma_f32_16x16x32_bf16 a[48:51], a[216:219], v[232:235], a[48:51]\n\t" \
  "ds_read_b128 a[200:203], v32 offset:2048\n\t" \
  "v_add_f32_e32 v248, v248, v255\n\t" \
  "v_mfma_f32_16x16x32_bf16 a[52:55], a[216:219], v[236:239], a[52:55]\n\t" \
  "v_cvt_pk_bf16_f32 v225, v254, v255\n\t" \
  "v_mfma_f32_16x16x32_bf16 a[56:59], a[220:223], v[232:235], a[56:59]\n\t" \
  "ds_read_b128 a[204:207], v32 offset:3072\n\t" \
  "buffer_load_dwordx4 v34, %11, s27 offen offset:3072 sc1 lds\n\t" \
  "v_mfma_f32_16x16x32_bf16 a[60:63], a[220:223], v[236:239], a[60:63]\n\t" \
  "s_waitcnt lgkmcnt(4)\n\t" \
  "v_mfma_f32_16x16x32_bf16 a[64:67], a[224:227], v[232:235], a[64:67]\n\t" \
  "ds_read_b128 a[208:211], v32 offset:4096\n\t" \
  "v_readlane_b32 s26, v27, s25\n\t" \
  "v_mfma_f32_16x16x32_bf16 a[68:71], a[224:227], v[236:239], a[68:71]\n\t" \
  "v_exp_f32_e32 v252, v196\n\t" \
  "v_mfma_f32_16x16x32_bf16 a[72:75], a[228:231], v[232:235], a[72:75]\n\t" \
  "ds_read_b128 a[212:215], v32 offset:5120\n\t" \
  "v_readlane_b32 s27, v28, s25\n\t" \
  "v_mfma_f32_16x16x32_bf16 a[76:79], a[228:231], v[236:239], a[76:79]\n\t" \
  "v_exp_f32_e32 v253, v197\n\t" \
  "v_mfma_f32_16x16x32_bf16 a[80:83], a[232:235], v[232:235], a[80:83]\n\t" \
  "ds_read_b128 a[216:219], v32 offset:6144\n\t" \
  "s_add_u32 s25, s25, 1\n\t" \
  "v_mfma_f32_16x16x32_bf16 a[84:87], a[232:235], v[236:239], a[84:87]\n\t" \
  "v_add_f32_e32 v249, v251, v252\n\t" \
  "v_add_f32_e32 v249, v249, v253\n\t" \
  "v_mfma_f32_16x16x32_bf16 a[88:91], a[236:239], v[232:235], a[88:91]\n\t" \
  "ds_read_b128 a[220:223], v32 offset:7168\n\t" \
  "s_mov_b32 s29, s28\n\t" \
  "v_mfma_f32_16x16x32_bf16 a[92:95], a[236:239], v[236:239], a[92:95]\n\t" \
  "v_exp_f32_e32 v254, v198\n\t" \
  "v_mfma_f32_16x16x32_bf16 a[96:99], a[240:243], v[232:235], a[96:99]\n\t" \
  "ds_read_b128 a[224:227], v32 offset:8192\n\t" \
  "s_mov_b32 s28, s30\n\t" \
  "v_mfma_f32_16x16x32_bf16 a[100:103], a[240:243], v[236:239], a[100:103]\n\t" \
  "v_cvt_pk_bf16_f32 v228, v252, v253\n\t" \
  "v_add_f32_e32 v249, v249, v254\n\t" \
  "v_mfma_f32_16x16x32_bf16 a[104:107], a[244:247], v[232:235], a[104:107]\n\t" \
  "ds_read_b128 a[228:231], v32 offset:9216\n\t" \
  "v_mfma_f32_16x16x32_bf16 a[108:111], a[244:247], v[236:239], a[108:111]\n\t" \
  "v_exp_f32_e32 v255, v199\n\t" \
  "v_mfma_f32_16x16x32_bf16 a[112:115], a[248:251], v[232:235], a[112:115]\n\t" \
  "ds_read_b128 a[232:235], v32 offset:10240\n\t" \
  "v_add_f32_e32 v249, v249, v255\n\t" \
  "s_sub_u32 s88, s88, 1\n\t" \
  "v_mfma_f32_16x16x32_bf16 a[116:119], a[248:251], v[236:239], a[116:119]\n\t" \
  "v_cvt_pk_bf16_f32 v229, v254, v255\n\t" \
  "s_cmp_lt_u32 s88, 4\n\t" \
  "v_mfma_f32_16x16x32_bf16 a[120:123], a[252:255], v[232:235], a[120:123]\n\t" \
  "ds_read_b128 a[236:239], v32 offset:11264\n\t" \
  "v_mfma_f32_16x16x32_bf16 a[124:127], a[252:255], v[236:239], a[124:127]\n\t" \
  "s_cbranch_scc1 MK_HK_%=\n\t" \
  "MK_HKR_%=:\n\t" \
  "s_sub_u32 s33, s33, 2\n\t" \
  "s_cmp_lg_u32 s33, 0\n\t" \
  "s_cbranch_scc1 MK_FAST_%=\n\t" \
  "s_branch MK_FEND_%=\n\t" \
  "MK_HK_%=:\n\t" \
  "s_cmp_eq_u32 s88, 1\n\t" \
  "s_cbranch_scc1 MK_HK1_%=\n\t" \
  "s_cmp_eq_u32 s88, 0\n\t" \
  "s_cbranch_scc1 MK_HK0_%=\n\t" \
  "s_cmp_eq_u32 s88, 2\n\t" \
  "s_cbranch_scc1 MK_HK2_%=\n\t" \
  "v_bfe_u32 v112, %1, 4, 4\n\t" \
  "v_mul_u32_u24_e32 v112, 896, v112\n\t" \
  "v_lshrrev_b32_e32 v113, 8, %1\n\t" \
  "v_lshl_add_u32 v112, v113, 4, v112\n\t" \
  "s_mov_b32 s66, 0x40000000\n\t" \
  "v_readlane_b32 s92, %13, 18\n\t" \
  "v_readlane_b32 s93, %13, 19\n\t" \
  "v_readlane_b32 s94, %13, 20\n\t" \
  "v_readlane_b32 s95, %13, 21\n\t" \
  "v_readlane_b32 s43, v30, 32\n\t" \
  "buffer_store_dwordx4 v[40:43], %1, %11, s40 offen offset:0 sc1\n\t" \
  "s_bitcmp1_b32 s43, 0\n\t" \
  "s_cselect_b64 s[64:65], s[94:95], s[92:93]\n\t" \
  "s_and_b32 s43, s43, -2\n\t" \
  "s_add_u32 s44, s43, 14336\n\t" \
  "buffer_load_dwordx4 v[72:75], v112, s[64:67], s43 offen\n\t" \
  "buffer_load_dwordx4 v[76:79], v112, s[64:67], s44 offen\n\t" \
  "v_readlane_b32 s43, v30, 35\n\t" \
  "buffer_store_dwordx4 v[44:47], %1, %11, s70 offen offset:0 sc1\n\t" \
  "s_bitcmp1_b32 s43, 0\n\t" \
  "s_cselect_b64 s[64:65], s[94:95], s[92:93]\n\t" \
  "s_and_b32 s43, s43, -2\n\t" \
  "s_add_u32 s44, s43, 14336\n\t" \
  "buffer_load_dwordx4 v[80:83], v112, s[64:67], s43 offen\n\t" \
  "buffer_load_dwordx4 v[84:87], v112, s[64:67], s44 offen\n\t" \
  "v_readlane_b32 s43, v30, 38\n\t" \
  "buffer_store_dwordx4 v[48:51], %1, %11, s40 offen offset:1024 sc1\n\t" \
  "s_bitcmp1_b32 s43, 0\n\t" \
  "s_cselect_b64 s[64:65], s[94:95], s[92:93]\n\t" \
  "s_and_b32 s43, s43, -2\n\t" \
  "s_add_u32 s44, s43, 14336\n\t" \
  "buffer_load_dwordx4 v[88:91], v112, s[64:67], s43 offen\n\t" \
  "buffer_load_dwordx4 v[92:95], v112, s[64:67], s44 offen\n\t" \
  "s_branch MK_HKR_%=\n\t" \
  "MK_HK2_%=:\n\t" \
  "buffer_store_dwordx4 v[52:55], %1, %11, s70 offen offset:1024 sc1\n\t" \
  "buffer_store_dwordx4 v[56:59], %1, %11, s40 offen offset:2048 sc1\n\t" \
  "buffer_store_dwordx4 v[60:63], %1, %11, s70 offen offset:2048 sc1\n\t" \
  "buffer_store_dwordx4 v[64:67], %1, %11, s40 offen offset:3072 sc1\n\t" \
  "buffer_store_dwordx4 v[68:71], %1, %11, s70 offen offset:3072 sc1\n\t" \
  "s_branch MK_HKR_%=\n\t" \
  "MK_HK1_%=:\n\t" \
  "v_mfma_f32_16x16x32_bf16 v[96:99], v[72:75], %7, 0\n\t" \
  "v_mfma_f32_16x16x32_bf16 v[100:103], v[76:79], %7, 0\n\t" \
  "v_mfma_f32_16x16x32_bf16 v[104:107], v[72:75], %8, 0\n\t" \
  "v_mfma_f32_16x16x32_bf16 v[108:111], v[76:79], %8, 0\n\t" \
  "v_mfma_f32_16x16x32_bf16 v[112:115], v[80:83], %7, 0\n\t" \
  "v_mfma_f32_16x16x32_bf16 v[116:119], v[84:87], %7, 0\n\t" \
  "v_mfma_f32_16x16x32_bf16 v[120:123], v[80:83], %8, 0\n\t" \
  "v_mfma_f32_16x16x32_bf16 v[124:127], v[84:87], %8, 0\n\t" \
  "v_mfma_f32_16x16x32_bf16 v[72:75], v[88:91], %7, 0\n\t" \
  "v_mfma_f32_16x16x32_bf16 v[76:79], v[92:95], %7, 0\n\t" \
  "v_mfma_f32_16x16x32_bf16 v[80:83], v[88:91], %8, 0\n\t" \
  "v_mfma_f32_16x16x32_bf16 v[84:87], v[92:95], %8, 0\n\t" \
  "v_readlane_b32 s43, v30, 33\n\t" \
  "v_cvt_pk_bf16_f32 v96, v96, v97\n\t" \
  "v_cvt_pk_bf16_f32 v97, v98, v99\n\t" \
  "v_cvt_pk_bf16_f32 v98, v100, v101\n\t" \
  "v_cvt_pk_bf16_f32 v99, v102, v103\n\t" \
  "s_nop 0\n\t" \
  "buffer_store_dwordx4 v[96:99], %1, %11, s43 offen sc1\n\t" \
  "v_readlane_b32 s43, v30, 34\n\t" \
  "v_cvt_pk_bf16_f32 v104, v104, v105\n\t" \
  "v_cvt_pk_bf16_f32 v105, v106, v107\n\t" \
  "v_cvt_pk_bf16_f32 v106, v108, v109\n\t" \
  "v_cvt_pk_bf16_f32 v107, v110, v111\n\t" \
  "s_nop 0\n\t" \
  "buffer_store_dwordx4 v[104:107], %1, %11, s43 offen sc1\n\t" \
  "v_readlane_b32 s43, v30, 36\n\t" \
  "v_cvt_pk_bf16_f32 v112, v112, v113\n\t" \
  "v_cvt_pk_bf16_f32 v113, v114, v115\n\t" \
  "v_cvt_pk_bf16_f32 v114, v116, v117\n\t" \
  "v_cvt_pk_bf16_f32 v115, v118, v119\n\t" \
  "s_nop 0\n\t" \
  "buffer_store_dwordx4 v[112:115], %1, %11, s43 offen sc1\n\t" \
  "v_readlane_b32 s43, v30, 37\n\t" \
  "v_cvt_pk_bf16_f32 v120, v120, v121\n\t" \
  "v_cvt_pk_bf16_f32 v121, v122, v123\n\t" \
  "v_cvt_pk_bf16_f32 v122, v124, v125\n\t" \
  "v_cvt_pk_bf16_f32 v123, v126, v127\n\t" \
  "s_nop 0\n\t" \
  "buffer_store_dwordx4 v[120:123], %1, %11, s43 offen sc1\n\t" \
  "v_readlane_b32 s43, v30, 39\n\t" \
  "v_cvt_pk_bf16_f32 v72, v72, v73\n\t" \
  "v_cvt_pk_bf16_f32 v73, v74, v75\n\t" \
  "v_cvt_pk_bf16_f32 v74, v76, v77\n\t" \
  "v_cvt_pk_bf16_f32 v75, v78, v79\n\t" \
  "s_nop 0\n\t" \
  "buffer_store_dwordx4 v[72:75], %1, %11, s43 offen sc1\n\t" \
  "v_readlane_b32 s43, v30, 40\n\t" \
  "v_cvt_pk_bf16_f32 v80, v80, v81\n\t" \
  "v_cvt_pk_bf16_f32 v81, v82, v83\n\t" \
  "v_cvt_pk_bf16_f32 v82, v84, v85\n\t" \
  "v_cvt_pk_bf16_f32 v83, v86, v87\n\t" \
  "s_nop 0\n\t" \
  "buffer_store_dwordx4 v[80:83], %1, %11, s43 offen sc1\n\t" \
  "s_branch MK_HKR_%=\n\t" \
  "MK_HK0_%=:\n\t" \
  "v_readlane_b32 s43, v29, 34\n\t" \
  "v_mov_b32_e32 v112, s90\n\t" \
  "s_nop 3\n\t" \
  "buffer_store_dword v112, %12, %11, s43 offen offset:0 sc1\n\t" \
  "s_branch MK_HKR_%=\n\t" \
  "MK_FEND_%=:\n\t" \
  "s_and_b32 s43, s41, 1\n\t" \
  "s_cbranch_scc0 MK_T5_%=\n\t" \
  "s_waitcnt vmcnt(16)\n\t" \
  "s_barrier\n\t" \
  "s_waitcnt lgkmcnt(4)\n\t" \
  "v_mfma_f32_16x16x32_bf16 v[208:211], a[192:195], v[128:131], v[240:243]\n\t" \
  "s_add_u32 s30, s28, 0x8000\n\t" \
  "ds_read_b128 a[240:243], v32 offset:12288\n\t" \
  "v_mfma_f32_16x16x32_bf16 v[212:215], a[192:195], v[132:135], v[244:247]\n\t" \
  "s_cmp_lg_u32 s28, 0x20000\n\t" \
  "s_cselect_b32 s30, s30, 0\n\t" \
  "v_mfma_f32_16x16x32_bf16 v[208:211], a[196:199], v[136:139], v[208:211]\n\t" \
  "ds_read_b128 a[244:247], v32 offset:13312\n\t" \
  "s_add_u32 m0, s29, s31\n\t" \
  "v_mfma_f32_16x16x32_bf16 v[212:215], a[196:199], v[140:143], v[212:215]\n\t" \
  "v_exp_f32_e32 v252, v200\n\t" \
  "v_mfma_f32_16x16x32_bf16 v[208:211], a[200:203], v[144:147], v[208:211]\n\t" \
  "ds_read_b128 a[248:251], v32 offset:14336\n\t" \
  "buffer_load_dwordx4 v34, %11, s26 offen offset:0 sc1 lds\n\t" \
  "v_mfma_f32_16x16x32_bf16 v[212:215], a[200:203], v[148:151], v[212:215]\n\t" \
  "v_exp_f32_e32 v253, v201\n\t" \
  "v_mfma_f32_16x16x32_bf16 v[208:211], a[204:207], v[152:155], v[208:211]\n\t" \
  "v_add_u32_e32 v33, s30, %1\n\t" \
  "ds_read_b128 a[252:255], v32 offset:15360\n\t" \
  "v_mfma_f32_16x16x32_bf16 v[212:215], a[204:207], v[156:159], v[212:215]\n\t" \
  "v_exp_f32_e32 v254, v202\n\t" \
  "v_mfma_f32_16x16x32_bf16 v[208:211], a[208:211], v[160:163], v[208:211]\n\t" \
  "ds_read_b128 a[192:195], v32 offset:16384\n\t" \
  "v_add_f32_e32 v248, v248, v252\n\t" \
  "v_mfma_f32_16x16x32_bf16 v[212:215], a[208:211], v[164:167], v[212:215]\n\t" \
  "v_add_f32_e32 v248, v248, v253\n\t" \
  "v_cvt_pk_bf16_f32 v226, v252, v253\n\t" \
  "v_mfma_f32_16x16x32_bf16 v[208:211], a[212:215], v[168:171], v[208:211]\n\t" \
  "ds_read_b128 a[196:199], v32 offset:17408\n\t" \
  "buffer_load_dwordx4 v34, %11, s26 offen offset:1024 sc1 lds\n\t" \
  "v_mfma_f32_16x16x32_bf16 v[212:215], a[212:215], v[172:175], v[212:215]\n\t" \
  "v_exp_f32_e32 v255, v203\n\t" \
  "v_mfma_f32_16x16x32_bf16 v[208:211], a[216:219], v[176:179], v[208:211]\n\t" \
  "ds_read_b128 a[200:203], v32 offset:18432\n\t" \
  "v_add_f32_e32 v248, v248, v254\n\t" \
  "v_mfma_f32_16x16x32_bf16 v[212:215], a[216:219], v[180:183], v[212:215]\n\t" \
  "v_exp_f32_e32 v252, v204\n\t" \
  "v_mfma_f32_16x16x32_bf16 v[208:211], a[220:223], v[184:187], v[208:211]\n\t" \
  "ds_read_b128 a[204:207], v32 offset:19456\n\t" \
  "v_add_f32_e32 v248, v248, v255\n\t" \
  "v_mfma_f32_16x16x32_bf16 v[212:215], a[220:223], v[188:191], v[212:215]\n\t" \
  "v_cvt_pk_bf16_f32 v227, v254, v255\n\t" \
  "s_waitcnt lgkmcnt(4)\n\t" \
  "v_mfma_f32_16x16x32_bf16 v[216:219], a[224:227], v[128:131], v[240:243]\n\t" \
  "ds_read_b128 a[208:211], v32 offset:20480\n\t" \
  "buffer_load_dwordx4 v34, %11, s26 offen offset:2048 sc1 lds\n\t" \
  "v_mfma_f32_16x16x32_bf16 v[220:223], a[224:227], v[132:135], v[244:247]\n\t" \
  "v_exp_f32_e32 v253, v205\n\t" \
  "v_mfma_f32_16x16x32_bf16 v[216:219], a[228:231], v[136:139], v[216:219]\n\t" \
  "ds_read_b128 a[212:215], v32 offset:21504\n\t" \
  "v_add_f32_e32 v249, v249, v252\n\t" \
  "v_mfma_f32_16x16x32_bf16 v[220:223], a[228:231], v[140:143], v[220:223]\n\t" \
  "v_add_f32_e32 v249, v249, v253\n\t" \
  "v_cvt_pk_bf16_f32 v230, v252, v253\n\t" \
  "v_mfma_f32_16x16x32_bf16 v[216:219], a[232:235], v[144:147], v[216:219]\n\t" \
  "ds_read_b128 a[216:219], v32 offset:22528\n\t" \
  "v_mfma_f32_16x16x32_bf16 v[220:223], a[232:235], v[148:151], v[220:223]\n\t" \
  "v_exp_f32_e32 v254, v206\n\t" \
  "v_mfma_f32_16x16x32_bf16 v[216:219], a[236:239], v[152:155], v[216:219]\n\t" \
  "ds_read_b128 a[220:223], v32 offset:23552\n\t" \
  "buffer_load_dwordx4 v34, %11, s26 offen offset:3072 sc1 lds\n\t" \
  "v_mfma_f32_16x16x32_bf16 v[220:223], a[236:239], v[156:159], v[220:223]\n\t" \
  "v_exp_f32_e32 v255, v207\n\t" \
  "v_mfma_f32_16x16x32_bf16 v[216:219], a[240:243], v[160:163], v[216:219]\n\t" \
  "ds_read_b128 a[224:227], v32 offset:24576\n\t" \
  "v_add_f32_e32 v249, v249, v254\n\t" \
  "v_mfma_f32_16x16x32_bf16 v[220:223], a[240:243], v[164:167], v[220:223]\n\t" \
  "v_add_f32_e32 v249, v249, v255\n\t" \
  "v_cvt_pk_bf16_f32 v231, v254, v255\n\t" \
  "v_mfma_f32_16x16x32_bf16 v[216:219], a[244:247], v[168:171], v[216:219]\n\t" \
  "ds_read_b128 a[228:231], v32 offset:25600\n\t" \
  "s_add_u32 m0, m0, 0x4000\n\t" \
  "v_mfma_f32_16x16x32_bf16 v[220:223], a[244:247], v[172:175], v[220:223]\n\t" \
  "v_max_f32_e32 v38, v248, v249\n\t" \
  "v_mfma_f32_16x16x32_bf16 v[216:219], a[248:251], v[176:179], v[216:219]\n\t" \
  "ds_read_b128 a[232:235], v32 offset:26624\n\t" \
  "buffer_load_dwordx4 v34, %11, s27 offen offset:0 sc1 lds\n\t" \
  "v_cmp_nge_f32_e32 vcc, s45, v38\n\t" \
  "v_mfma_f32_16x16x32_bf16 v[220:223], a[248:251], v[180:183], v[220:223]\n\t" \
  "v_mfma_f32_16x16x32_bf16 v[216:219], a[252:255], v[184:187], v[216:219]\n\t" \
  "ds_read_b128 a[236:239], v32 offset:27648\n\t" \
  "v_mfma_f32_16x16x32_bf16 v[220:223], a[252:255], v[188:191], v[220:223]\n\t" \
  "s_cbranch_vccnz MK_T0_1_%=\n\t" \
  "MK_R0_1_%=:\n\t" \
  "s_waitcnt lgkmcnt(4)\n\t" \
  "v_mfma_f32_16x16x32_bf16 a[0:3], a[192:195], v[224:227], a[0:3]\n\t" \
  "ds_read_b128 a[240:243], v32 offset:28672\n\t" \
  "v_mfma_f32_16x16x32_bf16 a[4:7], a[192:195], v[228:231], a[4:7]\n\t" \
  "v_exp_f32_e32 v252, v208\n\t" \
  "v_mfma_f32_16x16x32_bf16 a[8:11], a[196:199], v[224:227], a[8:11]\n\t" \
  "ds_read_b128 a[244:247], v32 offset:29696\n\t" \
  "buffer_load_dwordx4 v34, %11, s27 offen offset:1024 sc1 lds\n\t" \
  "v_mfma_f32_16x16x32_bf16 a[12:15], a[196:199], v[228:231], a[12:15]\n\t" \
  "v_exp_f32_e32 v253, v209\n\t" \
  "v_mfma_f32_16x16x32_bf16 a[16:19], a[200:203], v[224:227], a[16:19]\n\t" \
  "ds_read_b128 a[248:251], v32 offset:30720\n\t" \
  "v_add_f32_e32 v250, v248, v252\n\t" \
  "v_mfma_f32_16x16x32_bf16 a[20:23], a[200:203], v[228:231], a[20:23]\n\t" \
  "v_add_f32_e32 v250, v250, v253\n\t" \
  "v_cvt_pk_bf16_f32 v232, v252, v253\n\t" \
  "v_mfma_f32_16x16x32_bf16 a[24:27], a[204:207], v[224:227], a[24:27]\n\t" \
  "ds_read_b128 a[252:255], v32 offset:31744\n\t" \
  "v_mfma_f32_16x16x32_bf16 a[28:31], a[204:207], v[228:231], a[28:31]\n\t" \
  "v_exp_f32_e32 v254, v210\n\t" \
  "v_mfma_f32_16x16x32_bf16 a[32:35], a[208:211], v[224:227], a[32:35]\n\t" \
  "ds_read_b128 a[192:195], v33 offset:16384\n\t" \
  "buffer_load_dwordx4 v34, %11, s27 offen offset:2048 sc1 lds\n\t" \
  "v_mfma_f32_16x16x32_bf16 a[36:39], a[208:211], v[228:231], a[36:39]\n\t" \
  "v_add_f32_e32 v250, v250, v254\n\t" \
  "v_mfma_f32_16x16x32_bf16 a[40:43], a[212:215], v[224:227], a[40:43]\n\t" \
  "ds_read_b128 a[196:199], v33 offset:17408\n\t" \
  "v_mfma_f32_16x16x32_bf16 a[44:47], a[212:215], v[228:231], a[44:47]\n\t" \
  "v_exp_f32_e32 v255, v211\n\t" \
  "v_mfma_f32_16x16x32_bf16 a[48:51], a[216:219], v[224:227], a[48:51]\n\t" \
  "ds_read_b128 a[200:203], v33 offset:18432\n\t" \
  "v_add_f32_e32 v250, v250, v255\n\t" \
  "v_mfma_f32_16x16x32_bf16 a[52:55], a[216:219], v[228:231], a[52:55]\n\t" \
  "v_cvt_pk_bf16_f32 v233, v254, v255\n\t" \
  "v_mfma_f32_16x16x32_bf16 a[56:59], a[220:223], v[224:227], a[56:59]\n\t" \
  "ds_read_b128 a[204:207], v33 offset:19456\n\t" \
  "buffer_load_dwordx4 v34, %11, s27 offen offset:3072 sc1 lds\n\t" \
  "v_mfma_f32_16x16x32_bf16 a[60:63], a[220:223], v[228:231], a[60:63]\n\t" \
  "s_waitcnt lgkmcnt(4)\n\t" \
  "v_mfma_f32_16x16x32_bf16 a[64:67], a[224:227], v[224:227], a[64:67]\n\t" \
  "ds_read_b128 a[208:211], v33 offset:20480\n\t" \
  "v_readlane_b32 s26, v27, s25\n\t" \
  "v_mfma_f32_16x16x32_bf16 a[68:71], a[224:227], v[228:231], a[68:71]\n\t" \
  "v_exp_f32_e32 v252, v212\n\t" \
  "v_mfma_f32_16x16x32_bf16 a[72:75], a[228:231], v[224:227], a[72:75]\n\t" \
  "ds_read_b128 a[212:215], v33 offset:21504\n\t" \
  "v_readlane_b32 s27, v28, s25\n\t" \
  "v_mfma_f32_16x16x32_bf16 a[76:79], a[228:231], v[228:231], a[76:79]\n\t" \
  "v_exp_f32_e32 v253, v213\n\t" \
  "v_mfma_f32_16x16x32_bf16 a[80:83], a[232:235], v[224:227], a[80:83]\n\t" \
  "ds_read_b128 a[216:219], v33 offset:22528\n\t" \
  "s_add_u32 s25, s25, 1\n\t" \
  "v_mfma_f32_16x16x32_bf16 a[84:87], a[232:235], v[228:231], a[84:87]\n\t" \
  "v_add_f32_e32 v251, v249, v252\n\t" \
  "v_add_f32_e32 v251, v251, v253\n\t" \
  "v_mfma_f32_16x16x32_bf16 a[88:91], a[236:239], v[224:227], a[88:91]\n\t" \
  "ds_read_b128 a[220:223], v33 offset:23552\n\t" \
  "s_mov_b32 s29, s28\n\t" \
  "v_mfma_f32_16x16x32_bf16 a[92:95], a[236:239], v[228:231], a[92:95]\n\t" \
  "v_exp_f32_e32 v254, v214\n\t" \
  "v_mfma_f32_16x16x32_bf16 a[96:99], a[240:243], v[224:227], a[96:99]\n\t" \
  "ds_read_b128 a[224:227], v33 offset:24576\n\t" \
  "s_mov_b32 s28, s30\n\t" \
  "v_mfma_f32_16x16x32_bf16 a[100:103], a[240:243], v[228:231], a[100:103]\n\t" \
  "v_cvt_pk_bf16_f32 v236, v252, v253\n\t" \
  "v_add_f32_e32 v251, v251, v254\n\t" \
  "v_mfma_f32_16x16x32_bf16 a[104:107], a[244:247], v[224:227], a[104:107]\n\t" \
  "ds_read_b128 a[228:231], v33 offset:25600\n\t" \
  "v_mfma_f32_16x16x32_bf16 a[108:111], a[244:247], v[228:231], a[108:111]\n\t" \
  "v_exp_f32_e32 v255, v215\n\t" \
  "v_mfma_f32_16x16x32_bf16 a[112:115], a[248:251], v[224:227], a[112:115]\n\t" \
  "ds_read_b128 a[232:235], v33 offset:26624\n\t" \
  "v_add_f32_e32 v251, v251, v255\n\t" \
  "v_mfma_f32_16x16x32_bf16 a[116:119], a[248:251], v[228:231], a[116:119]\n\t" \
  "v_cvt_pk_bf16_f32 v237, v254, v255\n\t" \
  "v_mfma_f32_16x16x32_bf16 a[120:123], a[252:255], v[224:227], a[120:123]\n\t" \
  "ds_read_b128 a[236:239], v33 offset:27648\n\t" \
  "v_mfma_f32_16x16x32_bf16 a[124:127], a[252:255], v[228:231], a[124:127]\n\t" \
  "s_waitcnt vmcnt(16)\n\t" \
  "s_barrier\n\t" \
  "v_exp_f32_e32 v252, v216\n\t" \
  "v_exp_f32_e32 v253, v217\n\t" \
  "v_add_f32_e32 v250, v250, v252\n\t" \
  "v_add_f32_e32 v250, v250, v253\n\t" \
  "v_cvt_pk_bf16_f32 v234, v252, v253\n\t" \
  "v_exp_f32_e32 v254, v218\n\t" \
  "v_exp_f32_e32 v255, v219\n\t" \
  "v_add_f32_e32 v250, v250, v254\n\t" \
  "v_add_f32_e32 v250, v250, v255\n\t" \
  "v_cvt_pk_bf16_f32 v235, v254, v255\n\t" \
  "v_exp_f32_e32 v252, v220\n\t" \
  "v_exp_f32_e32 v253, v221\n\t" \
  "v_add_f32_e32 v251, v251, v252\n\t" \
  "v_add_f32_e32 v251, v251, v253\n\t" \
  "v_cvt_pk_bf16_f32 v238, v252, v253\n\t" \
  "v_exp_f32_e32 v254, v222\n\t" \
  "v_exp_f32_e32 v255, v223\n\t" \
  "v_add_f32_e32 v251, v251, v254\n\t" \
  "v_add_f32_e32 v251, v251, v255\n\t" \
  "v_cvt_pk_bf16_f32 v239, v254, v255\n\t" \
  "v_max_f32_e32 v38, v250, v251\n\t" \
  "v_cmp_nge_f32_e32 vcc, s45, v38\n\t" \
  "s_nop 1\n\t" \
  "s_cbranch_vccnz MK_T1_1_%=\n\t" \
  "MK_R1_1_%=:\n\t" \
  "v_mov_b32_e32 v252, v240\n\t" \
  "v_mov_b32_e32 v253, v244\n\t" \
  "v_mov_b32_e32 v254, v250\n\t" \
  "v_mov_b32_e32 v255, v251\n\t" \
  "buffer_store_dwordx4 v[252:255], %1, s[56:59], s20 offen sc1\n\t" \
  "s_waitcnt lgkmcnt(4)\n\t" \
  "v_mfma_f32_16x16x32_bf16 a[0:3], a[192:195], v[232:235], a[0:3]\n\t" \
  "s_add_u32 s30, s28, 0x8000\n\t" \
  "ds_read_b128 a[240:243], v33 offset:28672\n\t" \
  "v_accvgpr_read_b32 v128, a128\n\t" \
  "v_accvgpr_read_b32 v129, a129\n\t" \
  "v_mfma_f32_16x16x32_bf16 a[4:7], a[192:195], v[236:239], a[4:7]\n\t" \
  "s_cmp_lg_u32 s28, 0x20000\n\t" \
  "s_cselect_b32 s30, s30, 0\n\t" \
  "v_accvgpr_read_b32 v130, a130\n\t" \
  "v_mfma_f32_16x16x32_bf16 a[8:11], a[196:199], v[232:235], a[8:11]\n\t" \
  "ds_read_b128 a[244:247], v33 offset:29696\n\t" \
  "s_add_u32 m0, s29, s31\n\t" \
  "v_accvgpr_read_b32 v131, a131\n\t" \
  "v_mfma_f32_16x16x32_bf16 a[12:15], a[196:199], v[236:239], a[12:15]\n\t" \
  "v_accvgpr_read_b32 v132, a132\n\t" \
  "v_accvgpr_read_b32 v133, a133\n\t" \
  "v_accvgpr_read_b32 v134, a134\n\t" \
  "v_mfma_f32_16x16x32_bf16 a[16:19], a[200:203], v[232:235], a[16:19]\n\t" \
  "ds_read_b128 a[248:251], v33 offset:30720\n\t" \
  "buffer_load_dwordx4 v34, %11, s26 offen offset:0 sc1 lds\n\t" \
  "v_accvgpr_read_b32 v135, a135\n\t" \
  "v_mfma_f32_16x16x32_bf16 a[20:23], a[200:203], v[236:239], a[20:23]\n\t" \
  "v_accvgpr_read_b32 v136, a136\n\t" \
  "v_accvgpr_read_b32 v137, a137\n\t" \
  "v_accvgpr_read_b32 v138, a138\n\t" \
  "v_mfma_f32_16x16x32_bf16 a[24:27], a[204:207], v[232:235], a[24:27]\n\t" \
  "v_add_u32_e32 v32, s30, %1\n\t" \
  "ds_read_b128 a[252:255], v33 offset:31744\n\t" \
  "v_accvgpr_read_b32 v139, a139\n\t" \
  "v_mfma_f32_16x16x32_bf16 a[28:31], a[204:207], v[236:239], a[28:31]\n\t" \
  "buffer_load_dwordx4 v34, %11, s26 offen offset:1024 sc1 lds\n\t" \
  "v_accvgpr_read_b32 v140, a140\n\t" \
  "v_accvgpr_read_b32 v141, a141\n\t" \
  "v_mfma_f32_16x16x32_bf16 a[32:35], a[208:211], v[232:235], a[32:35]\n\t" \
  "ds_read_b128 a[192:195], v32 offset:0\n\t" \
  "v_accvgpr_read_b32 v142, a142\n\t" \
  "v_accvgpr_read_b32 v143, a143\n\t" \
  "v_accvgpr_read_b32 v144, a144\n\t" \
  "v_mfma_f32_16x16x32_bf16 a[36:39], a[208:211], v[236:239], a[36:39]\n\t" \
  "v_accvgpr_read_b32 v145, a145\n\t" \
  "v_accvgpr_read_b32 v146, a146\n\t" \
  "v_accvgpr_read_b32 v147, a147\n\t" \
  "v_mfma_f32_16x16x32_bf16 a[40:43], a[212:215], v[232:235], a[40:43]\n\t" \
  "ds_read_b128 a[196:199], v32 offset:1024\n\t" \
  "buffer_load_dwordx4 v34, %11, s26 offen offset:2048 sc1 lds\n\t" \
  "v_accvgpr_read_b32 v148, a148\n\t" \
  "v_mfma_f32_16x16x32_bf16 a[44:47], a[212:215], v[236:239], a[44:47]\n\t" \
  "v_accvgpr_read_b32 v149, a149\n\t" \
  "v_accvgpr_read_b32 v150, a150\n\t" \
  "v_accvgpr_read_b32 v151, a151\n\t" \
  "v_mfma_f32_16x16x32_bf16 a[48:51], a[216:219], v[232:235], a[48:51]\n\t" \
  "ds_read_b128 a[200:203], v32 offset:2048\n\t" \
  "v_accvgpr_read_b32 v152, a152\n\t" \
  "v_accvgpr_read_b32 v153, a153\n\t" \
  "v_mfma_f32_16x16x32_bf16 a[52:55], a[216:219], v[236:239], a[52:55]\n\t" \
  "buffer_load_dwordx4 v34, %11, s26 offen offset:3072 sc1 lds\n\t" \
  "v_accvgpr_read_b32 v154, a154\n\t" \
  "v_accvgpr_read_b32 v155, a155\n\t" \
  "v_mfma_f32_16x16x32_bf16 a[56:59], a[220:223], v[232:235], a[56:59]\n\t" \
  "ds_read_b128 a[204:207], v32 offset:3072\n\t" \
  "s_add_u32 m0, m0, 0x4000\n\t" \
  "v_accvgpr_read_b32 v156, a156\n\t" \
  "v_mfma_f32_16x16x32_bf16 a[60:63], a[220:223], v[236:239], a[60:63]\n\t" \
  "v_accvgpr_read_b32 v157, a157\n\t" \
  "v_accvgpr_read_b32 v158, a158\n\t" \
  "s_waitcnt lgkmcnt(4)\n\t" \
  "v_mfma_f32_16x16x32_bf16 a[64:67], a[224:227], v[232:235], a[64:67]\n\t" \
  "ds_read_b128 a[208:211], v32 offset:4096\n\t" \
  "buffer_load_dwordx4 v34, %11, s27 offen offset:0 sc1 lds\n\t" \
  "v_accvgpr_read_b32 v159, a159\n\t" \
  "v_accvgpr_read_b32 v160, a160\n\t" \
  "v_mfma_f32_16x16x32_bf16 a[68:71], a[224:227], v[236:239], a[68:71]\n\t" \
  "v_accvgpr_read_b32 v161, a161\n\t" \
  "v_accvgpr_read_b32 v162, a162\n\t" \
  "v_accvgpr_read_b32 v163, a163\n\t" \
  "v_mfma_f32_16x16x32_bf16 a[72:75], a[228:231], v[232:235], a[72:75]\n\t" \
  "ds_read_b128 a[212:215], v32 offset:5120\n\t" \
  "v_accvgpr_read_b32 v164, a164\n\t" \
  "v_accvgpr_read_b32 v165, a165\n\t" \
  "v_mfma_f32_16x16x32_bf16 a[76:79], a[228:231], v[236:239], a[76:79]\n\t" \
  "buffer_load_dwordx4 v34, %11, s27 offen offset:1024 sc1 lds\n\t" \
  "v_accvgpr_read_b32 v166, a166\n\t" \
  "v_accvgpr_read_b32 v167, a167\n\t" \
  "v_mfma_f32_16x16x32_bf16 a[80:83], a[232:235], v[232:235], a[80:83]\n\t" \
  "ds_read_b128 a[216:219], v32 offset:6144\n\t" \
  "v_accvgpr_read_b32 v168, a168\n\t" \
  "v_accvgpr_read_b32 v169, a169\n\t" \
  "v_mfma_f32_16x16x32_bf16 a[84:87], a[232:235], v[236:239], a[84:87]\n\t" \
  "v_accvgpr_read_b32 v170, a170\n\t" \
  "v_accvgpr_read_b32 v171, a171\n\t" \
  "v_accvgpr_read_b32 v172, a172\n\t" \
  "v_mfma_f32_16x16x32_bf16 a[88:91], a[236:239], v[232:235], a[88:91]\n\t" \
  "ds_read_b128 a[220:223], v32 offset:7168\n\t" \
  "buffer_load_dwordx4 v34, %11, s27 offen offset:2048 sc1 lds\n\t" \
  "v_accvgpr_read_b32 v173, a173\n\t" \
  "v_mfma_f32_16x16x32_bf16 a[92:95], a[236:239], v[236:239], a[92:95]\n\t" \
  "v_accvgpr_read_b32 v174, a174\n\t" \
  "v_accvgpr_read_b32 v175, a175\n\t" \
  "v_accvgpr_read_b32 v176, a176\n\t" \
  "v_mfma_f32_16x16x32_bf16 a[96:99], a[240:243], v[232:235], a[96:99]\n\t" \
  "ds_read_b128 a[224:227], v32 offset:8192\n\t" \
  "v_accvgpr_read_b32 v177, a177\n\t" \
  "v_accvgpr_read_b32 v178, a178\n\t" \
  "v_accvgpr_read_b32 v179, a179\n\t" \
  "v_mfma_f32_16x16x32_bf16 a[100:103], a[240:243], v[236:239], a[100:103]\n\t" \
  "buffer_load_dwordx4 v34, %11, s27 offen offset:3072 sc1 lds\n\t" \
  "v_accvgpr_read_b32 v180, a180\n\t" \
  "v_accvgpr_read_b32 v181, a181\n\t" \
  "v_mfma_f32_16x16x32_bf16 a[104:107], a[244:247], v[232:235], a[104:107]\n\t" \
  "ds_read_b128 a[228:231], v32 offset:9216\n\t" \
  "v_readlane_b32 s26, v27, s25\n\t" \
  "v_accvgpr_read_b32 v182, a182\n\t" \
  "v_mfma_f32_16x16x32_bf16 a[108:111], a[244:247], v[236:239], a[108:111]\n\t" \
  "v_readlane_b32 s27, v28, s25\n\t" \
  "v_accvgpr_read_b32 v183, a183\n\t" \
  "v_accvgpr_read_b32 v184, a184\n\t" \
  "v_mfma_f32_16x16x32_bf16 a[112:115], a[248:251], v[232:235], a[112:115]\n\t" \
  "ds_read_b128 a[232:235], v32 offset:10240\n\t" \
  "s_add_u32 s25, s25, 1\n\t" \
  "v_accvgpr_read_b32 v185, a185\n\t" \
  "v_mfma_f32_16x16x32_bf16 a[116:119], a[248:251], v[236:239], a[116:119]\n\t" \
  "s_mov_b32 s29, s28\n\t" \
  "v_accvgpr_read_b32 v186, a186\n\t" \
  "v_accvgpr_read_b32 v187, a187\n\t" \
  "v_mfma_f32_16x16x32_bf16 a[120:123], a[252:255], v[232:235], a[120:123]\n\t" \
  "ds_read_b128 a[236:239], v32 offset:11264\n\t" \
  "s_mov_b32 s28, s30\n\t" \
  "v_accvgpr_read_b32 v188, a188\n\t" \
  "v_mfma_f32_16x16x32_bf16 a[124:127], a[252:255], v[236:239], a[124:127]\n\t" \
  "v_accvgpr_read_b32 v189, a189\n\t" \
  "v_accvgpr_read_b32 v190, a190\n\t" \
  "v_accvgpr_read_b32 v191, a191\n\t" \
  "s_mov_b32 s46, 8\n\t" \
  "s_mov_b32 s37, s68\n\t" \
  "v_mov_b32_e32 v33, v32\n\t" \
  "s_add_u32 s43, s46, 0\n\t" \
  "v_readlane_b32 s34, v29, s43\n\t" \
  "s_add_u32 s43, s46, 1\n\t" \
  "v_readlane_b32 s88, v29, s43\n\t" \
  "s_add_u32 s43, s46, 4\n\t" \
  "v_readlane_b32 s38, v29, s43\n\t" \
  "s_add_u32 s43, s46, 5\n\t" \
  "v_readlane_b32 s39, v29, s43\n\t" \
  "s_add_u32 s43, s46, 7\n\t" \
  "v_readlane_b32 s41, v29, s43\n\t" \
  "v_accvgpr_read_b32 v72, a0\n\t" \
  "v_accvgpr_read_b32 v73, a1\n\t" \
  "v_accvgpr_read_b32 v74, a2\n\t" \
  "v_accvgpr_read_b32 v75, a3\n\t" \
  "v_accvgpr_read_b32 v76, a8\n\t" \
  "v_accvgpr_read_b32 v77, a9\n\t" \
  "v_accvgpr_read_b32 v78, a10\n\t" \
  "v_accvgpr_read_b32 v79, a11\n\t" \
  "v_cvt_pk_bf16_f32 v80, v72, v73\n\t" \
  "v_cvt_pk_bf16_f32 v81, v74, v75\n\t" \
  "v_cvt_pk_bf16_f32 v82, v76, v77\n\t" \
  "v_cvt_pk_bf16_f32 v83, v78, v79\n\t" \
  "buffer_store_dwordx4 v[80:83], %1, s[56:59], s16 offen offset:0 sc1\n\t" \
  "v_accvgpr_read_b32 v84, a4\n\t" \
  "v_accvgpr_read_b32 v85, a5\n\t" \
  "v_accvgpr_read_b32 v86, a6\n\t" \
  "v_accvgpr_read_b32 v87, a7\n\t" \
  "v_accvgpr_read_b32 v88, a12\n\t" \
  "v_accvgpr_read_b32 v89, a13\n\t" \
  "v_accvgpr_read_b32 v90, a14\n\t" \
  "v_accvgpr_read_b32 v91, a15\n\t" \
  "v_cvt_pk_bf16_f32 v92, v84, v85\n\t" \
  "v_cvt_pk_bf16_f32 v93, v86, v87\n\t" \
  "v_cvt_pk_bf16_f32 v94, v88, v89\n\t" \
  "v_cvt_pk_bf16_f32 v95, v90, v91\n\t" \
  "buffer_store_dwordx4 v[92:95], %1, s[56:59], s16 offen offset:1024 sc1\n\t" \
  "v_accvgpr_read_b32 v96, a16\n\t" \
  "v_accvgpr_read_b32 v97, a17\n\t" \
  "v_accvgpr_read_b32 v98, a18\n\t" \
  "v_accvgpr_read_b32 v99, a19\n\t" \
  "v_accvgpr_read_b32 v100, a24\n\t" \
  "v_accvgpr_read_b32 v101, a25\n\t" \
  "v_accvgpr_read_b32 v102, a26\n\t" \
  "v_accvgpr_read_b32 v103, a27\n\t" \
  "v_cvt_pk_bf16_f32 v104, v96, v97\n\t" \
  "v_cvt_pk_bf16_f32 v105, v98, v99\n\t" \
  "v_cvt_pk_bf16_f32 v106, v100, v101\n\t" \
  "v_cvt_pk_bf16_f32 v107, v102, v103\n\t" \
  "buffer_store_dwordx4 v[104:107], %1, s[56:59], s16 offen offset:2048 sc1\n\t" \
  "v_accvgpr_write_b32 a0, 0\n\t" \
  "v_accvgpr_write_b32 a1, 0\n\t" \
  "v_accvgpr_write_b32 a2, 0\n\t" \
  "v_accvgpr_write_b32 a3, 0\n\t" \
  "v_accvgpr_read_b32 v108, a20\n\t" \
  "v_accvgpr_read_b32 v109, a21\n\t" \
  "v_accvgpr_read_b32 v110, a22\n\t" \
  "v_accvgpr_read_b32 v111, a23\n\t" \
  "v_accvgpr_read_b32 v112, a28\n\t" \
  "v_accvgpr_read_b32 v113, a29\n\t" \
  "v_accvgpr_read_b32 v114, a30\n\t" \
  "v_accvgpr_read_b32 v115, a31\n\t" \
  "v_cvt_pk_bf16_f32 v116, v108, v109\n\t" \
  "v_cvt_pk_bf16_f32 v117, v110, v111\n\t" \
  "v_cvt_pk_bf16_f32 v118, v112, v113\n\t" \
  "v_cvt_pk_bf16_f32 v119, v114, v115\n\t" \
  "buffer_store_dwordx4 v[116:119], %1, s[56:59], s16 offen offset:3072 sc1\n\t" \
  "v_accvgpr_write_b32 a4, 0\n\t" \
  "v_accvgpr_write_b32 a5, 0\n\t" \
  "v_accvgpr_write_b32 a6, 0\n\t" \
  "v_accvgpr_write_b32 a7, 0\n\t" \
  "v_accvgpr_read_b32 v72, a32\n\t" \
  "v_accvgpr_read_b32 v73, a33\n\t" \
  "v_accvgpr_read_b32 v74, a34\n\t" \
  "v_accvgpr_read_b32 v75, a35\n\t" \
  "v_accvgpr_read_b32 v76, a40\n\t" \
  "v_accvgpr_read_b32 v77, a41\n\t" \
  "v_accvgpr_read_b32 v78, a42\n\t" \
  "v_accvgpr_read_b32 v79, a43\n\t" \
  "v_cvt_pk_bf16_f32 v80, v72, v73\n\t" \
  "v_cvt_pk_bf16_f32 v81, v74, v75\n\t" \
  "v_cvt_pk_bf16_f32 v82, v76, v77\n\t" \
  "v_cvt_pk_bf16_f32 v83, v78, v79\n\t" \
  "buffer_store_dwordx4 v[80:83], %1, s[56:59], s17 offen offset:0 sc1\n\t" \
  "v_accvgpr_write_b32 a8, 0\n\t" \
  "v_accvgpr_write_b32 a9, 0\n\t" \
  "v_accvgpr_write_b32 a10, 0\n\t" \
  "v_accvgpr_write_b32 a11, 0\n\t" \
  "v_accvgpr_read_b32 v84, a36\n\t" \
  "v_accvgpr_read_b32 v85, a37\n\t" \
  "v_accvgpr_read_b32 v86, a38\n\t" \
  "v_accvgpr_read_b32 v87, a39\n\t" \
  "v_accvgpr_read_b32 v88, a44\n\t" \
  "v_accvgpr_read_b32 v89, a45\n\t" \
  "v_accvgpr_read_b32 v90, a46\n\t" \
  "v_accvgpr_read_b32 v91, a47\n\t" \
  "v_cvt_pk_bf16_f32 v92, v84, v85\n\t" \
  "v_cvt_pk_bf16_f32 v93, v86, v87\n\t" \
  "v_cvt_pk_bf16_f32 v94, v88, v89\n\t" \
  "v_cvt_pk_bf16_f32 v95, v90, v91\n\t" \
  "buffer_store_dwordx4 v[92:95], %1, s[56:59], s17 offen offset:1024 sc1\n\t" \
  "v_accvgpr_write_b32 a12, 0\n\t" \
  "v_accvgpr_write_b32 a13, 0\n\t" \
  "v_accvgpr_write_b32 a14, 0\n\t" \
  "v_accvgpr_write_b32 a15, 0\n\t" \
  "v_accvgpr_read_b32 v96, a48\n\t" \
  "v_accvgpr_read_b32 v97, a49\n\t" \
  "v_accvgpr_read_b32 v98, a50\n\t" \
  "v_accvgpr_read_b32 v99, a51\n\t" \
  "v_accvgpr_read_b32 v100, a56\n\t" \
  "v_accvgpr_read_b32 v101, a57\n\t" \
  "v_accvgpr_read_b32 v102, a58\n\t" \
  "v_accvgpr_read_b32 v103, a59\n\t" \
  "v_cvt_pk_bf16_f32 v104, v96, v97\n\t" \
  "v_cvt_pk_bf16_f32 v105, v98, v99\n\t" \
  "v_cvt_pk_bf16_f32 v106, v100, v101\n\t" \
  "v_cvt_pk_bf16_f32 v107, v102, v103\n\t" \
  "buffer_store_dwordx4 v[104:107], %1, s[56:59], s17 offen offset:2048 sc1\n\t" \
  "v_accvgpr_write_b32 a16, 0\n\t" \
  "v_accvgpr_write_b32 a17, 0\n\t" \
  "v_accvgpr_write_b32 a18, 0\n\t" \
  "v_accvgpr_write_b32 a19, 0\n\t" \
  "v_accvgpr_read_b32 v108, a52\n\t" \
  "v_accvgpr_read_b32 v109, a53\n\t" \
  "v_accvgpr_read_b32 v110, a54\n\t" \
  "v_accvgpr_read_b32 v111, a55\n\t" \
  "v_accvgpr_read_b32 v112, a60\n\t" \
  "v_accvgpr_read_b32 v113, a61\n\t" \
  "v_accvgpr_read_b32 v114, a62\n\t" \
  "v_accvgpr_read_b32 v115, a63\n\t" \
  "v_cvt_pk_bf16_f32 v116, v108, v109\n\t" \
  "v_cvt_pk_bf16_f32 v117, v110, v111\n\t" \
  "v_cvt_pk_bf16_f32 v118, v112, v113\n\t" \
  "v_cvt_pk_bf16_f32 v119, v114, v115\n\t" \
  "buffer_store_dwordx4 v[116:119], %1, s[56:59], s17 offen offset:3072 sc1\n\t" \
  "v_accvgpr_write_b32 a20, 0\n\t" \
  "v_accvgpr_write_b32 a21, 0\n\t" \
  "v_accvgpr_write_b32 a22, 0\n\t" \
  "v_accvgpr_write_b32 a23, 0\n\t" \
  "v_accvgpr_read_b32 v72, a64\n\t" \
  "v_accvgpr_read_b32 v73, a65\n\t" \
  "v_accvgpr_read_b32 v74, a66\n\t" \
  "v_accvgpr_read_b32 v75, a67\n\t" \
  "v_accvgpr_read_b32 v76, a72\n\t" \
  "v_accvgpr_read_b32 v77, a73\n\t" \
  "v_accvgpr_read_b32 v78, a74\n\t" \
  "v_accvgpr_read_b32 v79, a75\n\t" \
  "v_cvt_pk_bf16_f32 v80, v72, v73\n\t" \
  "v_cvt_pk_bf16_f32 v81, v74, v75\n\t" \
  "v_cvt_pk_bf16_f32 v82, v76, v77\n\t" \
  "v_cvt_pk_bf16_f32 v83, v78, v79\n\t" \
  "buffer_store_dwordx4 v[80:83], %1, s[56:59], s18 offen offset:0 sc1\n\t" \
  "v_accvgpr_write_b32 a24, 0\n\t" \
  "v_accvgpr_write_b32 a25, 0\n\t" \
  "v_accvgpr_write_b32 a26, 0\n\t" \
  "v_accvgpr_write_b32 a27, 0\n\t" \
  "v_accvgpr_read_b32 v84, a68\n\t" \
  "v_accvgpr_read_b32 v85, a69\n\t" \
  "v_accvgpr_read_b32 v86, a70\n\t" \
  "v_accvgpr_read_b32 v87, a71\n\t" \
  "v_accvgpr_read_b32 v88, a76\n\t" \
  "v_accvgpr_read_b32 v89, a77\n\t" \
  "v_accvgpr_read_b32 v90, a78\n\t" \
  "v_accvgpr_read_b32 v91, a79\n\t" \
  "v_cvt_pk_bf16_f32 v92, v84, v85\n\t" \
  "v_cvt_pk_bf16_f32 v93, v86, v87\n\t" \
  "v_cvt_pk_bf16_f32 v94, v88, v89\n\t" \
  "v_cvt_pk_bf16_f32 v95, v90, v91\n\t" \
  "buffer_store_dwordx4 v[92:95], %1, s[56:59], s18 offen offset:1024 sc1\n\t" \
  "v_accvgpr_write_b32 a28, 0\n\t" \
  "v_accvgpr_write_b32 a29, 0\n\t" \
  "v_accvgpr_write_b32 a30, 0\n\t" \
  "v_accvgpr_write_b32 a31, 0\n\t" \
  "v_accvgpr_read_b32 v96, a80\n\t" \
  "v_accvgpr_read_b32 v97, a81\n\t" \
  "v_accvgpr_read_b32 v98, a82\n\t" \
  "v_accvgpr_read_b32 v99, a83\n\t" \
  "v_accvgpr_read_b32 v100, a88\n\t" \
  "v_accvgpr_read_b32 v101, a89\n\t" \
  "v_accvgpr_read_b32 v102, a90\n\t" \
  "v_accvgpr_read_b32 v103, a91\n\t" \
  "v_cvt_pk_bf16_f32 v104, v96, v97\n\t" \
  "v_cvt_pk_bf16_f32 v105, v98, v99\n\t" \
  "v_cvt_pk_bf16_f32 v106, v100, v101\n\t" \
  "v_cvt_pk_bf16_f32 v107, v102, v103\n\t" \
  "buffer_store_dwordx4 v[104:107], %1, s[56:59], s18 offen offset:2048 sc1\n\t" \
  "v_accvgpr_write_b32 a32, 0\n\t" \
  "v_accvgpr_write_b32 a33, 0\n\t" \
  "v_accvgpr_write_b32 a34, 0\n\t" \
  "v_accvgpr_write_b32 a35, 0\n\t" \
  "v_accvgpr_read_b32 v108, a84\n\t" \
  "v_accvgpr_read_b32 v109, a85\n\t" \
  "v_accvgpr_read_b32 v110, a86\n\t" \
  "v_accvgpr_read_b32 v111, a87\n\t" \
  "v_accvgpr_read_b32 v112, a92\n\t" \
  "v_accvgpr_read_b32 v113, a93\n\t" \
  "v_accvgpr_read_b32 v114, a94\n\t" \
  "v_accvgpr_read_b32 v115, a95\n\t" \
  "v_cvt_pk_bf16_f32 v116, v108, v109\n\t" \
  "v_cvt_pk_bf16_f32 v117, v110, v111\n\t" \
  "v_cvt_pk_bf16_f32 v118, v112, v113\n\t" \
  "v_cvt_pk_bf16_f32 v119, v114, v115\n\t" \
  "buffer_store_dwordx4 v[116:119], %1, s[56:59], s18 offen offset:3072 sc1\n\t" \
  "v_accvgpr_write_b32 a36, 0\n\t" \
  "v_accvgpr_write_b32 a37, 0\n\t" \
  "v_accvgpr_write_b32 a38, 0\n\t" \
  "v_accvgpr_write_b32 a39, 0\n\t" \
  "v_accvgpr_read_b32 v72, a96\n\t" \
  "v_accvgpr_read_b32 v73, a97\n\t" \
  "v_accvgpr_read_b32 v74, a98\n\t" \
  "v_accvgpr_read_b32 v75, a99\n\t" \
  "v_accvgpr_read_b32 v76, a104\n\t" \
  "v_accvgpr_read_b32 v77, a105\n\t" \
  "v_accvgpr_read_b32 v78, a106\n\t" \
  "v_accvgpr_read_b32 v79, a107\n\t" \
  "v_cvt_pk_bf16_f32 v80, v72, v73\n\t" \
  "v_cvt_pk_bf16_f32 v81, v74, v75\n\t" \
  "v_cvt_pk_bf16_f32 v82, v76, v77\n\t" \
  "v_cvt_pk_bf16_f32 v83, v78, v79\n\t" \
  "buffer_store_dwordx4 v[80:83], %1, s[56:59], s19 offen offset:0 sc1\n\t" \
  "v_accvgpr_write_b32 a40, 0\n\t" \
  "v_accvgpr_write_b32 a41, 0\n\t" \
  "v_accvgpr_write_b32 a42, 0\n\t" \
  "v_accvgpr_write_b32 a43, 0\n\t" \
  "v_accvgpr_read_b32 v84, a100\n\t" \
  "v_accvgpr_read_b32 v85, a101\n\t" \
  "v_accvgpr_read_b32 v86, a102\n\t" \
  "v_accvgpr_read_b32 v87, a103\n\t" \
  "v_accvgpr_read_b32 v88, a108\n\t" \
  "v_accvgpr_read_b32 v89, a109\n\t" \
  "v_accvgpr_read_b32 v90, a110\n\t" \
  "v_accvgpr_read_b32 v91, a111\n\t" \
  "v_cvt_pk_bf16_f32 v92, v84, v85\n\t" \
  "v_cvt_pk_bf16_f32 v93, v86, v87\n\t" \
  "v_cvt_pk_bf16_f32 v94, v88, v89\n\t" \
  "v_cvt_pk_bf16_f32 v95, v90, v91\n\t" \
  "buffer_store_dwordx4 v[92:95], %1, s[56:59], s19 offen offset:1024 sc1\n\t" \
  "v_accvgpr_write_b32 a44, 0\n\t" \
  "v_accvgpr_write_b32 a45, 0\n\t" \
  "v_accvgpr_write_b32 a46, 0\n\t" \
  "v_accvgpr_write_b32 a47, 0\n\t" \
  "v_accvgpr_read_b32 v96, a112\n\t" \
  "v_accvgpr_read_b32 v97, a113\n\t" \
  "v_accvgpr_read_b32 v98, a114\n\t" \
  "v_accvgpr_read_b32 v99, a115\n\t" \
  "v_accvgpr_read_b32 v100, a120\n\t" \
  "v_accvgpr_read_b32 v101, a121\n\t" \
  "v_accvgpr_read_b32 v102, a122\n\t" \
  "v_accvgpr_read_b32 v103, a123\n\t" \
  "v_cvt_pk_bf16_f32 v104, v96, v97\n\t" \
  "v_cvt_pk_bf16_f32 v105, v98, v99\n\t" \
  "v_cvt_pk_bf16_f32 v106, v100, v101\n\t" \
  "v_cvt_pk_bf16_f32 v107, v102, v103\n\t" \
  "buffer_store_dwordx4 v[104:107], %1, s[56:59], s19 offen offset:2048 sc1\n\t" \
  "v_accvgpr_write_b32 a48, 0\n\t" \
  "v_accvgpr_write_b32 a49, 0\n\t" \
  "v_accvgpr_write_b32 a50, 0\n\t" \
  "v_accvgpr_write_b32 a51, 0\n\t" \
  "v_accvgpr_read_b32 v108, a116\n\t" \
  "v_accvgpr_read_b32 v109, a117\n\t" \
  "v_accvgpr_read_b32 v110, a118\n\t" \
  "v_accvgpr_read_b32 v111, a119\n\t" \
  "v_accvgpr_read_b32 v112, a124\n\t" \
  "v_accvgpr_read_b32 v113, a125\n\t" \
  "v_accvgpr_read_b32 v114, a126\n\t" \
  "v_accvgpr_read_b32 v115, a127\n\t" \
  "v_cvt_pk_bf16_f32 v116, v108, v109\n\t" \
  "v_cvt_pk_bf16_f32 v117, v110, v111\n\t" \
  "v_cvt_pk_bf16_f32 v118, v112, v113\n\t" \
  "v_cvt_pk_bf16_f32 v119, v114, v115\n\t" \
  "buffer_store_dwordx4 v[116:119], %1, s[56:59], s19 offen offset:3072 sc1\n\t" \
  "v_accvgpr_write_b32 a52, 0\n\t" \
  "v_accvgpr_write_b32 a53, 0\n\t" \
  "v_accvgpr_write_b32 a54, 0\n\t" \
  "v_accvgpr_write_b32 a55, 0\n\t" \
  "buffer_load_dwordx4 v[124:127], %1, %11, s80 offen sc1\n\t" \
  "s_waitcnt vmcnt(16)\n\t" \
  "s_barrier\n\t" \
  "v_mov_b32_e32 v240, 0\n\t" \
  "v_mov_b32_e32 v241, 0\n\t" \
  "v_mov_b32_e32 v242, 0\n\t" \
  "v_mov_b32_e32 v243, 0\n\t" \
  "v_mov_b32_e32 v244, 0\n\t" \
  "v_mov_b32_e32 v245, 0\n\t" \
  "v_mov_b32_e32 v246, 0\n\t" \
  "v_mov_b32_e32 v247, 0\n\t" \
  "v_mov_b32_e32 v250, 0\n\t" \
  "v_mov_b32_e32 v251, 0\n\t" \
  "v_add_u32_e32 v35, s38, %2\n\t" \
  "v_cmp_gt_i32_e64 s[48:49], 0, v35\n\t" \
  "v_cmp_gt_i32_e64 s[50:51], 1, v35\n\t" \
  "v_cmp_gt_i32_e64 s[52:53], 2, v35\n\t" \
  "v_cmp_gt_i32_e64 s[54:55], 3, v35\n\t" \
  "v_cndmask_b32_e64 v192, v240, v37, s[48:49]\n\t" \
  "v_cndmask_b32_e64 v193, v241, v37, s[50:51]\n\t" \
  "v_cndmask_b32_e64 v194, v242, v37, s[52:53]\n\t" \
  "v_cndmask_b32_e64 v195, v243, v37, s[54:55]\n\t" \
  "s_sub_u32 s38, s38, 32\n\t" \
  "s_waitcnt lgkmcnt(4)\n\t" \
  "v_mfma_f32_16x16x32_bf16 v[192:195], a[192:195], v[128:131], v[192:195]\n\t" \
  "s_add_u32 s30, s28, 0x8000\n\t" \
  "ds_read_b128 a[240:243], v33 offset:12288\n\t" \
  "v_cmp_gt_i32_e64 s[48:49], 16, v35\n\t" \
  "v_accvgpr_write_b32 a56, 0\n\t" \
  "v_mfma_f32_16x16x32_bf16 v[196:199], a[192:195], v[132:135], v[244:247]\n\t" \
  "s_cmp_lg_u32 s28, 0x20000\n\t" \
  "s_cselect_b32 s30, s30, 0\n\t" \
  "v_cmp_gt_i32_e64 s[50:51], 17, v35\n\t" \
  "v_accvgpr_write_b32 a57, 0\n\t" \
  "v_mfma_f32_16x16x32_bf16 v[192:195], a[196:199], v[136:139], v[192:195]\n\t" \
  "ds_read_b128 a[244:247], v33 offset:13312\n\t" \
  "s_add_u32 m0, s29, s31\n\t" \
  "v_accvgpr_write_b32 a58, 0\n\t" \
  "v_mfma_f32_16x16x32_bf16 v[196:199], a[196:199], v[140:143], v[196:199]\n\t" \
  "v_cmp_gt_i32_e64 s[52:53], 18, v35\n\t" \
  "v_accvgpr_write_b32 a59, 0\n\t" \
  "v_accvgpr_write_b32 a60, 0\n\t" \
  "v_accvgpr_write_b32 a61, 0\n\t" \
  "v_mfma_f32_16x16x32_bf16 v[192:195], a[200:203], v[144:147], v[192:195]\n\t" \
  "ds_read_b128 a[248:251], v33 offset:14336\n\t" \
  "buffer_load_dwordx4 v34, %11, s26 offen offset:0 sc1 lds\n\t" \
  "v_accvgpr_write_b32 a62, 0\n\t" \
  "v_accvgpr_write_b32 a63, 0\n\t" \
  "v_mfma_f32_16x16x32_bf16 v[196:199], a[200:203], v[148:151], v[196:199]\n\t" \
  "v_cmp_gt_i32_e64 s[54:55], 19, v35\n\t" \
  "v_accvgpr_write_b32 a64, 0\n\t" \
  "v_accvgpr_write_b32 a65, 0\n\t" \
  "v_mfma_f32_16x16x32_bf16 v[192:195], a[204:207], v[152:155], v[192:195]\n\t" \
  "v_add_u32_e32 v32, s30, %1\n\t" \
  "ds_read_b128 a[252:255], v33 offset:15360\n\t" \
  "v_accvgpr_write_b32 a66, 0\n\t" \
  "v_accvgpr_write_b32 a67, 0\n\t" \
  "v_mfma_f32_16x16x32_bf16 v[196:199], a[204:207], v[156:159], v[196:199]\n\t" \
  "buffer_load_dwordx4 v34, %11, s26 offen offset:1024 sc1 lds\n\t" \
  "v_cndmask_b32_e64 v200, v240, v37, s[48:49]\n\t" \
  "v_accvgpr_write_b32 a68, 0\n\t" \
  "v_mfma_f32_16x16x32_bf16 v[192:195], a[208:211], v[160:163], v[192:195]\n\t" \
  "ds_read_b128 a[192:195], v32 offset:0\n\t" \
  "v_cndmask_b32_e64 v201, v241, v37, s[50:51]\n\t" \
  "v_accvgpr_write_b32 a69, 0\n\t" \
  "v_accvgpr_write_b32 a70, 0\n\t" \
  "v_mfma_f32_16x16x32_bf16 v[196:199], a[208:211], v[164:167], v[196:199]\n\t" \
  "v_accvgpr_write_b32 a71, 0\n\t" \
  "v_accvgpr_write_b32 a72, 0\n\t" \
  "v_accvgpr_write_b32 a73, 0\n\t" \
  "v_accvgpr_write_b32 a74, 0\n\t" \
  "v_mfma_f32_16x16x32_bf16 v[192:195], a[212:215], v[168:171], v[192:195]\n\t" \
  "ds_read_b128 a[196:199], v32 offset:1024\n\t" \
  "buffer_load_dwordx4 v34, %11, s26 offen offset:2048 sc1 lds\n\t" \
  "v_cndmask_b32_e64 v202, v242, v37, s[52:53]\n\t" \
  "v_mfma_f32_16x16x32_bf16 v[196:199], a[212:215], v[172:175], v[196:199]\n\t" \
  "v_accvgpr_write_b32 a75, 0\n\t" \
  "v_accvgpr_write_b32 a76, 0\n\t" \
  "v_accvgpr_write_b32 a77, 0\n\t" \
  "v_accvgpr_write_b32 a78, 0\n\t" \
  "v_mfma_f32_16x16x32_bf16 v[192:195], a[216:219], v[176:179], v[192:195]\n\t" \
  "ds_read_b128 a[200:203], v32 offset:2048\n\t" \
  "v_cndmask_b32_e64 v203, v243, v37, s[54:55]\n\t" \
  "v_accvgpr_write_b32 a79, 0\n\t" \
  "v_accvgpr_write_b32 a80, 0\n\t" \
  "v_mfma_f32_16x16x32_bf16 v[196:199], a[216:219], v[180:183], v[196:199]\n\t" \
  "buffer_load_dwordx4 v34, %11, s26 offen offset:3072 sc1 lds\n\t" \
  "v_accvgpr_write_b32 a81, 0\n\t" \
  "v_accvgpr_write_b32 a82, 0\n\t" \
  "v_mfma_f32_16x16x32_bf16 v[192:195], a[220:223], v[184:187], v[192:195]\n\t" \
  "ds_read_b128 a[204:207], v32 offset:3072\n\t" \
  "s_add_u32 m0, m0, 0x4000\n\t" \
  "v_accvgpr_write_b32 a83, 0\n\t" \
  "v_accvgpr_write_b32 a84, 0\n\t" \
  "v_mfma_f32_16x16x32_bf16 v[196:199], a[220:223], v[188:191], v[196:199]\n\t" \
  "v_accvgpr_write_b32 a85, 0\n\t" \
  "v_accvgpr_write_b32 a86, 0\n\t" \
  "s_waitcnt lgkmcnt(4)\n\t" \
  "v_mfma_f32_16x16x32_bf16 v[200:203], a[224:227], v[128:131], v[200:203]\n\t" \
  "ds_read_b128 a[208:211], v32 offset:4096\n\t" \
  "buffer_load_dwordx4 v34, %11, s27 offen offset:0 sc1 lds\n\t" \
  "v_accvgpr_write_b32 a87, 0\n\t" \
  "v_accvgpr_write_b32 a88, 0\n\t" \
  "v_mfma_f32_16x16x32_bf16 v[204:207], a[224:227], v[132:135], v[244:247]\n\t" \
  "v_accvgpr_write_b32 a89, 0\n\t" \
  "v_accvgpr_write_b32 a90, 0\n\t" \
  "v_accvgpr_write_b32 a91, 0\n\t" \
  "v_accvgpr_write_b32 a92, 0\n\t" \
  "v_mfma_f32_16x16x32_bf16 v[200:203], a[228:231], v[136:139], v[200:203]\n\t" \
  "ds_read_b128 a[212:215], v32 offset:5120\n\t" \
  "v_accvgpr_write_b32 a93, 0\n\t" \
  "v_accvgpr_write_b32 a94, 0\n\t" \
  "v_mfma_f32_16x16x32_bf16 v[204:207], a[228:231], v[140:143], v[204:207]\n\t" \
  "buffer_load_dwordx4 v34, %11, s27 offen offset:1024 sc1 lds\n\t" \
  "v_accvgpr_write_b32 a95, 0\n\t" \
  "v_accvgpr_write_b32 a96, 0\n\t" \
  "v_accvgpr_write_b32 a97, 0\n\t" \
  "v_mfma_f32_16x16x32_bf16 v[200:203], a[232:235], v[144:147], v[200:203]\n\t" \
  "ds_read_b128 a[216:219], v32 offset:6144\n\t" \
  "v_accvgpr_write_b32 a98, 0\n\t" \
  "v_accvgpr_write_b32 a99, 0\n\t" \
  "v_accvgpr_write_b32 a100, 0\n\t" \
  "v_mfma_f32_16x16x32_bf16 v[204:207], a[232:235], v[148:151], v[204:207]\n\t" \
  "v_accvgpr_write_b32 a101, 0\n\t" \
  "v_accvgpr_write_b32 a102, 0\n\t" \
  "v_accvgpr_write_b32 a103, 0\n\t" \
  "v_mfma_f32_16x16x32_bf16 v[200:203], a[236:239], v[152:155], v[200:203]\n\t" \
  "ds_read_b128 a[220:223], v32 offset:7168\n\t" \
  "buffer_load_dwordx4 v34, %11, s27 offen offset:2048 sc1 lds\n\t" \
  "v_accvgpr_write_b32 a104, 0\n\t" \
  "v_accvgpr_write_b32 a105, 0\n\t" \
  "v_mfma_f32_16x16x32_bf16 v[204:207], a[236:239], v[156:159], v[204:207]\n\t" \
  "v_accvgpr_write_b32 a106, 0\n\t" \
  "v_accvgpr_write_b32 a107, 0\n\t" \
  "v_accvgpr_write_b32 a108, 0\n\t" \
  "v_mfma_f32_16x16x32_bf16 v[200:203], a[240:243], v[160:163], v[200:203]\n\t" \
  "ds_read_b128 a[224:227], v32 offset:8192\n\t" \
  "v_accvgpr_write_b32 a109, 0\n\t" \
  "v_accvgpr_write_b32 a110, 0\n\t" \
  "v_accvgpr_write_b32 a111, 0\n\t" \
  "v_mfma_f32_16x16x32_bf16 v[204:207], a[240:243], v[164:167], v[204:207]\n\t" \
  "buffer_load_dwordx4 v34, %11, s27 offen offset:3072 sc1 lds\n\t" \
  "v_accvgpr_write_b32 a112, 0\n\t" \
  "v_accvgpr_write_b32 a113, 0\n\t" \
  "v_accvgpr_write_b32 a114, 0\n\t" \
  "v_mfma_f32_16x16x32_bf16 v[200:203], a[244:247], v[168:171], v[200:203]\n\t" \
  "ds_read_b128 a[228:231], v32 offset:9216\n\t" \
  "v_readlane_b32 s26, v27, s25\n\t" \
  "v_accvgpr_write_b32 a115, 0\n\t" \
  "v_mfma_f32_16x16x32_bf16 v[204:207], a[244:247], v[172:175], v[204:207]\n\t" \
  "v_readlane_b32 s27, v28, s25\n\t" \
  "v_accvgpr_write_b32 a116, 0\n\t" \
  "v_accvgpr_write_b32 a117, 0\n\t" \
  "v_accvgpr_write_b32 a118, 0\n\t" \
  "v_mfma_f32_16x16x32_bf16 v[200:203], a[248:251], v[176:179], v[200:203]\n\t" \
  "ds_read_b128 a[232:235], v32 offset:10240\n\t" \
  "s_add_u32 s25, s25, 1\n\t" \
  "v_accvgpr_write_b32 a119, 0\n\t" \
  "v_accvgpr_write_b32 a120, 0\n\t" \
  "v_mfma_f32_16x16x32_bf16 v[204:207], a[248:251], v[180:183], v[204:207]\n\t" \
  "s_mov_b32 s29, s28\n\t" \
  "v_accvgpr_write_b32 a121, 0\n\t" \
  "v_accvgpr_write_b32 a122, 0\n\t" \
  "v_mfma_f32_16x16x32_bf16 v[200:203], a[252:255], v[184:187], v[200:203]\n\t" \
  "ds_read_b128 a[236:239], v32 offset:11264\n\t" \
  "s_mov_b32 s28, s30\n\t" \
  "v_accvgpr_write_b32 a123, 0\n\t" \
  "v_accvgpr_write_b32 a124, 0\n\t" \
  "v_mfma_f32_16x16x32_bf16 v[204:207], a[252:255], v[188:191], v[204:207]\n\t" \
  "v_accvgpr_write_b32 a125, 0\n\t" \
  "v_accvgpr_write_b32 a126, 0\n\t" \
  "v_accvgpr_write_b32 a127, 0\n\t" \
  "s_branch MK_AT1_%=\n\t" \
  "MK_T5_%=:\n\t" \
  "s_waitcnt vmcnt(16)\n\t" \
  "s_barrier\n\t" \
  "s_waitcnt lgkmcnt(4)\n\t" \
  "v_mfma_f32_16x16x32_bf16 v[208:211], a[192:195], v[128:131], v[240:243]\n\t" \
  "s_add_u32 s30, s28, 0x8000\n\t" \
  "ds_read_b128 a[240:243], v32 offset:12288\n\t" \
  "v_mfma_f32_16x16x32_bf16 v[212:215], a[192:195], v[132:135], v[244:247]\n\t" \
  "s_cmp_lg_u32 s28, 0x20000\n\t" \
  "s_cselect_b32 s30, s30, 0\n\t" \
  "v_mfma_f32_16x16x32_bf16 v[208:211], a[196:199], v[136:139], v[208:211]\n\t" \
  "ds_read_b128 a[244:247], v32 offset:13312\n\t" \
  "s_add_u32 m0, s29, s31\n\t" \
  "v_mfma_f32_16x16x32_bf16 v[212:215], a[196:199], v[140:143], v[212:215]\n\t" \
  "v_exp_f32_e32 v252, v200\n\t" \
  "buffer_load_dwordx4 v[124:127], %1, %11, s80 offen sc1\n\t" \
  "v_mfma_f32_16x16x32_bf16 v[208:211], a[200:203], v[144:147], v[208:211]\n\t" \
  "ds_read_b128 a[248:251], v32 offset:14336\n\t" \
  "buffer_load_dwordx4 v34, %11, s26 offen offset:0 sc1 lds\n\t" \
  "v_mfma_f32_16x16x32_bf16 v[212:215], a[200:203], v[148:151], v[212:215]\n\t" \
  "v_exp_f32_e32 v253, v201\n\t" \
  "v_mfma_f32_16x16x32_bf16 v[208:211], a[204:207], v[152:155], v[208:211]\n\t" \
  "v_add_u32_e32 v33, s30, %1\n\t" \
  "ds_read_b128 a[252:255], v32 offset:15360\n\t" \
  "v_mfma_f32_16x16x32_bf16 v[212:215], a[204:207], v[156:159], v[212:215]\n\t" \
  "v_exp_f32_e32 v254, v202\n\t" \
  "v_mfma_f32_16x16x32_bf16 v[208:211], a[208:211], v[160:163], v[208:211]\n\t" \
  "ds_read_b128 a[192:195], v32 offset:16384\n\t" \
  "v_add_f32_e32 v248, v248, v252\n\t" \
  "v_mfma_f32_16x16x32_bf16 v[212:215], a[208:211], v[164:167], v[212:215]\n\t" \
  "v_add_f32_e32 v248, v248, v253\n\t" \
  "v_cvt_pk_bf16_f32 v226, v252, v253\n\t" \
  "v_mfma_f32_16x16x32_bf16 v[208:211], a[212:215], v[168:171], v[208:211]\n\t" \
  "ds_read_b128 a[196:199], v32 offset:17408\n\t" \
  "buffer_load_dwordx4 v34, %11, s26 offen offset:1024 sc1 lds\n\t" \
  "v_mfma_f32_16x16x32_bf16 v[212:215], a[212:215], v[172:175], v[212:215]\n\t" \
  "v_exp_f32_e32 v255, v203\n\t" \
  "v_mfma_f32_16x16x32_bf16 v[208:211], a[216:219], v[176:179], v[208:211]\n\t" \
  "ds_read_b128 a[200:203], v32 offset:18432\n\t" \
  "v_add_f32_e32 v248, v248, v254\n\t" \
  "v_mfma_f32_16x16x32_bf16 v[212:215], a[216:219], v[180:183], v[212:215]\n\t" \
  "v_exp_f32_e32 v252, v204\n\t" \
  "v_mfma_f32_16x16x32_bf16 v[208:211], a[220:223], v[184:187], v[208:211]\n\t" \
  "ds_read_b128 a[204:207], v32 offset:19456\n\t" \
  "v_add_f32_e32 v248, v248, v255\n\t" \
  "v_mfma_f32_16x16x32_bf16 v[212:215], a[220:223], v[188:191], v[212:215]\n\t" \
  "v_cvt_pk_bf16_f32 v227, v254, v255\n\t" \
  "s_waitcnt lgkmcnt(4)\n\t" \
  "v_mfma_f32_16x16x32_bf16 v[216:219], a[224:227], v[128:131], v[240:243]\n\t" \
  "ds_read_b128 a[208:211], v32 offset:20480\n\t" \
  "buffer_load_dwordx4 v34, %11, s26 offen offset:2048 sc1 lds\n\t" \
  "v_mfma_f32_16x16x32_bf16 v[220:223], a[224:227], v[132:135], v[244:247]\n\t" \
  "v_exp_f32_e32 v253, v205\n\t" \
  "v_mfma_f32_16x16x32_bf16 v[216:219], a[228:231], v[136:139], v[216:219]\n\t" \
  "ds_read_b128 a[212:215], v32 offset:21504\n\t" \
  "v_add_f32_e32 v249, v249, v252\n\t" \
  "v_mfma_f32_16x16x32_bf16 v[220:223], a[228:231], v[140:143], v[220:223]\n\t" \
  "v_add_f32_e32 v249, v249, v253\n\t" \
  "v_cvt_pk_bf16_f32 v230, v252, v253\n\t" \
  "v_mfma_f32_16x16x32_bf16 v[216:219], a[232:235], v[144:147], v[216:219]\n\t" \
  "ds_read_b128 a[216:219], v32 offset:22528\n\t" \
  "v_mfma_f32_16x16x32_bf16 v[220:223], a[232:235], v[148:151], v[220:223]\n\t" \
  "v_exp_f32_e32 v254, v206\n\t" \
  "v_mfma_f32_16x16x32_bf16 v[216:219], a[236:239], v[152:155], v[216:219]\n\t" \
  "ds_read_b128 a[220:223], v32 offset:23552\n\t" \
  "buffer_load_dwordx4 v34, %11, s26 offen offset:3072 sc1 lds\n\t" \
  "v_mfma_f32_16x16x32_bf16 v[220:223], a[236:239], v[156:159], v[220:223]\n\t" \
  "v_exp_f32_e32 v255, v207\n\t" \
  "v_mfma_f32_16x16x32_bf16 v[216:219], a[240:243], v[160:163], v[216:219]\n\t" \
  "ds_read_b128 a[224:227], v32 offset:24576\n\t" \
  "v_add_f32_e32 v249, v249, v254\n\t" \
  "v_mfma_f32_16x16x32_bf16 v[220:223], a[240:243], v[164:167], v[220:223]\n\t" \
  "v_add_f32_e32 v249, v249, v255\n\t" \
  "v_cvt_pk_bf16_f32 v231, v254, v255\n\t" \
  "v_mfma_f32_16x16x32_bf16 v[216:219], a[244:247], v[168:171], v[216:219]\n\t" \
  "ds_read_b128 a[228:231], v32 offset:25600\n\t" \
  "s_add_u32 m0, m0, 0x4000\n\t" \
  "v_mfma_f32_16x16x32_bf16 v[220:223], a[244:247], v[172:175], v[220:223]\n\t" \
  "v_max_f32_e32 v38, v248, v249\n\t" \
  "v_mfma_f32_16x16x32_bf16 v[216:219], a[248:251], v[176:179], v[216:219]\n\t" \
  "ds_read_b128 a[232:235], v32 offset:26624\n\t" \
  "buffer_load_dwordx4 v34, %11, s27 offen offset:0 sc1 lds\n\t" \
  "v_cmp_nge_f32_e32 vcc, s45, v38\n\t" \
  "v_mfma_f32_16x16x32_bf16 v[220:223], a[248:251], v[180:183], v[220:223]\n\t" \
  "v_mfma_f32_16x16x32_bf16 v[216:219], a[252:255], v[184:187], v[216:219]\n\t" \
  "ds_read_b128 a[236:239], v32 offset:27648\n\t" \
  "v_mfma_f32_16x16x32_bf16 v[220:223], a[252:255], v[188:191], v[220:223]\n\t" \
  "s_cbranch_vccnz MK_T0_2_%=\n\t" \
  "MK_R0_2_%=:\n\t" \
  "s_waitcnt lgkmcnt(4)\n\t" \
  "v_mfma_f32_16x16x32_bf16 a[0:3], a[192:195], v[224:227], a[0:3]\n\t" \
  "ds_read_b128 a[240:243], v32 offset:28672\n\t" \
  "buffer_store_dword v39, %12, %11, s36 offen offset:0 sc1\n\t" \
  "v_mfma_f32_16x16x32_bf16 a[4:7], a[192:195], v[228:231], a[4:7]\n\t" \
  "v_exp_f32_e32 v252, v208\n\t" \
  "v_mfma_f32_16x16x32_bf16 a[8:11], a[196:199], v[224:227], a[8:11]\n\t" \
  "ds_read_b128 a[244:247], v32 offset:29696\n\t" \
  "buffer_load_dwordx4 v34, %11, s27 offen offset:1024 sc1 lds\n\t" \
  "v_readlane_b32 s72, v29, 45\n\t" \
  "v_mfma_f32_16x16x32_bf16 a[12:15], a[196:199], v[228:231], a[12:15]\n\t" \
  "v_exp_f32_e32 v253, v209\n\t" \
  "v_mfma_f32_16x16x32_bf16 a[16:19], a[200:203], v[224:227], a[16:19]\n\t" \
  "ds_read_b128 a[248:251], v32 offset:30720\n\t" \
  "v_add_f32_e32 v250, v248, v252\n\t" \
  "v_mfma_f32_16x16x32_bf16 a[20:23], a[200:203], v[228:231], a[20:23]\n\t" \
  "v_add_f32_e32 v250, v250, v253\n\t" \
  "v_cvt_pk_bf16_f32 v232, v252, v253\n\t" \
  "v_mfma_f32_16x16x32_bf16 a[24:27], a[204:207], v[224:227], a[24:27]\n\t" \
  "ds_read_b128 a[252:255], v32 offset:31744\n\t" \
  "v_readlane_b32 s74, v29, 46\n\t" \
  "v_mfma_f32_16x16x32_bf16 a[28:31], a[204:207], v[228:231], a[28:31]\n\t" \
  "v_exp_f32_e32 v254, v210\n\t" \
  "v_mfma_f32_16x16x32_bf16 a[32:35], a[208:211], v[224:227], a[32:35]\n\t" \
  "ds_read_b128 a[192:195], v33 offset:0\n\t" \
  "buffer_load_dwordx4 v34, %11, s27 offen offset:2048 sc1 lds\n\t" \
  "v_mfma_f32_16x16x32_bf16 a[36:39], a[208:211], v[228:231], a[36:39]\n\t" \
  "v_add_f32_e32 v250, v250, v254\n\t" \
  "v_mfma_f32_16x16x32_bf16 a[40:43], a[212:215], v[224:227], a[40:43]\n\t" \
  "ds_read_b128 a[196:199], v33 offset:1024\n\t" \
  "s_mov_b32 s36, s37\n\t" \
  "v_mfma_f32_16x16x32_bf16 a[44:47], a[212:215], v[228:231], a[44:47]\n\t" \
  "v_exp_f32_e32 v255, v211\n\t" \
  "s_nop 0\n\t" \
  "v_mfma_f32_16x16x32_bf16 a[48:51], a[216:219], v[224:227], a[48:51]\n\t" \
  "ds_read_b128 a[200:203], v33 offset:2048\n\t" \
  "v_add_f32_e32 v250, v250, v255\n\t" \
  "v_mfma_f32_16x16x32_bf16 a[52:55], a[216:219], v[228:231], a[52:55]\n\t" \
  "v_cvt_pk_bf16_f32 v233, v254, v255\n\t" \
  "v_mfma_f32_16x16x32_bf16 a[56:59], a[220:223], v[224:227], a[56:59]\n\t" \
  "ds_read_b128 a[204:207], v33 offset:3072\n\t" \
  "buffer_load_dwordx4 v34, %11, s27 offen offset:3072 sc1 lds\n\t" \
  "v_mfma_f32_16x16x32_bf16 a[60:63], a[220:223], v[228:231], a[60:63]\n\t" \
  "s_add_u32 s73, s72, 188416\n\t" \
  "s_waitcnt lgkmcnt(4)\n\t" \
  "v_mfma_f32_16x16x32_bf16 a[64:67], a[224:227], v[224:227], a[64:67]\n\t" \
  "ds_read_b128 a[208:211], v33 offset:4096\n\t" \
  "v_readlane_b32 s26, v27, s25\n\t" \
  "v_mfma_f32_16x16x32_bf16 a[68:71], a[224:227], v[228:231], a[68:71]\n\t" \
  "v_exp_f32_e32 v252, v212\n\t" \
  "v_mfma_f32_16x16x32_bf16 a[72:75], a[228:231], v[224:227], a[72:75]\n\t" \
  "ds_read_b128 a[212:215], v33 offset:5120\n\t" \
  "v_readlane_b32 s27, v28, s25\n\t" \
  "v_mfma_f32_16x16x32_bf16 a[76:79], a[228:231], v[228:231], a[76:79]\n\t" \
  "v_exp_f32_e32 v253, v213\n\t" \
  "v_mfma_f32_16x16x32_bf16 a[80:83], a[232:235], v[224:227], a[80:83]\n\t" \
  "ds_read_b128 a[216:219], v33 offset:6144\n\t" \
  "s_add_u32 s25, s25, 1\n\t" \
  "s_add_u32 s76, s72, 4096\n\t" \
  "v_mfma_f32_16x16x32_bf16 a[84:87], a[232:235], v[228:231], a[84:87]\n\t" \
  "v_add_f32_e32 v251, v249, v252\n\t" \
  "v_add_f32_e32 v251, v251, v253\n\t" \
  "v_mfma_f32_16x16x32_bf16 a[88:91], a[236:239], v[224:227], a[88:91]\n\t" \
  "ds_read_b128 a[220:223], v33 offset:7168\n\t" \
  "s_mov_b32 s29, s28\n\t" \
  "v_mfma_f32_16x16x32_bf16 a[92:95], a[236:239], v[228:231], a[92:95]\n\t" \
  "v_exp_f32_e32 v254, v214\n\t" \
  "v_mfma_f32_16x16x32_bf16 a[96:99], a[240:243], v[224:227], a[96:99]\n\t" \
  "ds_read_b128 a[224:227], v33 offset:8192\n\t" \
  "s_mov_b32 s28, s30\n\t" \
  "s_add_u32 s77, s73, 4096\n\t" \
  "v_mfma_f32_16x16x32_bf16 a[100:103], a[240:243], v[228:231], a[100:103]\n\t" \
  "v_cvt_pk_bf16_f32 v236, v252, v253\n\t" \
  "v_add_f32_e32 v251, v251, v254\n\t" \
  "v_mfma_f32_16x16x32_bf16 a[104:107], a[244:247], v[224:227], a[104:107]\n\t" \
  "ds_read_b128 a[228:231], v33 offset:9216\n\t" \
  "s_mov_b32 s37, 0x60000000\n\t" \
  "v_mfma_f32_16x16x32_bf16 a[108:111], a[244:247], v[228:231], a[108:111]\n\t" \
  "v_exp_f32_e32 v255, v215\n\t" \
  "v_mfma_f32_16x16x32_bf16 a[112:115], a[248:251], v[224:227], a[112:115]\n\t" \
  "ds_read_b128 a[232:235], v33 offset:10240\n\t" \
  "v_add_f32_e32 v251, v251, v255\n\t" \
  "v_mfma_f32_16x16x32_bf16 a[116:119], a[248:251], v[228:231], a[116:119]\n\t" \
  "v_cvt_pk_bf16_f32 v237, v254, v255\n\t" \
  "buffer_load_dwordx4 v[120:123], %1, s[84:87], s89 offen\n\t" \
  "v_mfma_f32_16x16x32_bf16 a[120:123], a[252:255], v[224:227], a[120:123]\n\t" \
  "ds_read_b128 a[236:239], v33 offset:11264\n\t" \
  "v_mfma_f32_16x16x32_bf16 a[124:127], a[252:255], v[228:231], a[124:127]\n\t" \
  "s_waitcnt vmcnt(10)\n\t" \
  "s_barrier\n\t" \
  "v_cmp_ne_u32_e32 vcc, s90, v124\n\t" \
  "v_cmp_ne_u32_e64 s[48:49], s90, v125\n\t" \
  "v_cmp_ne_u32_e64 s[50:51], s90, v126\n\t" \
  "v_cmp_ne_u32_e64 s[52:53], s90, v127\n\t" \
  "s_or_b64 vcc, vcc, s[48:49]\n\t" \
  "s_or_b64 vcc, vcc, s[50:51]\n\t" \
  "s_or_b64 vcc, vcc, s[52:53]\n\t" \
  "s_cbranch_vccnz MK_F4P_%=\n\t" \
  "MK_F4R_%=:\n\t" \
  "v_add_u32_e32 v35, s38, %2\n\t" \
  "v_cmp_gt_i32_e64 s[48:49], 0, v35\n\t" \
  "v_cmp_gt_i32_e64 s[50:51], 1, v35\n\t" \
  "v_cmp_gt_i32_e64 s[52:53], 2, v35\n\t" \
  "v_cmp_gt_i32_e64 s[54:55], 3, v35\n\t" \
  "v_cndmask_b32_e64 v192, v240, v37, s[48:49]\n\t" \
  "v_cndmask_b32_e64 v193, v241, v37, s[50:51]\n\t" \
  "v_cndmask_b32_e64 v194, v242, v37, s[52:53]\n\t" \
  "v_cndmask_b32_e64 v195, v243, v37, s[54:55]\n\t" \
  "s_sub_u32 s38, s38, 32\n\t" \
  "s_waitcnt lgkmcnt(4)\n\t" \
  "v_mfma_f32_16x16x32_bf16 v[192:195], a[192:195], v[128:131], v[192:195]\n\t" \
  "s_add_u32 s30, s28, 0x8000\n\t" \
  "ds_read_b128 a[240:243], v33 offset:12288\n\t" \
  "v_cmp_gt_i32_e64 s[48:49], 16, v35\n\t" \
  "v_mfma_f32_16x16x32_bf16 v[196:199], a[192:195], v[132:135], v[244:247]\n\t" \
  "s_cmp_lg_u32 s28, 0x20000\n\t" \
  "s_cselect_b32 s30, s30, 0\n\t" \
  "v_cmp_gt_i32_e64 s[50:51], 17, v35\n\t" \
  "v_mfma_f32_16x16x32_bf16 v[192:195], a[196:199], v[136:139], v[192:195]\n\t" \
  "ds_read_b128 a[244:247], v33 offset:13312\n\t" \
  "s_add_u32 m0, s29, s31\n\t" \
  "v_mfma_f32_16x16x32_bf16 v[196:199], a[196:199], v[140:143], v[196:199]\n\t" \
  "v_cmp_gt_i32_e64 s[52:53], 18, v35\n\t" \
  "v_mfma_f32_16x16x32_bf16 v[192:195], a[200:203], v[144:147], v[192:195]\n\t" \
  "ds_read_b128 a[248:251], v33 offset:14336\n\t" \
  "buffer_load_dwordx4 v34, %11, s26 offen offset:0 sc1 lds\n\t" \
  "v_mfma_f32_16x16x32_bf16 v[196:199], a[200:203], v[148:151], v[196:199]\n\t" \
  "v_cmp_gt_i32_e64 s[54:55], 19, v35\n\t" \
  "v_mfma_f32_16x16x32_bf16 v[192:195], a[204:207], v[152:155], v[192:195]\n\t" \
  "v_add_u32_e32 v32, s30, %1\n\t" \
  "ds_read_b128 a[252:255], v33 offset:15360\n\t" \
  "buffer_load_dwordx4 a[128:131], %1, %11, s72 offen offset:0 sc1\n\t" \
  "v_mfma_f32_16x16x32_bf16 v[196:199], a[204:207], v[156:159], v[196:199]\n\t" \
  "v_cndmask_b32_e64 v200, v240, v37, s[48:49]\n\t" \
  "v_mfma_f32_16x16x32_bf16 v[192:195], a[208:211], v[160:163], v[192:195]\n\t" \
  "ds_read_b128 a[192:195], v33 offset:16384\n\t" \
  "v_cndmask_b32_e64 v201, v241, v37, s[50:51]\n\t" \
  "v_mfma_f32_16x16x32_bf16 v[196:199], a[208:211], v[164:167], v[196:199]\n\t" \
  "v_exp_f32_e32 v252, v216\n\t" \
  "v_mfma_f32_16x16x32_bf16 v[192:195], a[212:215], v[168:171], v[192:195]\n\t" \
  "ds_read_b128 a[196:199], v33 offset:17408\n\t" \
  "buffer_load_dwordx4 v34, %11, s26 offen offset:1024 sc1 lds\n\t" \
  "v_cndmask_b32_e64 v202, v242, v37, s[52:53]\n\t" \
  "v_mfma_f32_16x16x32_bf16 v[196:199], a[212:215], v[172:175], v[196:199]\n\t" \
  "v_exp_f32_e32 v253, v217\n\t" \
  "v_add_f32_e32 v250, v250, v252\n\t" \
  "v_mfma_f32_16x16x32_bf16 v[192:195], a[216:219], v[176:179], v[192:195]\n\t" \
  "ds_read_b128 a[200:203], v33 offset:18432\n\t" \
  "v_cndmask_b32_e64 v203, v243, v37, s[54:55]\n\t" \
  "v_add_f32_e32 v250, v250, v253\n\t" \
  "v_mfma_f32_16x16x32_bf16 v[196:199], a[216:219], v[180:183], v[196:199]\n\t" \
  "v_exp_f32_e32 v254, v218\n\t" \
  "v_cvt_pk_bf16_f32 v234, v252, v253\n\t" \
  "v_mfma_f32_16x16x32_bf16 v[192:195], a[220:223], v[184:187], v[192:195]\n\t" \
  "ds_read_b128 a[204:207], v33 offset:19456\n\t" \
  "v_add_f32_e32 v250, v250, v254\n\t" \
  "buffer_load_dwordx4 a[132:135], %1, %11, s73 offen offset:0 sc1\n\t" \
  "v_mfma_f32_16x16x32_bf16 v[196:199], a[220:223], v[188:191], v[196:199]\n\t" \
  "s_waitcnt lgkmcnt(4)\n\t" \
  "v_mfma_f32_16x16x32_bf16 v[200:203], a[224:227], v[128:131], v[200:203]\n\t" \
  "ds_read_b128 a[208:211], v33 offset:20480\n\t" \
  "buffer_load_dwordx4 v34, %11, s26 offen offset:2048 sc1 lds\n\t" \
  "v_mfma_f32_16x16x32_bf16 v[204:207], a[224:227], v[132:135], v[244:247]\n\t" \
  "v_exp_f32_e32 v255, v219\n\t" \
  "buffer_load_dwordx4 a[136:139], %1, %11, s72 offen offset:1024 sc1\n\t" \
  "v_mfma_f32_16x16x32_bf16 v[200:203], a[228:231], v[136:139], v[200:203]\n\t" \
  "ds_read_b128 a[212:215], v33 offset:21504\n\t" \
  "v_add_f32_e32 v250, v250, v255\n\t" \
  "v_cvt_pk_bf16_f32 v235, v254, v255\n\t" \
  "v_mfma_f32_16x16x32_bf16 v[204:207], a[228:231], v[140:143], v[204:207]\n\t" \
  "v_exp_f32_e32 v252, v220\n\t" \
  "v_mfma_f32_16x16x32_bf16 v[200:203], a[232:235], v[144:147], v[200:203]\n\t" \
  "ds_read_b128 a[216:219], v33 offset:22528\n\t" \
  "v_add_f32_e32 v251, v251, v252\n\t" \
  "buffer_load_dword v36, off, %11, s68 sc1\n\t" \
  "v_mfma_f32_16x16x32_bf16 v[204:207], a[232:235], v[148:151], v[204:207]\n\t" \
  "v_exp_f32_e32 v253, v221\n\t" \
  "v_mfma_f32_16x16x32_bf16 v[200:203], a[236:239], v[152:155], v[200:203]\n\t" \
  "ds_read_b128 a[220:223], v33 offset:23552\n\t" \
  "buffer_load_dwordx4 v34, %11, s26 offen offset:3072 sc1 lds\n\t" \
  "v_add_f32_e32 v251, v251, v253\n\t" \
  "v_mfma_f32_16x16x32_bf16 v[204:207], a[236:239], v[156:159], v[204:207]\n\t" \
  "v_exp_f32_e32 v254, v222\n\t" \
  "v_cvt_pk_bf16_f32 v238, v252, v253\n\t" \
  "v_mfma_f32_16x16x32_bf16 v[200:203], a[240:243], v[160:163], v[200:203]\n\t" \
  "ds_read_b128 a[224:227], v33 offset:24576\n\t" \
  "v_add_f32_e32 v251, v251, v254\n\t" \
  "buffer_load_dwordx4 a[140:143], %1, %11, s73 offen offset:1024 sc1\n\t" \
  "v_mfma_f32_16x16x32_bf16 v[204:207], a[240:243], v[164:167], v[204:207]\n\t" \
  "v_exp_f32_e32 v255, v223\n\t" \
  "v_mfma_f32_16x16x32_bf16 v[200:203], a[244:247], v[168:171], v[200:203]\n\t" \
  "ds_read_b128 a[228:231], v33 offset:25600\n\t" \
  "s_add_u32 m0, m0, 0x4000\n\t" \
  "v_add_f32_e32 v251, v251, v255\n\t" \
  "v_mfma_f32_16x16x32_bf16 v[204:207], a[244:247], v[172:175], v[204:207]\n\t" \
  "v_cvt_pk_bf16_f32 v239, v254, v255\n\t" \
  "v_max_f32_e32 v38, v250, v251\n\t" \
  "v_mfma_f32_16x16x32_bf16 v[200:203], a[248:251], v[176:179], v[200:203]\n\t" \
  "ds_read_b128 a[232:235], v33 offset:26624\n\t" \
  "buffer_load_dwordx4 v34, %11, s27 offen offset:0 sc1 lds\n\t" \
  "v_cmp_nge_f32_e32 vcc, s45, v38\n\t" \
  "v_mfma_f32_16x16x32_bf16 v[204:207], a[248:251], v[180:183], v[204:207]\n\t" \
  "buffer_load_dwordx4 a[144:147], %1, %11, s72 offen offset:2048 sc1\n\t" \
  "v_mfma_f32_16x16x32_bf16 v[200:203], a[252:255], v[184:187], v[200:203]\n\t" \
  "ds_read_b128 a[236:239], v33 offset:27648\n\t" \
  "v_mfma_f32_16x16x32_bf16 v[204:207], a[252:255], v[188:191], v[204:207]\n\t" \
  "s_cbranch_vccnz MK_T1_2_%=\n\t" \
  "MK_R1_2_%=:\n\t" \
  "s_waitcnt lgkmcnt(4)\n\t" \
  "v_mfma_f32_16x16x32_bf16 a[0:3], a[192:195], v[232:235], a[0:3]\n\t" \
  "ds_read_b128 a[240:243], v33 offset:28672\n\t" \
  "v_mfma_f32_16x16x32_bf16 a[4:7], a[192:195], v[236:239], a[4:7]\n\t" \
  "v_exp_f32_e32 v252, v192\n\t" \
  "v_mfma_f32_16x16x32_bf16 a[8:11], a[196:199], v[232:235], a[8:11]\n\t" \
  "ds_read_b128 a[244:247], v33 offset:29696\n\t" \
  "buffer_load_dwordx4 v34, %11, s27 offen offset:1024 sc1 lds\n\t" \
  "buffer_load_dwordx4 a[148:151], %1, %11, s73 offen offset:2048 sc1\n\t" \
  "v_mfma_f32_16x16x32_bf16 a[12:15], a[196:199], v[236:239], a[12:15]\n\t" \
  "v_exp_f32_e32 v253, v193\n\t" \
  "v_mfma_f32_16x16x32_bf16 a[16:19], a[200:203], v[232:235], a[16:19]\n\t" \
  "ds_read_b128 a[248:251], v33 offset:30720\n\t" \
  "v_add_f32_e32 v248, v250, v252\n\t" \
  "v_mfma_f32_16x16x32_bf16 a[20:23], a[200:203], v[236:239], a[20:23]\n\t" \
  "v_add_f32_e32 v248, v248, v253\n\t" \
  "v_cvt_pk_bf16_f32 v224, v252, v253\n\t" \
  "v_mfma_f32_16x16x32_bf16 a[24:27], a[204:207], v[232:235], a[24:27]\n\t" \
  "ds_read_b128 a[252:255], v33 offset:31744\n\t" \
  "v_mfma_f32_16x16x32_bf16 a[28:31], a[204:207], v[236:239], a[28:31]\n\t" \
  "v_exp_f32_e32 v254, v194\n\t" \
  "v_mfma_f32_16x16x32_bf16 a[32:35], a[208:211], v[232:235], a[32:35]\n\t" \
  "ds_read_b128 a[192:195], v32 offset:0\n\t" \
  "buffer_load_dwordx4 v34, %11, s27 offen offset:2048 sc1 lds\n\t" \
  "buffer_load_dwordx4 a[152:155], %1, %11, s72 offen offset:3072 sc1\n\t" \
  "v_mfma_f32_16x16x32_bf16 a[36:39], a[208:211], v[236:239], a[36:39]\n\t" \
  "v_add_f32_e32 v248, v248, v254\n\t" \
  "v_mfma_f32_16x16x32_bf16 a[40:43], a[212:215], v[232:235], a[40:43]\n\t" \
  "ds_read_b128 a[196:199], v32 offset:1024\n\t" \
  "v_mfma_f32_16x16x32_bf16 a[44:47], a[212:215], v[236:239], a[44:47]\n\t" \
  "v_exp_f32_e32 v255, v195\n\t" \
  "v_mfma_f32_16x16x32_bf16 a[48:51], a[216:219], v[232:235], a[48:51]\n\t" \
  "ds_read_b128 a[200:203], v32 offset:2048\n\t" \
  "v_add_f32_e32 v248, v248, v255\n\t" \
  "v_mfma_f32_16x16x32_bf16 a[52:55], a[216:219], v[236:239], a[52:55]\n\t" \
  "v_cvt_pk_bf16_f32 v225, v254, v255\n\t" \
  "buffer_load_dwordx4 a[156:159], %1, %11, s73 offen offset:3072 sc1\n\t" \
  "v_mfma_f32_16x16x32_bf16 a[56:59], a[220:223], v[232:235], a[56:59]\n\t" \
  "ds_read_b128 a[204:207], v32 offset:3072\n\t" \
  "buffer_load_dwordx4 v34, %11, s27 offen offset:3072 sc1 lds\n\t" \
  "v_mfma_f32_16x16x32_bf16 a[60:63], a[220:223], v[236:239], a[60:63]\n\t" \
  "s_waitcnt lgkmcnt(4)\n\t" \
  "v_mfma_f32_16x16x32_bf16 a[64:67], a[224:227], v[232:235], a[64:67]\n\t" \
  "ds_read_b128 a[208:211], v32 offset:4096\n\t" \
  "v_readlane_b32 s26, v27, s25\n\t" \
  "v_mfma_f32_16x16x32_bf16 a[68:71], a[224:227], v[236:239], a[68:71]\n\t" \
  "v_exp_f32_e32 v252, v196\n\t" \
  "v_mfma_f32_16x16x32_bf16 a[72:75], a[228:231], v[232:235], a[72:75]\n\t" \
  "ds_read_b128 a[212:215], v32 offset:5120\n\t" \
  "v_readlane_b32 s27, v28, s25\n\t" \
  "v_mfma_f32_16x16x32_bf16 a[76:79], a[228:231], v[236:239], a[76:79]\n\t" \
  "v_exp_f32_e32 v253, v197\n\t" \
  "buffer_load_dwordx4 a[160:163], %1, %11, s76 offen offset:0 sc1\n\t" \
  "v_mfma_f32_16x16x32_bf16 a[80:83], a[232:235], v[232:235], a[80:83]\n\t" \
  "ds_read_b128 a[216:219], v32 offset:6144\n\t" \
  "s_add_u32 s25, s25, 1\n\t" \
  "v_mfma_f32_16x16x32_bf16 a[84:87], a[232:235], v[236:239], a[84:87]\n\t" \
  "v_add_f32_e32 v249, v251, v252\n\t" \
  "v_add_f32_e32 v249, v249, v253\n\t" \
  "v_mfma_f32_16x16x32_bf16 a[88:91], a[236:239], v[232:235], a[88:91]\n\t" \
  "ds_read_b128 a[220:223], v32 offset:7168\n\t" \
  "s_mov_b32 s29, s28\n\t" \
  "v_mfma_f32_16x16x32_bf16 a[92:95], a[236:239], v[236:239], a[92:95]\n\t" \
  "v_exp_f32_e32 v254, v198\n\t" \
  "v_mfma_f32_16x16x32_bf16 a[96:99], a[240:243], v[232:235], a[96:99]\n\t" \
  "ds_read_b128 a[224:227], v32 offset:8192\n\t" \
  "s_mov_b32 s28, s30\n\t" \
  "v_mfma_f32_16x16x32_bf16 a[100:103], a[240:243], v[236:239], a[100:103]\n\t" \
  "v_cvt_pk_bf16_f32 v228, v252, v253\n\t" \
  "v_add_f32_e32 v249, v249, v254\n\t" \
  "buffer_load_dwordx4 a[164:167], %1, %11, s77 offen offset:0 sc1\n\t" \
  "v_mfma_f32_16x16x32_bf16 a[104:107], a[244:247], v[232:235], a[104:107]\n\t" \
  "ds_read_b128 a[228:231], v32 offset:9216\n\t" \
  "v_mfma_f32_16x16x32_bf16 a[108:111], a[244:247], v[236:239], a[108:111]\n\t" \
  "v_exp_f32_e32 v255, v199\n\t" \
  "v_mfma_f32_16x16x32_bf16 a[112:115], a[248:251], v[232:235], a[112:115]\n\t" \
  "ds_read_b128 a[232:235], v32 offset:10240\n\t" \
  "v_add_f32_e32 v249, v249, v255\n\t" \
  "v_mfma_f32_16x16x32_bf16 a[116:119], a[248:251], v[236:239], a[116:119]\n\t" \
  "v_cvt_pk_bf16_f32 v229, v254, v255\n\t" \
  "v_mfma_f32_16x16x32_bf16 a[120:123], a[252:255], v[232:235], a[120:123]\n\t" \
  "ds_read_b128 a[236:239], v32 offset:11264\n\t" \
  "v_mfma_f32_16x16x32_bf16 a[124:127], a[252:255], v[236:239], a[124:127]\n\t" \
  "s_branch MK_T3B_%=\n\t" \
  "MK_T3_%=:\n\t" \
  "s_mov_b32 s36, s37\n\t" \
  "v_readlane_b32 s72, v29, 45\n\t" \
  "v_readlane_b32 s74, v29, 46\n\t" \
  "s_nop 0\n\t" \
  "s_add_u32 s73, s72, 188416\n\t" \
  "s_add_u32 s76, s72, 4096\n\t" \
  "s_add_u32 s77, s73, 4096\n\t" \
  "v_cmp_ne_u32_e32 vcc, s90, v124\n\t" \
  "v_cmp_ne_u32_e64 s[48:49], s90, v125\n\t" \
  "v_cmp_ne_u32_e64 s[50:51], s90, v126\n\t" \
  "v_cmp_ne_u32_e64 s[52:53], s90, v127\n\t" \
  "s_or_b64 vcc, vcc, s[48:49]\n\t" \
  "s_or_b64 vcc, vcc, s[50:51]\n\t" \
  "s_or_b64 vcc, vcc, s[52:53]\n\t" \
  "s_cbranch_vccnz MK_F4SP_%=\n\t" \
  "MK_F4SR_%=:\n\t" \
  "buffer_load_dwordx4 a[128:131], %1, %11, s72 offen offset:0 sc1\n\t" \
  "buffer_load_dwordx4 a[132:135], %1, %11, s73 offen offset:0 sc1\n\t" \
  "buffer_load_dwordx4 a[136:139], %1, %11, s72 offen offset:1024 sc1\n\t" \
  "buffer_load_dwordx4 a[140:143], %1, %11, s73 offen offset:1024 sc1\n\t" \
  "buffer_load_dwordx4 a[144:147], %1, %11, s72 offen offset:2048 sc1\n\t" \
  "buffer_load_dwordx4 a[148:151], %1, %11, s73 offen offset:2048 sc1\n\t" \
  "buffer_load_dwordx4 a[152:155], %1, %11, s72 offen offset:3072 sc1\n\t" \
  "buffer_load_dwordx4 a[156:159], %1, %11, s73 offen offset:3072 sc1\n\t" \
  "buffer_load_dwordx4 a[160:163], %1, %11, s76 offen offset:0 sc1\n\t" \
  "buffer_load_dwordx4 a[164:167], %1, %11, s77 offen offset:0 sc1\n\t" \
  "MK_T3B_%=:\n\t" \
  "s_waitcnt vmcnt(16)\n\t" \
  "s_barrier\n\t" \
  "s_and_b32 s43, s41, 2\n\t" \
  "s_cselect_b64 s[54:55], -1, 0\n\t" \
  "v_cmp_ne_u32_e32 vcc, s90, v36\n\t" \
  "s_and_b64 vcc, vcc, s[54:55]\n\t" \
  "s_cbranch_vccnz MK_F2P_%=\n\t" \
  "MK_F2R_%=:\n\t" \
  "v_add_u32_e32 v35, s38, %2\n\t" \
  "v_cmp_gt_i32_e64 s[48:49], 0, v35\n\t" \
  "v_cmp_gt_i32_e64 s[50:51], 1, v35\n\t" \
  "v_cmp_gt_i32_e64 s[52:53], 2, v35\n\t" \
  "v_cmp_gt_i32_e64 s[54:55], 3, v35\n\t" \
  "v_cndmask_b32_e64 v208, v240, v37, s[48:49]\n\t" \
  "v_cndmask_b32_e64 v209, v241, v37, s[50:51]\n\t" \
  "v_cndmask_b32_e64 v210, v242, v37, s[52:53]\n\t" \
  "v_cndmask_b32_e64 v211, v243, v37, s[54:55]\n\t" \
  "s_sub_u32 s38, s38, 32\n\t" \
  "s_waitcnt lgkmcnt(4)\n\t" \
  "v_mfma_f32_16x16x32_bf16 v[208:211], a[192:195], v[128:131], v[208:211]\n\t" \
  "s_add_u32 s30, s28, 0x8000\n\t" \
  "ds_read_b128 a[240:243], v32 offset:12288\n\t" \
  "v_cmp_gt_i32_e64 s[48:49], 16, v35\n\t" \
  "buffer_store_dword v39, %12, %11, s36 offen offset:0 sc1\n\t" \
  "v_mfma_f32_16x16x32_bf16 v[212:215], a[192:195], v[132:135], v[244:247]\n\t" \
  "s_cmp_lg_u32 s28, 0x20000\n\t" \
  "s_cselect_b32 s30, s30, 0\n\t" \
  "v_cmp_gt_i32_e64 s[50:51], 17, v35\n\t" \
  "v_mfma_f32_16x16x32_bf16 v[208:211], a[196:199], v[136:139], v[208:211]\n\t" \
  "ds_read_b128 a[244:247], v32 offset:13312\n\t" \
  "s_add_u32 m0, s29, s31\n\t" \
  "buffer_load_dwordx4 v[40:43], %1, s[60:63], s20 offen offset:0 sc1\n\t" \
  "v_mfma_f32_16x16x32_bf16 v[212:215], a[196:199], v[140:143], v[212:215]\n\t" \
  "v_cmp_gt_i32_e64 s[52:53], 18, v35\n\t" \
  "v_mfma_f32_16x16x32_bf16 v[208:211], a[200:203], v[144:147], v[208:211]\n\t" \
  "ds_read_b128 a[248:251], v32 offset:14336\n\t" \
  "buffer_load_dwordx4 v34, %11, s26 offen offset:0 sc1 lds\n\t" \
  "buffer_load_dwordx4 v[44:47], %1, s[60:63], s16 offen offset:0 sc1\n\t" \
  "v_mfma_f32_16x16x32_bf16 v[212:215], a[200:203], v[148:151], v[212:215]\n\t" \
  "v_cmp_gt_i32_e64 s[54:55], 19, v35\n\t" \
  "v_mfma_f32_16x16x32_bf16 v[208:211], a[204:207], v[152:155], v[208:211]\n\t" \
  "v_add_u32_e32 v33, s30, %1\n\t" \
  "ds_read_b128 a[252:255], v32 offset:15360\n\t" \
  "v_mfma_f32_16x16x32_bf16 v[212:215], a[204:207], v[156:159], v[212:215]\n\t" \
  "v_cndmask_b32_e64 v216, v240, v37, s[48:49]\n\t" \
  "v_mfma_f32_16x16x32_bf16 v[208:211], a[208:211], v[160:163], v[208:211]\n\t" \
  "ds_read_b128 a[192:195], v32 offset:16384\n\t" \
  "v_cndmask_b32_e64 v217, v241, v37, s[50:51]\n\t" \
  "buffer_load_dwordx4 v[48:51], %1, s[60:63], s16 offen offset:2048 sc1\n\t" \
  "v_mfma_f32_16x16x32_bf16 v[212:215], a[208:211], v[164:167], v[212:215]\n\t" \
  "v_exp_f32_e32 v252, v200\n\t" \
  "v_mfma_f32_16x16x32_bf16 v[208:211], a[212:215], v[168:171], v[208:211]\n\t" \
  "ds_read_b128 a[196:199], v32 offset:17408\n\t" \
  "buffer_load_dwordx4 v34, %11, s26 offen offset:1024 sc1 lds\n\t" \
  "v_cndmask_b32_e64 v218, v242, v37, s[52:53]\n\t" \
  "v_mfma_f32_16x16x32_bf16 v[212:215], a[212:215], v[172:175], v[212:215]\n\t" \
  "v_exp_f32_e32 v253, v201\n\t" \
  "v_add_f32_e32 v248, v248, v252\n\t" \
  "v_mfma_f32_16x16x32_bf16 v[208:211], a[216:219], v[176:179], v[208:211]\n\t" \
  "ds_read_b128 a[200:203], v32 offset:18432\n\t" \
  "v_cndmask_b32_e64 v219, v243, v37, s[54:55]\n\t" \
  "v_add_f32_e32 v248, v248, v253\n\t" \
  "v_mfma_f32_16x16x32_bf16 v[212:215], a[216:219], v[180:183], v[212:215]\n\t" \
  "v_exp_f32_e32 v254, v202\n\t" \
  "v_cvt_pk_bf16_f32 v226, v252, v253\n\t" \
  "v_mfma_f32_16x16x32_bf16 v[208:211], a[220:223], v[184:187], v[208:211]\n\t" \
  "ds_read_b128 a[204:207], v32 offset:19456\n\t" \
  "v_add_f32_e32 v248, v248, v254\n\t" \
  "buffer_load_dwordx4 v[52:55], %1, s[60:63], s17 offen offset:0 sc1\n\t" \
  "v_mfma_f32_16x16x32_bf16 v[212:215], a[220:223], v[188:191], v[212:215]\n\t" \
  "buffer_load_dwordx4 v[56:59], %1, s[60:63], s17 offen offset:2048 sc1\n\t" \
  "s_waitcnt lgkmcnt(4)\n\t" \
  "v_mfma_f32_16x16x32_bf16 v[216:219], a[224:227], v[128:131], v[216:219]\n\t" \
  "ds_read_b128 a[208:211], v32 offset:20480\n\t" \
  "buffer_load_dwordx4 v34, %11, s26 offen offset:2048 sc1 lds\n\t" \
  "v_mfma_f32_16x16x32_bf16 v[220:223], a[224:227], v[132:135], v[244:247]\n\t" \
  "v_exp_f32_e32 v255, v203\n\t" \
  "v_mfma_f32_16x16x32_bf16 v[216:219], a[228:231], v[136:139], v[216:219]\n\t" \
  "ds_read_b128 a[212:215], v32 offset:21504\n\t" \
  "v_add_f32_e32 v248, v248, v255\n\t" \
  "v_cvt_pk_bf16_f32 v227, v254, v255\n\t" \
  "v_mfma_f32_16x16x32_bf16 v[220:223], a[228:231], v[140:143], v[220:223]\n\t" \
  "v_exp_f32_e32 v252, v204\n\t" \
  "buffer_load_dwordx4 v[60:63], %1, s[60:63], s18 offen offset:0 sc1\n\t" \
  "v_mfma_f32_16x16x32_bf16 v[216:219], a[232:235], v[144:147], v[216:219]\n\t" \
  "ds_read_b128 a[216:219], v32 offset:22528\n\t" \
  "v_add_f32_e32 v249, v249, v252\n\t" \
  "v_mfma_f32_16x16x32_bf16 v[220:223], a[232:235], v[148:151], v[220:223]\n\t" \
  "v_exp_f32_e32 v253, v205\n\t" \
  "v_mfma_f32_16x16x32_bf16 v[216:219], a[236:239], v[152:155], v[216:219]\n\t" \
  "ds_read_b128 a[220:223], v32 offset:23552\n\t" \
  "buffer_load_dwordx4 v34, %11, s26 offen offset:3072 sc1 lds\n\t" \
  "v_add_f32_e32 v249, v249, v253\n\t" \
  "v_mfma_f32_16x16x32_bf16 v[220:223], a[236:239], v[156:159], v[220:223]\n\t" \
  "v_exp_f32_e32 v254, v206\n\t" \
  "v_cvt_pk_bf16_f32 v230, v252, v253\n\t" \
  "v_mfma_f32_16x16x32_bf16 v[216:219], a[240:243], v[160:163], v[216:219]\n\t" \
  "ds_read_b128 a[224:227], v32 offset:24576\n\t" \
  "v_add_f32_e32 v249, v249, v254\n\t" \
  "buffer_load_dwordx4 v[64:67], %1, s[60:63], s18 offen offset:2048 sc1\n\t" \
  "v_mfma_f32_16x16x32_bf16 v[220:223], a[240:243], v[164:167], v[220:223]\n\t" \
  "v_exp_f32_e32 v255, v207\n\t" \
  "v_mfma_f32_16x16x32_bf16 v[216:219], a[244:247], v[168:171], v[216:219]\n\t" \
  "ds_read_b128 a[228:231], v32 offset:25600\n\t" \
  "s_add_u32 m0, m0, 0x4000\n\t" \
  "v_add_f32_e32 v249, v249, v255\n\t" \
  "v_mfma_f32_16x16x32_bf16 v[220:223], a[244:247], v[172:175], v[220:223]\n\t" \
  "v_cvt_pk_bf16_f32 v231, v254, v255\n\t" \
  "v_max_f32_e32 v38, v248, v249\n\t" \
  "buffer_load_dwordx4 v[68:71], %1, s[60:63], s19 offen offset:0 sc1\n\t" \
  "v_mfma_f32_16x16x32_bf16 v[216:219], a[248:251], v[176:179], v[216:219]\n\t" \
  "ds_read_b128 a[232:235], v32 offset:26624\n\t" \
  "buffer_load_dwordx4 v34, %11, s27 offen offset:0 sc1 lds\n\t" \
  "v_cmp_nge_f32_e32 vcc, s45, v38\n\t" \
  "v_mfma_f32_16x16x32_bf16 v[220:223], a[248:251], v[180:183], v[220:223]\n\t" \
  "v_mfma_f32_16x16x32_bf16 v[216:219], a[252:255], v[184:187], v[216:219]\n\t" \
  "ds_read_b128 a[236:239], v32 offset:27648\n\t" \
  "buffer_load_dwordx4 v[72:75], %1, s[60:63], s19 offen offset:2048 sc1\n\t" \
  "v_mfma_f32_16x16x32_bf16 v[220:223], a[252:255], v[188:191], v[220:223]\n\t" \
  "s_cbranch_vccnz MK_T0_3_%=\n\t" \
  "MK_R0_3_%=:\n\t" \
  "s_waitcnt lgkmcnt(4)\n\t" \
  "v_mfma_f32_16x16x32_bf16 a[0:3], a[192:195], v[224:227], a[0:3]\n\t" \
  "ds_read_b128 a[240:243], v32 offset:28672\n\t" \
  "v_mfma_f32_16x16x32_bf16 a[4:7], a[192:195], v[228:231], a[4:7]\n\t" \
  "v_exp_f32_e32 v252, v208\n\t" \
  "buffer_load_dwordx4 v[76:79], %1, s[60:63], s16 offen offset:1024 sc1\n\t" \
  "v_mfma_f32_16x16x32_bf16 a[8:11], a[196:199], v[224:227], a[8:11]\n\t" \
  "ds_read_b128 a[244:247], v32 offset:29696\n\t" \
  "buffer_load_dwordx4 v34, %11, s27 offen offset:1024 sc1 lds\n\t" \
  "v_readlane_b32 s80, v29, 47\n\t" \
  "v_mfma_f32_16x16x32_bf16 a[12:15], a[196:199], v[228:231], a[12:15]\n\t" \
  "v_exp_f32_e32 v253, v209\n\t" \
  "v_mfma_f32_16x16x32_bf16 a[16:19], a[200:203], v[224:227], a[16:19]\n\t" \
  "ds_read_b128 a[248:251], v32 offset:30720\n\t" \
  "v_add_f32_e32 v250, v248, v252\n\t" \
  "v_mfma_f32_16x16x32_bf16 a[20:23], a[200:203], v[228:231], a[20:23]\n\t" \
  "v_add_f32_e32 v250, v250, v253\n\t" \
  "v_cvt_pk_bf16_f32 v232, v252, v253\n\t" \
  "buffer_load_dwordx4 v[80:83], %1, s[60:63], s16 offen offset:3072 sc1\n\t" \
  "v_mfma_f32_16x16x32_bf16 a[24:27], a[204:207], v[224:227], a[24:27]\n\t" \
  "ds_read_b128 a[252:255], v32 offset:31744\n\t" \
  "v_mfma_f32_16x16x32_bf16 a[28:31], a[204:207], v[228:231], a[28:31]\n\t" \
  "v_exp_f32_e32 v254, v210\n\t" \
  "v_readlane_b32 s81, v29, 48\n\t" \
  "v_mfma_f32_16x16x32_bf16 a[32:35], a[208:211], v[224:227], a[32:35]\n\t" \
  "ds_read_b128 a[192:195], v33 offset:0\n\t" \
  "buffer_load_dwordx4 v34, %11, s27 offen offset:2048 sc1 lds\n\t" \
  "v_mfma_f32_16x16x32_bf16 a[36:39], a[208:211], v[228:231], a[36:39]\n\t" \
  "v_add_f32_e32 v250, v250, v254\n\t" \
  "buffer_load_dwordx4 v[84:87], %1, s[60:63], s17 offen offset:1024 sc1\n\t" \
  "v_mfma_f32_16x16x32_bf16 a[40:43], a[212:215], v[224:227], a[40:43]\n\t" \
  "ds_read_b128 a[196:199], v33 offset:1024\n\t" \
  "v_mfma_f32_16x16x32_bf16 a[44:47], a[212:215], v[228:231], a[44:47]\n\t" \
  "v_exp_f32_e32 v255, v211\n\t" \
  "v_mfma_f32_16x16x32_bf16 a[48:51], a[216:219], v[224:227], a[48:51]\n\t" \
  "ds_read_b128 a[200:203], v33 offset:2048\n\t" \
  "v_add_f32_e32 v250, v250, v255\n\t" \
  "buffer_load_dwordx4 v[88:91], %1, s[60:63], s17 offen offset:3072 sc1\n\t" \
  "v_mfma_f32_16x16x32_bf16 a[52:55], a[216:219], v[228:231], a[52:55]\n\t" \
  "v_cvt_pk_bf16_f32 v233, v254, v255\n\t" \
  "v_readlane_b32 s47, v29, 49\n\t" \
  "v_mfma_f32_16x16x32_bf16 a[56:59], a[220:223], v[224:227], a[56:59]\n\t" \
  "ds_read_b128 a[204:207], v33 offset:3072\n\t" \
  "buffer_load_dwordx4 v34, %11, s27 offen offset:3072 sc1 lds\n\t" \
  "v_mfma_f32_16x16x32_bf16 a[60:63], a[220:223], v[228:231], a[60:63]\n\t" \
  "s_waitcnt lgkmcnt(4)\n\t" \
  "v_mfma_f32_16x16x32_bf16 a[64:67], a[224:227], v[224:227], a[64:67]\n\t" \
  "ds_read_b128 a[208:211], v33 offset:4096\n\t" \
  "v_readlane_b32 s26, v27, s25\n\t" \
  "buffer_load_dwordx4 v[92:95], %1, s[60:63], s18 offen offset:1024 sc1\n\t" \
  "v_mfma_f32_16x16x32_bf16 a[68:71], a[224:227], v[228:231], a[68:71]\n\t" \
  "v_exp_f32_e32 v252, v212\n\t" \
  "v_mfma_f32_16x16x32_bf16 a[72:75], a[228:231], v[224:227], a[72:75]\n\t" \
  "ds_read_b128 a[212:215], v33 offset:5120\n\t" \
  "v_readlane_b32 s27, v28, s25\n\t" \
  "v_mfma_f32_16x16x32_bf16 a[76:79], a[228:231], v[228:231], a[76:79]\n\t" \
  "v_exp_f32_e32 v253, v213\n\t" \
  "s_add_u32 s75, s74, 188416\n\t" \
  "v_mfma_f32_16x16x32_bf16 a[80:83], a[232:235], v[224:227], a[80:83]\n\t" \
  "ds_read_b128 a[216:219], v33 offset:6144\n\t" \
  "s_add_u32 s25, s25, 1\n\t" \
  "buffer_load_dwordx4 v[96:99], %1, s[60:63], s18 offen offset:3072 sc1\n\t" \
  "v_mfma_f32_16x16x32_bf16 a[84:87], a[232:235], v[228:231], a[84:87]\n\t" \
  "v_add_f32_e32 v251, v249, v252\n\t" \
  "v_add_f32_e32 v251, v251, v253\n\t" \
  "s_mul_i32 s35, s91, 188416\n\t" \
  "v_mfma_f32_16x16x32_bf16 a[88:91], a[236:239], v[224:227], a[88:91]\n\t" \
  "ds_read_b128 a[220:223], v33 offset:7168\n\t" \
  "s_mov_b32 s29, s28\n\t" \
  "s_add_u32 s35, s35, s39\n\t" \
  "v_mfma_f32_16x16x32_bf16 a[92:95], a[236:239], v[228:231], a[92:95]\n\t" \
  "v_exp_f32_e32 v254, v214\n\t" \
  "buffer_load_dwordx4 v[100:103], %1, s[60:63], s19 offen offset:1024 sc1\n\t" \
  "v_mfma_f32_16x16x32_bf16 a[96:99], a[240:243], v[224:227], a[96:99]\n\t" \
  "ds_read_b128 a[224:227], v33 offset:8192\n\t" \
  "s_mov_b32 s28, s30\n\t" \
  "s_add_u32 s69, s39, 4096\n\t" \
  "v_mfma_f32_16x16x32_bf16 a[100:103], a[240:243], v[228:231], a[100:103]\n\t" \
  "v_cvt_pk_bf16_f32 v236, v252, v253\n\t" \
  "v_add_f32_e32 v251, v251, v254\n\t" \
  "s_add_u32 s70, s35, 4096\n\t" \
  "v_mfma_f32_16x16x32_bf16 a[104:107], a[244:247], v[224:227], a[104:107]\n\t" \
  "ds_read_b128 a[228:231], v33 offset:9216\n\t" \
  "v_mfma_f32_16x16x32_bf16 a[108:111], a[244:247], v[228:231], a[108:111]\n\t" \
  "v_exp_f32_e32 v255, v215\n\t" \
  "buffer_load_dwordx4 v[104:107], %1, s[60:63], s19 offen offset:3072 sc1\n\t" \
  "v_mfma_f32_16x16x32_bf16 a[112:115], a[248:251], v[224:227], a[112:115]\n\t" \
  "ds_read_b128 a[232:235], v33 offset:10240\n\t" \
  "v_add_f32_e32 v251, v251, v255\n\t" \
  "s_and_b32 s43, s41, 2\n\ts_cselect_b64 s[92:93], -1, 0\n\t" \
  "v_mfma_f32_16x16x32_bf16 a[116:119], a[248:251], v[228:231], a[116:119]\n\t" \
  "v_cvt_pk_bf16_f32 v237, v254, v255\n\t" \
  "s_lshl_b32 s43, s91, 4\n\t" \
  "v_mfma_f32_16x16x32_bf16 a[120:123], a[252:255], v[224:227], a[120:123]\n\t" \
  "ds_read_b128 a[236:239], v33 offset:11264\n\t" \
  "s_add_u32 s38, s38, s43\n\t" \
  "v_mfma_f32_16x16x32_bf16 a[124:127], a[252:255], v[228:231], a[124:127]\n\t" \
  "s_waitcnt vmcnt(16)\n\t" \
  "s_barrier\n\t" \
  "v_add_u32_e32 v35, s38, %2\n\t" \
  "v_cmp_gt_i32_e64 s[48:49], 0, v35\n\t" \
  "v_cmp_gt_i32_e64 s[50:51], 1, v35\n\t" \
  "v_cmp_gt_i32_e64 s[52:53], 2, v35\n\t" \
  "v_cmp_gt_i32_e64 s[54:55], 3, v35\n\t" \
  "v_cndmask_b32_e64 v196, v244, v37, s[48:49]\n\t" \
  "v_cndmask_b32_e64 v197, v245, v37, s[50:51]\n\t" \
  "v_cndmask_b32_e64 v198, v246, v37, s[52:53]\n\t" \
  "v_cndmask_b32_e64 v199, v247, v37, s[54:55]\n\t" \
  "s_sub_u32 s38, s38, 32\n\t" \
  "s_waitcnt lgkmcnt(4)\n\t" \
  "v_mfma_f32_16x16x32_bf16 v[196:199], a[192:195], v[132:135], v[196:199]\n\t" \
  "s_add_u32 s30, s28, 0x8000\n\t" \
  "ds_read_b128 a[240:243], v33 offset:12288\n\t" \
  "v_cmp_gt_i32_e64 s[48:49], 16, v35\n\t" \
  "v_cmp_gt_i32_e64 s[50:51], 17, v35\n\t" \
  "v_mfma_f32_16x16x32_bf16 v[196:199], a[196:199], v[140:143], v[196:199]\n\t" \
  "s_cmp_lg_u32 s28, 0x20000\n\t" \
  "s_cselect_b32 s30, s30, 0\n\t" \
  "ds_read_b128 a[244:247], v33 offset:13312\n\t" \
  "v_cmp_gt_i32_e64 s[52:53], 18, v35\n\t" \
  "v_mfma_f32_16x16x32_bf16 v[196:199], a[200:203], v[148:151], v[196:199]\n\t" \
  "ds_read_b128 a[248:251], v33 offset:14336\n\t" \
  "s_add_u32 m0, s29, s31\n\t" \
  "v_cmp_gt_i32_e64 s[54:55], 19, v35\n\t" \
  "v_exp_f32_e32 v252, v216\n\t" \
  "v_mfma_f32_16x16x32_bf16 v[196:199], a[204:207], v[156:159], v[196:199]\n\t" \
  "ds_read_b128 a[252:255], v33 offset:15360\n\t" \
  "v_cndmask_b32_e64 v204, v244, v37, s[48:49]\n\t" \
  "v_cndmask_b32_e64 v205, v245, v37, s[50:51]\n\t" \
  "v_add_f32_e32 v250, v250, v252\n\t" \
  "v_mfma_f32_16x16x32_bf16 v[196:199], a[208:211], v[164:167], v[196:199]\n\t" \
  "ds_read_b128 a[192:195], v33 offset:16384\n\t" \
  "buffer_load_dwordx4 v34, %11, s26 offen offset:0 sc1 lds\n\t" \
  "v_cndmask_b32_e64 v206, v246, v37, s[52:53]\n\t" \
  "v_mfma_f32_16x16x32_bf16 v[196:199], a[212:215], v[172:175], v[196:199]\n\t" \
  "ds_read_b128 a[196:199], v33 offset:17408\n\t" \
  "v_cndmask_b32_e64 v207, v247, v37, s[54:55]\n\t" \
  "v_exp_f32_e32 v253, v217\n\t" \
  "v_mfma_f32_16x16x32_bf16 v[196:199], a[216:219], v[180:183], v[196:199]\n\t" \
  "v_add_u32_e32 v32, s30, %1\n\t" \
  "ds_read_b128 a[200:203], v33 offset:18432\n\t" \
  "v_exp_f32_e32 v254, v218\n\t" \
  "v_add_f32_e32 v250, v250, v253\n\t" \
  "v_mfma_f32_16x16x32_bf16 v[196:199], a[220:223], v[188:191], v[196:199]\n\t" \
  "ds_read_b128 a[204:207], v33 offset:19456\n\t" \
  "v_exp_f32_e32 v255, v219\n\t" \
  "v_cvt_pk_bf16_f32 v234, v252, v253\n\t" \
  "s_waitcnt lgkmcnt(4)\n\t" \
  "v_mfma_f32_16x16x32_bf16 v[204:207], a[224:227], v[132:135], v[204:207]\n\t" \
  "ds_read_b128 a[208:211], v33 offset:20480\n\t" \
  "buffer_load_dwordx4 v34, %11, s26 offen offset:1024 sc1 lds\n\t" \
  "v_exp_f32_e32 v252, v220\n\t" \
  "v_add_f32_e32 v250, v250, v254\n\t" \
  "v_mfma_f32_16x16x32_bf16 v[204:207], a[228:231], v[140:143], v[204:207]\n\t" \
  "ds_read_b128 a[212:215], v33 offset:21504\n\t" \
  "v_exp_f32_e32 v253, v221\n\t" \
  "v_add_f32_e32 v250, v250, v255\n\t" \
  "v_cvt_pk_bf16_f32 v235, v254, v255\n\t" \
  "v_mfma_f32_16x16x32_bf16 v[204:207], a[232:235], v[148:151], v[204:207]\n\t" \
  "ds_read_b128 a[216:219], v33 offset:22528\n\t" \
  "v_exp_f32_e32 v254, v222\n\t" \
  "v_add_f32_e32 v251, v251, v252\n\t" \
  "v_add_f32_e32 v251, v251, v253\n\t" \
  "v_mfma_f32_16x16x32_bf16 v[204:207], a[236:239], v[156:159], v[204:207]\n\t" \
  "ds_read_b128 a[220:223], v33 offset:23552\n\t" \
  "v_exp_f32_e32 v255, v223\n\t" \
  "v_cvt_pk_bf16_f32 v238, v252, v253\n\t" \
  "v_add_f32_e32 v251, v251, v254\n\t" \
  "v_mfma_f32_16x16x32_bf16 v[204:207], a[240:243], v[164:167], v[204:207]\n\t" \
  "ds_read_b128 a[224:227], v33 offset:24576\n\t" \
  "buffer_load_dwordx4 v34, %11, s26 offen offset:2048 sc1 lds\n\t" \
  "v_add_f32_e32 v251, v251, v255\n\t" \
  "v_cvt_pk_bf16_f32 v239, v254, v255\n\t" \
  "v_max_f32_e32 v38, v250, v251\n\t" \
  "v_mfma_f32_16x16x32_bf16 v[204:207], a[244:247], v[172:175], v[204:207]\n\t" \
  "ds_read_b128 a[228:231], v33 offset:25600\n\t" \
  "v_cmp_nge_f32_e32 vcc, s45, v38\n\t" \
  "v_mfma_f32_16x16x32_bf16 v[204:207], a[248:251], v[180:183], v[204:207]\n\t" \
  "ds_read_b128 a[232:235], v33 offset:26624\n\t" \
  "v_mfma_f32_16x16x32_bf16 v[204:207], a[252:255], v[188:191], v[204:207]\n\t" \
  "ds_read_b128 a[236:239], v33 offset:27648\n\t" \
  "s_cbranch_vccnz MK_T1_3_%=\n\t" \
  "MK_R1_3_%=:\n\t" \
  "s_waitcnt lgkmcnt(4)\n\t" \
  "v_mfma_f32_16x16x32_bf16 a[0:3], a[192:195], v[232:235], a[0:3]\n\t" \
  "ds_read_b128 a[240:243], v33 offset:28672\n\t" \
  "buffer_load_dwordx4 v34, %11, s26 offen offset:3072 sc1 lds\n\t" \
  "v_mfma_f32_16x16x32_bf16 a[4:7], a[192:195], v[236:239], a[4:7]\n\t" \
  "v_exp_f32_e32 v252, v196\n\t" \
  "s_waitcnt vmcnt(28)\n\t" \
  "v_mfma_f32_16x16x32_bf16 a[8:11], a[196:199], v[232:235], a[8:11]\n\t" \
  "ds_read_b128 a[244:247], v33 offset:29696\n\t" \
  "s_add_u32 m0, m0, 0x4000\n\t" \
  "v_add_f32_e32 v249, v251, v252\n\t" \
  "buffer_load_dwordx4 a[168:171], %1, %11, s76 offen offset:1024 sc1\n\t" \
  "v_mfma_f32_16x16x32_bf16 a[12:15], a[196:199], v[236:239], a[12:15]\n\t" \
  "v_mov_b32_e32 v193, 0x7f000000\n\t" \
  "v_cndmask_b32_e64 v40, v193, v40, s[92:93]\n\t" \
  "v_mfma_f32_16x16x32_bf16 a[16:19], a[200:203], v[232:235], a[16:19]\n\t" \
  "ds_read_b128 a[248:251], v33 offset:30720\n\t" \
  "buffer_load_dwordx4 v34, %11, s27 offen offset:0 sc1 lds\n\t" \
  "v_min_f32_e32 v193, v240, v40\n\t" \
  "v_mfma_f32_16x16x32_bf16 a[20:23], a[200:203], v[236:239], a[20:23]\n\t" \
  "v_sub_f32_e32 v192, v193, v240\n\t" \
  "v_mfma_f32_16x16x32_bf16 a[24:27], a[204:207], v[232:235], a[24:27]\n\t" \
  "ds_read_b128 a[252:255], v33 offset:31744\n\t" \
  "buffer_load_dwordx4 a[172:175], %1, %11, s77 offen offset:1024 sc1\n\t" \
  "v_sub_f32_e32 v194, v193, v40\n\t" \
  "v_mfma_f32_16x16x32_bf16 a[28:31], a[204:207], v[236:239], a[28:31]\n\t" \
  "v_exp_f32_e32 v253, v197\n\t" \
  "v_mfma_f32_16x16x32_bf16 a[32:35], a[208:211], v[232:235], a[32:35]\n\t" \
  "ds_read_b128 a[192:195], v32 offset:0\n\t" \
  "buffer_load_dwordx4 v34, %11, s27 offen offset:1024 sc1 lds\n\t" \
  "v_add_f32_e32 v249, v249, v253\n\t" \
  "v_cvt_pk_bf16_f32 v228, v252, v253\n\t" \
  "v_mfma_f32_16x16x32_bf16 a[36:39], a[208:211], v[236:239], a[36:39]\n\t" \
  "v_exp_f32_e32 v192, v192\n\t" \
  "v_mfma_f32_16x16x32_bf16 a[40:43], a[212:215], v[232:235], a[40:43]\n\t" \
  "ds_read_b128 a[196:199], v32 offset:1024\n\t" \
  "v_exp_f32_e32 v194, v194\n\t" \
  "v_mfma_f32_16x16x32_bf16 a[44:47], a[212:215], v[236:239], a[44:47]\n\t" \
  "buffer_load_dwordx4 a[176:179], %1, %11, s76 offen offset:2048 sc1\n\t" \
  "v_mul_f32_e32 v193, v250, v192\n\t" \
  "v_fmac_f32_e32 v193, v42, v194\n\t" \
  "v_mfma_f32_16x16x32_bf16 a[48:51], a[216:219], v[232:235], a[48:51]\n\t" \
  "ds_read_b128 a[200:203], v32 offset:2048\n\t" \
  "buffer_load_dwordx4 v34, %11, s27 offen offset:2048 sc1 lds\n\t" \
  "v_mov_b32_e32 v195, v193\n\t" \
  "v_mfma_f32_16x16x32_bf16 a[52:55], a[216:219], v[236:239], a[52:55]\n\t" \
  "v_mfma_f32_16x16x32_bf16 a[56:59], a[220:223], v[232:235], a[56:59]\n\t" \
  "ds_read_b128 a[204:207], v32 offset:3072\n\t" \
  "s_nop 1\n\t" \
  "v_mfma_f32_16x16x32_bf16 a[60:63], a[220:223], v[236:239], a[60:63]\n\t" \
  "v_permlane16_swap_b32_e32 v193, v195\n\t" \
  "s_waitcnt lgkmcnt(4)\n\t" \
  "v_mfma_f32_16x16x32_bf16 a[64:67], a[224:227], v[232:235], a[64:67]\n\t" \
  "ds_read_b128 a[208:211], v32 offset:4096\n\t" \
  "buffer_load_dwordx4 v34, %11, s27 offen offset:3072 sc1 lds\n\t" \
  "buffer_load_dwordx4 a[180:183], %1, %11, s77 offen offset:2048 sc1\n\t" \
  "v_mfma_f32_16x16x32_bf16 a[68:71], a[224:227], v[236:239], a[68:71]\n\t" \
  "v_exp_f32_e32 v254, v198\n\t" \
  "s_nop 0\n\t" \
  "v_mfma_f32_16x16x32_bf16 a[72:75], a[228:231], v[232:235], a[72:75]\n\t" \
  "ds_read_b128 a[212:215], v32 offset:5120\n\t" \
  "v_readlane_b32 s26, v27, s25\n\t" \
  "v_add_f32_e32 v249, v249, v254\n\t" \
  "v_mfma_f32_16x16x32_bf16 a[76:79], a[228:231], v[236:239], a[76:79]\n\t" \
  "v_add_f32_e32 v193, v193, v195\n\t" \
  "v_mov_b32_e32 v195, v193\n\t" \
  "v_mfma_f32_16x16x32_bf16 a[80:83], a[232:235], v[232:235], a[80:83]\n\t" \
  "ds_read_b128 a[216:219], v32 offset:6144\n\t" \
  "v_readlane_b32 s27, v28, s25\n\t" \
  "s_nop 1\n\t" \
  "v_mfma_f32_16x16x32_bf16 a[84:87], a[232:235], v[236:239], a[84:87]\n\t" \
  "buffer_load_dwordx4 a[184:187], %1, %11, s76 offen offset:3072 sc1\n\t" \
  "v_permlane32_swap_b32_e32 v193, v195\n\t" \
  "v_mfma_f32_16x16x32_bf16 a[88:91], a[236:239], v[232:235], a[88:91]\n\t" \
  "ds_read_b128 a[220:223], v32 offset:7168\n\t" \
  "s_add_u32 s25, s25, 1\n\t" \
  "s_nop 0\n\t" \
  "v_mfma_f32_16x16x32_bf16 a[92:95], a[236:239], v[236:239], a[92:95]\n\t" \
  "v_exp_f32_e32 v255, v199\n\t" \
  "v_add_f32_e32 v193, v193, v195\n\t" \
  "v_mfma_f32_16x16x32_bf16 a[96:99], a[240:243], v[232:235], a[96:99]\n\t" \
  "ds_read_b128 a[224:227], v32 offset:8192\n\t" \
  "s_mov_b32 s29, s28\n\t" \
  "v_add_f32_e32 v249, v249, v255\n\t" \
  "v_cvt_pk_bf16_f32 v229, v254, v255\n\t" \
  "v_mfma_f32_16x16x32_bf16 a[100:103], a[240:243], v[236:239], a[100:103]\n\t" \
  "v_rcp_f32_e32 v193, v193\n\t" \
  "v_mfma_f32_16x16x32_bf16 a[104:107], a[244:247], v[232:235], a[104:107]\n\t" \
  "ds_read_b128 a[228:231], v32 offset:9216\n\t" \
  "s_mov_b32 s28, s30\n\t" \
  "buffer_load_dwordx4 a[188:191], %1, %11, s77 offen offset:3072 sc1\n\t" \
  "v_mfma_f32_16x16x32_bf16 a[108:111], a[244:247], v[236:239], a[108:111]\n\t" \
  "s_nop 0\n\t" \
  "v_mul_f32_e32 v192, v192, v193\n\t" \
  "v_mfma_f32_16x16x32_bf16 a[112:115], a[248:251], v[232:235], a[112:115]\n\t" \
  "ds_read_b128 a[232:235], v32 offset:10240\n\t" \
  "v_mul_f32_e32 v194, v194, v193\n\t" \
  "v_mfma_f32_16x16x32_bf16 a[116:119], a[248:251], v[236:239], a[116:119]\n\t" \
  "v_mov_b32_e32 v193, v192\n\t" \
  "v_mfma_f32_16x16x32_bf16 a[120:123], a[252:255], v[232:235], a[120:123]\n\t" \
  "ds_read_b128 a[236:239], v32 offset:11264\n\t" \
  "v_mov_b32_e32 v195, v194\n\t" \
  "v_mfma_f32_16x16x32_bf16 a[124:127], a[252:255], v[236:239], a[124:127]\n\t" \
  "s_waitcnt vmcnt(16)\n\t" \
  "s_barrier\n\t" \
  "v_add_u32_e32 v35, s38, %2\n\t" \
  "v_cmp_gt_i32_e64 s[48:49], 0, v35\n\t" \
  "v_cmp_gt_i32_e64 s[50:51], 1, v35\n\t" \
  "v_cmp_gt_i32_e64 s[52:53], 2, v35\n\t" \
  "v_cmp_gt_i32_e64 s[54:55], 3, v35\n\t" \
  "v_cndmask_b32_e64 v212, v244, v37, s[48:49]\n\t" \
  "v_cndmask_b32_e64 v213, v245, v37, s[50:51]\n\t" \
  "v_cndmask_b32_e64 v214, v246, v37, s[52:53]\n\t" \
  "v_cndmask_b32_e64 v215, v247, v37, s[54:55]\n\t" \
  "s_sub_u32 s38, s38, 32\n\t" \
  "s_waitcnt lgkmcnt(4)\n\t" \
  "v_mfma_f32_16x16x32_bf16 v[212:215], a[192:195], v[132:135], v[212:215]\n\t" \
  "s_add_u32 s30, s28, 0x8000\n\t" \
  "ds_read_b128 a[240:243], v32 offset:12288\n\t" \
  "v_cmp_gt_i32_e64 s[48:49], 16, v35\n\t" \
  "v_cmp_gt_i32_e64 s[50:51], 17, v35\n\t" \
  "v_mfma_f32_16x16x32_bf16 v[212:215], a[196:199], v[140:143], v[212:215]\n\t" \
  "s_cmp_lg_u32 s28, 0x20000\n\t" \
  "s_cselect_b32 s30, s30, 0\n\t" \
  "ds_read_b128 a[244:247], v32 offset:13312\n\t" \
  "v_cmp_gt_i32_e64 s[52:53], 18, v35\n\t" \
  "v_mfma_f32_16x16x32_bf16 v[212:215], a[200:203], v[148:151], v[212:215]\n\t" \
  "ds_read_b128 a[248:251], v32 offset:14336\n\t" \
  "s_add_u32 m0, s29, s31\n\t" \
  "v_cmp_gt_i32_e64 s[54:55], 19, v35\n\t" \
  "v_mfma_f32_16x16x32_bf16 v[212:215], a[204:207], v[156:159], v[212:215]\n\t" \
  "ds_read_b128 a[252:255], v32 offset:15360\n\t" \
  "v_cndmask_b32_e64 v220, v244, v37, s[48:49]\n\t" \
  "v_cndmask_b32_e64 v221, v245, v37, s[50:51]\n\t" \
  "s_waitcnt vmcnt(25)\n\t" \
  "v_mfma_f32_16x16x32_bf16 v[212:215], a[208:211], v[164:167], v[212:215]\n\t" \
  "ds_read_b128 a[192:195], v32 offset:16384\n\t" \
  "buffer_load_dwordx4 v34, %11, s26 offen offset:0 sc1 lds\n\t" \
  "v_cndmask_b32_e64 v222, v246, v37, s[52:53]\n\t" \
  "v_accvgpr_read_b32 v128, a0\n\t" \
  "v_accvgpr_read_b32 v129, a1\n\t" \
  "v_accvgpr_read_b32 v130, a2\n\t" \
  "v_accvgpr_read_b32 v131, a3\n\t" \
  "v_accvgpr_read_b32 v136, a8\n\t" \
  "v_accvgpr_read_b32 v137, a9\n\t" \
  "v_accvgpr_read_b32 v138, a10\n\t" \
  "v_accvgpr_read_b32 v139, a11\n\t" \
  "v_mfma_f32_16x16x32_bf16 v[212:215], a[212:215], v[172:175], v[212:215]\n\t" \
  "ds_read_b128 a[196:199], v32 offset:17408\n\t" \
  "v_cndmask_b32_e64 v223, v247, v37, s[54:55]\n\t" \
  "v_exp_f32_e32 v252, v204\n\t" \
  "v_lshlrev_b32_e32 v144, 16, v44\n\t" \
  "v_and_b32_e32 v145, 0xffff0000, v44\n\t" \
  "v_lshlrev_b32_e32 v146, 16, v45\n\t" \
  "v_and_b32_e32 v147, 0xffff0000, v45\n\t" \
  "v_lshlrev_b32_e32 v152, 16, v46\n\t" \
  "v_and_b32_e32 v153, 0xffff0000, v46\n\t" \
  "v_lshlrev_b32_e32 v154, 16, v47\n\t" \
  "v_mfma_f32_16x16x32_bf16 v[212:215], a[216:219], v[180:183], v[212:215]\n\t" \
  "v_add_u32_e32 v33, s30, %1\n\t" \
  "ds_read_b128 a[200:203], v32 offset:18432\n\t" \
  "v_add_f32_e32 v249, v249, v252\n\t" \
  "v_and_b32_e32 v155, 0xffff0000, v47\n\t" \
  "v_pk_mul_f32 v[128:129], v[128:129], v[192:193]\n\t" \
  "v_pk_mul_f32 v[130:131], v[130:131], v[192:193]\n\t" \
  "v_pk_mul_f32 v[136:137], v[136:137], v[192:193]\n\t" \
  "v_pk_mul_f32 v[138:139], v[138:139], v[192:193]\n\t" \
  "v_pk_fma_f32 v[128:129], v[144:145], v[194:195], v[128:129]\n\t" \
  "v_pk_fma_f32 v[130:131], v[146:147], v[194:195], v[130:131]\n\t" \
  "v_pk_fma_f32 v[136:137], v[152:153], v[194:195], v[136:137]\n\t" \
  "v_mfma_f32_16x16x32_bf16 v[212:215], a[220:223], v[188:191], v[212:215]\n\t" \
  "ds_read_b128 a[204:207], v32 offset:19456\n\t" \
  "buffer_load_dwordx4 v34, %11, s26 offen offset:1024 sc1 lds\n\t" \
  "v_pk_fma_f32 v[138:139], v[154:155], v[194:195], v[138:139]\n\t" \
  "v_cvt_pk_bf16_f32 v44, v128, v129\n\t" \
  "v_cvt_pk_bf16_f32 v45, v130, v131\n\t" \
  "v_cvt_pk_bf16_f32 v46, v136, v137\n\t" \
  "v_cvt_pk_bf16_f32 v47, v138, v139\n\t" \
  "buffer_store_dwordx4 v[44:47], %1, %11, s39 offen offset:0 sc1\n\t" \
  "v_accvgpr_read_b32 v160, a16\n\t" \
  "v_accvgpr_read_b32 v161, a17\n\t" \
  "s_waitcnt lgkmcnt(4)\n\t" \
  "v_mfma_f32_16x16x32_bf16 v[220:223], a[224:227], v[132:135], v[220:223]\n\t" \
  "ds_read_b128 a[208:211], v32 offset:20480\n\t" \
  "v_exp_f32_e32 v253, v205\n\t" \
  "v_accvgpr_read_b32 v162, a18\n\t" \
  "v_accvgpr_read_b32 v163, a19\n\t" \
  "v_accvgpr_read_b32 v168, a24\n\t" \
  "v_accvgpr_read_b32 v169, a25\n\t" \
  "v_accvgpr_read_b32 v170, a26\n\t" \
  "v_accvgpr_read_b32 v171, a27\n\t" \
  "v_lshlrev_b32_e32 v176, 16, v48\n\t" \
  "v_and_b32_e32 v177, 0xffff0000, v48\n\t" \
  "v_mfma_f32_16x16x32_bf16 v[220:223], a[228:231], v[140:143], v[220:223]\n\t" \
  "ds_read_b128 a[212:215], v32 offset:21504\n\t" \
  "v_exp_f32_e32 v254, v206\n\t" \
  "v_add_f32_e32 v249, v249, v253\n\t" \
  "v_cvt_pk_bf16_f32 v230, v252, v253\n\t" \
  "v_lshlrev_b32_e32 v178, 16, v49\n\t" \
  "v_and_b32_e32 v179, 0xffff0000, v49\n\t" \
  "v_lshlrev_b32_e32 v184, 16, v50\n\t" \
  "v_and_b32_e32 v185, 0xffff0000, v50\n\t" \
  "v_lshlrev_b32_e32 v186, 16, v51\n\t" \
  "v_and_b32_e32 v187, 0xffff0000, v51\n\t" \
  "v_mfma_f32_16x16x32_bf16 v[220:223], a[232:235], v[148:151], v[220:223]\n\t" \
  "ds_read_b128 a[216:219], v32 offset:22528\n\t" \
  "buffer_load_dwordx4 v34, %11, s26 offen offset:2048 sc1 lds\n\t" \
  "v_add_f32_e32 v249, v249, v254\n\t" \
  "v_pk_mul_f32 v[160:161], v[160:161], v[192:193]\n\t" \
  "v_pk_mul_f32 v[162:163], v[162:163], v[192:193]\n\t" \
  "v_pk_mul_f32 v[168:169], v[168:169], v[192:193]\n\t" \
  "v_pk_mul_f32 v[170:171], v[170:171], v[192:193]\n\t" \
  "v_pk_fma_f32 v[160:161], v[176:177], v[194:195], v[160:161]\n\t" \
  "v_pk_fma_f32 v[162:163], v[178:179], v[194:195], v[162:163]\n\t" \
  "v_pk_fma_f32 v[168:169], v[184:185], v[194:195], v[168:169]\n\t" \
  "v_pk_fma_f32 v[170:171], v[186:187], v[194:195], v[170:171]\n\t" \
  "v_mfma_f32_16x16x32_bf16 v[220:223], a[236:239], v[156:159], v[220:223]\n\t" \
  "ds_read_b128 a[220:223], v32 offset:23552\n\t" \
  "v_exp_f32_e32 v255, v207\n\t" \
  "v_cvt_pk_bf16_f32 v48, v160, v161\n\t" \
  "v_cvt_pk_bf16_f32 v49, v162, v163\n\t" \
  "v_cvt_pk_bf16_f32 v50, v168, v169\n\t" \
  "v_cvt_pk_bf16_f32 v51, v170, v171\n\t" \
  "buffer_store_dwordx4 v[48:51], %1, %11, s39 offen offset:1024 sc1\n\t" \
  "v_accvgpr_read_b32 v128, a32\n\t" \
  "v_accvgpr_read_b32 v129, a33\n\t" \
  "v_accvgpr_read_b32 v130, a34\n\t" \
  "v_mfma_f32_16x16x32_bf16 v[220:223], a[240:243], v[164:167], v[220:223]\n\t" \
  "ds_read_b128 a[224:227], v32 offset:24576\n\t" \
  "v_add_f32_e32 v249, v249, v255\n\t" \
  "v_cvt_pk_bf16_f32 v231, v254, v255\n\t" \
  "v_cmp_nge_f32_e32 vcc, s45, v249\n\t" \
  "v_accvgpr_read_b32 v131, a35\n\t" \
  "v_accvgpr_read_b32 v136, a40\n\t" \
  "v_accvgpr_read_b32 v137, a41\n\t" \
  "v_accvgpr_read_b32 v138, a42\n\t" \
  "v_accvgpr_read_b32 v139, a43\n\t" \
  "v_lshlrev_b32_e32 v144, 16, v52\n\t" \
  "v_and_b32_e32 v145, 0xffff0000, v52\n\t" \
  "v_mfma_f32_16x16x32_bf16 v[220:223], a[244:247], v[172:175], v[220:223]\n\t" \
  "ds_read_b128 a[228:231], v32 offset:25600\n\t" \
  "buffer_load_dwordx4 v34, %11, s26 offen offset:3072 sc1 lds\n\t" \
  "v_lshlrev_b32_e32 v146, 16, v53\n\t" \
  "v_and_b32_e32 v147, 0xffff0000, v53\n\t" \
  "v_lshlrev_b32_e32 v152, 16, v54\n\t" \
  "v_and_b32_e32 v153, 0xffff0000, v54\n\t" \
  "v_lshlrev_b32_e32 v154, 16, v55\n\t" \
  "v_and_b32_e32 v155, 0xffff0000, v55\n\t" \
  "v_pk_mul_f32 v[128:129], v[128:129], v[192:193]\n\t" \
  "v_pk_mul_f32 v[130:131], v[130:131], v[192:193]\n\t" \
  "v_pk_mul_f32 v[136:137], v[136:137], v[192:193]\n\t" \
  "v_mfma_f32_16x16x32_bf16 v[220:223], a[248:251], v[180:183], v[220:223]\n\t" \
  "ds_read_b128 a[232:235], v32 offset:26624\n\t" \
  "s_add_u32 m0, m0, 0x4000\n\t" \
  "v_pk_mul_f32 v[138:139], v[138:139], v[192:193]\n\t" \
  "v_pk_fma_f32 v[128:129], v[144:145], v[194:195], v[128:129]\n\t" \
  "v_pk_fma_f32 v[130:131], v[146:147], v[194:195], v[130:131]\n\t" \
  "v_pk_fma_f32 v[136:137], v[152:153], v[194:195], v[136:137]\n\t" \
  "v_pk_fma_f32 v[138:139], v[154:155], v[194:195], v[138:139]\n\t" \
  "v_cvt_pk_bf16_f32 v52, v128, v129\n\t" \
  "v_cvt_pk_bf16_f32 v53, v130, v131\n\t" \
  "v_cvt_pk_bf16_f32 v54, v136, v137\n\t" \
  "v_cvt_pk_bf16_f32 v55, v138, v139\n\t" \
  "v_mfma_f32_16x16x32_bf16 v[220:223], a[252:255], v[188:191], v[220:223]\n\t" \
  "ds_read_b128 a[236:239], v32 offset:27648\n\t" \
  "s_cbranch_vccnz MK_T0_4_%=\n\t" \
  "MK_R0_4_%=:\n\t" \
  "buffer_store_dwordx4 v[52:55], %1, %11, s39 offen offset:2048 sc1\n\t" \
  "v_accvgpr_read_b32 v160, a48\n\t" \
  "v_accvgpr_read_b32 v161, a49\n\t" \
  "v_accvgpr_read_b32 v162, a50\n\t" \
  "v_accvgpr_read_b32 v163, a51\n\t" \
  "v_accvgpr_read_b32 v168, a56\n\t" \
  "v_accvgpr_read_b32 v169, a57\n\t" \
  "v_accvgpr_read_b32 v170, a58\n\t" \
  "s_waitcnt lgkmcnt(4)\n\t" \
  "v_mfma_f32_16x16x32_bf16 a[4:7], a[192:195], v[228:231], a[4:7]\n\t" \
  "ds_read_b128 a[240:243], v32 offset:28672\n\t" \
  "buffer_load_dwordx4 v34, %11, s27 offen offset:0 sc1 lds\n\t" \
  "v_accvgpr_read_b32 v171, a59\n\t" \
  "v_lshlrev_b32_e32 v176, 16, v56\n\t" \
  "v_and_b32_e32 v177, 0xffff0000, v56\n\t" \
  "v_lshlrev_b32_e32 v178, 16, v57\n\t" \
  "v_and_b32_e32 v179, 0xffff0000, v57\n\t" \
  "v_lshlrev_b32_e32 v184, 16, v58\n\t" \
  "v_and_b32_e32 v185, 0xffff0000, v58\n\t" \
  "v_lshlrev_b32_e32 v186, 16, v59\n\t" \
  "v_and_b32_e32 v187, 0xffff0000, v59\n\t" \
  "v_mfma_f32_16x16x32_bf16 a[12:15], a[196:199], v[228:231], a[12:15]\n\t" \
  "ds_read_b128 a[244:247], v32 offset:29696\n\t" \
  "v_exp_f32_e32 v252, v212\n\t" \
  "v_pk_mul_f32 v[160:161], v[160:161], v[192:193]\n\t" \
  "v_pk_mul_f32 v[162:163], v[162:163], v[192:193]\n\t" \
  "v_pk_mul_f32 v[168:169], v[168:169], v[192:193]\n\t" \
  "v_pk_mul_f32 v[170:171], v[170:171], v[192:193]\n\t" \
  "v_pk_fma_f32 v[160:161], v[176:177], v[194:195], v[160:161]\n\t" \
  "v_pk_fma_f32 v[162:163], v[178:179], v[194:195], v[162:163]\n\t" \
  "v_pk_fma_f32 v[168:169], v[184:185], v[194:195], v[168:169]\n\t" \
  "v_pk_fma_f32 v[170:171], v[186:187], v[194:195], v[170:171]\n\t" \
  "v_mfma_f32_16x16x32_bf16 a[20:23], a[200:203], v[228:231], a[20:23]\n\t" \
  "ds_read_b128 a[248:251], v32 offset:30720\n\t" \
  "v_exp_f32_e32 v253, v213\n\t" \
  "v_add_f32_e32 v251, v249, v252\n\t" \
  "v_cvt_pk_bf16_f32 v56, v160, v161\n\t" \
  "v_cvt_pk_bf16_f32 v57, v162, v163\n\t" \
  "v_cvt_pk_bf16_f32 v58, v168, v169\n\t" \
  "v_cvt_pk_bf16_f32 v59, v170, v171\n\t" \
  "buffer_store_dwordx4 v[56:59], %1, %11, s39 offen offset:3072 sc1\n\t" \
  "v_accvgpr_read_b32 v128, a64\n\t" \
  "v_accvgpr_read_b32 v129, a65\n\t" \
  "v_mfma_f32_16x16x32_bf16 a[28:31], a[204:207], v[228:231], a[28:31]\n\t" \
  "ds_read_b128 a[252:255], v32 offset:31744\n\t" \
  "buffer_load_dwordx4 v34, %11, s27 offen offset:1024 sc1 lds\n\t" \
  "v_add_f32_e32 v251, v251, v253\n\t" \
  "v_cvt_pk_bf16_f32 v236, v252, v253\n\t" \
  "v_accvgpr_read_b32 v130, a66\n\t" \
  "v_accvgpr_read_b32 v131, a67\n\t" \
  "v_accvgpr_read_b32 v136, a72\n\t" \
  "v_accvgpr_read_b32 v137, a73\n\t" \
  "v_accvgpr_read_b32 v138, a74\n\t" \
  "v_accvgpr_read_b32 v139, a75\n\t" \
  "v_lshlrev_b32_e32 v144, 16, v60\n\t" \
  "v_mfma_f32_16x16x32_bf16 a[36:39], a[208:211], v[228:231], a[36:39]\n\t" \
  "ds_read_b128 a[192:195], v33 offset:16384\n\t" \
  "v_exp_f32_e32 v254, v214\n\t" \
  "v_and_b32_e32 v145, 0xffff0000, v60\n\t" \
  "v_lshlrev_b32_e32 v146, 16, v61\n\t" \
  "v_and_b32_e32 v147, 0xffff0000, v61\n\t" \
  "v_lshlrev_b32_e32 v152, 16, v62\n\t" \
  "v_and_b32_e32 v153, 0xffff0000, v62\n\t" \
  "v_lshlrev_b32_e32 v154, 16, v63\n\t" \
  "v_and_b32_e32 v155, 0xffff0000, v63\n\t" \
  "v_pk_mul_f32 v[128:129], v[128:129], v[192:193]\n\t" \
  "v_mfma_f32_16x16x32_bf16 a[44:47], a[212:215], v[228:231], a[44:47]\n\t" \
  "ds_read_b128 a[196:199], v33 offset:17408\n\t" \
  "v_exp_f32_e32 v255, v215\n\t" \
  "v_add_f32_e32 v251, v251, v254\n\t" \
  "v_pk_mul_f32 v[130:131], v[130:131], v[192:193]\n\t" \
  "v_pk_mul_f32 v[136:137], v[136:137], v[192:193]\n\t" \
  "v_pk_mul_f32 v[138:139], v[138:139], v[192:193]\n\t" \
  "v_pk_fma_f32 v[128:129], v[144:145], v[194:195], v[128:129]\n\t" \
  "v_pk_fma_f32 v[130:131], v[146:147], v[194:195], v[130:131]\n\t" \
  "v_pk_fma_f32 v[136:137], v[152:153], v[194:195], v[136:137]\n\t" \
  "v_pk_fma_f32 v[138:139], v[154:155], v[194:195], v[138:139]\n\t" \
  "v_mfma_f32_16x16x32_bf16 a[52:55], a[216:219], v[228:231], a[52:55]\n\t" \
  "ds_read_b128 a[200:203], v33 offset:18432\n\t" \
  "buffer_load_dwordx4 v34, %11, s27 offen offset:2048 sc1 lds\n\t" \
  "v_add_f32_e32 v251, v251, v255\n\t" \
  "v_cvt_pk_bf16_f32 v237, v254, v255\n\t" \
  "v_cvt_pk_bf16_f32 v60, v128, v129\n\t" \
  "v_cvt_pk_bf16_f32 v61, v130, v131\n\t" \
  "v_cvt_pk_bf16_f32 v62, v136, v137\n\t" \
  "v_cvt_pk_bf16_f32 v63, v138, v139\n\t" \
  "buffer_store_dwordx4 v[60:63], %1, %11, s69 offen offset:0 sc1\n\t" \
  "v_accvgpr_read_b32 v160, a80\n\t" \
  "v_accvgpr_read_b32 v161, a81\n\t" \
  "v_mfma_f32_16x16x32_bf16 a[60:63], a[220:223], v[228:231], a[60:63]\n\t" \
  "ds_read_b128 a[204:207], v33 offset:19456\n\t" \
  "v_accvgpr_read_b32 v162, a82\n\t" \
  "v_accvgpr_read_b32 v163, a83\n\t" \
  "v_accvgpr_read_b32 v168, a88\n\t" \
  "v_accvgpr_read_b32 v169, a89\n\t" \
  "v_accvgpr_read_b32 v170, a90\n\t" \
  "v_accvgpr_read_b32 v171, a91\n\t" \
  "v_lshlrev_b32_e32 v176, 16, v64\n\t" \
  "v_and_b32_e32 v177, 0xffff0000, v64\n\t" \
  "v_lshlrev_b32_e32 v178, 16, v65\n\t" \
  "s_waitcnt lgkmcnt(4)\n\t" \
  "v_mfma_f32_16x16x32_bf16 a[68:71], a[224:227], v[228:231], a[68:71]\n\t" \
  "ds_read_b128 a[208:211], v33 offset:20480\n\t" \
  "v_and_b32_e32 v179, 0xffff0000, v65\n\t" \
  "v_lshlrev_b32_e32 v184, 16, v66\n\t" \
  "v_and_b32_e32 v185, 0xffff0000, v66\n\t" \
  "v_lshlrev_b32_e32 v186, 16, v67\n\t" \
  "v_and_b32_e32 v187, 0xffff0000, v67\n\t" \
  "v_pk_mul_f32 v[160:161], v[160:161], v[192:193]\n\t" \
  "v_pk_mul_f32 v[162:163], v[162:163], v[192:193]\n\t" \
  "v_pk_mul_f32 v[168:169], v[168:169], v[192:193]\n\t" \
  "v_pk_mul_f32 v[170:171], v[170:171], v[192:193]\n\t" \
  "v_pk_fma_f32 v[160:161], v[176:177], v[194:195], v[160:161]\n\t" \
  "v_mfma_f32_16x16x32_bf16 a[76:79], a[228:231], v[228:231], a[76:79]\n\t" \
  "ds_read_b128 a[212:215], v33 offset:21504\n\t" \
  "buffer_load_dwordx4 v34, %11, s27 offen offset:3072 sc1 lds\n\t" \
  "v_pk_fma_f32 v[162:163], v[178:179], v[194:195], v[162:163]\n\t" \
  "v_pk_fma_f32 v[168:169], v[184:185], v[194:195], v[168:169]\n\t" \
  "v_pk_fma_f32 v[170:171], v[186:187], v[194:195], v[170:171]\n\t" \
  "v_cvt_pk_bf16_f32 v64, v160, v161\n\t" \
  "v_cvt_pk_bf16_f32 v65, v162, v163\n\t" \
  "v_cvt_pk_bf16_f32 v66, v168, v169\n\t" \
  "v_cvt_pk_bf16_f32 v67, v170, v171\n\t" \
  "buffer_store_dwordx4 v[64:67], %1, %11, s69 offen offset:1024 sc1\n\t" \
  "v_accvgpr_read_b32 v128, a96\n\t" \
  "v_mfma_f32_16x16x32_bf16 a[84:87], a[232:235], v[228:231], a[84:87]\n\t" \
  "ds_read_b128 a[216:219], v33 offset:22528\n\t" \
  "v_readlane_b32 s26, v27, s25\n\t" \
  "v_accvgpr_read_b32 v129, a97\n\t" \
  "v_accvgpr_read_b32 v130, a98\n\t" \
  "v_accvgpr_read_b32 v131, a99\n\t" \
  "v_accvgpr_read_b32 v136, a104\n\t" \
  "v_accvgpr_read_b32 v137, a105\n\t" \
  "v_accvgpr_read_b32 v138, a106\n\t" \
  "v_accvgpr_read_b32 v139, a107\n\t" \
  "v_lshlrev_b32_e32 v144, 16, v68\n\t" \
  "v_and_b32_e32 v145, 0xffff0000, v68\n\t" \
  "v_mfma_f32_16x16x32_bf16 a[92:95], a[236:239], v[228:231], a[92:95]\n\t" \
  "ds_read_b128 a[220:223], v33 offset:23552\n\t" \
  "v_readlane_b32 s27, v28, s25\n\t" \
  "v_lshlrev_b32_e32 v146, 16, v69\n\t" \
  "v_and_b32_e32 v147, 0xffff0000, v69\n\t" \
  "v_lshlrev_b32_e32 v152, 16, v70\n\t" \
  "v_and_b32_e32 v153, 0xffff0000, v70\n\t" \
  "v_lshlrev_b32_e32 v154, 16, v71\n\t" \
  "v_and_b32_e32 v155, 0xffff0000, v71\n\t" \
  "v_pk_mul_f32 v[128:129], v[128:129], v[192:193]\n\t" \
  "v_pk_mul_f32 v[130:131], v[130:131], v[192:193]\n\t" \
  "v_pk_mul_f32 v[136:137], v[136:137], v[192:193]\n\t" \
  "v_mfma_f32_16x16x32_bf16 a[100:103], a[240:243], v[228:231], a[100:103]\n\t" \
  "ds_read_b128 a[224:227], v33 offset:24576\n\t" \
  "s_add_u32 s25, s25, 1\n\t" \
  "v_pk_mul_f32 v[138:139], v[138:139], v[192:193]\n\t" \
  "v_pk_fma_f32 v[128:129], v[144:145], v[194:195], v[128:129]\n\t" \
  "v_pk_fma_f32 v[130:131], v[146:147], v[194:195], v[130:131]\n\t" \
  "v_pk_fma_f32 v[136:137], v[152:153], v[194:195], v[136:137]\n\t" \
  "v_pk_fma_f32 v[138:139], v[154:155], v[194:195], v[138:139]\n\t" \
  "v_cvt_pk_bf16_f32 v68, v128, v129\n\t" \
  "v_cvt_pk_bf16_f32 v69, v130, v131\n\t" \
  "v_cvt_pk_bf16_f32 v70, v136, v137\n\t" \
  "v_cvt_pk_bf16_f32 v71, v138, v139\n\t" \
  "v_mfma_f32_16x16x32_bf16 a[108:111], a[244:247], v[228:231], a[108:111]\n\t" \
  "ds_read_b128 a[228:231], v33 offset:25600\n\t" \
  "s_mov_b32 s29, s28\n\t" \
  "buffer_store_dwordx4 v[68:71], %1, %11, s69 offen offset:2048 sc1\n\t" \
  "v_accvgpr_read_b32 v160, a112\n\t" \
  "v_accvgpr_read_b32 v161, a113\n\t" \
  "v_accvgpr_read_b32 v162, a114\n\t" \
  "v_accvgpr_read_b32 v163, a115\n\t" \
  "v_accvgpr_read_b32 v168, a120\n\t" \
  "v_accvgpr_read_b32 v169, a121\n\t" \
  "v_accvgpr_read_b32 v170, a122\n\t" \
  "v_accvgpr_read_b32 v171, a123\n\t" \
  "v_mfma_f32_16x16x32_bf16 a[116:119], a[248:251], v[228:231], a[116:119]\n\t" \
  "ds_read_b128 a[232:235], v33 offset:26624\n\t" \
  "s_mov_b32 s28, s30\n\t" \
  "v_lshlrev_b32_e32 v176, 16, v72\n\t" \
  "v_and_b32_e32 v177, 0xffff0000, v72\n\t" \
  "v_lshlrev_b32_e32 v178, 16, v73\n\t" \
  "v_and_b32_e32 v179, 0xffff0000, v73\n\t" \
  "v_lshlrev_b32_e32 v184, 16, v74\n\t" \
  "v_and_b32_e32 v185, 0xffff0000, v74\n\t" \
  "v_lshlrev_b32_e32 v186, 16, v75\n\t" \
  "v_and_b32_e32 v187, 0xffff0000, v75\n\t" \
  "v_pk_mul_f32 v[160:161], v[160:161], v[192:193]\n\t" \
  "v_mfma_f32_16x16x32_bf16 a[124:127], a[252:255], v[228:231], a[124:127]\n\t" \
  "ds_read_b128 a[236:239], v33 offset:27648\n\t" \
  "v_pk_mul_f32 v[162:163], v[162:163], v[192:193]\n\t" \
  "v_pk_mul_f32 v[168:169], v[168:169], v[192:193]\n\t" \
  "v_pk_mul_f32 v[170:171], v[170:171], v[192:193]\n\t" \
  "v_pk_fma_f32 v[160:161], v[176:177], v[194:195], v[160:161]\n\t" \
  "v_pk_fma_f32 v[162:163], v[178:179], v[194:195], v[162:163]\n\t" \
  "v_pk_fma_f32 v[168:169], v[184:185], v[194:195], v[168:169]\n\t" \
  "v_pk_fma_f32 v[170:171], v[186:187], v[194:195], v[170:171]\n\t" \
  "v_cvt_pk_bf16_f32 v72, v160, v161\n\t" \
  "v_cvt_pk_bf16_f32 v73, v162, v163\n\t" \
  "v_cvt_pk_bf16_f32 v74, v168, v169\n\t" \
  "v_cvt_pk_bf16_f32 v75, v170, v171\n\t" \
  "buffer_store_dwordx4 v[72:75], %1, %11, s69 offen offset:3072 sc1\n\t" \
  "s_waitcnt vmcnt(34)\n\t" \
  "s_barrier\n\t" \
  "v_exp_f32_e32 v252, v220\n\t" \
  "v_exp_f32_e32 v253, v221\n\t" \
  "v_add_f32_e32 v251, v251, v252\n\t" \
  "v_add_f32_e32 v251, v251, v253\n\t" \
  "v_cvt_pk_bf16_f32 v238, v252, v253\n\t" \
  "v_exp_f32_e32 v254, v222\n\t" \
  "v_exp_f32_e32 v255, v223\n\t" \
  "v_add_f32_e32 v251, v251, v254\n\t" \
  "v_add_f32_e32 v251, v251, v255\n\t" \
  "v_cvt_pk_bf16_f32 v239, v254, v255\n\t" \
  "v_cmp_nge_f32_e32 vcc, s45, v251\n\t" \
  "s_nop 1\n\t" \
  "s_cbranch_vccnz MK_T1_4_%=\n\t" \
  "MK_R1_4_%=:\n\t" \
  "s_waitcnt lgkmcnt(4)\n\t" \
  "v_mfma_f32_16x16x32_bf16 a[4:7], a[192:195], v[236:239], a[4:7]\n\t" \
  "s_add_u32 s30, s28, 0x8000\n\t" \
  "ds_read_b128 a[240:243], v33 offset:28672\n\t" \
  "v_mov_b32_e32 v197, 0x7f000000\n\t" \
  "v_cndmask_b32_e64 v41, v197, v41, s[92:93]\n\t" \
  "v_mfma_f32_16x16x32_bf16 a[12:15], a[196:199], v[236:239], a[12:15]\n\t" \
  "s_cmp_lg_u32 s28, 0x20000\n\t" \
  "s_cselect_b32 s30, s30, 0\n\t" \
  "ds_read_b128 a[244:247], v33 offset:29696\n\t" \
  "v_min_f32_e32 v197, v244, v41\n\t" \
  "v_sub_f32_e32 v196, v197, v244\n\t" \
  "v_mfma_f32_16x16x32_bf16 a[20:23], a[200:203], v[236:239], a[20:23]\n\t" \
  "ds_read_b128 a[248:251], v33 offset:30720\n\t" \
  "s_add_u32 m0, s29, s31\n\t" \
  "v_sub_f32_e32 v198, v197, v41\n\t" \
  "v_mfma_f32_16x16x32_bf16 a[28:31], a[204:207], v[236:239], a[28:31]\n\t" \
  "v_add_u32_e32 v32, s30, %1\n\t" \
  "ds_read_b128 a[252:255], v33 offset:31744\n\t" \
  "v_exp_f32_e32 v196, v196\n\t" \
  "v_mfma_f32_16x16x32_bf16 a[36:39], a[208:211], v[236:239], a[36:39]\n\t" \
  "ds_read_b128 a[192:195], v32 offset:0\n\t" \
  "buffer_load_dwordx4 v34, %11, s26 offen offset:0 sc1 lds\n\t" \
  "v_exp_f32_e32 v198, v198\n\t" \
  "v_mul_f32_e32 v197, v251, v196\n\t" \
  "v_mfma_f32_16x16x32_bf16 a[44:47], a[212:215], v[236:239], a[44:47]\n\t" \
  "ds_read_b128 a[196:199], v32 offset:1024\n\t" \
  "buffer_load_dwordx4 v34, %11, s26 offen offset:1024 sc1 lds\n\t" \
  "v_fmac_f32_e32 v197, v43, v198\n\t" \
  "v_mov_b32_e32 v199, v197\n\t" \
  "v_mfma_f32_16x16x32_bf16 a[52:55], a[216:219], v[236:239], a[52:55]\n\t" \
  "ds_read_b128 a[200:203], v32 offset:2048\n\t" \
  "buffer_load_dwordx4 v34, %11, s26 offen offset:2048 sc1 lds\n\t" \
  "s_nop 1\n\t" \
  "v_mfma_f32_16x16x32_bf16 a[60:63], a[220:223], v[236:239], a[60:63]\n\t" \
  "ds_read_b128 a[204:207], v32 offset:3072\n\t" \
  "buffer_load_dwordx4 v34, %11, s26 offen offset:3072 sc1 lds\n\t" \
  "s_add_u32 m0, m0, 0x4000\n\t" \
  "v_permlane16_swap_b32_e32 v197, v199\n\t" \
  "s_waitcnt lgkmcnt(4)\n\t" \
  "v_mfma_f32_16x16x32_bf16 a[68:71], a[224:227], v[236:239], a[68:71]\n\t" \
  "ds_read_b128 a[208:211], v32 offset:4096\n\t" \
  "buffer_load_dwordx4 v34, %11, s27 offen offset:0 sc1 lds\n\t" \
  "s_nop 0\n\t" \
  "v_add_f32_e32 v197, v197, v199\n\t" \
  "v_mov_b32_e32 v199, v197\n\t" \
  "v_mfma_f32_16x16x32_bf16 a[76:79], a[228:231], v[236:239], a[76:79]\n\t" \
  "ds_read_b128 a[212:215], v32 offset:5120\n\t" \
  "buffer_load_dwordx4 v34, %11, s27 offen offset:1024 sc1 lds\n\t" \
  "s_nop 1\n\t" \
  "v_mfma_f32_16x16x32_bf16 a[84:87], a[232:235], v[236:239], a[84:87]\n\t" \
  "ds_read_b128 a[216:219], v32 offset:6144\n\t" \
  "buffer_load_dwordx4 v34, %11, s27 offen offset:2048 sc1 lds\n\t" \
  "v_permlane32_swap_b32_e32 v197, v199\n\t" \
  "s_nop 0\n\t" \
  "v_mfma_f32_16x16x32_bf16 a[92:95], a[236:239], v[236:239], a[92:95]\n\t" \
  "ds_read_b128 a[220:223], v32 offset:7168\n\t" \
  "buffer_load_dwordx4 v34, %11, s27 offen offset:3072 sc1 lds\n\t" \
  "v_add_f32_e32 v197, v197, v199\n\t" \
  "v_mfma_f32_16x16x32_bf16 a[100:103], a[240:243], v[236:239], a[100:103]\n\t" \
  "ds_read_b128 a[224:227], v32 offset:8192\n\t" \
  "v_readlane_b32 s26, v27, s25\n\t" \
  "v_rcp_f32_e32 v197, v197\n\t" \
  "s_nop 0\n\t" \
  "v_mfma_f32_16x16x32_bf16 a[108:111], a[244:247], v[236:239], a[108:111]\n\t" \
  "ds_read_b128 a[228:231], v32 offset:9216\n\t" \
  "v_readlane_b32 s27, v28, s25\n\t" \
  "v_mul_f32_e32 v196, v196, v197\n\t" \
  "v_mfma_f32_16x16x32_bf16 a[116:119], a[248:251], v[236:239], a[116:119]\n\t" \
  "ds_read_b128 a[232:235], v32 offset:10240\n\t" \
  "s_add_u32 s25, s25, 1\n\t" \
  "v_mul_f32_e32 v198, v198, v197\n\t" \
  "v_mov_b32_e32 v197, v196\n\t" \
  "v_mfma_f32_16x16x32_bf16 a[124:127], a[252:255], v[236:239], a[124:127]\n\t" \
  "ds_read_b128 a[236:239], v32 offset:11264\n\t" \
  "s_mov_b32 s29, s28\n\t" \
  "s_mov_b32 s28, s30\n\t" \
  "v_mov_b32_e32 v199, v198\n\t" \
  "s_waitcnt vmcnt(38)\n\t" \
  "v_readlane_b32 s56, %13, 6\n\t" \
  "v_readlane_b32 s57, %13, 7\n\t" \
  "v_readlane_b32 s60, %13, 8\n\t" \
  "v_readlane_b32 s61, %13, 9\n\t" \
  "v_readlane_b32 s64, %13, 10\n\t" \
  "v_readlane_b32 s65, %13, 11\n\t" \
  "s_mov_b32 s58, 1835008\n\t" \
  "s_mov_b32 s62, 1835008\n\t" \
  "s_mov_b32 s66, 896\n\t" \
  "v_add_u32_e32 v248, 0, %1\n\t" \
  "v_add_u32_e32 v249, 4096, %1\n\t" \
  "v_add_u32_e32 v250, 8192, %1\n\t" \
  "v_add_u32_e32 v251, 12288, %1\n\t" \
  "v_add_u32_e32 v252, 16384, %1\n\t" \
  "v_add_u32_e32 v253, 20480, %1\n\t" \
  "v_add_u32_e32 v254, 24576, %1\n\t" \
  "v_add_u32_e32 v255, 28672, %1\n\t" \
  "v_accvgpr_read_b32 v128, a4\n\t" \
  "v_accvgpr_read_b32 v129, a5\n\t" \
  "v_accvgpr_read_b32 v130, a6\n\t" \
  "v_accvgpr_read_b32 v131, a7\n\t" \
  "v_accvgpr_read_b32 v136, a12\n\t" \
  "v_accvgpr_read_b32 v137, a13\n\t" \
  "v_accvgpr_read_b32 v138, a14\n\t" \
  "v_accvgpr_read_b32 v139, a15\n\t" \
  "v_lshlrev_b32_e32 v144, 16, v76\n\t" \
  "v_and_b32_e32 v145, 0xffff0000, v76\n\t" \
  "v_lshlrev_b32_e32 v146, 16, v77\n\t" \
  "v_and_b32_e32 v147, 0xffff0000, v77\n\t" \
  "v_lshlrev_b32_e32 v152, 16, v78\n\t" \
  "v_and_b32_e32 v153, 0xffff0000, v78\n\t" \
  "v_lshlrev_b32_e32 v154, 16, v79\n\t" \
  "v_and_b32_e32 v155, 0xffff0000, v79\n\t" \
  "v_pk_mul_f32 v[128:129], v[128:129], v[196:197]\n\t" \
  "v_pk_mul_f32 v[130:131], v[130:131], v[196:197]\n\t" \
  "v_pk_mul_f32 v[136:137], v[136:137], v[196:197]\n\t" \
  "v_pk_mul_f32 v[138:139], v[138:139], v[196:197]\n\t" \
  "v_pk_fma_f32 v[128:129], v[144:145], v[198:199], v[128:129]\n\t" \
  "v_pk_fma_f32 v[130:131], v[146:147], v[198:199], v[130:131]\n\t" \
  "v_pk_fma_f32 v[136:137], v[152:153], v[198:199], v[136:137]\n\t" \
  "v_pk_fma_f32 v[138:139], v[154:155], v[198:199], v[138:139]\n\t" \
  "v_cvt_pk_bf16_f32 v76, v128, v129\n\t" \
  "v_cvt_pk_bf16_f32 v77, v130, v131\n\t" \
  "v_cvt_pk_bf16_f32 v78, v136, v137\n\t" \
  "v_cvt_pk_bf16_f32 v79, v138, v139\n\t" \
  "buffer_store_dwordx4 v[76:79], %1, %11, s35 offen offset:0 sc1\n\t" \
  "v_accvgpr_read_b32 v160, a20\n\t" \
  "v_accvgpr_read_b32 v161, a21\n\t" \
  "v_accvgpr_read_b32 v162, a22\n\t" \
  "v_accvgpr_read_b32 v163, a23\n\t" \
  "v_accvgpr_read_b32 v168, a28\n\t" \
  "v_accvgpr_read_b32 v169, a29\n\t" \
  "v_accvgpr_read_b32 v170, a30\n\t" \
  "v_accvgpr_read_b32 v171, a31\n\t" \
  "v_lshlrev_b32_e32 v176, 16, v80\n\t" \
  "v_and_b32_e32 v177, 0xffff0000, v80\n\t" \
  "v_lshlrev_b32_e32 v178, 16, v81\n\t" \
  "v_and_b32_e32 v179, 0xffff0000, v81\n\t" \
  "v_lshlrev_b32_e32 v184, 16, v82\n\t" \
  "v_and_b32_e32 v185, 0xffff0000, v82\n\t" \
  "v_lshlrev_b32_e32 v186, 16, v83\n\t" \
  "v_and_b32_e32 v187, 0xffff0000, v83\n\t" \
  "v_pk_mul_f32 v[160:161], v[160:161], v[196:197]\n\t" \
  "v_pk_mul_f32 v[162:163], v[162:163], v[196:197]\n\t" \
  "v_pk_mul_f32 v[168:169], v[168:169], v[196:197]\n\t" \
  "v_pk_mul_f32 v[170:171], v[170:171], v[196:197]\n\t" \
  "v_pk_fma_f32 v[160:161], v[176:177], v[198:199], v[160:161]\n\t" \
  "v_pk_fma_f32 v[162:163], v[178:179], v[198:199], v[162:163]\n\t" \
  "v_pk_fma_f32 v[168:169], v[184:185], v[198:199], v[168:169]\n\t" \
  "v_pk_fma_f32 v[170:171], v[186:187], v[198:199], v[170:171]\n\t" \
  "v_cvt_pk_bf16_f32 v80, v160, v161\n\t" \
  "v_cvt_pk_bf16_f32 v81, v162, v163\n\t" \
  "v_cvt_pk_bf16_f32 v82, v168, v169\n\t" \
  "v_cvt_pk_bf16_f32 v83, v170, v171\n\t" \
  "buffer_store_dwordx4 v[80:83], %1, %11, s35 offen offset:1024 sc1\n\t" \
  "v_accvgpr_read_b32 v128, a36\n\t" \
  "v_accvgpr_read_b32 v129, a37\n\t" \
  "v_accvgpr_read_b32 v130, a38\n\t" \
  "v_accvgpr_read_b32 v131, a39\n\t" \
  "v_accvgpr_read_b32 v136, a44\n\t" \
  "v_accvgpr_read_b32 v137, a45\n\t" \
  "v_accvgpr_read_b32 v138, a46\n\t" \
  "v_accvgpr_read_b32 v139, a47\n\t" \
  "v_lshlrev_b32_e32 v144, 16, v84\n\t" \
  "v_and_b32_e32 v145, 0xffff0000, v84\n\t" \
  "v_lshlrev_b32_e32 v146, 16, v85\n\t" \
  "v_and_b32_e32 v147, 0xffff0000, v85\n\t" \
  "v_lshlrev_b32_e32 v152, 16, v86\n\t" \
  "v_and_b32_e32 v153, 0xffff0000, v86\n\t" \
  "v_lshlrev_b32_e32 v154, 16, v87\n\t" \
  "v_and_b32_e32 v155, 0xffff0000, v87\n\t" \
  "v_pk_mul_f32 v[128:129], v[128:129], v[196:197]\n\t" \
  "v_pk_mul_f32 v[130:131], v[130:131], v[196:197]\n\t" \
  "v_pk_mul_f32 v[136:137], v[136:137], v[196:197]\n\t" \
  "v_pk_mul_f32 v[138:139], v[138:139], v[196:197]\n\t" \
  "v_pk_fma_f32 v[128:129], v[144:145], v[198:199], v[128:129]\n\t" \
  "v_pk_fma_f32 v[130:131], v[146:147], v[198:199], v[130:131]\n\t" \
  "v_pk_fma_f32 v[136:137], v[152:153], v[198:199], v[136:137]\n\t" \
  "v_pk_fma_f32 v[138:139], v[154:155], v[198:199], v[138:139]\n\t" \
  "v_cvt_pk_bf16_f32 v84, v128, v129\n\t" \
  "v_cvt_pk_bf16_f32 v85, v130, v131\n\t" \
  "v_cvt_pk_bf16_f32 v86, v136, v137\n\t" \
  "v_cvt_pk_bf16_f32 v87, v138, v139\n\t" \
  "buffer_store_dwordx4 v[84:87], %1, %11, s35 offen offset:2048 sc1\n\t" \
  "v_accvgpr_read_b32 v160, a52\n\t" \
  "v_accvgpr_read_b32 v161, a53\n\t" \
  "v_accvgpr_read_b32 v162, a54\n\t" \
  "v_accvgpr_read_b32 v163, a55\n\t" \
  "v_accvgpr_read_b32 v168, a60\n\t" \
  "v_accvgpr_read_b32 v169, a61\n\t" \
  "v_accvgpr_read_b32 v170, a62\n\t" \
  "v_accvgpr_read_b32 v171, a63\n\t" \
  "v_lshlrev_b32_e32 v176, 16, v88\n\t" \
  "v_and_b32_e32 v177, 0xffff0000, v88\n\t" \
  "v_lshlrev_b32_e32 v178, 16, v89\n\t" \
  "v_and_b32_e32 v179, 0xffff0000, v89\n\t" \
  "v_lshlrev_b32_e32 v184, 16, v90\n\t" \
  "v_and_b32_e32 v185, 0xffff0000, v90\n\t" \
  "v_lshlrev_b32_e32 v186, 16, v91\n\t" \
  "v_and_b32_e32 v187, 0xffff0000, v91\n\t" \
  "v_pk_mul_f32 v[160:161], v[160:161], v[196:197]\n\t" \
  "v_pk_mul_f32 v[162:163], v[162:163], v[196:197]\n\t" \
  "v_pk_mul_f32 v[168:169], v[168:169], v[196:197]\n\t" \
  "v_pk_mul_f32 v[170:171], v[170:171], v[196:197]\n\t" \
  "v_pk_fma_f32 v[160:161], v[176:177], v[198:199], v[160:161]\n\t" \
  "v_pk_fma_f32 v[162:163], v[178:179], v[198:199], v[162:163]\n\t" \
  "v_pk_fma_f32 v[168:169], v[184:185], v[198:199], v[168:169]\n\t" \
  "v_pk_fma_f32 v[170:171], v[186:187], v[198:199], v[170:171]\n\t" \
  "v_cvt_pk_bf16_f32 v88, v160, v161\n\t" \
  "v_cvt_pk_bf16_f32 v89, v162, v163\n\t" \
  "v_cvt_pk_bf16_f32 v90, v168, v169\n\t" \
  "v_cvt_pk_bf16_f32 v91, v170, v171\n\t" \
  "buffer_store_dwordx4 v[88:91], %1, %11, s35 offen offset:3072 sc1\n\t" \
  "v_accvgpr_read_b32 v128, a68\n\t" \
  "v_accvgpr_read_b32 v129, a69\n\t" \
  "v_accvgpr_read_b32 v130, a70\n\t" \
  "v_accvgpr_read_b32 v131, a71\n\t" \
  "v_accvgpr_read_b32 v136, a76\n\t" \
  "v_accvgpr_read_b32 v137, a77\n\t" \
  "v_accvgpr_read_b32 v138, a78\n\t" \
  "v_accvgpr_read_b32 v139, a79\n\t" \
  "v_lshlrev_b32_e32 v144, 16, v92\n\t" \
  "v_and_b32_e32 v145, 0xffff0000, v92\n\t" \
  "v_lshlrev_b32_e32 v146, 16, v93\n\t" \
  "v_and_b32_e32 v147, 0xffff0000, v93\n\t" \
  "v_lshlrev_b32_e32 v152, 16, v94\n\t" \
  "v_and_b32_e32 v153, 0xffff0000, v94\n\t" \
  "v_lshlrev_b32_e32 v154, 16, v95\n\t" \
  "v_and_b32_e32 v155, 0xffff0000, v95\n\t" \
  "v_pk_mul_f32 v[128:129], v[128:129], v[196:197]\n\t" \
  "v_pk_mul_f32 v[130:131], v[130:131], v[196:197]\n\t" \
  "v_pk_mul_f32 v[136:137], v[136:137], v[196:197]\n\t" \
  "v_pk_mul_f32 v[138:139], v[138:139], v[196:197]\n\t" \
  "v_pk_fma_f32 v[128:129], v[144:145], v[198:199], v[128:129]\n\t" \
  "v_pk_fma_f32 v[130:131], v[146:147], v[198:199], v[130:131]\n\t" \
  "v_pk_fma_f32 v[136:137], v[152:153], v[198:199], v[136:137]\n\t" \
  "v_pk_fma_f32 v[138:139], v[154:155], v[198:199], v[138:139]\n\t" \
  "v_cvt_pk_bf16_f32 v92, v128, v129\n\t" \
  "v_cvt_pk_bf16_f32 v93, v130, v131\n\t" \
  "v_cvt_pk_bf16_f32 v94, v136, v137\n\t" \
  "v_cvt_pk_bf16_f32 v95, v138, v139\n\t" \
  "buffer_store_dwordx4 v[92:95], %1, %11, s70 offen offset:0 sc1\n\t" \
  "v_accvgpr_read_b32 v160, a84\n\t" \
  "v_accvgpr_read_b32 v161, a85\n\t" \
  "v_accvgpr_read_b32 v162, a86\n\t" \
  "v_accvgpr_read_b32 v163, a87\n\t" \
  "v_accvgpr_read_b32 v168, a92\n\t" \
  "v_accvgpr_read_b32 v169, a93\n\t" \
  "v_accvgpr_read_b32 v170, a94\n\t" \
  "v_accvgpr_read_b32 v171, a95\n\t" \
  "v_lshlrev_b32_e32 v176, 16, v96\n\t" \
  "v_and_b32_e32 v177, 0xffff0000, v96\n\t" \
  "v_lshlrev_b32_e32 v178, 16, v97\n\t" \
  "v_and_b32_e32 v179, 0xffff0000, v97\n\t" \
  "v_lshlrev_b32_e32 v184, 16, v98\n\t" \
  "v_and_b32_e32 v185, 0xffff0000, v98\n\t" \
  "v_lshlrev_b32_e32 v186, 16, v99\n\t" \
  "v_and_b32_e32 v187, 0xffff0000, v99\n\t" \
  "v_pk_mul_f32 v[160:161], v[160:161], v[196:197]\n\t" \
  "v_pk_mul_f32 v[162:163], v[162:163], v[196:197]\n\t" \
  "v_pk_mul_f32 v[168:169], v[168:169], v[196:197]\n\t" \
  "v_pk_mul_f32 v[170:171], v[170:171], v[196:197]\n\t" \
  "v_pk_fma_f32 v[160:161], v[176:177], v[198:199], v[160:161]\n\t" \
  "v_pk_fma_f32 v[162:163], v[178:179], v[198:199], v[162:163]\n\t" \
  "v_pk_fma_f32 v[168:169], v[184:185], v[198:199], v[168:169]\n\t" \
  "v_pk_fma_f32 v[170:171], v[186:187], v[198:199], v[170:171]\n\t" \
  "v_cvt_pk_bf16_f32 v96, v160, v161\n\t" \
  "v_cvt_pk_bf16_f32 v97, v162, v163\n\t" \
  "v_cvt_pk_bf16_f32 v98, v168, v169\n\t" \
  "v_cvt_pk_bf16_f32 v99, v170, v171\n\t" \
  "buffer_store_dwordx4 v[96:99], %1, %11, s70 offen offset:1024 sc1\n\t" \
  "v_accvgpr_read_b32 v128, a100\n\t" \
  "v_accvgpr_read_b32 v129, a101\n\t" \
  "v_accvgpr_read_b32 v130, a102\n\t" \
  "v_accvgpr_read_b32 v131, a103\n\t" \
  "v_accvgpr_read_b32 v136, a108\n\t" \
  "v_accvgpr_read_b32 v137, a109\n\t" \
  "v_accvgpr_read_b32 v138, a110\n\t" \
  "v_accvgpr_read_b32 v139, a111\n\t" \
  "v_lshlrev_b32_e32 v144, 16, v100\n\t" \
  "v_and_b32_e32 v145, 0xffff0000, v100\n\t" \
  "v_lshlrev_b32_e32 v146, 16, v101\n\t" \
  "v_and_b32_e32 v147, 0xffff0000, v101\n\t" \
  "v_lshlrev_b32_e32 v152, 16, v102\n\t" \
  "v_and_b32_e32 v153, 0xffff0000, v102\n\t" \
  "v_lshlrev_b32_e32 v154, 16, v103\n\t" \
  "v_and_b32_e32 v155, 0xffff0000, v103\n\t" \
  "v_pk_mul_f32 v[128:129], v[128:129], v[196:197]\n\t" \
  "v_pk_mul_f32 v[130:131], v[130:131], v[196:197]\n\t" \
  "v_pk_mul_f32 v[136:137], v[136:137], v[196:197]\n\t" \
  "v_pk_mul_f32 v[138:139], v[138:139], v[196:197]\n\t" \
  "v_pk_fma_f32 v[128:129], v[144:145], v[198:199], v[128:129]\n\t" \
  "v_pk_fma_f32 v[130:131], v[146:147], v[198:199], v[130:131]\n\t" \
  "v_pk_fma_f32 v[136:137], v[152:153], v[198:199], v[136:137]\n\t" \
  "v_pk_fma_f32 v[138:139], v[154:155], v[198:199], v[138:139]\n\t" \
  "v_cvt_pk_bf16_f32 v100, v128, v129\n\t" \
  "v_cvt_pk_bf16_f32 v101, v130, v131\n\t" \
  "v_cvt_pk_bf16_f32 v102, v136, v137\n\t" \
  "v_cvt_pk_bf16_f32 v103, v138, v139\n\t" \
  "buffer_store_dwordx4 v[100:103], %1, %11, s70 offen offset:2048 sc1\n\t" \
  "v_accvgpr_read_b32 v160, a116\n\t" \
  "v_accvgpr_read_b32 v161, a117\n\t" \
  "v_accvgpr_read_b32 v162, a118\n\t" \
  "v_accvgpr_read_b32 v163, a119\n\t" \
  "v_accvgpr_read_b32 v168, a124\n\t" \
  "v_accvgpr_read_b32 v169, a125\n\t" \
  "v_accvgpr_read_b32 v170, a126\n\t" \
  "v_accvgpr_read_b32 v171, a127\n\t" \
  "v_lshlrev_b32_e32 v176, 16, v104\n\t" \
  "v_and_b32_e32 v177, 0xffff0000, v104\n\t" \
  "v_lshlrev_b32_e32 v178, 16, v105\n\t" \
  "v_and_b32_e32 v179, 0xffff0000, v105\n\t" \
  "v_lshlrev_b32_e32 v184, 16, v106\n\t" \
  "v_and_b32_e32 v185, 0xffff0000, v106\n\t" \
  "v_lshlrev_b32_e32 v186, 16, v107\n\t" \
  "v_and_b32_e32 v187, 0xffff0000, v107\n\t" \
  "v_pk_mul_f32 v[160:161], v[160:161], v[196:197]\n\t" \
  "v_pk_mul_f32 v[162:163], v[162:163], v[196:197]\n\t" \
  "v_pk_mul_f32 v[168:169], v[168:169], v[196:197]\n\t" \
  "v_pk_mul_f32 v[170:171], v[170:171], v[196:197]\n\t" \
  "v_pk_fma_f32 v[160:161], v[176:177], v[198:199], v[160:161]\n\t" \
  "v_pk_fma_f32 v[162:163], v[178:179], v[198:199], v[162:163]\n\t" \
  "v_pk_fma_f32 v[168:169], v[184:185], v[198:199], v[168:169]\n\t" \
  "v_pk_fma_f32 v[170:171], v[186:187], v[198:199], v[170:171]\n\t" \
  "v_cvt_pk_bf16_f32 v104, v160, v161\n\t" \
  "v_cvt_pk_bf16_f32 v105, v162, v163\n\t" \
  "v_cvt_pk_bf16_f32 v106, v168, v169\n\t" \
  "v_cvt_pk_bf16_f32 v107, v170, v171\n\t" \
  "buffer_store_dwordx4 v[104:107], %1, %11, s70 offen offset:3072 sc1\n\t" \
  "buffer_load_dwordx4 a[0:3], v250, %11, s72 offen offset:0 sc1\n\t" \
  "buffer_load_dwordx4 a[4:7], v250, %11, s73 offen offset:0 sc1\n\t" \
  "buffer_load_dwordx4 a[8:11], v250, %11, s72 offen offset:1024 sc1\n\t" \
  "buffer_load_dwordx4 a[12:15], v250, %11, s73 offen offset:1024 sc1\n\t" \
  "buffer_load_dwordx4 a[16:19], v250, %11, s72 offen offset:2048 sc1\n\t" \
  "buffer_load_dwordx4 a[20:23], v250, %11, s73 offen offset:2048 sc1\n\t" \
  "buffer_load_dwordx4 a[24:27], v250, %11, s72 offen offset:3072 sc1\n\t" \
  "buffer_load_dwordx4 a[28:31], v250, %11, s73 offen offset:3072 sc1\n\t" \
  "buffer_load_dwordx4 a[32:35], v251, %11, s72 offen offset:0 sc1\n\t" \
  "buffer_load_dwordx4 a[36:39], v251, %11, s73 offen offset:0 sc1\n\t" \
  "buffer_load_dwordx4 a[40:43], v251, %11, s72 offen offset:1024 sc1\n\t" \
  "buffer_load_dwordx4 a[44:47], v251, %11, s73 offen offset:1024 sc1\n\t" \
  "v_mov_b32_e32 v38, s90\n\t" \
  "v_bfe_u32 v39, %1, 2, 4\n\t" \
  "v_and_b32_e32 v39, 4, v39\n\t" \
  "s_waitcnt vmcnt(12)\n\t" \
  "s_barrier\n\t" \
  "buffer_store_dword v38, %12, %11, s71 offen offset:0 sc1\n\t" \
  "s_waitcnt lgkmcnt(4)\n\t" \
  "v_mfma_f32_16x16x32_bf16 v[192:195], a[192:195], a[128:131], 0\n\t" \
  "s_add_u32 s30, s28, 0x8000\n\t" \
  "ds_read_b128 a[240:243], v32 offset:12288\n\t" \
  "v_mfma_f32_16x16x32_bf16 v[196:199], a[192:195], a[132:135], 0\n\t" \
  "s_cmp_lg_u32 s28, 0x20000\n\t" \
  "s_cselect_b32 s30, s30, 0\n\t" \
  "v_mfma_f32_16x16x32_bf16 v[200:203], a[196:199], a[128:131], 0\n\t" \
  "ds_read_b128 a[244:247], v32 offset:13312\n\t" \
  "s_add_u32 m0, s29, s31\n\t" \
  "v_mfma_f32_16x16x32_bf16 v[204:207], a[196:199], a[132:135], 0\n\t" \
  "v_readlane_b32 s34, v29, 24\n\t" \
  "v_mfma_f32_16x16x32_bf16 v[208:211], a[200:203], a[128:131], 0\n\t" \
  "ds_read_b128 a[248:251], v32 offset:14336\n\t" \
  "buffer_load_dwordx4 v34, %11, s26 offen offset:0 sc1 lds\n\t" \
  "v_mfma_f32_16x16x32_bf16 v[212:215], a[200:203], a[132:135], 0\n\t" \
  "v_readlane_b32 s38, v29, 25\n\t" \
  "v_mfma_f32_16x16x32_bf16 v[216:219], a[204:207], a[128:131], 0\n\t" \
  "v_add_u32_e32 v33, s30, %1\n\t" \
  "ds_read_b128 a[252:255], v32 offset:15360\n\t" \
  "v_mfma_f32_16x16x32_bf16 v[220:223], a[204:207], a[132:135], 0\n\t" \
  "v_mfma_f32_16x16x32_bf16 v[224:227], a[208:211], a[128:131], 0\n\t" \
  "ds_read_b128 a[192:195], v32 offset:16384\n\t" \
  "v_readlane_b32 s39, v29, 26\n\t" \
  "v_mfma_f32_16x16x32_bf16 v[228:231], a[208:211], a[132:135], 0\n\t" \
  "v_mfma_f32_16x16x32_bf16 v[232:235], a[212:215], a[128:131], 0\n\t" \
  "ds_read_b128 a[196:199], v32 offset:17408\n\t" \
  "buffer_load_dwordx4 v34, %11, s26 offen offset:1024 sc1 lds\n\t" \
  "v_mfma_f32_16x16x32_bf16 v[236:239], a[212:215], a[132:135], 0\n\t" \
  "v_readlane_b32 s69, v29, 27\n\t" \
  "v_mfma_f32_16x16x32_bf16 v[240:243], a[216:219], a[128:131], 0\n\t" \
  "ds_read_b128 a[200:203], v32 offset:18432\n\t" \
  "v_mfma_f32_16x16x32_bf16 v[244:247], a[216:219], a[132:135], 0\n\t" \
  "v_readlane_b32 s70, v29, 28\n\t" \
  "v_mfma_f32_16x16x32_bf16 v[192:195], a[220:223], a[136:139], v[192:195]\n\t" \
  "ds_read_b128 a[204:207], v32 offset:19456\n\t" \
  "v_mfma_f32_16x16x32_bf16 v[196:199], a[220:223], a[140:143], v[196:199]\n\t" \
  "s_waitcnt lgkmcnt(4)\n\t" \
  "v_mfma_f32_16x16x32_bf16 v[200:203], a[224:227], a[136:139], v[200:203]\n\t" \
  "ds_read_b128 a[208:211], v32 offset:20480\n\t" \
  "buffer_load_dwordx4 v34, %11, s26 offen offset:2048 sc1 lds\n\t" \
  "v_mfma_f32_16x16x32_bf16 v[204:207], a[224:227], a[140:143], v[204:207]\n\t" \
  "v_readlane_b32 s35, v29, 29\n\t" \
  "v_mfma_f32_16x16x32_bf16 v[208:211], a[228:231], a[136:139], v[208:211]\n\t" \
  "ds_read_b128 a[212:215], v32 offset:21504\n\t" \
  "v_mfma_f32_16x16x32_bf16 v[212:215], a[228:231], a[140:143], v[212:215]\n\t" \
  "v_readlane_b32 s40, v29, 38\n\t" \
  "v_mfma_f32_16x16x32_bf16 v[216:219], a[232:235], a[136:139], v[216:219]\n\t" \
  "ds_read_b128 a[216:219], v32 offset:22528\n\t" \
  "v_mfma_f32_16x16x32_bf16 v[220:223], a[232:235], a[140:143], v[220:223]\n\t" \
  "v_mfma_f32_16x16x32_bf16 v[224:227], a[236:239], a[136:139], v[224:227]\n\t" \
  "ds_read_b128 a[220:223], v32 offset:23552\n\t" \
  "buffer_load_dwordx4 v34, %11, s26 offen offset:3072 sc1 lds\n\t" \
  "v_mfma_f32_16x16x32_bf16 v[228:231], a[236:239], a[140:143], v[228:231]\n\t" \
  "v_readlane_b32 s41, v29, 39\n\t" \
  "v_mfma_f32_16x16x32_bf16 v[232:235], a[240:243], a[136:139], v[232:235]\n\t" \
  "ds_read_b128 a[224:227], v32 offset:24576\n\t" \
  "v_readlane_b32 s33, v29, 51\n\t" \
  "v_mfma_f32_16x16x32_bf16 v[236:239], a[240:243], a[140:143], v[236:239]\n\t" \
  "v_mfma_f32_16x16x32_bf16 v[240:243], a[244:247], a[136:139], v[240:243]\n\t" \
  "ds_read_b128 a[228:231], v32 offset:25600\n\t" \
  "s_add_u32 m0, m0, 0x4000\n\t" \
  "v_mfma_f32_16x16x32_bf16 v[244:247], a[244:247], a[140:143], v[244:247]\n\t" \
  "v_readlane_b32 s42, v29, 60\n\t" \
  "v_mfma_f32_16x16x32_bf16 v[192:195], a[248:251], a[144:147], v[192:195]\n\t" \
  "ds_read_b128 a[232:235], v32 offset:26624\n\t" \
  "buffer_load_dwordx4 v34, %11, s27 offen offset:0 sc1 lds\n\t" \
  "v_mfma_f32_16x16x32_bf16 v[196:199], a[248:251], a[148:151], v[196:199]\n\t" \
  "v_mfma_f32_16x16x32_bf16 v[200:203], a[252:255], a[144:147], v[200:203]\n\t" \
  "ds_read_b128 a[236:239], v32 offset:27648\n\t" \
  "v_readlane_b32 s46, v29, 61\n\t" \
  "v_mfma_f32_16x16x32_bf16 v[204:207], a[252:255], a[148:151], v[204:207]\n\t" \
  "s_waitcnt lgkmcnt(4)\n\t" \
  "v_mfma_f32_16x16x32_bf16 v[208:211], a[192:195], a[144:147], v[208:211]\n\t" \
  "ds_read_b128 a[240:243], v32 offset:28672\n\t" \
  "v_readlane_b32 s88, v29, 52\n\t" \
  "v_mfma_f32_16x16x32_bf16 v[212:215], a[192:195], a[148:151], v[212:215]\n\t" \
  "v_mfma_f32_16x16x32_bf16 v[216:219], a[196:199], a[144:147], v[216:219]\n\t" \
  "ds_read_b128 a[244:247], v32 offset:29696\n\t" \
  "buffer_load_dwordx4 v34, %11, s27 offen offset:1024 sc1 lds\n\t" \
  "v_mfma_f32_16x16x32_bf16 v[220:223], a[196:199], a[148:151], v[220:223]\n\t" \
  "v_readlane_b32 s37, v29, 50\n\t" \
  "v_mfma_f32_16x16x32_bf16 v[224:227], a[200:203], a[144:147], v[224:227]\n\t" \
  "ds_read_b128 a[248:251], v32 offset:30720\n\t" \
  "v_mfma_f32_16x16x32_bf16 v[228:231], a[200:203], a[148:151], v[228:231]\n\t" \
  "v_mfma_f32_16x16x32_bf16 v[232:235], a[204:207], a[144:147], v[232:235]\n\t" \
  "ds_read_b128 a[252:255], v32 offset:31744\n\t" \
  "v_readlane_b32 s36, v29, 57\n\t" \
  "v_mfma_f32_16x16x32_bf16 v[236:239], a[204:207], a[148:151], v[236:239]\n\t" \
  "v_mfma_f32_16x16x32_bf16 v[240:243], a[208:211], a[144:147], v[240:243]\n\t" \
  "ds_read_b128 a[192:195], v33 offset:0\n\t" \
  "buffer_load_dwordx4 v34, %11, s27 offen offset:2048 sc1 lds\n\t" \
  "v_mfma_f32_16x16x32_bf16 v[244:247], a[208:211], a[148:151], v[244:247]\n\t" \
  "s_add_u32 s76, s47, 0\n\t" \
  "v_mfma_f32_16x16x32_bf16 v[192:195], a[212:215], a[152:155], v[192:195]\n\t" \
  "ds_read_b128 a[196:199], v33 offset:1024\n\t" \
  "v_mfma_f32_16x16x32_bf16 v[196:199], a[212:215], a[156:159], v[196:199]\n\t" \
  "v_mfma_f32_16x16x32_bf16 v[200:203], a[216:219], a[152:155], v[200:203]\n\t" \
  "ds_read_b128 a[200:203], v33 offset:2048\n\t" \
  "v_mfma_f32_16x16x32_bf16 v[204:207], a[216:219], a[156:159], v[204:207]\n\t" \
  "v_mfma_f32_16x16x32_bf16 v[208:211], a[220:223], a[152:155], v[208:211]\n\t" \
  "ds_read_b128 a[204:207], v33 offset:3072\n\t" \
  "buffer_load_dwordx4 v34, %11, s27 offen offset:3072 sc1 lds\n\t" \
  "v_mfma_f32_16x16x32_bf16 v[212:215], a[220:223], a[156:159], v[212:215]\n\t" \
  "s_add_u32 s77, s47, 14336\n\t" \
  "s_waitcnt lgkmcnt(4)\n\t" \
  "v_mfma_f32_16x16x32_bf16 v[216:219], a[224:227], a[152:155], v[216:219]\n\t" \
  "ds_read_b128 a[208:211], v33 offset:4096\n\t" \
  "v_readlane_b32 s26, v27, s25\n\t" \
  "v_mfma_f32_16x16x32_bf16 v[220:223], a[224:227], a[156:159], v[220:223]\n\t" \
  "v_mfma_f32_16x16x32_bf16 v[224:227], a[228:231], a[152:155], v[224:227]\n\t" \
  "ds_read_b128 a[212:215], v33 offset:5120\n\t" \
  "v_readlane_b32 s27, v28, s25\n\t" \
  "v_mfma_f32_16x16x32_bf16 v[228:231], a[228:231], a[156:159], v[228:231]\n\t" \
  "s_add_u32 s78, s47, 28672\n\t" \
  "v_mfma_f32_16x16x32_bf16 v[232:235], a[232:235], a[152:155], v[232:235]\n\t" \
  "ds_read_b128 a[216:219], v33 offset:6144\n\t" \
  "s_add_u32 s25, s25, 1\n\t" \
  "v_mfma_f32_16x16x32_bf16 v[236:239], a[232:235], a[156:159], v[236:239]\n\t" \
  "v_mfma_f32_16x16x32_bf16 v[240:243], a[236:239], a[152:155], v[240:243]\n\t" \
  "ds_read_b128 a[220:223], v33 offset:7168\n\t" \
  "s_mov_b32 s29, s28\n\t" \
  "v_mfma_f32_16x16x32_bf16 v[244:247], a[236:239], a[156:159], v[244:247]\n\t" \
  "v_mfma_f32_16x16x32_bf16 v[192:195], a[240:243], a[160:163], v[192:195]\n\t" \
  "ds_read_b128 a[224:227], v33 offset:8192\n\t" \
  "s_mov_b32 s28, s30\n\t" \
  "v_mfma_f32_16x16x32_bf16 v[196:199], a[240:243], a[164:167], v[196:199]\n\t" \
  "s_add_u32 s79, s47, 43008\n\t" \
  "v_mfma_f32_16x16x32_bf16 v[200:203], a[244:247], a[160:163], v[200:203]\n\t" \
  "ds_read_b128 a[228:231], v33 offset:9216\n\t" \
  "v_mfma_f32_16x16x32_bf16 v[204:207], a[244:247], a[164:167], v[204:207]\n\t" \
  "v_mfma_f32_16x16x32_bf16 v[208:211], a[248:251], a[160:163], v[208:211]\n\t" \
  "ds_read_b128 a[232:235], v33 offset:10240\n\t" \
  "v_mfma_f32_16x16x32_bf16 v[212:215], a[248:251], a[164:167], v[212:215]\n\t" \
  "v_mfma_f32_16x16x32_bf16 v[216:219], a[252:255], a[160:163], v[216:219]\n\t" \
  "ds_read_b128 a[236:239], v33 offset:11264\n\t" \
  "v_mfma_f32_16x16x32_bf16 v[220:223], a[252:255], a[164:167], v[220:223]\n\t" \
  "s_barrier\n\t" \
  "s_waitcnt lgkmcnt(4)\n\t" \
  "v_mfma_f32_16x16x32_bf16 v[224:227], a[192:195], a[160:163], v[224:227]\n\t" \
  "s_add_u32 s30, s28, 0x8000\n\t" \
  "ds_read_b128 a[240:243], v33 offset:12288\n\t" \
  "v_mfma_f32_16x16x32_bf16 v[228:231], a[192:195], a[164:167], v[228:231]\n\t" \
  "s_cmp_lg_u32 s28, 0x20000\n\t" \
  "s_cselect_b32 s30, s30, 0\n\t" \
  "v_mfma_f32_16x16x32_bf16 v[232:235], a[196:199], a[160:163], v[232:235]\n\t" \
  "ds_read_b128 a[244:247], v33 offset:13312\n\t" \
  "s_add_u32 m0, s29, s31\n\t" \
  "v_mfma_f32_16x16x32_bf16 v[236:239], a[196:199], a[164:167], v[236:239]\n\t" \
  "v_subrev_u32_e32 v37, s88, %9\n\t" \
  "v_mfma_f32_16x16x32_bf16 v[240:243], a[200:203], a[160:163], v[240:243]\n\t" \
  "ds_read_b128 a[248:251], v33 offset:14336\n\t" \
  "buffer_load_dwordx4 v34, %11, s26 offen offset:0 sc1 lds\n\t" \
  "v_mfma_f32_16x16x32_bf16 v[244:247], a[200:203], a[164:167], v[244:247]\n\t" \
  "v_lshrrev_b32_e32 v35, 8, %1\n\t" \
  "v_mfma_f32_16x16x32_bf16 v[192:195], a[204:207], a[168:171], v[192:195]\n\t" \
  "v_add_u32_e32 v32, s30, %1\n\t" \
  "ds_read_b128 a[252:255], v33 offset:15360\n\t" \
  "v_mfma_f32_16x16x32_bf16 v[196:199], a[204:207], a[172:175], v[196:199]\n\t" \
  "v_lshlrev_b32_e32 v35, 3, v35\n\t" \
  "v_mfma_f32_16x16x32_bf16 v[200:203], a[208:211], a[168:171], v[200:203]\n\t" \
  "ds_read_b128 a[192:195], v33 offset:16384\n\t" \
  "buffer_store_dword v38, %12, %11, s42 offen offset:0 sc1\n\t" \
  "v_mfma_f32_16x16x32_bf16 v[204:207], a[208:211], a[172:175], v[204:207]\n\t" \
  "v_mfma_f32_16x16x32_bf16 v[208:211], a[212:215], a[168:171], v[208:211]\n\t" \
  "ds_read_b128 a[196:199], v33 offset:17408\n\t" \
  "buffer_load_dwordx4 v34, %11, s26 offen offset:1024 sc1 lds\n\t" \
  "v_mfma_f32_16x16x32_bf16 v[212:215], a[212:215], a[172:175], v[212:215]\n\t" \
  "v_mfma_f32_16x16x32_bf16 v[216:219], a[216:219], a[168:171], v[216:219]\n\t" \
  "ds_read_b128 a[200:203], v33 offset:18432\n\t" \
  "v_mfma_f32_16x16x32_bf16 v[220:223], a[216:219], a[172:175], v[220:223]\n\t" \
  "v_mfma_f32_16x16x32_bf16 v[224:227], a[220:223], a[168:171], v[224:227]\n\t" \
  "ds_read_b128 a[204:207], v33 offset:19456\n\t" \
  "v_mfma_f32_16x16x32_bf16 v[228:231], a[220:223], a[172:175], v[228:231]\n\t" \
  "s_waitcnt lgkmcnt(4)\n\t" \
  "v_mfma_f32_16x16x32_bf16 v[232:235], a[224:227], a[168:171], v[232:235]\n\t" \
  "ds_read_b128 a[208:211], v33 offset:20480\n\t" \
  "buffer_load_dwordx4 v34, %11, s26 offen offset:2048 sc1 lds\n\t" \
  "v_mfma_f32_16x16x32_bf16 v[236:239], a[224:227], a[172:175], v[236:239]\n\t" \
  "v_mfma_f32_16x16x32_bf16 v[240:243], a[228:231], a[168:171], v[240:243]\n\t" \
  "ds_read_b128 a[212:215], v33 offset:21504\n\t" \
  "v_mfma_f32_16x16x32_bf16 v[244:247], a[228:231], a[172:175], v[244:247]\n\t" \
  "v_mfma_f32_16x16x32_bf16 v[192:195], a[232:235], a[176:179], v[192:195]\n\t" \
  "ds_read_b128 a[216:219], v33 offset:22528\n\t" \
  "v_mfma_f32_16x16x32_bf16 v[196:199], a[232:235], a[180:183], v[196:199]\n\t" \
  "v_mfma_f32_16x16x32_bf16 v[200:203], a[236:239], a[176:179], v[200:203]\n\t" \
  "ds_read_b128 a[220:223], v33 offset:23552\n\t" \
  "buffer_load_dwordx4 v34, %11, s26 offen offset:3072 sc1 lds\n\t" \
  "v_mfma_f32_16x16x32_bf16 v[204:207], a[236:239], a[180:183], v[204:207]\n\t" \
  "v_mfma_f32_16x16x32_bf16 v[208:211], a[240:243], a[176:179], v[208:211]\n\t" \
  "ds_read_b128 a[224:227], v33 offset:24576\n\t" \
  "v_mfma_f32_16x16x32_bf16 v[212:215], a[240:243], a[180:183], v[212:215]\n\t" \
  "v_mfma_f32_16x16x32_bf16 v[216:219], a[244:247], a[176:179], v[216:219]\n\t" \
  "ds_read_b128 a[228:231], v33 offset:25600\n\t" \
  "s_add_u32 m0, m0, 0x4000\n\t" \
  "v_mfma_f32_16x16x32_bf16 v[220:223], a[244:247], a[180:183], v[220:223]\n\t" \
  "v_mfma_f32_16x16x32_bf16 v[224:227], a[248:251], a[176:179], v[224:227]\n\t" \
  "ds_read_b128 a[232:235], v33 offset:26624\n\t" \
  "buffer_load_dwordx4 v34, %11, s27 offen offset:0 sc1 lds\n\t" \
  "v_mfma_f32_16x16x32_bf16 v[228:231], a[248:251], a[180:183], v[228:231]\n\t" \
  "v_mfma_f32_16x16x32_bf16 v[232:235], a[252:255], a[176:179], v[232:235]\n\t" \
  "ds_read_b128 a[236:239], v33 offset:27648\n\t" \
  "buffer_load_dword v36, v39, %11, s80 offen sc1\n\t" \
  "v_mfma_f32_16x16x32_bf16 v[236:239], a[252:255], a[180:183], v[236:239]\n\t" \
  "s_waitcnt lgkmcnt(4)\n\t" \
  "v_mfma_f32_16x16x32_bf16 v[240:243], a[192:195], a[176:179], v[240:243]\n\t" \
  "ds_read_b128 a[240:243], v33 offset:28672\n\t" \
  "v_mfma_f32_16x16x32_bf16 v[244:247], a[192:195], a[180:183], v[244:247]\n\t" \
  "v_mfma_f32_16x16x32_bf16 v[192:195], a[196:199], a[184:187], v[192:195]\n\t" \
  "ds_read_b128 a[244:247], v33 offset:29696\n\t" \
  "buffer_load_dwordx4 v34, %11, s27 offen offset:1024 sc1 lds\n\t" \
  "v_mfma_f32_16x16x32_bf16 v[196:199], a[196:199], a[188:191], v[196:199]\n\t" \
  "v_mfma_f32_16x16x32_bf16 v[200:203], a[200:203], a[184:187], v[200:203]\n\t" \
  "ds_read_b128 a[248:251], v33 offset:30720\n\t" \
  "v_mfma_f32_16x16x32_bf16 v[204:207], a[200:203], a[188:191], v[204:207]\n\t" \
  "v_mfma_f32_16x16x32_bf16 v[208:211], a[204:207], a[184:187], v[208:211]\n\t" \
  "ds_read_b128 a[252:255], v33 offset:31744\n\t" \
  "v_mfma_f32_16x16x32_bf16 v[212:215], a[204:207], a[188:191], v[212:215]\n\t" \
  "v_mfma_f32_16x16x32_bf16 v[216:219], a[208:211], a[184:187], v[216:219]\n\t" \
  "ds_read_b128 a[192:195], v32 offset:0\n\t" \
  "buffer_load_dwordx4 v34, %11, s27 offen offset:2048 sc1 lds\n\t" \
  "v_mfma_f32_16x16x32_bf16 v[220:223], a[208:211], a[188:191], v[220:223]\n\t" \
  "v_mfma_f32_16x16x32_bf16 v[224:227], a[212:215], a[184:187], v[224:227]\n\t" \
  "ds_read_b128 a[196:199], v32 offset:1024\n\t" \
  "v_mfma_f32_16x16x32_bf16 v[228:231], a[212:215], a[188:191], v[228:231]\n\t" \
  "v_mfma_f32_16x16x32_bf16 v[232:235], a[216:219], a[184:187], v[232:235]\n\t" \
  "ds_read_b128 a[200:203], v32 offset:2048\n\t" \
  "v_mfma_f32_16x16x32_bf16 v[236:239], a[216:219], a[188:191], v[236:239]\n\t" \
  "v_mfma_f32_16x16x32_bf16 v[240:243], a[220:223], a[184:187], v[240:243]\n\t" \
  "ds_read_b128 a[204:207], v32 offset:3072\n\t" \
  "buffer_load_dwordx4 v34, %11, s27 offen offset:3072 sc1 lds\n\t" \
  "v_mfma_f32_16x16x32_bf16 v[244:247], a[220:223], a[188:191], v[244:247]\n\t" \
  "s_waitcnt lgkmcnt(4)\n\t" \
  "s_waitcnt vmcnt(29)\n\t" \
  "v_mfma_f32_16x16x32_bf16 v[192:195], a[224:227], a[0:3], v[192:195]\n\t" \
  "ds_read_b128 a[208:211], v32 offset:4096\n\t" \
  "v_readlane_b32 s26, v27, s25\n\t" \
  "v_mfma_f32_16x16x32_bf16 v[196:199], a[224:227], a[4:7], v[196:199]\n\t" \
  "v_mfma_f32_16x16x32_bf16 v[200:203], a[228:231], a[0:3], v[200:203]\n\t" \
  "ds_read_b128 a[212:215], v32 offset:5120\n\t" \
  "v_readlane_b32 s27, v28, s25\n\t" \
  "v_mfma_f32_16x16x32_bf16 v[204:207], a[228:231], a[4:7], v[204:207]\n\t" \
  "v_mfma_f32_16x16x32_bf16 v[208:211], a[232:235], a[0:3], v[208:211]\n\t" \
  "ds_read_b128 a[216:219], v32 offset:6144\n\t" \
  "s_add_u32 s25, s25, 1\n\t" \
  "v_mfma_f32_16x16x32_bf16 v[212:215], a[232:235], a[4:7], v[212:215]\n\t" \
  "buffer_load_dwordx2 a[160:161], v37, s[56:59], s40 offen offset:0\n\t" \
  "buffer_load_dwordx2 a[162:163], v37, s[56:59], s41 offen offset:0\n\t" \
  "v_mfma_f32_16x16x32_bf16 v[216:219], a[236:239], a[0:3], v[216:219]\n\t" \
  "ds_read_b128 a[220:223], v32 offset:7168\n\t" \
  "s_mov_b32 s29, s28\n\t" \
  "v_mfma_f32_16x16x32_bf16 v[220:223], a[236:239], a[4:7], v[220:223]\n\t" \
  "buffer_load_dwordx2 a[164:165], %9, s[56:59], s37 offen offset:0\n\t" \
  "v_mfma_f32_16x16x32_bf16 v[224:227], a[240:243], a[0:3], v[224:227]\n\t" \
  "ds_read_b128 a[224:227], v32 offset:8192\n\t" \
  "s_mov_b32 s28, s30\n\t" \
  "v_mfma_f32_16x16x32_bf16 v[228:231], a[240:243], a[4:7], v[228:231]\n\t" \
  "buffer_load_dwordx2 a[166:167], %10, s[56:59], s37 offen offset:64\n\t" \
  "v_mfma_f32_16x16x32_bf16 v[232:235], a[244:247], a[0:3], v[232:235]\n\t" \
  "ds_read_b128 a[228:231], v32 offset:9216\n\t" \
  "buffer_load_dwordx2 a[168:169], v35, s[64:67], s33 offen offset:0\n\t" \
  "v_mfma_f32_16x16x32_bf16 v[236:239], a[244:247], a[4:7], v[236:239]\n\t" \
  "buffer_load_dwordx2 a[170:171], v35, s[64:67], s33 offen offset:0\n\t" \
  "v_mfma_f32_16x16x32_bf16 v[240:243], a[248:251], a[0:3], v[240:243]\n\t" \
  "ds_read_b128 a[232:235], v32 offset:10240\n\t" \
  "v_mfma_f32_16x16x32_bf16 v[244:247], a[248:251], a[4:7], v[244:247]\n\t" \
  "buffer_load_dwordx2 a[172:173], v35, s[64:67], s36 offen offset:0\n\t" \
  "s_waitcnt vmcnt(34)\n\t" \
  "v_mfma_f32_16x16x32_bf16 v[192:195], a[252:255], a[8:11], v[192:195]\n\t" \
  "ds_read_b128 a[236:239], v32 offset:11264\n\t" \
  "buffer_load_dwordx2 a[174:175], v35, s[64:67], s36 offen offset:64\n\t" \
  "v_mfma_f32_16x16x32_bf16 v[196:199], a[252:255], a[12:15], v[196:199]\n\t" \
  "s_waitcnt vmcnt(11)\n\t" \
  "s_barrier\n\t" \
  "v_cmp_ne_u32_e32 vcc, s90, v36\n\t" \
  "s_cbranch_vccnz MK_F3P_%=\n\t" \
  "MK_F3R_%=:\n\t" \
  "s_waitcnt lgkmcnt(4)\n\t" \
  "v_mfma_f32_16x16x32_bf16 v[200:203], a[192:195], a[8:11], v[200:203]\n\t" \
  "s_add_u32 s30, s28, 0x8000\n\t" \
  "ds_read_b128 a[240:243], v32 offset:12288\n\t" \
  "v_mfma_f32_16x16x32_bf16 v[204:207], a[192:195], a[12:15], v[204:207]\n\t" \
  "s_cmp_lg_u32 s28, 0x20000\n\t" \
  "s_cselect_b32 s30, s30, 0\n\t" \
  "v_mfma_f32_16x16x32_bf16 v[208:211], a[196:199], a[8:11], v[208:211]\n\t" \
  "ds_read_b128 a[244:247], v32 offset:13312\n\t" \
  "s_add_u32 m0, s29, s31\n\t" \
  "v_mfma_f32_16x16x32_bf16 v[212:215], a[196:199], a[12:15], v[212:215]\n\t" \
  "buffer_load_dwordx4 a[48:51], v248, %11, s74 offen offset:0 sc1\n\t" \
  "v_mfma_f32_16x16x32_bf16 v[216:219], a[200:203], a[8:11], v[216:219]\n\t" \
  "ds_read_b128 a[248:251], v32 offset:14336\n\t" \
  "buffer_load_dwordx4 v34, %11, s26 offen offset:0 sc1 lds\n\t" \
  "v_mfma_f32_16x16x32_bf16 v[220:223], a[200:203], a[12:15], v[220:223]\n\t" \
  "buffer_load_dwordx4 a[52:55], v248, %11, s75 offen offset:0 sc1\n\t" \
  "v_mfma_f32_16x16x32_bf16 v[224:227], a[204:207], a[8:11], v[224:227]\n\t" \
  "v_add_u32_e32 v33, s30, %1\n\t" \
  "ds_read_b128 a[252:255], v32 offset:15360\n\t" \
  "v_mfma_f32_16x16x32_bf16 v[228:231], a[204:207], a[12:15], v[228:231]\n\t" \
  "v_mfma_f32_16x16x32_bf16 v[232:235], a[208:211], a[8:11], v[232:235]\n\t" \
  "ds_read_b128 a[192:195], v32 offset:16384\n\t" \
  "buffer_load_dwordx4 a[56:59], v248, %11, s74 offen offset:1024 sc1\n\t" \
  "v_mfma_f32_16x16x32_bf16 v[236:239], a[208:211], a[12:15], v[236:239]\n\t" \
  "v_mfma_f32_16x16x32_bf16 v[240:243], a[212:215], a[8:11], v[240:243]\n\t" \
  "ds_read_b128 a[196:199], v32 offset:17408\n\t" \
  "buffer_load_dwordx4 v34, %11, s26 offen offset:1024 sc1 lds\n\t" \
  "v_mfma_f32_16x16x32_bf16 v[244:247], a[212:215], a[12:15], v[244:247]\n\t" \
  "v_mfma_f32_16x16x32_bf16 v[192:195], a[216:219], a[16:19], v[192:195]\n\t" \
  "ds_read_b128 a[200:203], v32 offset:18432\n\t" \
  "buffer_load_dwordx4 a[60:63], v248, %11, s75 offen offset:1024 sc1\n\t" \
  "v_mfma_f32_16x16x32_bf16 v[196:199], a[216:219], a[20:23], v[196:199]\n\t" \
  "v_mfma_f32_16x16x32_bf16 v[200:203], a[220:223], a[16:19], v[200:203]\n\t" \
  "ds_read_b128 a[204:207], v32 offset:19456\n\t" \
  "v_mfma_f32_16x16x32_bf16 v[204:207], a[220:223], a[20:23], v[204:207]\n\t" \
  "s_waitcnt lgkmcnt(4)\n\t" \
  "v_mfma_f32_16x16x32_bf16 v[208:211], a[224:227], a[16:19], v[208:211]\n\t" \
  "ds_read_b128 a[208:211], v32 offset:20480\n\t" \
  "buffer_load_dwordx4 v34, %11, s26 offen offset:2048 sc1 lds\n\t" \
  "v_mfma_f32_16x16x32_bf16 v[212:215], a[224:227], a[20:23], v[212:215]\n\t" \
  "buffer_load_dwordx4 a[64:67], v248, %11, s74 offen offset:2048 sc1\n\t" \
  "v_mfma_f32_16x16x32_bf16 v[216:219], a[228:231], a[16:19], v[216:219]\n\t" \
  "ds_read_b128 a[212:215], v32 offset:21504\n\t" \
  "v_mfma_f32_16x16x32_bf16 v[220:223], a[228:231], a[20:23], v[220:223]\n\t" \
  "buffer_load_dwordx4 a[68:71], v248, %11, s75 offen offset:2048 sc1\n\t" \
  "v_mfma_f32_16x16x32_bf16 v[224:227], a[232:235], a[16:19], v[224:227]\n\t" \
  "ds_read_b128 a[216:219], v32 offset:22528\n\t" \
  "v_mfma_f32_16x16x32_bf16 v[228:231], a[232:235], a[20:23], v[228:231]\n\t" \
  "v_mfma_f32_16x16x32_bf16 v[232:235], a[236:239], a[16:19], v[232:235]\n\t" \
  "ds_read_b128 a[220:223], v32 offset:23552\n\t" \
  "buffer_load_dwordx4 v34, %11, s26 offen offset:3072 sc1 lds\n\t" \
  "v_mfma_f32_16x16x32_bf16 v[236:239], a[236:239], a[20:23], v[236:239]\n\t" \
  "buffer_load_dwordx4 a[72:75], v248, %11, s74 offen offset:3072 sc1\n\t" \
  "v_mfma_f32_16x16x32_bf16 v[240:243], a[240:243], a[16:19], v[240:243]\n\t" \
  "ds_read_b128 a[224:227], v32 offset:24576\n\t" \
  "v_mfma_f32_16x16x32_bf16 v[244:247], a[240:243], a[20:23], v[244:247]\n\t" \
  "v_mfma_f32_16x16x32_bf16 v[192:195], a[244:247], a[24:27], v[192:195]\n\t" \
  "ds_read_b128 a[228:231], v32 offset:25600\n\t" \
  "s_add_u32 m0, m0, 0x4000\n\t" \
  "v_mfma_f32_16x16x32_bf16 v[196:199], a[244:247], a[28:31], v[196:199]\n\t" \
  "buffer_load_dwordx4 a[76:79], v248, %11, s75 offen offset:3072 sc1\n\t" \
  "v_mfma_f32_16x16x32_bf16 v[200:203], a[248:251], a[24:27], v[200:203]\n\t" \
  "ds_read_b128 a[232:235], v32 offset:26624\n\t" \
  "buffer_load_dwordx4 v34, %11, s27 offen offset:0 sc1 lds\n\t" \
  "v_mfma_f32_16x16x32_bf16 v[204:207], a[248:251], a[28:31], v[204:207]\n\t" \
  "v_mfma_f32_16x16x32_bf16 v[208:211], a[252:255], a[24:27], v[208:211]\n\t" \
  "ds_read_b128 a[236:239], v32 offset:27648\n\t" \
  "v_mfma_f32_16x16x32_bf16 v[212:215], a[252:255], a[28:31], v[212:215]\n\t" \
  "buffer_load_dwordx4 a[80:83], v249, %11, s74 offen offset:0 sc1\n\t" \
  "s_waitcnt lgkmcnt(4)\n\t" \
  "v_mfma_f32_16x16x32_bf16 v[216:219], a[192:195], a[24:27], v[216:219]\n\t" \
  "ds_read_b128 a[240:243], v32 offset:28672\n\t" \
  "v_mfma_f32_16x16x32_bf16 v[220:223], a[192:195], a[28:31], v[220:223]\n\t" \
  "v_mfma_f32_16x16x32_bf16 v[224:227], a[196:199], a[24:27], v[224:227]\n\t" \
  "ds_read_b128 a[244:247], v32 offset:29696\n\t" \
  "buffer_load_dwordx4 v34, %11, s27 offen offset:1024 sc1 lds\n\t" \
  "v_mfma_f32_16x16x32_bf16 v[228:231], a[196:199], a[28:31], v[228:231]\n\t" \
  "buffer_load_dwordx4 a[84:87], v249, %11, s75 offen offset:0 sc1\n\t" \
  "v_mfma_f32_16x16x32_bf16 v[232:235], a[200:203], a[24:27], v[232:235]\n\t" \
  "ds_read_b128 a[248:251], v32 offset:30720\n\t" \
  "v_mfma_f32_16x16x32_bf16 v[236:239], a[200:203], a[28:31], v[236:239]\n\t" \
  "v_mfma_f32_16x16x32_bf16 v[240:243], a[204:207], a[24:27], v[240:243]\n\t" \
  "ds_read_b128 a[252:255], v32 offset:31744\n\t" \
  "buffer_load_dwordx4 a[88:91], v249, %11, s74 offen offset:1024 sc1\n\t" \
  "v_mfma_f32_16x16x32_bf16 v[244:247], a[204:207], a[28:31], v[244:247]\n\t" \
  "v_mfma_f32_16x16x32_bf16 v[192:195], a[208:211], a[32:35], v[192:195]\n\t" \
  "ds_read_b128 a[192:195], v33 offset:0\n\t" \
  "buffer_load_dwordx4 v34, %11, s27 offen offset:2048 sc1 lds\n\t" \
  "v_mfma_f32_16x16x32_bf16 v[196:199], a[208:211], a[36:39], v[196:199]\n\t" \
  "v_mfma_f32_16x16x32_bf16 v[200:203], a[212:215], a[32:35], v[200:203]\n\t" \
  "ds_read_b128 a[196:199], v33 offset:1024\n\t" \
  "buffer_load_dwordx4 a[92:95], v249, %11, s75 offen offset:1024 sc1\n\t" \
  "v_mfma_f32_16x16x32_bf16 v[204:207], a[212:215], a[36:39], v[204:207]\n\t" \
  "v_mfma_f32_16x16x32_bf16 v[208:211], a[216:219], a[32:35], v[208:211]\n\t" \
  "ds_read_b128 a[200:203], v33 offset:2048\n\t" \
  "v_mfma_f32_16x16x32_bf16 v[212:215], a[216:219], a[36:39], v[212:215]\n\t" \
  "v_mfma_f32_16x16x32_bf16 v[216:219], a[220:223], a[32:35], v[216:219]\n\t" \
  "ds_read_b128 a[204:207], v33 offset:3072\n\t" \
  "buffer_load_dwordx4 v34, %11, s27 offen offset:3072 sc1 lds\n\t" \
  "v_mfma_f32_16x16x32_bf16 v[220:223], a[220:223], a[36:39], v[220:223]\n\t" \
  "buffer_load_dwordx4 a[96:99], v249, %11, s74 offen offset:2048 sc1\n\t" \
  "s_waitcnt lgkmcnt(4)\n\t" \
  "v_mfma_f32_16x16x32_bf16 v[224:227], a[224:227], a[32:35], v[224:227]\n\t" \
  "ds_read_b128 a[208:211], v33 offset:4096\n\t" \
  "v_readlane_b32 s26, v27, s25\n\t" \
  "v_mfma_f32_16x16x32_bf16 v[228:231], a[224:227], a[36:39], v[228:231]\n\t" \
  "buffer_load_dwordx4 a[100:103], v249, %11, s75 offen offset:2048 sc1\n\t" \
  "v_mfma_f32_16x16x32_bf16 v[232:235], a[228:231], a[32:35], v[232:235]\n\t" \
  "ds_read_b128 a[212:215], v33 offset:5120\n\t" \
  "v_readlane_b32 s27, v28, s25\n\t" \
  "v_mfma_f32_16x16x32_bf16 v[236:239], a[228:231], a[36:39], v[236:239]\n\t" \
  "v_mfma_f32_16x16x32_bf16 v[240:243], a[232:235], a[32:35], v[240:243]\n\t" \
  "ds_read_b128 a[216:219], v33 offset:6144\n\t" \
  "s_add_u32 s25, s25, 1\n\t" \
  "v_mfma_f32_16x16x32_bf16 v[244:247], a[232:235], a[36:39], v[244:247]\n\t" \
  "buffer_load_dwordx4 a[104:107], v249, %11, s74 offen offset:3072 sc1\n\t" \
  "v_mfma_f32_16x16x32_bf16 v[192:195], a[236:239], a[40:43], v[192:195]\n\t" \
  "ds_read_b128 a[220:223], v33 offset:7168\n\t" \
  "s_mov_b32 s29, s28\n\t" \
  "v_mfma_f32_16x16x32_bf16 v[196:199], a[236:239], a[44:47], v[196:199]\n\t" \
  "v_mfma_f32_16x16x32_bf16 v[200:203], a[240:243], a[40:43], v[200:203]\n\t" \
  "ds_read_b128 a[224:227], v33 offset:8192\n\t" \
  "s_mov_b32 s28, s30\n\t" \
  "v_mfma_f32_16x16x32_bf16 v[204:207], a[240:243], a[44:47], v[204:207]\n\t" \
  "buffer_load_dwordx4 a[108:111], v249, %11, s75 offen offset:3072 sc1\n\t" \
  "v_mfma_f32_16x16x32_bf16 v[208:211], a[244:247], a[40:43], v[208:211]\n\t" \
  "ds_read_b128 a[228:231], v33 offset:9216\n\t" \
  "v_mfma_f32_16x16x32_bf16 v[212:215], a[244:247], a[44:47], v[212:215]\n\t" \
  "v_mfma_f32_16x16x32_bf16 v[216:219], a[248:251], a[40:43], v[216:219]\n\t" \
  "ds_read_b128 a[232:235], v33 offset:10240\n\t" \
  "v_mfma_f32_16x16x32_bf16 v[220:223], a[248:251], a[44:47], v[220:223]\n\t" \
  "v_mfma_f32_16x16x32_bf16 v[224:227], a[252:255], a[40:43], v[224:227]\n\t" \
  "ds_read_b128 a[236:239], v33 offset:11264\n\t" \
  "v_mfma_f32_16x16x32_bf16 v[228:231], a[252:255], a[44:47], v[228:231]\n\t" \
  "s_barrier\n\t" \
  "s_waitcnt lgkmcnt(4)\n\t" \
  "v_mfma_f32_16x16x32_bf16 v[232:235], a[192:195], a[40:43], v[232:235]\n\t" \
  "s_add_u32 s30, s28, 0x8000\n\t" \
  "ds_read_b128 a[240:243], v33 offset:12288\n\t" \
  "v_mfma_f32_16x16x32_bf16 v[236:239], a[192:195], a[44:47], v[236:239]\n\t" \
  "s_cmp_lg_u32 s28, 0x20000\n\t" \
  "s_cselect_b32 s30, s30, 0\n\t" \
  "v_mfma_f32_16x16x32_bf16 v[240:243], a[196:199], a[40:43], v[240:243]\n\t" \
  "ds_read_b128 a[244:247], v33 offset:13312\n\t" \
  "s_add_u32 m0, s29, s31\n\t" \
  "v_mfma_f32_16x16x32_bf16 v[244:247], a[196:199], a[44:47], v[244:247]\n\t" \
  "buffer_load_dwordx4 a[112:115], v250, %11, s74 offen offset:0 sc1\n\t" \
  "s_waitcnt vmcnt(22)\n\t" \
  "v_mfma_f32_16x16x32_bf16 v[192:195], a[200:203], a[48:51], v[192:195]\n\t" \
  "ds_read_b128 a[248:251], v33 offset:14336\n\t" \
  "buffer_load_dwordx4 v34, %11, s26 offen offset:0 sc1 lds\n\t" \
  "v_mfma_f32_16x16x32_bf16 v[196:199], a[200:203], a[52:55], v[196:199]\n\t" \
  "buffer_load_dwordx4 a[116:119], v250, %11, s75 offen offset:0 sc1\n\t" \
  "v_mfma_f32_16x16x32_bf16 v[200:203], a[204:207], a[48:51], v[200:203]\n\t" \
  "v_add_u32_e32 v32, s30, %1\n\t" \
  "ds_read_b128 a[252:255], v33 offset:15360\n\t" \
  "v_mfma_f32_16x16x32_bf16 v[204:207], a[204:207], a[52:55], v[204:207]\n\t" \
  "v_mfma_f32_16x16x32_bf16 v[208:211], a[208:211], a[48:51], v[208:211]\n\t" \
  "ds_read_b128 a[192:195], v33 offset:16384\n\t" \
  "buffer_load_dwordx4 a[120:123], v250, %11, s74 offen offset:1024 sc1\n\t" \
  "v_mfma_f32_16x16x32_bf16 v[212:215], a[208:211], a[52:55], v[212:215]\n\t" \
  "v_mfma_f32_16x16x32_bf16 v[216:219], a[212:215], a[48:51], v[216:219]\n\t" \
  "ds_read_b128 a[196:199], v33 offset:17408\n\t" \
  "buffer_load_dwordx4 v34, %11, s26 offen offset:1024 sc1 lds\n\t" \
  "v_mfma_f32_16x16x32_bf16 v[220:223], a[212:215], a[52:55], v[220:223]\n\t" \
  "v_mfma_f32_16x16x32_bf16 v[224:227], a[216:219], a[48:51], v[224:227]\n\t" \
  "ds_read_b128 a[200:203], v33 offset:18432\n\t" \
  "buffer_load_dwordx4 a[124:127], v250, %11, s75 offen offset:1024 sc1\n\t" \
  "v_mfma_f32_16x16x32_bf16 v[228:231], a[216:219], a[52:55], v[228:231]\n\t" \
  "v_mfma_f32_16x16x32_bf16 v[232:235], a[220:223], a[48:51], v[232:235]\n\t" \
  "ds_read_b128 a[204:207], v33 offset:19456\n\t" \
  "v_mfma_f32_16x16x32_bf16 v[236:239], a[220:223], a[52:55], v[236:239]\n\t" \
  "s_waitcnt lgkmcnt(4)\n\t" \
  "s_waitcnt vmcnt(24)\n\t" \
  "v_mfma_f32_16x16x32_bf16 v[192:195], a[224:227], a[56:59], v[192:195]\n\t" \
  "ds_read_b128 a[208:211], v33 offset:20480\n\t" \
  "buffer_load_dwordx4 v34, %11, s26 offen offset:2048 sc1 lds\n\t" \
  "v_mfma_f32_16x16x32_bf16 v[196:199], a[224:227], a[60:63], v[196:199]\n\t" \
  "buffer_load_dwordx4 v[40:43], v250, %11, s74 offen offset:2048 sc1\n\t" \
  "v_mfma_f32_16x16x32_bf16 v[200:203], a[228:231], a[56:59], v[200:203]\n\t" \
  "ds_read_b128 a[212:215], v33 offset:21504\n\t" \
  "v_mfma_f32_16x16x32_bf16 v[204:207], a[228:231], a[60:63], v[204:207]\n\t" \
  "buffer_load_dwordx4 v[44:47], v250, %11, s75 offen offset:2048 sc1\n\t" \
  "v_mfma_f32_16x16x32_bf16 v[208:211], a[232:235], a[56:59], v[208:211]\n\t" \
  "ds_read_b128 a[216:219], v33 offset:22528\n\t" \
  "v_mfma_f32_16x16x32_bf16 v[212:215], a[232:235], a[60:63], v[212:215]\n\t" \
  "v_mfma_f32_16x16x32_bf16 v[216:219], a[236:239], a[56:59], v[216:219]\n\t" \
  "ds_read_b128 a[220:223], v33 offset:23552\n\t" \
  "buffer_load_dwordx4 v34, %11, s26 offen offset:3072 sc1 lds\n\t" \
  "v_mfma_f32_16x16x32_bf16 v[220:223], a[236:239], a[60:63], v[220:223]\n\t" \
  "buffer_load_dwordx4 v[48:51], v250, %11, s74 offen offset:3072 sc1\n\t" \
  "v_mfma_f32_16x16x32_bf16 v[224:227], a[240:243], a[56:59], v[224:227]\n\t" \
  "ds_read_b128 a[224:227], v33 offset:24576\n\t" \
  "v_mfma_f32_16x16x32_bf16 v[228:231], a[240:243], a[60:63], v[228:231]\n\t" \
  "v_mfma_f32_16x16x32_bf16 v[232:235], a[244:247], a[56:59], v[232:235]\n\t" \
  "ds_read_b128 a[228:231], v33 offset:25600\n\t" \
  "s_add_u32 m0, m0, 0x4000\n\t" \
  "v_mfma_f32_16x16x32_bf16 v[236:239], a[244:247], a[60:63], v[236:239]\n\t" \
  "buffer_load_dwordx4 v[52:55], v250, %11, s75 offen offset:3072 sc1\n\t" \
  "s_waitcnt vmcnt(27)\n\t" \
  "v_mfma_f32_16x16x32_bf16 v[192:195], a[248:251], a[64:67], v[192:195]\n\t" \
  "ds_read_b128 a[232:235], v33 offset:26624\n\t" \
  "buffer_load_dwordx4 v34, %11, s27 offen offset:0 sc1 lds\n\t" \
  "v_mfma_f32_16x16x32_bf16 v[196:199], a[248:251], a[68:71], v[196:199]\n\t" \
  "v_mfma_f32_16x16x32_bf16 v[200:203], a[252:255], a[64:67], v[200:203]\n\t" \
  "ds_read_b128 a[236:239], v33 offset:27648\n\t" \
  "buffer_load_dword v36, v39, %11, s80 offen offset:8 sc1\n\t" \
  "v_mfma_f32_16x16x32_bf16 v[204:207], a[252:255], a[68:71], v[204:207]\n\t" \
  "buffer_load_dwordx4 v[56:59], v251, %11, s74 offen offset:0 sc1\n\t" \
  "s_waitcnt lgkmcnt(4)\n\t" \
  "v_mfma_f32_16x16x32_bf16 v[208:211], a[192:195], a[64:67], v[208:211]\n\t" \
  "ds_read_b128 a[240:243], v33 offset:28672\n\t" \
  "v_mfma_f32_16x16x32_bf16 v[212:215], a[192:195], a[68:71], v[212:215]\n\t" \
  "v_mfma_f32_16x16x32_bf16 v[216:219], a[196:199], a[64:67], v[216:219]\n\t" \
  "ds_read_b128 a[244:247], v33 offset:29696\n\t" \
  "buffer_load_dwordx4 v34, %11, s27 offen offset:1024 sc1 lds\n\t" \
  "v_mfma_f32_16x16x32_bf16 v[220:223], a[196:199], a[68:71], v[220:223]\n\t" \
  "buffer_load_dwordx4 v[60:63], v251, %11, s75 offen offset:0 sc1\n\t" \
  "v_mfma_f32_16x16x32_bf16 v[224:227], a[200:203], a[64:67], v[224:227]\n\t" \
  "ds_read_b128 a[248:251], v33 offset:30720\n\t" \
  "v_mfma_f32_16x16x32_bf16 v[228:231], a[200:203], a[68:71], v[228:231]\n\t" \
  "v_mfma_f32_16x16x32_bf16 v[232:235], a[204:207], a[64:67], v[232:235]\n\t" \
  "ds_read_b128 a[252:255], v33 offset:31744\n\t" \
  "buffer_load_dwordx4 v[64:67], v251, %11, s74 offen offset:1024 sc1\n\t" \
  "v_mfma_f32_16x16x32_bf16 v[236:239], a[204:207], a[68:71], v[236:239]\n\t" \
  "s_waitcnt vmcnt(30)\n\t" \
  "v_mfma_f32_16x16x32_bf16 v[192:195], a[208:211], a[72:75], v[192:195]\n\t" \
  "ds_read_b128 a[192:195], v32 offset:0\n\t" \
  "buffer_load_dwordx4 v34, %11, s27 offen offset:2048 sc1 lds\n\t" \
  "v_mfma_f32_16x16x32_bf16 v[196:199], a[208:211], a[76:79], v[196:199]\n\t" \
  "v_mfma_f32_16x16x32_bf16 v[200:203], a[212:215], a[72:75], v[200:203]\n\t" \
  "ds_read_b128 a[196:199], v32 offset:1024\n\t" \
  "buffer_load_dwordx4 v[68:71], v251, %11, s75 offen offset:1024 sc1\n\t" \
  "v_mfma_f32_16x16x32_bf16 v[204:207], a[212:215], a[76:79], v[204:207]\n\t" \
  "v_mfma_f32_16x16x32_bf16 v[208:211], a[216:219], a[72:75], v[208:211]\n\t" \
  "ds_read_b128 a[200:203], v32 offset:2048\n\t" \
  "v_mfma_f32_16x16x32_bf16 v[212:215], a[216:219], a[76:79], v[212:215]\n\t" \
  "v_mfma_f32_16x16x32_bf16 v[216:219], a[220:223], a[72:75], v[216:219]\n\t" \
  "ds_read_b128 a[204:207], v32 offset:3072\n\t" \
  "buffer_load_dwordx4 v34, %11, s27 offen offset:3072 sc1 lds\n\t" \
  "v_mfma_f32_16x16x32_bf16 v[220:223], a[220:223], a[76:79], v[220:223]\n\t" \
  "buffer_load_dwordx4 v[72:75], v251, %11, s74 offen offset:2048 sc1\n\t" \
  "s_waitcnt lgkmcnt(4)\n\t" \
  "v_mfma_f32_16x16x32_bf16 v[224:227], a[224:227], a[72:75], v[224:227]\n\t" \
  "ds_read_b128 a[208:211], v32 offset:4096\n\t" \
  "v_readlane_b32 s26, v27, s25\n\t" \
  "v_mfma_f32_16x16x32_bf16 v[228:231], a[224:227], a[76:79], v[228:231]\n\t" \
  "buffer_load_dwordx4 v[76:79], v251, %11, s75 offen offset:2048 sc1\n\t" \
  "v_mfma_f32_16x16x32_bf16 v[232:235], a[228:231], a[72:75], v[232:235]\n\t" \
  "ds_read_b128 a[212:215], v32 offset:5120\n\t" \
  "v_readlane_b32 s27, v28, s25\n\t" \
  "v_mfma_f32_16x16x32_bf16 v[236:239], a[228:231], a[76:79], v[236:239]\n\t" \
  "s_waitcnt vmcnt(31)\n\t" \
  "v_mfma_f32_16x16x32_bf16 v[192:195], a[232:235], a[80:83], v[192:195]\n\t" \
  "ds_read_b128 a[216:219], v32 offset:6144\n\t" \
  "s_add_u32 s25, s25, 1\n\t" \
  "v_mfma_f32_16x16x32_bf16 v[196:199], a[232:235], a[84:87], v[196:199]\n\t" \
  "buffer_load_dwordx4 v[80:83], v251, %11, s74 offen offset:3072 sc1\n\t" \
  "v_mfma_f32_16x16x32_bf16 v[200:203], a[236:239], a[80:83], v[200:203]\n\t" \
  "ds_read_b128 a[220:223], v32 offset:7168\n\t" \
  "s_mov_b32 s29, s28\n\t" \
  "v_mfma_f32_16x16x32_bf16 v[204:207], a[236:239], a[84:87], v[204:207]\n\t" \
  "v_mfma_f32_16x16x32_bf16 v[208:211], a[240:243], a[80:83], v[208:211]\n\t" \
  "ds_read_b128 a[224:227], v32 offset:8192\n\t" \
  "s_mov_b32 s28, s30\n\t" \
  "v_mfma_f32_16x16x32_bf16 v[212:215], a[240:243], a[84:87], v[212:215]\n\t" \
  "buffer_load_dwordx4 v[84:87], v251, %11, s75 offen offset:3072 sc1\n\t" \
  "v_mfma_f32_16x16x32_bf16 v[216:219], a[244:247], a[80:83], v[216:219]\n\t" \
  "ds_read_b128 a[228:231], v32 offset:9216\n\t" \
  "v_mfma_f32_16x16x32_bf16 v[220:223], a[244:247], a[84:87], v[220:223]\n\t" \
  "v_mfma_f32_16x16x32_bf16 v[224:227], a[248:251], a[80:83], v[224:227]\n\t" \
  "ds_read_b128 a[232:235], v32 offset:10240\n\t" \
  "v_mfma_f32_16x16x32_bf16 v[228:231], a[248:251], a[84:87], v[228:231]\n\t" \
  "v_mfma_f32_16x16x32_bf16 v[232:235], a[252:255], a[80:83], v[232:235]\n\t" \
  "ds_read_b128 a[236:239], v32 offset:11264\n\t" \
  "v_mfma_f32_16x16x32_bf16 v[236:239], a[252:255], a[84:87], v[236:239]\n\t" \
  "s_waitcnt vmcnt(11)\n\t" \
  "s_barrier\n\t" \
  "v_cmp_ne_u32_e32 vcc, s90, v36\n\t" \
  "s_cbranch_vccnz MK_F3LP_%=\n\t" \
  "MK_F3LR_%=:\n\t" \
  "s_waitcnt lgkmcnt(4)\n\t" \
  "v_mfma_f32_16x16x32_bf16 v[192:195], a[192:195], a[88:91], v[192:195]\n\t" \
  "s_add_u32 s30, s28, 0x8000\n\t" \
  "ds_read_b128 a[240:243], v32 offset:12288\n\t" \
  "v_mfma_f32_16x16x32_bf16 v[196:199], a[192:195], a[92:95], v[196:199]\n\t" \
  "s_cmp_lg_u32 s28, 0x20000\n\t" \
  "s_cselect_b32 s30, s30, 0\n\t" \
  "v_mfma_f32_16x16x32_bf16 v[200:203], a[196:199], a[88:91], v[200:203]\n\t" \
  "ds_read_b128 a[244:247], v32 offset:13312\n\t" \
  "s_add_u32 m0, s29, s31\n\t" \
  "v_mfma_f32_16x16x32_bf16 v[204:207], a[196:199], a[92:95], v[204:207]\n\t" \
  "buffer_load_dwordx4 v[88:91], v252, %11, s74 offen offset:0 sc1\n\t" \
  "v_mfma_f32_16x16x32_bf16 v[208:211], a[200:203], a[88:91], v[208:211]\n\t" \
  "ds_read_b128 a[248:251], v32 offset:14336\n\t" \
  "buffer_load_dwordx4 v34, %11, s26 offen offset:0 sc1 lds\n\t" \
  "v_mfma_f32_16x16x32_bf16 v[212:215], a[200:203], a[92:95], v[212:215]\n\t" \
  "v_mfma_f32_16x16x32_bf16 v[216:219], a[204:207], a[88:91], v[216:219]\n\t" \
  "v_add_u32_e32 v33, s30, %1\n\t" \
  "ds_read_b128 a[252:255], v32 offset:15360\n\t" \
  "v_mfma_f32_16x16x32_bf16 v[220:223], a[204:207], a[92:95], v[220:223]\n\t" \
  "buffer_load_dwordx4 v[92:95], v252, %11, s75 offen offset:0 sc1\n\t" \
  "v_mfma_f32_16x16x32_bf16 v[224:227], a[208:211], a[88:91], v[224:227]\n\t" \
  "ds_read_b128 a[192:195], v32 offset:16384\n\t" \
  "v_mfma_f32_16x16x32_bf16 v[228:231], a[208:211], a[92:95], v[228:231]\n\t" \
  "v_mfma_f32_16x16x32_bf16 v[232:235], a[212:215], a[88:91], v[232:235]\n\t" \
  "ds_read_b128 a[196:199], v32 offset:17408\n\t" \
  "buffer_load_dwordx4 v34, %11, s26 offen offset:1024 sc1 lds\n\t" \
  "v_mfma_f32_16x16x32_bf16 v[236:239], a[212:215], a[92:95], v[236:239]\n\t" \
  "buffer_load_dwordx4 v[96:99], v252, %11, s74 offen offset:1024 sc1\n\t" \
  "v_mfma_f32_16x16x32_bf16 v[192:195], a[216:219], a[96:99], v[192:195]\n\t" \
  "ds_read_b128 a[200:203], v32 offset:18432\n\t" \
  "v_mfma_f32_16x16x32_bf16 v[196:199], a[216:219], a[100:103], v[196:199]\n\t" \
  "v_mfma_f32_16x16x32_bf16 v[200:203], a[220:223], a[96:99], v[200:203]\n\t" \
  "ds_read_b128 a[204:207], v32 offset:19456\n\t" \
  "v_mfma_f32_16x16x32_bf16 v[204:207], a[220:223], a[100:103], v[204:207]\n\t" \
  "s_waitcnt lgkmcnt(4)\n\t" \
  "v_mfma_f32_16x16x32_bf16 v[208:211], a[224:227], a[96:99], v[208:211]\n\t" \
  "ds_read_b128 a[208:211], v32 offset:20480\n\t" \
  "buffer_load_dwordx4 v34, %11, s26 offen offset:2048 sc1 lds\n\t" \
  "v_mfma_f32_16x16x32_bf16 v[212:215], a[224:227], a[100:103], v[212:215]\n\t" \
  "buffer_load_dwordx4 v[100:103], v252, %11, s75 offen offset:1024 sc1\n\t" \
  "v_mfma_f32_16x16x32_bf16 v[216:219], a[228:231], a[96:99], v[216:219]\n\t" \
  "ds_read_b128 a[212:215], v32 offset:21504\n\t" \
  "v_mfma_f32_16x16x32_bf16 v[220:223], a[228:231], a[100:103], v[220:223]\n\t" \
  "v_mfma_f32_16x16x32_bf16 v[224:227], a[232:235], a[96:99], v[224:227]\n\t" \
  "ds_read_b128 a[216:219], v32 offset:22528\n\t" \
  "v_mfma_f32_16x16x32_bf16 v[228:231], a[232:235], a[100:103], v[228:231]\n\t" \
  "v_mfma_f32_16x16x32_bf16 v[232:235], a[236:239], a[96:99], v[232:235]\n\t" \
  "ds_read_b128 a[220:223], v32 offset:23552\n\t" \
  "buffer_load_dwordx4 v34, %11, s26 offen offset:3072 sc1 lds\n\t" \
  "v_mfma_f32_16x16x32_bf16 v[236:239], a[236:239], a[100:103], v[236:239]\n\t" \
  "buffer_load_dwordx4 v[104:107], v252, %11, s74 offen offset:2048 sc1\n\t" \
  "v_mfma_f32_16x16x32_bf16 v[192:195], a[240:243], a[104:107], v[192:195]\n\t" \
  "ds_read_b128 a[224:227], v32 offset:24576\n\t" \
  "v_mfma_f32_16x16x32_bf16 v[196:199], a[240:243], a[108:111], v[196:199]\n\t" \
  "v_mfma_f32_16x16x32_bf16 v[200:203], a[244:247], a[104:107], v[200:203]\n\t" \
  "ds_read_b128 a[228:231], v32 offset:25600\n\t" \
  "s_add_u32 m0, m0, 0x4000\n\t" \
  "v_mfma_f32_16x16x32_bf16 v[204:207], a[244:247], a[108:111], v[204:207]\n\t" \
  "v_mfma_f32_16x16x32_bf16 v[208:211], a[248:251], a[104:107], v[208:211]\n\t" \
  "ds_read_b128 a[232:235], v32 offset:26624\n\t" \
  "buffer_load_dwordx4 v34, %11, s27 offen offset:0 sc1 lds\n\t" \
  "v_mfma_f32_16x16x32_bf16 v[212:215], a[248:251], a[108:111], v[212:215]\n\t" \
  "buffer_load_dwordx4 v[108:111], v252, %11, s75 offen offset:2048 sc1\n\t" \
  "v_mfma_f32_16x16x32_bf16 v[216:219], a[252:255], a[104:107], v[216:219]\n\t" \
  "ds_read_b128 a[236:239], v32 offset:27648\n\t" \
  "v_mfma_f32_16x16x32_bf16 v[220:223], a[252:255], a[108:111], v[220:223]\n\t" \
  "s_waitcnt lgkmcnt(4)\n\t" \
  "v_mfma_f32_16x16x32_bf16 v[224:227], a[192:195], a[104:107], v[224:227]\n\t" \
  "ds_read_b128 a[240:243], v32 offset:28672\n\t" \
  "v_mfma_f32_16x16x32_bf16 v[228:231], a[192:195], a[108:111], v[228:231]\n\t" \
  "buffer_load_dwordx4 v[112:115], v252, %11, s74 offen offset:3072 sc1\n\t" \
  "v_mfma_f32_16x16x32_bf16 v[232:235], a[196:199], a[104:107], v[232:235]\n\t" \
  "ds_read_b128 a[244:247], v32 offset:29696\n\t" \
  "buffer_load_dwordx4 v34, %11, s27 offen offset:1024 sc1 lds\n\t" \
  "v_mfma_f32_16x16x32_bf16 v[236:239], a[196:199], a[108:111], v[236:239]\n\t" \
  "v_mfma_f32_16x16x32_bf16 v[192:195], a[200:203], a[112:115], v[192:195]\n\t" \
  "ds_read_b128 a[248:251], v32 offset:30720\n\t" \
  "v_mfma_f32_16x16x32_bf16 v[196:199], a[200:203], a[116:119], v[196:199]\n\t" \
  "v_mfma_f32_16x16x32_bf16 v[200:203], a[204:207], a[112:115], v[200:203]\n\t" \
  "ds_read_b128 a[252:255], v32 offset:31744\n\t" \
  "v_mfma_f32_16x16x32_bf16 v[204:207], a[204:207], a[116:119], v[204:207]\n\t" \
  "buffer_load_dwordx4 v[116:119], v252, %11, s75 offen offset:3072 sc1\n\t" \
  "v_mfma_f32_16x16x32_bf16 v[208:211], a[208:211], a[112:115], v[208:211]\n\t" \
  "ds_read_b128 a[192:195], v33 offset:0\n\t" \
  "buffer_load_dwordx4 v34, %11, s27 offen offset:2048 sc1 lds\n\t" \
  "v_mfma_f32_16x16x32_bf16 v[212:215], a[208:211], a[116:119], v[212:215]\n\t" \
  "v_mfma_f32_16x16x32_bf16 v[216:219], a[212:215], a[112:115], v[216:219]\n\t" \
  "ds_read_b128 a[196:199], v33 offset:1024\n\t" \
  "v_mfma_f32_16x16x32_bf16 v[220:223], a[212:215], a[116:119], v[220:223]\n\t" \
  "v_mfma_f32_16x16x32_bf16 v[224:227], a[216:219], a[112:115], v[224:227]\n\t" \
  "ds_read_b128 a[200:203], v33 offset:2048\n\t" \
  "buffer_load_dwordx4 v[120:123], v253, %11, s74 offen offset:0 sc1\n\t" \
  "v_mfma_f32_16x16x32_bf16 v[228:231], a[216:219], a[116:119], v[228:231]\n\t" \
  "v_mfma_f32_16x16x32_bf16 v[232:235], a[220:223], a[112:115], v[232:235]\n\t" \
  "ds_read_b128 a[204:207], v33 offset:3072\n\t" \
  "buffer_load_dwordx4 v34, %11, s27 offen offset:3072 sc1 lds\n\t" \
  "v_mfma_f32_16x16x32_bf16 v[236:239], a[220:223], a[116:119], v[236:239]\n\t" \
  "s_waitcnt lgkmcnt(4)\n\t" \
  "v_mfma_f32_16x16x32_bf16 v[192:195], a[224:227], a[120:123], v[192:195]\n\t" \
  "ds_read_b128 a[208:211], v33 offset:4096\n\t" \
  "v_readlane_b32 s26, v27, s25\n\t" \
  "v_mfma_f32_16x16x32_bf16 v[196:199], a[224:227], a[124:127], v[196:199]\n\t" \
  "v_mfma_f32_16x16x32_bf16 v[200:203], a[228:231], a[120:123], v[200:203]\n\t" \
  "ds_read_b128 a[212:215], v33 offset:5120\n\t" \
  "v_readlane_b32 s27, v28, s25\n\t" \
  "v_mfma_f32_16x16x32_bf16 v[204:207], a[228:231], a[124:127], v[204:207]\n\t" \
  "buffer_load_dwordx4 v[124:127], v253, %11, s75 offen offset:0 sc1\n\t" \
  "v_mfma_f32_16x16x32_bf16 v[208:211], a[232:235], a[120:123], v[208:211]\n\t" \
  "ds_read_b128 a[216:219], v33 offset:6144\n\t" \
  "s_add_u32 s25, s25, 1\n\t" \
  "v_mfma_f32_16x16x32_bf16 v[212:215], a[232:235], a[124:127], v[212:215]\n\t" \
  "v_mfma_f32_16x16x32_bf16 v[216:219], a[236:239], a[120:123], v[216:219]\n\t" \
  "ds_read_b128 a[220:223], v33 offset:7168\n\t" \
  "s_mov_b32 s29, s28\n\t" \
  "v_mfma_f32_16x16x32_bf16 v[220:223], a[236:239], a[124:127], v[220:223]\n\t" \
  "buffer_load_dwordx4 v[128:131], v253, %11, s74 offen offset:1024 sc1\n\t" \
  "v_mfma_f32_16x16x32_bf16 v[224:227], a[240:243], a[120:123], v[224:227]\n\t" \
  "ds_read_b128 a[224:227], v33 offset:8192\n\t" \
  "s_mov_b32 s28, s30\n\t" \
  "v_mfma_f32_16x16x32_bf16 v[228:231], a[240:243], a[124:127], v[228:231]\n\t" \
  "v_mfma_f32_16x16x32_bf16 v[232:235], a[244:247], a[120:123], v[232:235]\n\t" \
  "ds_read_b128 a[228:231], v33 offset:9216\n\t" \
  "v_mfma_f32_16x16x32_bf16 v[236:239], a[244:247], a[124:127], v[236:239]\n\t" \
  "v_mfma_f32_16x16x32_bf16 v[192:195], a[248:251], v[40:43], v[192:195]\n\t" \
  "ds_read_b128 a[232:235], v33 offset:10240\n\t" \
  "v_mfma_f32_16x16x32_bf16 v[196:199], a[248:251], v[44:47], v[196:199]\n\t" \
  "v_mfma_f32_16x16x32_bf16 v[200:203], a[252:255], v[40:43], v[200:203]\n\t" \
  "ds_read_b128 a[236:239], v33 offset:11264\n\t" \
  "v_mfma_f32_16x16x32_bf16 v[204:207], a[252:255], v[44:47], v[204:207]\n\t" \
  "s_barrier\n\t" \
  "s_waitcnt lgkmcnt(4)\n\t" \
  "v_mfma_f32_16x16x32_bf16 v[208:211], a[192:195], v[40:43], v[208:211]\n\t" \
  "s_add_u32 s30, s28, 0x8000\n\t" \
  "ds_read_b128 a[240:243], v33 offset:12288\n\t" \
  "v_mfma_f32_16x16x32_bf16 v[212:215], a[192:195], v[44:47], v[212:215]\n\t" \
  "s_cmp_lg_u32 s28, 0x20000\n\t" \
  "s_cselect_b32 s30, s30, 0\n\t" \
  "v_mfma_f32_16x16x32_bf16 v[216:219], a[196:199], v[40:43], v[216:219]\n\t" \
  "ds_read_b128 a[244:247], v33 offset:13312\n\t" \
  "s_add_u32 m0, s29, s31\n\t" \
  "v_mfma_f32_16x16x32_bf16 v[220:223], a[196:199], v[44:47], v[220:223]\n\t" \
  "buffer_load_dwordx4 v[132:135], v253, %11, s75 offen offset:1024 sc1\n\t" \
  "v_mfma_f32_16x16x32_bf16 v[224:227], a[200:203], v[40:43], v[224:227]\n\t" \
  "ds_read_b128 a[248:251], v33 offset:14336\n\t" \
  "buffer_load_dwordx4 v34, %11, s26 offen offset:0 sc1 lds\n\t" \
  "v_mfma_f32_16x16x32_bf16 v[228:231], a[200:203], v[44:47], v[228:231]\n\t" \
  "v_mfma_f32_16x16x32_bf16 v[232:235], a[204:207], v[40:43], v[232:235]\n\t" \
  "v_add_u32_e32 v32, s30, %1\n\t" \
  "ds_read_b128 a[252:255], v33 offset:15360\n\t" \
  "v_mfma_f32_16x16x32_bf16 v[236:239], a[204:207], v[44:47], v[236:239]\n\t" \
  "buffer_load_dwordx4 v[136:139], v253, %11, s74 offen offset:2048 sc1\n\t" \
  "v_mfma_f32_16x16x32_bf16 v[192:195], a[208:211], v[48:51], v[192:195]\n\t" \
  "ds_read_b128 a[192:195], v33 offset:16384\n\t" \
  "v_mfma_f32_16x16x32_bf16 v[196:199], a[208:211], v[52:55], v[196:199]\n\t" \
  "v_mfma_f32_16x16x32_bf16 v[200:203], a[212:215], v[48:51], v[200:203]\n\t" \
  "ds_read_b128 a[196:199], v33 offset:17408\n\t" \
  "buffer_load_dwordx4 v34, %11, s26 offen offset:1024 sc1 lds\n\t" \
  "v_mfma_f32_16x16x32_bf16 v[204:207], a[212:215], v[52:55], v[204:207]\n\t" \
  "buffer_load_dwordx4 v[140:143], v253, %11, s75 offen offset:2048 sc1\n\t" \
  "v_mfma_f32_16x16x32_bf16 v[208:211], a[216:219], v[48:51], v[208:211]\n\t" \
  "ds_read_b128 a[200:203], v33 offset:18432\n\t" \
  "v_mfma_f32_16x16x32_bf16 v[212:215], a[216:219], v[52:55], v[212:215]\n\t" \
  "v_mfma_f32_16x16x32_bf16 v[216:219], a[220:223], v[48:51], v[216:219]\n\t" \
  "ds_read_b128 a[204:207], v33 offset:19456\n\t" \
  "v_mfma_f32_16x16x32_bf16 v[220:223], a[220:223], v[52:55], v[220:223]\n\t" \
  "s_waitcnt lgkmcnt(4)\n\t" \
  "v_mfma_f32_16x16x32_bf16 v[224:227], a[224:227], v[48:51], v[224:227]\n\t" \
  "ds_read_b128 a[208:211], v33 offset:20480\n\t" \
  "buffer_load_dwordx4 v34, %11, s26 offen offset:2048 sc1 lds\n\t" \
  "v_mfma_f32_16x16x32_bf16 v[228:231], a[224:227], v[52:55], v[228:231]\n\t" \
  "buffer_load_dwordx4 v[144:147], v253, %11, s74 offen offset:3072 sc1\n\t" \
  "v_mfma_f32_16x16x32_bf16 v[232:235], a[228:231], v[48:51], v[232:235]\n\t" \
  "ds_read_b128 a[212:215], v33 offset:21504\n\t" \
  "v_mfma_f32_16x16x32_bf16 v[236:239], a[228:231], v[52:55], v[236:239]\n\t" \
  "s_waitcnt vmcnt(34)\n\t" \
  "v_mfma_f32_16x16x32_bf16 v[192:195], a[232:235], v[56:59], v[192:195]\n\t" \
  "ds_read_b128 a[216:219], v33 offset:22528\n\t" \
  "v_mfma_f32_16x16x32_bf16 v[196:199], a[232:235], v[60:63], v[196:199]\n\t" \
  "v_mfma_f32_16x16x32_bf16 v[200:203], a[236:239], v[56:59], v[200:203]\n\t" \
  "ds_read_b128 a[220:223], v33 offset:23552\n\t" \
  "buffer_load_dwordx4 v34, %11, s26 offen offset:3072 sc1 lds\n\t" \
  "v_mfma_f32_16x16x32_bf16 v[204:207], a[236:239], v[60:63], v[204:207]\n\t" \
  "buffer_load_dwordx4 v[148:151], v253, %11, s75 offen offset:3072 sc1\n\t" \
  "v_mfma_f32_16x16x32_bf16 v[208:211], a[240:243], v[56:59], v[208:211]\n\t" \
  "ds_read_b128 a[224:227], v33 offset:24576\n\t" \
  "v_mfma_f32_16x16x32_bf16 v[212:215], a[240:243], v[60:63], v[212:215]\n\t" \
  "v_mfma_f32_16x16x32_bf16 v[216:219], a[244:247], v[56:59], v[216:219]\n\t" \
  "ds_read_b128 a[228:231], v33 offset:25600\n\t" \
  "s_add_u32 m0, m0, 0x4000\n\t" \
  "v_mfma_f32_16x16x32_bf16 v[220:223], a[244:247], v[60:63], v[220:223]\n\t" \
  "v_mfma_f32_16x16x32_bf16 v[224:227], a[248:251], v[56:59], v[224:227]\n\t" \
  "ds_read_b128 a[232:235], v33 offset:26624\n\t" \
  "buffer_load_dwordx4 v34, %11, s27 offen offset:0 sc1 lds\n\t" \
  "v_mfma_f32_16x16x32_bf16 v[228:231], a[248:251], v[60:63], v[228:231]\n\t" \
  "buffer_load_dwordx4 v[152:155], v254, %11, s74 offen offset:0 sc1\n\t" \
  "v_mfma_f32_16x16x32_bf16 v[232:235], a[252:255], v[56:59], v[232:235]\n\t" \
  "ds_read_b128 a[236:239], v33 offset:27648\n\t" \
  "v_mfma_f32_16x16x32_bf16 v[236:239], a[252:255], v[60:63], v[236:239]\n\t" \
  "s_waitcnt lgkmcnt(4)\n\t" \
  "s_waitcnt vmcnt(35)\n\t" \
  "v_mfma_f32_16x16x32_bf16 v[192:195], a[192:195], v[64:67], v[192:195]\n\t" \
  "ds_read_b128 a[240:243], v33 offset:28672\n\t" \
  "v_mfma_f32_16x16x32_bf16 v[196:199], a[192:195], v[68:71], v[196:199]\n\t" \
  "buffer_load_dwordx4 v[156:159], v254, %11, s75 offen offset:0 sc1\n\t" \
  "v_mfma_f32_16x16x32_bf16 v[200:203], a[196:199], v[64:67], v[200:203]\n\t" \
  "ds_read_b128 a[244:247], v33 offset:29696\n\t" \
  "buffer_load_dwordx4 v34, %11, s27 offen offset:1024 sc1 lds\n\t" \
  "v_mfma_f32_16x16x32_bf16 v[204:207], a[196:199], v[68:71], v[204:207]\n\t" \
  "v_mfma_f32_16x16x32_bf16 v[208:211], a[200:203], v[64:67], v[208:211]\n\t" \
  "ds_read_b128 a[248:251], v33 offset:30720\n\t" \
  "v_mfma_f32_16x16x32_bf16 v[212:215], a[200:203], v[68:71], v[212:215]\n\t" \
  "v_mfma_f32_16x16x32_bf16 v[216:219], a[204:207], v[64:67], v[216:219]\n\t" \
  "ds_read_b128 a[252:255], v33 offset:31744\n\t" \
  "v_mfma_f32_16x16x32_bf16 v[220:223], a[204:207], v[68:71], v[220:223]\n\t" \
  "buffer_load_dwordx4 v[160:163], v254, %11, s74 offen offset:1024 sc1\n\t" \
  "v_mfma_f32_16x16x32_bf16 v[224:227], a[208:211], v[64:67], v[224:227]\n\t" \
  "ds_read_b128 a[192:195], v32 offset:0\n\t" \
  "buffer_load_dwordx4 v34, %11, s27 offen offset:2048 sc1 lds\n\t" \
  "v_mfma_f32_16x16x32_bf16 v[228:231], a[208:211], v[68:71], v[228:231]\n\t" \
  "v_mfma_f32_16x16x32_bf16 v[232:235], a[212:215], v[64:67], v[232:235]\n\t" \
  "ds_read_b128 a[196:199], v32 offset:1024\n\t" \
  "v_mfma_f32_16x16x32_bf16 v[236:239], a[212:215], v[68:71], v[236:239]\n\t" \
  "s_waitcnt vmcnt(36)\n\t" \
  "v_mfma_f32_16x16x32_bf16 v[192:195], a[216:219], v[72:75], v[192:195]\n\t" \
  "ds_read_b128 a[200:203], v32 offset:2048\n\t" \
  "buffer_load_dwordx4 v[164:167], v254, %11, s75 offen offset:1024 sc1\n\t" \
  "v_mfma_f32_16x16x32_bf16 v[196:199], a[216:219], v[76:79], v[196:199]\n\t" \
  "v_mfma_f32_16x16x32_bf16 v[200:203], a[220:223], v[72:75], v[200:203]\n\t" \
  "ds_read_b128 a[204:207], v32 offset:3072\n\t" \
  "buffer_load_dwordx4 v34, %11, s27 offen offset:3072 sc1 lds\n\t" \
  "v_mfma_f32_16x16x32_bf16 v[204:207], a[220:223], v[76:79], v[204:207]\n\t" \
  "s_waitcnt lgkmcnt(4)\n\t" \
  "v_mfma_f32_16x16x32_bf16 v[208:211], a[224:227], v[72:75], v[208:211]\n\t" \
  "ds_read_b128 a[208:211], v32 offset:4096\n\t" \
  "v_readlane_b32 s26, v27, s25\n\t" \
  "v_mfma_f32_16x16x32_bf16 v[212:215], a[224:227], v[76:79], v[212:215]\n\t" \
  "v_mfma_f32_16x16x32_bf16 v[216:219], a[228:231], v[72:75], v[216:219]\n\t" \
  "ds_read_b128 a[212:215], v32 offset:5120\n\t" \
  "v_readlane_b32 s27, v28, s25\n\t" \
  "v_mfma_f32_16x16x32_bf16 v[220:223], a[228:231], v[76:79], v[220:223]\n\t" \
  "buffer_load_dwordx4 v[168:171], v254, %11, s74 offen offset:2048 sc1\n\t" \
  "v_mfma_f32_16x16x32_bf16 v[224:227], a[232:235], v[72:75], v[224:227]\n\t" \
  "ds_read_b128 a[216:219], v32 offset:6144\n\t" \
  "s_add_u32 s25, s25, 1\n\t" \
  "v_mfma_f32_16x16x32_bf16 v[228:231], a[232:235], v[76:79], v[228:231]\n\t" \
  "v_mfma_f32_16x16x32_bf16 v[232:235], a[236:239], v[72:75], v[232:235]\n\t" \
  "ds_read_b128 a[220:223], v32 offset:7168\n\t" \
  "s_mov_b32 s29, s28\n\t" \
  "v_mfma_f32_16x16x32_bf16 v[236:239], a[236:239], v[76:79], v[236:239]\n\t" \
  "buffer_load_dwordx4 v[172:175], v254, %11, s75 offen offset:2048 sc1\n\t" \
  "s_waitcnt vmcnt(38)\n\t" \
  "v_mfma_f32_16x16x32_bf16 v[192:195], a[240:243], v[80:83], v[192:195]\n\t" \
  "ds_read_b128 a[224:227], v32 offset:8192\n\t" \
  "s_mov_b32 s28, s30\n\t" \
  "v_mfma_f32_16x16x32_bf16 v[196:199], a[240:243], v[84:87], v[196:199]\n\t" \
  "v_mfma_f32_16x16x32_bf16 v[200:203], a[244:247], v[80:83], v[200:203]\n\t" \
  "ds_read_b128 a[228:231], v32 offset:9216\n\t" \
  "v_mfma_f32_16x16x32_bf16 v[204:207], a[244:247], v[84:87], v[204:207]\n\t" \
  "v_mfma_f32_16x16x32_bf16 v[208:211], a[248:251], v[80:83], v[208:211]\n\t" \
  "ds_read_b128 a[232:235], v32 offset:10240\n\t" \
  "v_mfma_f32_16x16x32_bf16 v[212:215], a[248:251], v[84:87], v[212:215]\n\t" \
  "v_mfma_f32_16x16x32_bf16 v[216:219], a[252:255], v[80:83], v[216:219]\n\t" \
  "ds_read_b128 a[236:239], v32 offset:11264\n\t" \
  "v_mfma_f32_16x16x32_bf16 v[220:223], a[252:255], v[84:87], v[220:223]\n\t" \
  "s_barrier\n\t" \
  "s_waitcnt lgkmcnt(4)\n\t" \
  "v_mfma_f32_16x16x32_bf16 v[224:227], a[192:195], v[80:83], v[224:227]\n\t" \
  "s_add_u32 s30, s28, 0x8000\n\t" \
  "ds_read_b128 a[240:243], v32 offset:12288\n\t" \
  "v_mfma_f32_16x16x32_bf16 v[228:231], a[192:195], v[84:87], v[228:231]\n\t" \
  "s_cmp_lg_u32 s28, 0x20000\n\t" \
  "s_cselect_b32 s30, s30, 0\n\t" \
  "v_mfma_f32_16x16x32_bf16 v[232:235], a[196:199], v[80:83], v[232:235]\n\t" \
  "ds_read_b128 a[244:247], v32 offset:13312\n\t" \
  "s_add_u32 m0, s29, s31\n\t" \
  "v_mfma_f32_16x16x32_bf16 v[236:239], a[196:199], v[84:87], v[236:239]\n\t" \
  "buffer_load_dwordx4 v[176:179], v254, %11, s74 offen offset:3072 sc1\n\t" \
  "s_waitcnt vmcnt(36)\n\t" \
  "v_mfma_f32_16x16x32_bf16 v[192:195], a[200:203], v[88:91], v[192:195]\n\t" \
  "ds_read_b128 a[248:251], v32 offset:14336\n\t" \
  "buffer_load_dwordx4 v34, %11, s26 offen offset:0 sc1 lds\n\t" \
  "v_mfma_f32_16x16x32_bf16 v[196:199], a[200:203], v[92:95], v[196:199]\n\t" \
  "v_mfma_f32_16x16x32_bf16 v[200:203], a[204:207], v[88:91], v[200:203]\n\t" \
  "v_add_u32_e32 v33, s30, %1\n\t" \
  "ds_read_b128 a[252:255], v32 offset:15360\n\t" \
  "v_mfma_f32_16x16x32_bf16 v[204:207], a[204:207], v[92:95], v[204:207]\n\t" \
  "buffer_load_dwordx4 v[180:183], v254, %11, s75 offen offset:3072 sc1\n\t" \
  "v_mfma_f32_16x16x32_bf16 v[208:211], a[208:211], v[88:91], v[208:211]\n\t" \
  "ds_read_b128 a[192:195], v32 offset:16384\n\t" \
  "v_mfma_f32_16x16x32_bf16 v[212:215], a[208:211], v[92:95], v[212:215]\n\t" \
  "v_mfma_f32_16x16x32_bf16 v[216:219], a[212:215], v[88:91], v[216:219]\n\t" \
  "ds_read_b128 a[196:199], v32 offset:17408\n\t" \
  "buffer_load_dwordx4 v34, %11, s26 offen offset:1024 sc1 lds\n\t" \
  "v_mfma_f32_16x16x32_bf16 v[220:223], a[212:215], v[92:95], v[220:223]\n\t" \
  "v_mfma_f32_16x16x32_bf16 v[224:227], a[216:219], v[88:91], v[224:227]\n\t" \
  "ds_read_b128 a[200:203], v32 offset:18432\n\t" \
  "v_mfma_f32_16x16x32_bf16 v[228:231], a[216:219], v[92:95], v[228:231]\n\t" \
  "buffer_load_dwordx4 v[184:187], v255, %11, s74 offen offset:0 sc1\n\t" \
  "v_mfma_f32_16x16x32_bf16 v[232:235], a[220:223], v[88:91], v[232:235]\n\t" \
  "ds_read_b128 a[204:207], v32 offset:19456\n\t" \
  "v_mfma_f32_16x16x32_bf16 v[236:239], a[220:223], v[92:95], v[236:239]\n\t" \
  "s_waitcnt lgkmcnt(4)\n\t" \
  "s_waitcnt vmcnt(36)\n\t" \
  "v_mfma_f32_16x16x32_bf16 v[192:195], a[224:227], v[96:99], v[192:195]\n\t" \
  "ds_read_b128 a[208:211], v32 offset:20480\n\t" \
  "buffer_load_dwordx4 v34, %11, s26 offen offset:2048 sc1 lds\n\t" \
  "v_mfma_f32_16x16x32_bf16 v[196:199], a[224:227], v[100:103], v[196:199]\n\t" \
  "v_mfma_f32_16x16x32_bf16 v[200:203], a[228:231], v[96:99], v[200:203]\n\t" \
  "ds_read_b128 a[212:215], v32 offset:21504\n\t" \
  "v_mfma_f32_16x16x32_bf16 v[204:207], a[228:231], v[100:103], v[204:207]\n\t" \
  "buffer_load_dwordx4 v[188:191], v255, %11, s75 offen offset:0 sc1\n\t" \
  "v_mfma_f32_16x16x32_bf16 v[208:211], a[232:235], v[96:99], v[208:211]\n\t" \
  "ds_read_b128 a[216:219], v32 offset:22528\n\t" \
  "v_mfma_f32_16x16x32_bf16 v[212:215], a[232:235], v[100:103], v[212:215]\n\t" \
  "v_mfma_f32_16x16x32_bf16 v[216:219], a[236:239], v[96:99], v[216:219]\n\t" \
  "ds_read_b128 a[220:223], v32 offset:23552\n\t" \
  "buffer_load_dwordx4 v34, %11, s26 offen offset:3072 sc1 lds\n\t" \
  "v_mfma_f32_16x16x32_bf16 v[220:223], a[236:239], v[100:103], v[220:223]\n\t" \
  "v_mfma_f32_16x16x32_bf16 v[224:227], a[240:243], v[96:99], v[224:227]\n\t" \
  "ds_read_b128 a[224:227], v32 offset:24576\n\t" \
  "v_mfma_f32_16x16x32_bf16 v[228:231], a[240:243], v[100:103], v[228:231]\n\t" \
  "buffer_load_dwordx4 a[128:131], v255, %11, s74 offen offset:1024 sc1\n\t" \
  "v_mfma_f32_16x16x32_bf16 v[232:235], a[244:247], v[96:99], v[232:235]\n\t" \
  "ds_read_b128 a[228:231], v32 offset:25600\n\t" \
  "s_add_u32 m0, m0, 0x4000\n\t" \
  "v_mfma_f32_16x16x32_bf16 v[236:239], a[244:247], v[100:103], v[236:239]\n\t" \
  "s_waitcnt vmcnt(36)\n\t" \
  "v_mfma_f32_16x16x32_bf16 v[192:195], a[248:251], v[104:107], v[192:195]\n\t" \
  "ds_read_b128 a[232:235], v32 offset:26624\n\t" \
  "buffer_load_dwordx4 v34, %11, s27 offen offset:0 sc1 lds\n\t" \
  "v_mfma_f32_16x16x32_bf16 v[196:199], a[248:251], v[108:111], v[196:199]\n\t" \
  "v_mfma_f32_16x16x32_bf16 v[200:203], a[252:255], v[104:107], v[200:203]\n\t" \
  "ds_read_b128 a[236:239], v32 offset:27648\n\t" \
  "v_mfma_f32_16x16x32_bf16 v[204:207], a[252:255], v[108:111], v[204:207]\n\t" \
  "buffer_load_dwordx4 a[132:135], v255, %11, s75 offen offset:1024 sc1\n\t" \
  "s_waitcnt lgkmcnt(4)\n\t" \
  "v_mfma_f32_16x16x32_bf16 v[208:211], a[192:195], v[104:107], v[208:211]\n\t" \
  "ds_read_b128 a[240:243], v32 offset:28672\n\t" \
  "v_mfma_f32_16x16x32_bf16 v[212:215], a[192:195], v[108:111], v[212:215]\n\t" \
  "v_mfma_f32_16x16x32_bf16 v[216:219], a[196:199], v[104:107], v[216:219]\n\t" \
  "ds_read_b128 a[244:247], v32 offset:29696\n\t" \
  "buffer_load_dwordx4 v34, %11, s27 offen offset:1024 sc1 lds\n\t" \
  "v_mfma_f32_16x16x32_bf16 v[220:223], a[196:199], v[108:111], v[220:223]\n\t" \
  "v_mfma_f32_16x16x32_bf16 v[224:227], a[200:203], v[104:107], v[224:227]\n\t" \
  "ds_read_b128 a[248:251], v32 offset:30720\n\t" \
  "v_mfma_f32_16x16x32_bf16 v[228:231], a[200:203], v[108:111], v[228:231]\n\t" \
  "buffer_load_dwordx4 a[136:139], v255, %11, s74 offen offset:2048 sc1\n\t" \
  "v_mfma_f32_16x16x32_bf16 v[232:235], a[204:207], v[104:107], v[232:235]\n\t" \
  "ds_read_b128 a[252:255], v32 offset:31744\n\t" \
  "v_mfma_f32_16x16x32_bf16 v[236:239], a[204:207], v[108:111], v[236:239]\n\t" \
  "s_waitcnt vmcnt(37)\n\t" \
  "v_mfma_f32_16x16x32_bf16 v[192:195], a[208:211], v[112:115], v[192:195]\n\t" \
  "ds_read_b128 a[192:195], v33 offset:0\n\t" \
  "buffer_load_dwordx4 v34, %11, s27 offen offset:2048 sc1 lds\n\t" \
  "v_mfma_f32_16x16x32_bf16 v[196:199], a[208:211], v[116:119], v[196:199]\n\t" \
  "v_mfma_f32_16x16x32_bf16 v[200:203], a[212:215], v[112:115], v[200:203]\n\t" \
  "ds_read_b128 a[196:199], v33 offset:1024\n\t" \
  "v_mfma_f32_16x16x32_bf16 v[204:207], a[212:215], v[116:119], v[204:207]\n\t" \
  "buffer_load_dwordx4 a[140:143], v255, %11, s75 offen offset:2048 sc1\n\t" \
  "v_mfma_f32_16x16x32_bf16 v[208:211], a[216:219], v[112:115], v[208:211]\n\t" \
  "ds_read_b128 a[200:203], v33 offset:2048\n\t" \
  "v_mfma_f32_16x16x32_bf16 v[212:215], a[216:219], v[116:119], v[212:215]\n\t" \
  "v_mfma_f32_16x16x32_bf16 v[216:219], a[220:223], v[112:115], v[216:219]\n\t" \
  "ds_read_b128 a[204:207], v33 offset:3072\n\t" \
  "buffer_load_dwordx4 v34, %11, s27 offen offset:3072 sc1 lds\n\t" \
  "v_mfma_f32_16x16x32_bf16 v[220:223], a[220:223], v[116:119], v[220:223]\n\t" \
  "s_waitcnt lgkmcnt(4)\n\t" \
  "v_mfma_f32_16x16x32_bf16 v[224:227], a[224:227], v[112:115], v[224:227]\n\t" \
  "ds_read_b128 a[208:211], v33 offset:4096\n\t" \
  "v_readlane_b32 s26, v27, s25\n\t" \
  "v_mfma_f32_16x16x32_bf16 v[228:231], a[224:227], v[116:119], v[228:231]\n\t" \
  "buffer_load_dwordx4 a[144:147], v255, %11, s74 offen offset:3072 sc1\n\t" \
  "v_mfma_f32_16x16x32_bf16 v[232:235], a[228:231], v[112:115], v[232:235]\n\t" \
  "ds_read_b128 a[212:215], v33 offset:5120\n\t" \
  "v_readlane_b32 s27, v28, s25\n\t" \
  "v_mfma_f32_16x16x32_bf16 v[236:239], a[228:231], v[116:119], v[236:239]\n\t" \
  "s_waitcnt vmcnt(37)\n\t" \
  "v_mfma_f32_16x16x32_bf16 v[192:195], a[232:235], v[120:123], v[192:195]\n\t" \
  "ds_read_b128 a[216:219], v33 offset:6144\n\t" \
  "s_add_u32 s25, s25, 1\n\t" \
  "v_mfma_f32_16x16x32_bf16 v[196:199], a[232:235], v[124:127], v[196:199]\n\t" \
  "s_waitcnt vmcnt(35)\n\t" \
  "v_mfma_f32_16x16x32_bf16 v[192:195], a[236:239], v[128:131], v[192:195]\n\t" \
  "ds_read_b128 a[220:223], v33 offset:7168\n\t" \
  "s_mov_b32 s29, s28\n\t" \
  "v_mfma_f32_16x16x32_bf16 v[196:199], a[236:239], v[132:135], v[196:199]\n\t" \
  "buffer_load_dwordx4 a[148:151], v255, %11, s75 offen offset:3072 sc1\n\t" \
  "s_waitcnt vmcnt(32)\n\t" \
  "v_mfma_f32_16x16x32_bf16 v[192:195], a[240:243], v[136:139], v[192:195]\n\t" \
  "ds_read_b128 a[224:227], v33 offset:8192\n\t" \
  "s_mov_b32 s28, s30\n\t" \
  "v_mfma_f32_16x16x32_bf16 v[196:199], a[240:243], v[140:143], v[196:199]\n\t" \
  "s_waitcnt vmcnt(28)\n\t" \
  "v_mfma_f32_16x16x32_bf16 v[192:195], a[244:247], v[144:147], v[192:195]\n\t" \
  "ds_read_b128 a[228:231], v33 offset:9216\n\t" \
  "v_mfma_f32_16x16x32_bf16 v[196:199], a[244:247], v[148:151], v[196:199]\n\t" \
  "s_waitcnt vmcnt(25)\n\t" \
  "v_mfma_f32_16x16x32_bf16 v[192:195], a[248:251], v[152:155], v[192:195]\n\t" \
  "ds_read_b128 a[232:235], v33 offset:10240\n\t" \
  "v_mfma_f32_16x16x32_bf16 v[196:199], a[248:251], v[156:159], v[196:199]\n\t" \
  "s_waitcnt vmcnt(21)\n\t" \
  "v_mfma_f32_16x16x32_bf16 v[192:195], a[252:255], v[160:163], v[192:195]\n\t" \
  "ds_read_b128 a[236:239], v33 offset:11264\n\t" \
  "v_mfma_f32_16x16x32_bf16 v[196:199], a[252:255], v[164:167], v[196:199]\n\t" \
  "s_barrier\n\t" \
  "s_waitcnt lgkmcnt(4)\n\t" \
  "s_waitcnt vmcnt(18)\n\t" \
  "v_mfma_f32_16x16x32_bf16 v[192:195], a[192:195], v[168:171], v[192:195]\n\t" \
  "s_add_u32 s30, s28, 0x8000\n\t" \
  "ds_read_b128 a[240:243], v33 offset:12288\n\t" \
  "v_mfma_f32_16x16x32_bf16 v[196:199], a[192:195], v[172:175], v[196:199]\n\t" \
  "s_cmp_lg_u32 s28, 0x20000\n\t" \
  "s_cselect_b32 s30, s30, 0\n\t" \
  "s_waitcnt vmcnt(15)\n\t" \
  "v_mfma_f32_16x16x32_bf16 v[192:195], a[196:199], v[176:179], v[192:195]\n\t" \
  "ds_read_b128 a[244:247], v33 offset:13312\n\t" \
  "v_mfma_f32_16x16x32_bf16 v[196:199], a[196:199], v[180:183], v[196:199]\n\t" \
  "v_add_u32_e32 v36, s46, %1\n\t" \
  "s_waitcnt vmcnt(11)\n\t" \
  "v_mfma_f32_16x16x32_bf16 v[192:195], a[200:203], v[184:187], v[192:195]\n\t" \
  "ds_read_b128 a[248:251], v33 offset:14336\n\t" \
  "v_mfma_f32_16x16x32_bf16 v[196:199], a[200:203], v[188:191], v[196:199]\n\t" \
  "s_waitcnt vmcnt(7)\n\t" \
  "v_mfma_f32_16x16x32_bf16 v[192:195], a[204:207], a[128:131], v[192:195]\n\t" \
  "v_add_u32_e32 v32, s30, %1\n\t" \
  "ds_read_b128 a[252:255], v33 offset:15360\n\t" \
  "v_mfma_f32_16x16x32_bf16 v[196:199], a[204:207], a[132:135], v[196:199]\n\t" \
  "s_waitcnt vmcnt(3)\n\t" \
  "v_mfma_f32_16x16x32_bf16 v[192:195], a[208:211], a[136:139], v[192:195]\n\t" \
  "ds_read_b128 a[192:195], v33 offset:16384\n\t" \
  "v_mfma_f32_16x16x32_bf16 v[196:199], a[208:211], a[140:143], v[196:199]\n\t" \
  "s_waitcnt vmcnt(0)\n\t" \
  "v_mfma_f32_16x16x32_bf16 v[192:195], a[212:215], a[144:147], v[192:195]\n\t" \
  "ds_read_b128 a[196:199], v33 offset:17408\n\t" \
  "v_mfma_f32_16x16x32_bf16 v[196:199], a[212:215], a[148:151], v[196:199]\n\t" \
  "v_mfma_f32_16x16x32_bf16 v[200:203], a[216:219], v[120:123], v[200:203]\n\t" \
  "ds_read_b128 a[200:203], v33 offset:18432\n\t" \
  "v_mfma_f32_16x16x32_bf16 v[204:207], a[216:219], v[124:127], v[204:207]\n\t" \
  "v_mfma_f32_16x16x32_bf16 v[200:203], a[220:223], v[128:131], v[200:203]\n\t" \
  "ds_read_b128 a[204:207], v33 offset:19456\n\t" \
  "v_mfma_f32_16x16x32_bf16 v[204:207], a[220:223], v[132:135], v[204:207]\n\t" \
  "s_waitcnt lgkmcnt(4)\n\t" \
  "v_mfma_f32_16x16x32_bf16 v[200:203], a[224:227], v[136:139], v[200:203]\n\t" \
  "ds_read_b128 a[208:211], v33 offset:20480\n\t" \
  "v_mfma_f32_16x16x32_bf16 v[204:207], a[224:227], v[140:143], v[204:207]\n\t" \
  "v_cvt_pk_bf16_f32 v192, v192, v193\n\t" \
  "v_cvt_pk_bf16_f32 v194, v194, v195\n\t" \
  "v_mfma_f32_16x16x32_bf16 v[200:203], a[228:231], v[144:147], v[200:203]\n\t" \
  "ds_read_b128 a[212:215], v33 offset:21504\n\t" \
  "v_mov_b32_e32 v193, s90\n\t" \
  "v_mfma_f32_16x16x32_bf16 v[204:207], a[228:231], v[148:151], v[204:207]\n\t" \
  "v_mov_b32_e32 v195, s90\n\t" \
  "buffer_store_dwordx4 v[192:195], v248, %11, s81 offen offset:0 sc1\n\t" \
  "v_mfma_f32_16x16x32_bf16 v[200:203], a[232:235], v[152:155], v[200:203]\n\t" \
  "ds_read_b128 a[216:219], v33 offset:22528\n\t" \
  "v_cvt_pk_bf16_f32 v196, v196, v197\n\t" \
  "v_mfma_f32_16x16x32_bf16 v[204:207], a[232:235], v[156:159], v[204:207]\n\t" \
  "v_cvt_pk_bf16_f32 v198, v198, v199\n\t" \
  "v_mov_b32_e32 v197, s90\n\t" \
  "v_mfma_f32_16x16x32_bf16 v[200:203], a[236:239], v[160:163], v[200:203]\n\t" \
  "ds_read_b128 a[220:223], v33 offset:23552\n\t" \
  "v_mov_b32_e32 v199, s90\n\t" \
  "v_mfma_f32_16x16x32_bf16 v[204:207], a[236:239], v[164:167], v[204:207]\n\t" \
  "buffer_store_dwordx4 v[196:199], v248, %11, s81 offen offset:1024 sc1\n\t" \
  "v_mfma_f32_16x16x32_bf16 v[200:203], a[240:243], v[168:171], v[200:203]\n\t" \
  "ds_read_b128 a[224:227], v33 offset:24576\n\t" \
  "v_mfma_f32_16x16x32_bf16 v[204:207], a[240:243], v[172:175], v[204:207]\n\t" \
  "v_mfma_f32_16x16x32_bf16 v[200:203], a[244:247], v[176:179], v[200:203]\n\t" \
  "ds_read_b128 a[228:231], v33 offset:25600\n\t" \
  "v_mfma_f32_16x16x32_bf16 v[204:207], a[244:247], v[180:183], v[204:207]\n\t" \
  "v_mfma_f32_16x16x32_bf16 v[200:203], a[248:251], v[184:187], v[200:203]\n\t" \
  "ds_read_b128 a[232:235], v33 offset:26624\n\t" \
  "v_mfma_f32_16x16x32_bf16 v[204:207], a[248:251], v[188:191], v[204:207]\n\t" \
  "v_mfma_f32_16x16x32_bf16 v[200:203], a[252:255], a[128:131], v[200:203]\n\t" \
  "ds_read_b128 a[236:239], v33 offset:27648\n\t" \
  "v_mfma_f32_16x16x32_bf16 v[204:207], a[252:255], a[132:135], v[204:207]\n\t" \
  "s_waitcnt lgkmcnt(4)\n\t" \
  "v_mfma_f32_16x16x32_bf16 v[200:203], a[192:195], a[136:139], v[200:203]\n\t" \
  "ds_read_b128 a[240:243], v33 offset:28672\n\t" \
  "v_mfma_f32_16x16x32_bf16 v[204:207], a[192:195], a[140:143], v[204:207]\n\t" \
  "v_mfma_f32_16x16x32_bf16 v[200:203], a[196:199], a[144:147], v[200:203]\n\t" \
  "ds_read_b128 a[244:247], v33 offset:29696\n\t" \
  "v_mfma_f32_16x16x32_bf16 v[204:207], a[196:199], a[148:151], v[204:207]\n\t" \
  "v_mfma_f32_16x16x32_bf16 v[208:211], a[200:203], v[120:123], v[208:211]\n\t" \
  "ds_read_b128 a[248:251], v33 offset:30720\n\t" \
  "v_mfma_f32_16x16x32_bf16 v[212:215], a[200:203], v[124:127], v[212:215]\n\t" \
  "v_mfma_f32_16x16x32_bf16 v[208:211], a[204:207], v[128:131], v[208:211]\n\t" \
  "ds_read_b128 a[252:255], v33 offset:31744\n\t" \
  "v_mfma_f32_16x16x32_bf16 v[212:215], a[204:207], v[132:135], v[212:215]\n\t" \
  "v_mfma_f32_16x16x32_bf16 v[208:211], a[208:211], v[136:139], v[208:211]\n\t" \
  "ds_read_b128 a[192:195], v32 offset:0\n\t" \
  "v_mfma_f32_16x16x32_bf16 v[212:215], a[208:211], v[140:143], v[212:215]\n\t" \
  "v_cvt_pk_bf16_f32 v200, v200, v201\n\t" \
  "v_cvt_pk_bf16_f32 v202, v202, v203\n\t" \
  "v_mfma_f32_16x16x32_bf16 v[208:211], a[212:215], v[144:147], v[208:211]\n\t" \
  "ds_read_b128 a[196:199], v32 offset:1024\n\t" \
  "v_mov_b32_e32 v201, s90\n\t" \
  "v_mfma_f32_16x16x32_bf16 v[212:215], a[212:215], v[148:151], v[212:215]\n\t" \
  "v_mov_b32_e32 v203, s90\n\t" \
  "buffer_store_dwordx4 v[200:203], v248, %11, s81 offen offset:2048 sc1\n\t" \
  "v_mfma_f32_16x16x32_bf16 v[208:211], a[216:219], v[152:155], v[208:211]\n\t" \
  "ds_read_b128 a[200:203], v32 offset:2048\n\t" \
  "v_cvt_pk_bf16_f32 v204, v204, v205\n\t" \
  "v_mfma_f32_16x16x32_bf16 v[212:215], a[216:219], v[156:159], v[212:215]\n\t" \
  "v_cvt_pk_bf16_f32 v206, v206, v207\n\t" \
  "v_mov_b32_e32 v205, s90\n\t" \
  "v_mfma_f32_16x16x32_bf16 v[208:211], a[220:223], v[160:163], v[208:211]\n\t" \
  "ds_read_b128 a[204:207], v32 offset:3072\n\t" \
  "v_mov_b32_e32 v207, s90\n\t" \
  "v_mfma_f32_16x16x32_bf16 v[212:215], a[220:223], v[164:167], v[212:215]\n\t" \
  "buffer_store_dwordx4 v[204:207], v248, %11, s81 offen offset:3072 sc1\n\t" \
  "s_waitcnt lgkmcnt(4)\n\t" \
  "v_mfma_f32_16x16x32_bf16 v[208:211], a[224:227], v[168:171], v[208:211]\n\t" \
  "ds_read_b128 a[208:211], v32 offset:4096\n\t" \
  "s_mov_b32 s29, s28\n\t" \
  "v_mfma_f32_16x16x32_bf16 v[212:215], a[224:227], v[172:175], v[212:215]\n\t" \
  "v_mfma_f32_16x16x32_bf16 v[208:211], a[228:231], v[176:179], v[208:211]\n\t" \
  "ds_read_b128 a[212:215], v32 offset:5120\n\t" \
  "s_mov_b32 s28, s30\n\t" \
  "v_mfma_f32_16x16x32_bf16 v[212:215], a[228:231], v[180:183], v[212:215]\n\t" \
  "v_mfma_f32_16x16x32_bf16 v[208:211], a[232:235], v[184:187], v[208:211]\n\t" \
  "ds_read_b128 a[216:219], v32 offset:6144\n\t" \
  "v_mfma_f32_16x16x32_bf16 v[212:215], a[232:235], v[188:191], v[212:215]\n\t" \
  "v_mfma_f32_16x16x32_bf16 v[208:211], a[236:239], a[128:131], v[208:211]\n\t" \
  "ds_read_b128 a[220:223], v32 offset:7168\n\t" \
  "v_mfma_f32_16x16x32_bf16 v[212:215], a[236:239], a[132:135], v[212:215]\n\t" \
  "v_mfma_f32_16x16x32_bf16 v[208:211], a[240:243], a[136:139], v[208:211]\n\t" \
  "ds_read_b128 a[224:227], v32 offset:8192\n\t" \
  "v_mfma_f32_16x16x32_bf16 v[212:215], a[240:243], a[140:143], v[212:215]\n\t" \
  "v_mfma_f32_16x16x32_bf16 v[208:211], a[244:247], a[144:147], v[208:211]\n\t" \
  "ds_read_b128 a[228:231], v32 offset:9216\n\t" \
  "v_mfma_f32_16x16x32_bf16 v[212:215], a[244:247], a[148:151], v[212:215]\n\t" \
  "v_mfma_f32_16x16x32_bf16 v[216:219], a[248:251], v[120:123], v[216:219]\n\t" \
  "ds_read_b128 a[232:235], v32 offset:10240\n\t" \
  "v_mfma_f32_16x16x32_bf16 v[220:223], a[248:251], v[124:127], v[220:223]\n\t" \
  "v_mfma_f32_16x16x32_bf16 v[216:219], a[252:255], v[128:131], v[216:219]\n\t" \
  "ds_read_b128 a[236:239], v32 offset:11264\n\t" \
  "v_mfma_f32_16x16x32_bf16 v[220:223], a[252:255], v[132:135], v[220:223]\n\t" \
  "s_barrier\n\t" \
  "s_waitcnt lgkmcnt(4)\n\t" \
  "v_mfma_f32_16x16x32_bf16 v[216:219], a[192:195], v[136:139], v[216:219]\n\t" \
  "s_add_u32 s30, s28, 0x8000\n\t" \
  "ds_read_b128 a[240:243], v32 offset:12288\n\t" \
  "v_mfma_f32_16x16x32_bf16 v[220:223], a[192:195], v[140:143], v[220:223]\n\t" \
  "s_cmp_lg_u32 s28, 0x20000\n\t" \
  "s_cselect_b32 s30, s30, 0\n\t" \
  "v_mfma_f32_16x16x32_bf16 v[216:219], a[196:199], v[144:147], v[216:219]\n\t" \
  "ds_read_b128 a[244:247], v32 offset:13312\n\t" \
  "v_mfma_f32_16x16x32_bf16 v[220:223], a[196:199], v[148:151], v[220:223]\n\t" \
  "v_mfma_f32_16x16x32_bf16 v[216:219], a[200:203], v[152:155], v[216:219]\n\t" \
  "ds_read_b128 a[248:251], v32 offset:14336\n\t" \
  "v_mfma_f32_16x16x32_bf16 v[220:223], a[200:203], v[156:159], v[220:223]\n\t" \
  "v_mfma_f32_16x16x32_bf16 v[216:219], a[204:207], v[160:163], v[216:219]\n\t" \
  "v_add_u32_e32 v33, s30, %1\n\t" \
  "ds_read_b128 a[252:255], v32 offset:15360\n\t" \
  "v_mfma_f32_16x16x32_bf16 v[220:223], a[204:207], v[164:167], v[220:223]\n\t" \
  "v_cvt_pk_bf16_f32 v208, v208, v209\n\t" \
  "v_cvt_pk_bf16_f32 v210, v210, v211\n\t" \
  "v_mfma_f32_16x16x32_bf16 v[216:219], a[208:211], v[168:171], v[216:219]\n\t" \
  "ds_read_b128 a[192:195], v32 offset:16384\n\t" \
  "v_mov_b32_e32 v209, s90\n\t" \
  "v_mfma_f32_16x16x32_bf16 v[220:223], a[208:211], v[172:175], v[220:223]\n\t" \
  "v_mov_b32_e32 v211, s90\n\t" \
  "buffer_store_dwordx4 v[208:211], v249, %11, s81 offen offset:0 sc1\n\t" \
  "v_mfma_f32_16x16x32_bf16 v[216:219], a[212:215], v[176:179], v[216:219]\n\t" \
  "ds_read_b128 a[196:199], v32 offset:17408\n\t" \
  "v_cvt_pk_bf16_f32 v212, v212, v213\n\t" \
  "v_mfma_f32_16x16x32_bf16 v[220:223], a[212:215], v[180:183], v[220:223]\n\t" \
  "v_cvt_pk_bf16_f32 v214, v214, v215\n\t" \
  "v_mov_b32_e32 v213, s90\n\t" \
  "v_mfma_f32_16x16x32_bf16 v[216:219], a[216:219], v[184:187], v[216:219]\n\t" \
  "ds_read_b128 a[200:203], v32 offset:18432\n\t" \
  "v_mov_b32_e32 v215, s90\n\t" \
  "v_mfma_f32_16x16x32_bf16 v[220:223], a[216:219], v[188:191], v[220:223]\n\t" \
  "buffer_store_dwordx4 v[212:215], v249, %11, s81 offen offset:1024 sc1\n\t" \
  "v_mfma_f32_16x16x32_bf16 v[216:219], a[220:223], a[128:131], v[216:219]\n\t" \
  "ds_read_b128 a[204:207], v32 offset:19456\n\t" \
  "v_mfma_f32_16x16x32_bf16 v[220:223], a[220:223], a[132:135], v[220:223]\n\t" \
  "s_waitcnt lgkmcnt(4)\n\t" \
  "v_mfma_f32_16x16x32_bf16 v[216:219], a[224:227], a[136:139], v[216:219]\n\t" \
  "ds_read_b128 a[208:211], v32 offset:20480\n\t" \
  "v_mfma_f32_16x16x32_bf16 v[220:223], a[224:227], a[140:143], v[220:223]\n\t" \
  "v_mfma_f32_16x16x32_bf16 v[216:219], a[228:231], a[144:147], v[216:219]\n\t" \
  "ds_read_b128 a[212:215], v32 offset:21504\n\t" \
  "v_mfma_f32_16x16x32_bf16 v[220:223], a[228:231], a[148:151], v[220:223]\n\t" \
  "v_mfma_f32_16x16x32_bf16 v[224:227], a[232:235], v[120:123], v[224:227]\n\t" \
  "ds_read_b128 a[216:219], v32 offset:22528\n\t" \
  "v_mfma_f32_16x16x32_bf16 v[228:231], a[232:235], v[124:127], v[228:231]\n\t" \
  "v_mfma_f32_16x16x32_bf16 v[224:227], a[236:239], v[128:131], v[224:227]\n\t" \
  "ds_read_b128 a[220:223], v32 offset:23552\n\t" \
  "v_mfma_f32_16x16x32_bf16 v[228:231], a[236:239], v[132:135], v[228:231]\n\t" \
  "v_mfma_f32_16x16x32_bf16 v[224:227], a[240:243], v[136:139], v[224:227]\n\t" \
  "ds_read_b128 a[224:227], v32 offset:24576\n\t" \
  "v_mfma_f32_16x16x32_bf16 v[228:231], a[240:243], v[140:143], v[228:231]\n\t" \
  "v_cvt_pk_bf16_f32 v216, v216, v217\n\t" \
  "v_cvt_pk_bf16_f32 v218, v218, v219\n\t" \
  "v_mfma_f32_16x16x32_bf16 v[224:227], a[244:247], v[144:147], v[224:227]\n\t" \
  "ds_read_b128 a[228:231], v32 offset:25600\n\t" \
  "v_mov_b32_e32 v217, s90\n\t" \
  "v_mfma_f32_16x16x32_bf16 v[228:231], a[244:247], v[148:151], v[228:231]\n\t" \
  "v_mov_b32_e32 v219, s90\n\t" \
  "buffer_store_dwordx4 v[216:219], %1, %11, s34 offen offset:0 sc1\n\t" \
  "v_mfma_f32_16x16x32_bf16 v[224:227], a[248:251], v[152:155], v[224:227]\n\t" \
  "ds_read_b128 a[232:235], v32 offset:26624\n\t" \
  "v_cvt_pk_bf16_f32 v220, v220, v221\n\t" \
  "v_mfma_f32_16x16x32_bf16 v[228:231], a[248:251], v[156:159], v[228:231]\n\t" \
  "v_cvt_pk_bf16_f32 v222, v222, v223\n\t" \
  "v_mov_b32_e32 v221, s90\n\t" \
  "v_mfma_f32_16x16x32_bf16 v[224:227], a[252:255], v[160:163], v[224:227]\n\t" \
  "ds_read_b128 a[236:239], v32 offset:27648\n\t" \
  "v_mov_b32_e32 v223, s90\n\t" \
  "v_mfma_f32_16x16x32_bf16 v[228:231], a[252:255], v[164:167], v[228:231]\n\t" \
  "buffer_store_dwordx4 v[220:223], %1, %11, s34 offen offset:1024 sc1\n\t" \
  "s_waitcnt lgkmcnt(4)\n\t" \
  "v_mfma_f32_16x16x32_bf16 v[224:227], a[192:195], v[168:171], v[224:227]\n\t" \
  "ds_read_b128 a[240:243], v32 offset:28672\n\t" \
  "v_mfma_f32_16x16x32_bf16 v[228:231], a[192:195], v[172:175], v[228:231]\n\t" \
  "v_mfma_f32_16x16x32_bf16 v[224:227], a[196:199], v[176:179], v[224:227]\n\t" \
  "ds_read_b128 a[244:247], v32 offset:29696\n\t" \
  "v_mfma_f32_16x16x32_bf16 v[228:231], a[196:199], v[180:183], v[228:231]\n\t" \
  "v_mfma_f32_16x16x32_bf16 v[224:227], a[200:203], v[184:187], v[224:227]\n\t" \
  "ds_read_b128 a[248:251], v32 offset:30720\n\t" \
  "v_mfma_f32_16x16x32_bf16 v[228:231], a[200:203], v[188:191], v[228:231]\n\t" \
  "v_mfma_f32_16x16x32_bf16 v[224:227], a[204:207], a[128:131], v[224:227]\n\t" \
  "ds_read_b128 a[252:255], v32 offset:31744\n\t" \
  "v_mfma_f32_16x16x32_bf16 v[228:231], a[204:207], a[132:135], v[228:231]\n\t" \
  "v_mfma_f32_16x16x32_bf16 v[224:227], a[208:211], a[136:139], v[224:227]\n\t" \
  "ds_read_b128 a[192:195], v33 offset:0\n\t" \
  "v_mfma_f32_16x16x32_bf16 v[228:231], a[208:211], a[140:143], v[228:231]\n\t" \
  "v_mfma_f32_16x16x32_bf16 v[224:227], a[212:215], a[144:147], v[224:227]\n\t" \
  "ds_read_b128 a[196:199], v33 offset:1024\n\t" \
  "v_mfma_f32_16x16x32_bf16 v[228:231], a[212:215], a[148:151], v[228:231]\n\t" \
  "v_mfma_f32_16x16x32_bf16 v[232:235], a[216:219], v[120:123], v[232:235]\n\t" \
  "ds_read_b128 a[200:203], v33 offset:2048\n\t" \
  "v_mfma_f32_16x16x32_bf16 v[236:239], a[216:219], v[124:127], v[236:239]\n\t" \
  "v_mfma_f32_16x16x32_bf16 v[232:235], a[220:223], v[128:131], v[232:235]\n\t" \
  "ds_read_b128 a[204:207], v33 offset:3072\n\t" \
  "v_mfma_f32_16x16x32_bf16 v[236:239], a[220:223], v[132:135], v[236:239]\n\t" \
  "s_waitcnt lgkmcnt(4)\n\t" \
  "v_mfma_f32_16x16x32_bf16 v[232:235], a[224:227], v[136:139], v[232:235]\n\t" \
  "ds_read_b128 a[208:211], v33 offset:4096\n\t" \
  "s_mov_b32 s29, s28\n\t" \
  "v_mfma_f32_16x16x32_bf16 v[236:239], a[224:227], v[140:143], v[236:239]\n\t" \
  "v_cvt_pk_bf16_f32 v224, v224, v225\n\t" \
  "v_cvt_pk_bf16_f32 v226, v226, v227\n\t" \
  "v_mfma_f32_16x16x32_bf16 v[232:235], a[228:231], v[144:147], v[232:235]\n\t" \
  "ds_read_b128 a[212:215], v33 offset:5120\n\t" \
  "s_mov_b32 s28, s30\n\t" \
  "v_mfma_f32_16x16x32_bf16 v[236:239], a[228:231], v[148:151], v[236:239]\n\t" \
  "v_mov_b32_e32 v225, s90\n\t" \
  "v_mov_b32_e32 v227, s90\n\t" \
  "v_mfma_f32_16x16x32_bf16 v[232:235], a[232:235], v[152:155], v[232:235]\n\t" \
  "ds_read_b128 a[216:219], v33 offset:6144\n\t" \
  "buffer_store_dwordx4 v[224:227], %1, %11, s38 offen offset:0 sc1\n\t" \
  "v_mfma_f32_16x16x32_bf16 v[236:239], a[232:235], v[156:159], v[236:239]\n\t" \
  "v_cvt_pk_bf16_f32 v228, v228, v229\n\t" \
  "v_cvt_pk_bf16_f32 v230, v230, v231\n\t" \
  "v_mfma_f32_16x16x32_bf16 v[232:235], a[236:239], v[160:163], v[232:235]\n\t" \
  "ds_read_b128 a[220:223], v33 offset:7168\n\t" \
  "v_mov_b32_e32 v229, s90\n\t" \
  "v_mfma_f32_16x16x32_bf16 v[236:239], a[236:239], v[164:167], v[236:239]\n\t" \
  "v_mov_b32_e32 v231, s90\n\t" \
  "buffer_store_dwordx4 v[228:231], %1, %11, s38 offen offset:1024 sc1\n\t" \
  "v_mfma_f32_16x16x32_bf16 v[232:235], a[240:243], v[168:171], v[232:235]\n\t" \
  "ds_read_b128 a[224:227], v33 offset:8192\n\t" \
  "v_mfma_f32_16x16x32_bf16 v[236:239], a[240:243], v[172:175], v[236:239]\n\t" \
  "v_mfma_f32_16x16x32_bf16 v[232:235], a[244:247], v[176:179], v[232:235]\n\t" \
  "ds_read_b128 a[228:231], v33 offset:9216\n\t" \
  "v_mfma_f32_16x16x32_bf16 v[236:239], a[244:247], v[180:183], v[236:239]\n\t" \
  "v_mfma_f32_16x16x32_bf16 v[232:235], a[248:251], v[184:187], v[232:235]\n\t" \
  "ds_read_b128 a[232:235], v33 offset:10240\n\t" \
  "v_mfma_f32_16x16x32_bf16 v[236:239], a[248:251], v[188:191], v[236:239]\n\t" \
  "v_mfma_f32_16x16x32_bf16 v[232:235], a[252:255], a[128:131], v[232:235]\n\t" \
  "ds_read_b128 a[236:239], v33 offset:11264\n\t" \
  "v_mfma_f32_16x16x32_bf16 v[236:239], a[252:255], a[132:135], v[236:239]\n\t" \
  "s_barrier\n\t" \
  "s_waitcnt lgkmcnt(4)\n\t" \
  "v_mfma_f32_16x16x32_bf16 v[232:235], a[192:195], a[136:139], v[232:235]\n\t" \
  "s_add_u32 s30, s28, 0x8000\n\t" \
  "ds_read_b128 a[240:243], v33 offset:12288\n\t" \
  "v_mfma_f32_16x16x32_bf16 v[236:239], a[192:195], a[140:143], v[236:239]\n\t" \
  "s_cmp_lg_u32 s28, 0x20000\n\t" \
  "s_cselect_b32 s30, s30, 0\n\t" \
  "v_mfma_f32_16x16x32_bf16 v[232:235], a[196:199], a[144:147], v[232:235]\n\t" \
  "ds_read_b128 a[244:247], v33 offset:13312\n\t" \
  "v_mfma_f32_16x16x32_bf16 v[236:239], a[196:199], a[148:151], v[236:239]\n\t" \
  "v_mfma_f32_16x16x32_bf16 v[240:243], a[200:203], a[48:51], v[240:243]\n\t" \
  "ds_read_b128 a[248:251], v33 offset:14336\n\t" \
  "v_mfma_f32_16x16x32_bf16 v[244:247], a[200:203], a[52:55], v[244:247]\n\t" \
  "v_mfma_f32_16x16x32_bf16 v[240:243], a[204:207], a[56:59], v[240:243]\n\t" \
  "v_add_u32_e32 v32, s30, %1\n\t" \
  "ds_read_b128 a[252:255], v33 offset:15360\n\t" \
  "v_mfma_f32_16x16x32_bf16 v[244:247], a[204:207], a[60:63], v[244:247]\n\t" \
  "v_mfma_f32_16x16x32_bf16 v[240:243], a[208:211], a[64:67], v[240:243]\n\t" \
  "ds_read_b128 a[192:195], v33 offset:16384\n\t" \
  "v_mfma_f32_16x16x32_bf16 v[244:247], a[208:211], a[68:71], v[244:247]\n\t" \
  "v_cvt_pk_bf16_f32 v232, v232, v233\n\t" \
  "v_cvt_pk_bf16_f32 v234, v234, v235\n\t" \
  "v_mfma_f32_16x16x32_bf16 v[240:243], a[212:215], a[72:75], v[240:243]\n\t" \
  "ds_read_b128 a[196:199], v33 offset:17408\n\t" \
  "v_mov_b32_e32 v233, s90\n\t" \
  "v_mfma_f32_16x16x32_bf16 v[244:247], a[212:215], a[76:79], v[244:247]\n\t" \
  "v_mov_b32_e32 v235, s90\n\t" \
  "buffer_store_dwordx4 v[232:235], %1, %11, s39 offen offset:0 sc1\n\t" \
  "v_mfma_f32_16x16x32_bf16 v[240:243], a[216:219], a[80:83], v[240:243]\n\t" \
  "ds_read_b128 a[200:203], v33 offset:18432\n\t" \
  "v_cvt_pk_bf16_f32 v236, v236, v237\n\t" \
  "v_mfma_f32_16x16x32_bf16 v[244:247], a[216:219], a[84:87], v[244:247]\n\t" \
  "v_cvt_pk_bf16_f32 v238, v238, v239\n\t" \
  "v_mov_b32_e32 v237, s90\n\t" \
  "v_mfma_f32_16x16x32_bf16 v[240:243], a[220:223], a[88:91], v[240:243]\n\t" \
  "ds_read_b128 a[204:207], v33 offset:19456\n\t" \
  "v_mov_b32_e32 v239, s90\n\t" \
  "v_mfma_f32_16x16x32_bf16 v[244:247], a[220:223], a[92:95], v[244:247]\n\t" \
  "buffer_store_dwordx4 v[236:239], %1, %11, s39 offen offset:1024 sc1\n\t" \
  "s_waitcnt lgkmcnt(4)\n\t" \
  "v_mfma_f32_16x16x32_bf16 v[240:243], a[224:227], a[96:99], v[240:243]\n\t" \
  "ds_read_b128 a[208:211], v33 offset:20480\n\t" \
  "v_mfma_f32_16x16x32_bf16 v[244:247], a[224:227], a[100:103], v[244:247]\n\t" \
  "v_mfma_f32_16x16x32_bf16 v[240:243], a[228:231], a[104:107], v[240:243]\n\t" \
  "ds_read_b128 a[212:215], v33 offset:21504\n\t" \
  "v_mfma_f32_16x16x32_bf16 v[244:247], a[228:231], a[108:111], v[244:247]\n\t" \
  "v_mfma_f32_16x16x32_bf16 v[240:243], a[232:235], a[112:115], v[240:243]\n\t" \
  "ds_read_b128 a[216:219], v33 offset:22528\n\t" \
  "v_mfma_f32_16x16x32_bf16 v[244:247], a[232:235], a[116:119], v[244:247]\n\t" \
  "v_mfma_f32_16x16x32_bf16 v[240:243], a[236:239], a[120:123], v[240:243]\n\t" \
  "ds_read_b128 a[220:223], v33 offset:23552\n\t" \
  "v_mfma_f32_16x16x32_bf16 v[244:247], a[236:239], a[124:127], v[244:247]\n\t" \
  "v_mfma_f32_16x16x32_bf16 v[240:243], a[240:243], v[40:43], v[240:243]\n\t" \
  "ds_read_b128 a[224:227], v33 offset:24576\n\t" \
  "v_mfma_f32_16x16x32_bf16 v[244:247], a[240:243], v[44:47], v[244:247]\n\t" \
  "v_mfma_f32_16x16x32_bf16 v[240:243], a[244:247], v[48:51], v[240:243]\n\t" \
  "ds_read_b128 a[228:231], v33 offset:25600\n\t" \
  "v_mfma_f32_16x16x32_bf16 v[244:247], a[244:247], v[52:55], v[244:247]\n\t" \
  "v_mfma_f32_16x16x32_bf16 v[240:243], a[248:251], v[56:59], v[240:243]\n\t" \
  "ds_read_b128 a[232:235], v33 offset:26624\n\t" \
  "v_mfma_f32_16x16x32_bf16 v[244:247], a[248:251], v[60:63], v[244:247]\n\t" \
  "v_mfma_f32_16x16x32_bf16 v[240:243], a[252:255], v[64:67], v[240:243]\n\t" \
  "ds_read_b128 a[236:239], v33 offset:27648\n\t" \
  "v_mfma_f32_16x16x32_bf16 v[244:247], a[252:255], v[68:71], v[244:247]\n\t" \
  "s_waitcnt lgkmcnt(4)\n\t" \
  "v_mfma_f32_16x16x32_bf16 v[240:243], a[192:195], v[72:75], v[240:243]\n\t" \
  "ds_read_b128 a[240:243], v33 offset:28672\n\t" \
  "v_mfma_f32_16x16x32_bf16 v[244:247], a[192:195], v[76:79], v[244:247]\n\t" \
  "v_mfma_f32_16x16x32_bf16 v[240:243], a[196:199], v[80:83], v[240:243]\n\t" \
  "ds_read_b128 a[244:247], v33 offset:29696\n\t" \
  "v_mfma_f32_16x16x32_bf16 v[244:247], a[196:199], v[84:87], v[244:247]\n\t" \
  "v_mfma_f32_16x16x32_bf16 v[240:243], a[200:203], v[88:91], v[240:243]\n\t" \
  "ds_read_b128 a[248:251], v33 offset:30720\n\t" \
  "buffer_load_dwordx4 v[192:195], %1, %11, s76 offen sc1\n\t" \
  "v_mfma_f32_16x16x32_bf16 v[244:247], a[200:203], v[92:95], v[244:247]\n\t" \
  "v_mfma_f32_16x16x32_bf16 v[240:243], a[204:207], v[96:99], v[240:243]\n\t" \
  "ds_read_b128 a[252:255], v33 offset:31744\n\t" \
  "buffer_load_dwordx4 v[196:199], %1, %11, s77 offen sc1\n\t" \
  "v_mfma_f32_16x16x32_bf16 v[244:247], a[204:207], v[100:103], v[244:247]\n\t" \
  "v_mfma_f32_16x16x32_bf16 v[240:243], a[208:211], v[104:107], v[240:243]\n\t" \
  "ds_read_b128 a[192:195], v32 offset:0\n\t" \
  "buffer_load_dwordx4 v[200:203], %1, %11, s78 offen sc1\n\t" \
  "v_mfma_f32_16x16x32_bf16 v[244:247], a[208:211], v[108:111], v[244:247]\n\t" \
  "v_mfma_f32_16x16x32_bf16 v[240:243], a[212:215], v[112:115], v[240:243]\n\t" \
  "ds_read_b128 a[196:199], v32 offset:1024\n\t" \
  "buffer_load_dwordx4 v[204:207], %1, %11, s79 offen sc1\n\t" \
  "v_mfma_f32_16x16x32_bf16 v[244:247], a[212:215], v[116:119], v[244:247]\n\t" \
  "v_mfma_f32_16x16x32_bf16 v[240:243], a[216:219], v[120:123], v[240:243]\n\t" \
  "ds_read_b128 a[200:203], v32 offset:2048\n\t" \
  "buffer_load_dwordx4 v[208:211], v36, %11, s76 offen sc1\n\t" \
  "v_mfma_f32_16x16x32_bf16 v[244:247], a[216:219], v[124:127], v[244:247]\n\t" \
  "v_mfma_f32_16x16x32_bf16 v[240:243], a[220:223], v[128:131], v[240:243]\n\t" \
  "ds_read_b128 a[204:207], v32 offset:3072\n\t" \
  "buffer_load_dwordx4 v[212:215], v36, %11, s77 offen sc1\n\t" \
  "v_mfma_f32_16x16x32_bf16 v[244:247], a[220:223], v[132:135], v[244:247]\n\t" \
  "s_waitcnt lgkmcnt(4)\n\t" \
  "v_mfma_f32_16x16x32_bf16 v[240:243], a[224:227], v[136:139], v[240:243]\n\t" \
  "ds_read_b128 a[208:211], v32 offset:4096\n\t" \
  "s_mov_b32 s29, s28\n\t" \
  "v_mfma_f32_16x16x32_bf16 v[244:247], a[224:227], v[140:143], v[244:247]\n\t" \
  "buffer_load_dwordx4 v[216:219], v36, %11, s78 offen sc1\n\t" \
  "v_mfma_f32_16x16x32_bf16 v[240:243], a[228:231], v[144:147], v[240:243]\n\t" \
  "ds_read_b128 a[212:215], v32 offset:5120\n\t" \
  "s_mov_b32 s28, s30\n\t" \
  "v_mfma_f32_16x16x32_bf16 v[244:247], a[228:231], v[148:151], v[244:247]\n\t" \
  "buffer_load_dwordx4 v[220:223], v36, %11, s79 offen sc1\n\t" \
  "v_mfma_f32_16x16x32_bf16 v[240:243], a[232:235], v[152:155], v[240:243]\n\t" \
  "ds_read_b128 a[216:219], v32 offset:6144\n\t" \
  "buffer_load_dwordx4 v[224:227], %1, %11, s69 offen offset:0 sc1\n\t" \
  "v_mfma_f32_16x16x32_bf16 v[244:247], a[232:235], v[156:159], v[244:247]\n\t" \
  "v_mfma_f32_16x16x32_bf16 v[240:243], a[236:239], v[160:163], v[240:243]\n\t" \
  "ds_read_b128 a[220:223], v32 offset:7168\n\t" \
  "buffer_load_dwordx4 v[228:231], %1, %11, s69 offen offset:1024 sc1\n\t" \
  "v_mfma_f32_16x16x32_bf16 v[244:247], a[236:239], v[164:167], v[244:247]\n\t" \
  "v_mfma_f32_16x16x32_bf16 v[240:243], a[240:243], v[168:171], v[240:243]\n\t" \
  "ds_read_b128 a[224:227], v32 offset:8192\n\t" \
  "buffer_load_dwordx4 v[232:235], %1, %11, s70 offen offset:0 sc1\n\t" \
  "v_mfma_f32_16x16x32_bf16 v[244:247], a[240:243], v[172:175], v[244:247]\n\t" \
  "v_mfma_f32_16x16x32_bf16 v[240:243], a[244:247], v[176:179], v[240:243]\n\t" \
  "ds_read_b128 a[228:231], v32 offset:9216\n\t" \
  "buffer_load_dwordx4 v[236:239], %1, %11, s70 offen offset:1024 sc1\n\t" \
  "v_mfma_f32_16x16x32_bf16 v[244:247], a[244:247], v[180:183], v[244:247]\n\t" \
  "v_mfma_f32_16x16x32_bf16 v[240:243], a[248:251], v[184:187], v[240:243]\n\t" \
  "ds_read_b128 a[232:235], v32 offset:10240\n\t" \
  "buffer_load_dwordx4 v[40:43], %1, %11, s35 offen offset:0 sc1\n\t" \
  "v_mfma_f32_16x16x32_bf16 v[244:247], a[248:251], v[188:191], v[244:247]\n\t" \
  "v_mfma_f32_16x16x32_bf16 v[240:243], a[252:255], a[128:131], v[240:243]\n\t" \
  "ds_read_b128 a[236:239], v32 offset:11264\n\t" \
  "buffer_load_dwordx4 v[44:47], %1, %11, s35 offen offset:1024 sc1\n\t" \
  "v_mfma_f32_16x16x32_bf16 v[244:247], a[252:255], a[132:135], v[244:247]\n\t" \
  "s_barrier\n\t" \
  "s_waitcnt lgkmcnt(0)\n\t" \
  "v_mfma_f32_16x16x32_bf16 v[240:243], a[192:195], a[136:139], v[240:243]\n\t" \
  "v_mfma_f32_16x16x32_bf16 v[244:247], a[192:195], a[140:143], v[244:247]\n\t" \
  "v_mfma_f32_16x16x32_bf16 v[240:243], a[196:199], a[144:147], v[240:243]\n\t" \
  "v_mfma_f32_16x16x32_bf16 v[244:247], a[196:199], a[148:151], v[244:247]\n\t" \
  "v_accvgpr_read_b32 v120, a160\n\t" \
  "v_accvgpr_read_b32 v122, a168\n\t" \
  "v_accvgpr_read_b32 v121, a161\n\t" \
  "v_accvgpr_read_b32 v123, a169\n\t" \
  "v_lshlrev_b32_e32 v124, 16, v120\n\t" \
  "v_and_b32_e32 v125, 0xffff0000, v120\n\t" \
  "v_lshlrev_b32_e32 v126, 16, v121\n\t" \
  "v_and_b32_e32 v127, 0xffff0000, v121\n\t" \
  "v_lshlrev_b32_e32 v128, 16, v122\n\t" \
  "v_and_b32_e32 v129, 0xffff0000, v122\n\t" \
  "v_lshlrev_b32_e32 v130, 16, v123\n\t" \
  "v_and_b32_e32 v131, 0xffff0000, v123\n\t" \
  "v_pk_add_f32 v[104:105], v[124:125], v[128:129]\n\t" \
  "v_pk_add_f32 v[106:107], v[126:127], v[130:131]\n\t" \
  "v_accvgpr_read_b32 v120, a162\n\t" \
  "v_accvgpr_read_b32 v122, a170\n\t" \
  "v_accvgpr_read_b32 v121, a163\n\t" \
  "v_accvgpr_read_b32 v123, a171\n\t" \
  "v_lshlrev_b32_e32 v124, 16, v120\n\t" \
  "v_and_b32_e32 v125, 0xffff0000, v120\n\t" \
  "v_lshlrev_b32_e32 v126, 16, v121\n\t" \
  "v_and_b32_e32 v127, 0xffff0000, v121\n\t" \
  "v_lshlrev_b32_e32 v128, 16, v122\n\t" \
  "v_and_b32_e32 v129, 0xffff0000, v122\n\t" \
  "v_lshlrev_b32_e32 v130, 16, v123\n\t" \
  "v_and_b32_e32 v131, 0xffff0000, v123\n\t" \
  "v_pk_add_f32 v[108:109], v[124:125], v[128:129]\n\t" \
  "v_pk_add_f32 v[110:111], v[126:127], v[130:131]\n\t" \
  "v_accvgpr_read_b32 v120, a164\n\t" \
  "v_accvgpr_read_b32 v122, a172\n\t" \
  "v_accvgpr_read_b32 v121, a165\n\t" \
  "v_accvgpr_read_b32 v123, a173\n\t" \
  "v_lshlrev_b32_e32 v124, 16, v120\n\t" \
  "v_and_b32_e32 v125, 0xffff0000, v120\n\t" \
  "v_lshlrev_b32_e32 v126, 16, v121\n\t" \
  "v_and_b32_e32 v127, 0xffff0000, v121\n\t" \
  "v_lshlrev_b32_e32 v128, 16, v122\n\t" \
  "v_and_b32_e32 v129, 0xffff0000, v122\n\t" \
  "v_lshlrev_b32_e32 v130, 16, v123\n\t" \
  "v_and_b32_e32 v131, 0xffff0000, v123\n\t" \
  "v_pk_add_f32 v[112:113], v[124:125], v[128:129]\n\t" \
  "v_pk_add_f32 v[114:115], v[126:127], v[130:131]\n\t" \
  "v_accvgpr_read_b32 v120, a166\n\t" \
  "v_accvgpr_read_b32 v122, a174\n\t" \
  "v_accvgpr_read_b32 v121, a167\n\t" \
  "v_accvgpr_read_b32 v123, a175\n\t" \
  "v_lshlrev_b32_e32 v124, 16, v120\n\t" \
  "v_and_b32_e32 v125, 0xffff0000, v120\n\t" \
  "v_lshlrev_b32_e32 v126, 16, v121\n\t" \
  "v_and_b32_e32 v127, 0xffff0000, v121\n\t" \
  "v_lshlrev_b32_e32 v128, 16, v122\n\t" \
  "v_and_b32_e32 v129, 0xffff0000, v122\n\t" \
  "v_lshlrev_b32_e32 v130, 16, v123\n\t" \
  "v_and_b32_e32 v131, 0xffff0000, v123\n\t" \
  "v_pk_add_f32 v[116:117], v[124:125], v[128:129]\n\t" \
  "v_pk_add_f32 v[118:119], v[126:127], v[130:131]\n\t" \
  "v_pk_add_f32 v[104:105], v[104:105], v[240:241]\n\t" \
  "v_pk_add_f32 v[106:107], v[106:107], v[242:243]\n\t" \
  "v_pk_add_f32 v[108:109], v[108:109], v[244:245]\n\t" \
  "v_pk_add_f32 v[110:111], v[110:111], v[246:247]\n\t" \
  "s_mov_b32 s44, 0x40000\n\t" \
  "s_waitcnt vmcnt(6)\n\t" \
  "MK_EA1_%=:\n\t" \
  "v_mov_b32_e32 v120, s90\n\t" \
  "v_mov_b32_e32 v121, s90\n\t" \
  "v_min3_u32 v120, v120, v193, v195\n\t" \
  "v_max3_u32 v121, v121, v193, v195\n\t" \
  "v_min3_u32 v120, v120, v197, v199\n\t" \
  "v_max3_u32 v121, v121, v197, v199\n\t" \
  "v_min3_u32 v120, v120, v201, v203\n\t" \
  "v_max3_u32 v121, v121, v201, v203\n\t" \
  "v_min3_u32 v120, v120, v205, v207\n\t" \
  "v_max3_u32 v121, v121, v205, v207\n\t" \
  "v_min3_u32 v120, v120, v209, v211\n\t" \
  "v_max3_u32 v121, v121, v209, v211\n\t" \
  "v_min3_u32 v120, v120, v213, v215\n\t" \
  "v_max3_u32 v121, v121, v213, v215\n\t" \
  "v_min3_u32 v120, v120, v217, v219\n\t" \
  "v_max3_u32 v121, v121, v217, v219\n\t" \
  "v_min3_u32 v120, v120, v221, v223\n\t" \
  "v_max3_u32 v121, v121, v221, v223\n\t" \
  "v_cmp_ne_u32_e32 vcc, v120, v121\n\t" \
  "s_cbranch_vccnz MK_EAR_%=\n\t" \
  "MK_EAD_%=:\n\t" \
  "v_and_b32_e32 v193, 0xffff0000, v192\n\t" \
  "v_lshlrev_b32_e32 v192, 16, v192\n\t" \
  "v_and_b32_e32 v195, 0xffff0000, v194\n\t" \
  "v_lshlrev_b32_e32 v194, 16, v194\n\t" \
  "v_pk_add_f32 v[112:113], v[112:113], v[192:193]\n\t" \
  "v_pk_add_f32 v[114:115], v[114:115], v[194:195]\n\t" \
  "v_and_b32_e32 v197, 0xffff0000, v196\n\t" \
  "v_lshlrev_b32_e32 v196, 16, v196\n\t" \
  "v_and_b32_e32 v199, 0xffff0000, v198\n\t" \
  "v_lshlrev_b32_e32 v198, 16, v198\n\t" \
  "v_pk_add_f32 v[112:113], v[112:113], v[196:197]\n\t" \
  "v_pk_add_f32 v[114:115], v[114:115], v[198:199]\n\t" \
  "v_and_b32_e32 v201, 0xffff0000, v200\n\t" \
  "v_lshlrev_b32_e32 v200, 16, v200\n\t" \
  "v_and_b32_e32 v203, 0xffff0000, v202\n\t" \
  "v_lshlrev_b32_e32 v202, 16, v202\n\t" \
  "v_pk_add_f32 v[112:113], v[112:113], v[200:201]\n\t" \
  "v_pk_add_f32 v[114:115], v[114:115], v[202:203]\n\t" \
  "v_and_b32_e32 v205, 0xffff0000, v204\n\t" \
  "v_lshlrev_b32_e32 v204, 16, v204\n\t" \
  "v_and_b32_e32 v207, 0xffff0000, v206\n\t" \
  "v_lshlrev_b32_e32 v206, 16, v206\n\t" \
  "v_pk_add_f32 v[112:113], v[112:113], v[204:205]\n\t" \
  "v_pk_add_f32 v[114:115], v[114:115], v[206:207]\n\t" \
  "v_and_b32_e32 v209, 0xffff0000, v208\n\t" \
  "v_lshlrev_b32_e32 v208, 16, v208\n\t" \
  "v_and_b32_e32 v211, 0xffff0000, v210\n\t" \
  "v_lshlrev_b32_e32 v210, 16, v210\n\t" \
  "v_pk_add_f32 v[116:117], v[116:117], v[208:209]\n\t" \
  "v_pk_add_f32 v[118:119], v[118:119], v[210:211]\n\t" \
  "v_and_b32_e32 v213, 0xffff0000, v212\n\t" \
  "v_lshlrev_b32_e32 v212, 16, v212\n\t" \
  "v_and_b32_e32 v215, 0xffff0000, v214\n\t" \
  "v_lshlrev_b32_e32 v214, 16, v214\n\t" \
  "v_pk_add_f32 v[116:117], v[116:117], v[212:213]\n\t" \
  "v_pk_add_f32 v[118:119], v[118:119], v[214:215]\n\t" \
  "v_and_b32_e32 v217, 0xffff0000, v216\n\t" \
  "v_lshlrev_b32_e32 v216, 16, v216\n\t" \
  "v_and_b32_e32 v219, 0xffff0000, v218\n\t" \
  "v_lshlrev_b32_e32 v218, 16, v218\n\t" \
  "v_pk_add_f32 v[116:117], v[116:117], v[216:217]\n\t" \
  "v_pk_add_f32 v[118:119], v[118:119], v[218:219]\n\t" \
  "v_and_b32_e32 v221, 0xffff0000, v220\n\t" \
  "v_lshlrev_b32_e32 v220, 16, v220\n\t" \
  "v_and_b32_e32 v223, 0xffff0000, v222\n\t" \
  "v_lshlrev_b32_e32 v222, 16, v222\n\t" \
  "v_pk_add_f32 v[116:117], v[116:117], v[220:221]\n\t" \
  "v_pk_add_f32 v[118:119], v[118:119], v[222:223]\n\t" \
  "v_cvt_pk_bf16_f32 v112, v112, v113\n\t" \
  "v_cvt_pk_bf16_f32 v113, v114, v115\n\t" \
  "buffer_store_dwordx2 v[112:113], %9, s[60:63], s37 offen offset:0\n\t" \
  "v_cvt_pk_bf16_f32 v116, v116, v117\n\t" \
  "v_cvt_pk_bf16_f32 v117, v118, v119\n\t" \
  "buffer_store_dwordx2 v[116:117], %10, s[60:63], s37 offen offset:64\n\t" \
  "s_mov_b32 s44, 0x40000\n\t" \
  "s_waitcnt vmcnt(2)\n\t" \
  "MK_EB1_%=:\n\t" \
  "v_mov_b32_e32 v120, s90\n\t" \
  "v_mov_b32_e32 v121, s90\n\t" \
  "v_min3_u32 v120, v120, v225, v227\n\t" \
  "v_max3_u32 v121, v121, v225, v227\n\t" \
  "v_min3_u32 v120, v120, v229, v231\n\t" \
  "v_max3_u32 v121, v121, v229, v231\n\t" \
  "v_min3_u32 v120, v120, v233, v235\n\t" \
  "v_max3_u32 v121, v121, v233, v235\n\t" \
  "v_min3_u32 v120, v120, v237, v239\n\t" \
  "v_max3_u32 v121, v121, v237, v239\n\t" \
  "v_min3_u32 v120, v120, v41, v43\n\t" \
  "v_max3_u32 v121, v121, v41, v43\n\t" \
  "v_min3_u32 v120, v120, v45, v47\n\t" \
  "v_max3_u32 v121, v121, v45, v47\n\t" \
  "v_cmp_ne_u32_e32 vcc, v120, v121\n\t" \
  "s_cbranch_vccnz MK_EBR_%=\n\t" \
  "MK_EBD_%=:\n\t" \
  "v_and_b32_e32 v225, 0xffff0000, v224\n\t" \
  "v_lshlrev_b32_e32 v224, 16, v224\n\t" \
  "v_and_b32_e32 v227, 0xffff0000, v226\n\t" \
  "v_lshlrev_b32_e32 v226, 16, v226\n\t" \
  "v_pk_add_f32 v[104:105], v[104:105], v[224:225]\n\t" \
  "v_pk_add_f32 v[106:107], v[106:107], v[226:227]\n\t" \
  "v_and_b32_e32 v229, 0xffff0000, v228\n\t" \
  "v_lshlrev_b32_e32 v228, 16, v228\n\t" \
  "v_and_b32_e32 v231, 0xffff0000, v230\n\t" \
  "v_lshlrev_b32_e32 v230, 16, v230\n\t" \
  "v_pk_add_f32 v[108:109], v[108:109], v[228:229]\n\t" \
  "v_pk_add_f32 v[110:111], v[110:111], v[230:231]\n\t" \
  "v_and_b32_e32 v233, 0xffff0000, v232\n\t" \
  "v_lshlrev_b32_e32 v232, 16, v232\n\t" \
  "v_and_b32_e32 v235, 0xffff0000, v234\n\t" \
  "v_lshlrev_b32_e32 v234, 16, v234\n\t" \
  "v_pk_add_f32 v[104:105], v[104:105], v[232:233]\n\t" \
  "v_pk_add_f32 v[106:107], v[106:107], v[234:235]\n\t" \
  "v_and_b32_e32 v237, 0xffff0000, v236\n\t" \
  "v_lshlrev_b32_e32 v236, 16, v236\n\t" \
  "v_and_b32_e32 v239, 0xffff0000, v238\n\t" \
  "v_lshlrev_b32_e32 v238, 16, v238\n\t" \
  "v_pk_add_f32 v[108:109], v[108:109], v[236:237]\n\t" \
  "v_pk_add_f32 v[110:111], v[110:111], v[238:239]\n\t" \
  "v_and_b32_e32 v41, 0xffff0000, v40\n\t" \
  "v_lshlrev_b32_e32 v40, 16, v40\n\t" \
  "v_and_b32_e32 v43, 0xffff0000, v42\n\t" \
  "v_lshlrev_b32_e32 v42, 16, v42\n\t" \
  "v_pk_add_f32 v[104:105], v[104:105], v[40:41]\n\t" \
  "v_pk_add_f32 v[106:107], v[106:107], v[42:43]\n\t" \
  "v_and_b32_e32 v45, 0xffff0000, v44\n\t" \
  "v_lshlrev_b32_e32 v44, 16, v44\n\t" \
  "v_and_b32_e32 v47, 0xffff0000, v46\n\t" \
  "v_lshlrev_b32_e32 v46, 16, v46\n\t" \
  "v_pk_add_f32 v[108:109], v[108:109], v[44:45]\n\t" \
  "v_pk_add_f32 v[110:111], v[110:111], v[46:47]\n\t" \
  "v_cvt_pk_bf16_f32 v104, v104, v105\n\t" \
  "v_cvt_pk_bf16_f32 v105, v106, v107\n\t" \
  "buffer_store_dwordx2 v[104:105], v37, s[60:63], s40 offen offset:0\n\t" \
  "v_cvt_pk_bf16_f32 v108, v108, v109\n\t" \
  "v_cvt_pk_bf16_f32 v109, v110, v111\n\t" \
  "buffer_store_dwordx2 v[108:109], v37, s[60:63], s41 offen offset:0\n\t" \
  "s_branch MK_END_%=\n\t" \
  "MK_F0KP_%=:\n\t" \
  "s_mov_b32 s44, 0x40000\n\t" \
  "MK_F0KL_%=:\n\t" \
  "s_sleep 1\n\t" \
  "buffer_load_dwordx4 v[124:127], %1, %11, s80 offen sc1\n\t" \
  "s_waitcnt vmcnt(0)\n\t" \
  "s_sub_u32 s44, s44, 1\n\t" \
  "s_cbranch_scc1 MK_F0KR_%=\n\t" \
  "v_cmp_ne_u32_e32 vcc, s90, v124\n\t" \
  "v_cmp_ne_u32_e64 s[48:49], s90, v125\n\t" \
  "v_cmp_ne_u32_e64 s[50:51], s90, v126\n\t" \
  "v_cmp_ne_u32_e64 s[52:53], s90, v127\n\t" \
  "s_or_b64 vcc, vcc, s[48:49]\n\t" \
  "s_or_b64 vcc, vcc, s[50:51]\n\t" \
  "s_or_b64 vcc, vcc, s[52:53]\n\t" \
  "s_cbranch_vccnz MK_F0KL_%=\n\t" \
  "s_branch MK_F0KR_%=\n\t" \
  "MK_F0VP_%=:\n\t" \
  "s_mov_b32 s44, 0x40000\n\t" \
  "MK_F0VL_%=:\n\t" \
  "s_sleep 1\n\t" \
  "buffer_load_dwordx4 v[124:127], %1, %11, s80 offen sc1\n\t" \
  "s_waitcnt vmcnt(0)\n\t" \
  "s_sub_u32 s44, s44, 1\n\t" \
  "s_cbranch_scc1 MK_F0VR_%=\n\t" \
  "v_cmp_ne_u32_e32 vcc, s90, v124\n\t" \
  "v_cmp_ne_u32_e64 s[48:49], s90, v125\n\t" \
  "v_cmp_ne_u32_e64 s[50:51], s90, v126\n\t" \
  "v_cmp_ne_u32_e64 s[52:53], s90, v127\n\t" \
  "s_or_b64 vcc, vcc, s[48:49]\n\t" \
  "s_or_b64 vcc, vcc, s[50:51]\n\t" \
  "s_or_b64 vcc, vcc, s[52:53]\n\t" \
  "s_cbranch_vccnz MK_F0VL_%=\n\t" \
  "s_branch MK_F0VR_%=\n\t" \
  "MK_F1P_%=:\n\t" \
  "s_mov_b32 s44, 0x40000\n\t" \
  "MK_F1L_%=:\n\t" \
  "s_sleep 1\n\t" \
  "buffer_load_dword v126, v125, %11, s80 offen sc1\n\t" \
  "s_waitcnt vmcnt(0)\n\t" \
  "s_sub_u32 s44, s44, 1\n\t" \
  "s_cbranch_scc1 MK_F1R_%=\n\t" \
  "v_cmp_ne_u32_e32 vcc, s90, v126\n\t" \
  "s_cbranch_vccnz MK_F1L_%=\n\t" \
  "s_branch MK_F1R_%=\n\t" \
  "MK_F5P_%=:\n\t" \
  "s_mov_b32 s44, 0x40000\n\t" \
  "MK_F5L_%=:\n\t" \
  "s_sleep 1\n\t" \
  "buffer_load_dword v127, off, %11, s68 offset:4 sc1\n\t" \
  "s_waitcnt vmcnt(0)\n\t" \
  "s_sub_u32 s44, s44, 1\n\t" \
  "s_cbranch_scc1 MK_F5R_%=\n\t" \
  "v_cmp_ne_u32_e32 vcc, s90, v127\n\t" \
  "s_cbranch_vccnz MK_F5L_%=\n\t" \
  "s_branch MK_F5R_%=\n\t" \
  "MK_QL_%=:\n\t" \
  "v_cmp_ne_u32_e32 vcc, s90, v127\n\t" \
  "s_cbranch_vccnz MK_F5P_%=\n\t" \
  "MK_F5R_%=:\n\t" \
  "v_readlane_b32 s43, v29, 21\n\t" \
  "s_nop 0\n\t" \
  "s_add_u32 s72, s43, 17408\n\t" \
  "s_add_u32 s73, s43, 21504\n\t" \
  "s_add_u32 s74, s43, 25600\n\t" \
  "s_add_u32 s75, s43, 29696\n\t" \
  "buffer_load_dwordx4 v[128:131], %1, %11, s72 offen offset:0 sc1\n\t" \
  "buffer_load_dwordx4 v[132:135], %1, %11, s72 offen offset:1024 sc1\n\t" \
  "buffer_load_dwordx4 v[136:139], %1, %11, s72 offen offset:2048 sc1\n\t" \
  "buffer_load_dwordx4 v[140:143], %1, %11, s72 offen offset:3072 sc1\n\t" \
  "buffer_load_dwordx4 v[144:147], %1, %11, s73 offen offset:0 sc1\n\t" \
  "buffer_load_dwordx4 v[148:151], %1, %11, s73 offen offset:1024 sc1\n\t" \
  "buffer_load_dwordx4 v[152:155], %1, %11, s73 offen offset:2048 sc1\n\t" \
  "buffer_load_dwordx4 v[156:159], %1, %11, s73 offen offset:3072 sc1\n\t" \
  "buffer_load_dwordx4 v[160:163], %1, %11, s74 offen offset:0 sc1\n\t" \
  "buffer_load_dwordx4 v[164:167], %1, %11, s74 offen offset:1024 sc1\n\t" \
  "buffer_load_dwordx4 v[168:171], %1, %11, s74 offen offset:2048 sc1\n\t" \
  "buffer_load_dwordx4 v[172:175], %1, %11, s74 offen offset:3072 sc1\n\t" \
  "buffer_load_dwordx4 v[176:179], %1, %11, s75 offen offset:0 sc1\n\t" \
  "buffer_load_dwordx4 v[180:183], %1, %11, s75 offen offset:1024 sc1\n\t" \
  "buffer_load_dwordx4 v[184:187], %1, %11, s75 offen offset:2048 sc1\n\t" \
  "buffer_load_dwordx4 v[188:191], %1, %11, s75 offen offset:3072 sc1\n\t" \
  "s_branch MK_QLR_%=\n\t" \
  "MK_T0_0_%=:\n\t" \
  "s_mov_b32 s42, 0\n\t" \
  "s_branch MK_SLOW0_%=\n\t" \
  "MK_T1_0_%=:\n\t" \
  "s_mov_b32 s42, 0\n\t" \
  "s_branch MK_SLOW1_%=\n\t" \
  "MK_T0_1_%=:\n\t" \
  "s_mov_b32 s42, 1\n\t" \
  "s_branch MK_SLOW0_%=\n\t" \
  "MK_T1_1_%=:\n\t" \
  "s_mov_b32 s42, 1\n\t" \
  "s_branch MK_SLOW1_%=\n\t" \
  "MK_T0_2_%=:\n\t" \
  "s_mov_b32 s42, 2\n\t" \
  "s_branch MK_SLOW0_%=\n\t" \
  "MK_F4P_%=:\n\t" \
  "s_mov_b32 s44, 0x40000\n\t" \
  "MK_F4L_%=:\n\t" \
  "s_sleep 1\n\t" \
  "buffer_load_dwordx4 v[124:127], %1, %11, s80 offen sc1\n\t" \
  "s_waitcnt vmcnt(0)\n\t" \
  "s_sub_u32 s44, s44, 1\n\t" \
  "s_cbranch_scc1 MK_F4R_%=\n\t" \
  "v_cmp_ne_u32_e32 vcc, s90, v124\n\t" \
  "v_cmp_ne_u32_e64 s[48:49], s90, v125\n\t" \
  "v_cmp_ne_u32_e64 s[50:51], s90, v126\n\t" \
  "v_cmp_ne_u32_e64 s[52:53], s90, v127\n\t" \
  "s_or_b64 vcc, vcc, s[48:49]\n\t" \
  "s_or_b64 vcc, vcc, s[50:51]\n\t" \
  "s_or_b64 vcc, vcc, s[52:53]\n\t" \
  "s_cbranch_vccnz MK_F4L_%=\n\t" \
  "s_branch MK_F4R_%=\n\t" \
  "MK_T1_2_%=:\n\t" \
  "s_mov_b32 s42, 2\n\t" \
  "s_branch MK_SLOW1_%=\n\t" \
  "MK_F4SP_%=:\n\t" \
  "s_mov_b32 s44, 0x40000\n\t" \
  "MK_F4SL_%=:\n\t" \
  "s_sleep 1\n\t" \
  "buffer_load_dwordx4 v[124:127], %1, %11, s80 offen sc1\n\t" \
  "s_waitcnt vmcnt(0)\n\t" \
  "s_sub_u32 s44, s44, 1\n\t" \
  "s_cbranch_scc1 MK_F4SR_%=\n\t" \
  "v_cmp_ne_u32_e32 vcc, s90, v124\n\t" \
  "v_cmp_ne_u32_e64 s[48:49], s90, v125\n\t" \
  "v_cmp_ne_u32_e64 s[50:51], s90, v126\n\t" \
  "v_cmp_ne_u32_e64 s[52:53], s90, v127\n\t" \
  "s_or_b64 vcc, vcc, s[48:49]\n\t" \
  "s_or_b64 vcc, vcc, s[50:51]\n\t" \
  "s_or_b64 vcc, vcc, s[52:53]\n\t" \
  "s_cbranch_vccnz MK_F4SL_%=\n\t" \
  "s_branch MK_F4SR_%=\n\t" \
  "MK_F2P_%=:\n\t" \
  "s_mov_b32 s44, 0x40000\n\t" \
  "MK_F2L_%=:\n\t" \
  "s_sleep 1\n\t" \
  "buffer_load_dword v36, off, %11, s68 sc1\n\t" \
  "s_waitcnt vmcnt(0)\n\t" \
  "s_sub_u32 s44, s44, 1\n\t" \
  "s_cbranch_scc1 MK_F2R_%=\n\t" \
  "v_cmp_ne_u32_e32 vcc, s90, v36\n\t" \
  "s_cbranch_vccnz MK_F2L_%=\n\t" \
  "s_branch MK_F2R_%=\n\t" \
  "MK_T0_3_%=:\n\t" \
  "s_mov_b32 s42, 3\n\t" \
  "s_branch MK_SLOW0_%=\n\t" \
  "MK_T1_3_%=:\n\t" \
  "s_mov_b32 s42, 3\n\t" \
  "s_branch MK_SLOW1_%=\n\t" \
  "MK_T0_4_%=:\n\t" \
  "s_mov_b32 s42, 4\n\t" \
  "s_branch MK_SLOW0B_%=\n\t" \
  "MK_T1_4_%=:\n\t" \
  "s_mov_b32 s42, 4\n\t" \
  "s_branch MK_SLOW1B_%=\n\t" \
  "MK_F3P_%=:\n\t" \
  "s_mov_b32 s44, 0x40000\n\t" \
  "MK_F3L_%=:\n\t" \
  "s_sleep 1\n\t" \
  "buffer_load_dword v36, v39, %11, s80 offen sc1\n\t" \
  "s_waitcnt vmcnt(0)\n\t" \
  "s_sub_u32 s44, s44, 1\n\t" \
  "s_cbranch_scc1 MK_F3R_%=\n\t" \
  "v_cmp_ne_u32_e32 vcc, s90, v36\n\t" \
  "s_cbranch_vccnz MK_F3L_%=\n\t" \
  "s_branch MK_F3R_%=\n\t" \
  "MK_F3LP_%=:\n\t" \
  "s_mov_b32 s44, 0x40000\n\t" \
  "MK_F3LL_%=:\n\t" \
  "s_sleep 1\n\t" \
  "buffer_load_dword v36, v39, %11, s80 offen offset:8 sc1\n\t" \
  "s_waitcnt vmcnt(0)\n\t" \
  "s_sub_u32 s44, s44, 1\n\t" \
  "s_cbranch_scc1 MK_F3LR_%=\n\t" \
  "v_cmp_ne_u32_e32 vcc, s90, v36\n\t" \
  "s_cbranch_vccnz MK_F3LL_%=\n\t" \
  "s_branch MK_F3LR_%=\n\t" \
  "MK_EAR_%=:\n\t" \
  "s_sub_u32 s44, s44, 1\n\t" \
  "s_cbranch_scc1 MK_EAD_%=\n\t" \
  "s_sleep 2\n\t" \
  "buffer_load_dwordx4 v[192:195], %1, %11, s76 offen sc1\n\t" \
  "buffer_load_dwordx4 v[196:199], %1, %11, s77 offen sc1\n\t" \
  "buffer_load_dwordx4 v[200:203], %1, %11, s78 offen sc1\n\t" \
  "buffer_load_dwordx4 v[204:207], %1, %11, s79 offen sc1\n\t" \
  "buffer_load_dwordx4 v[208:211], v36, %11, s76 offen sc1\n\t" \
  "buffer_load_dwordx4 v[212:215], v36, %11, s77 offen sc1\n\t" \
  "buffer_load_dwordx4 v[216:219], v36, %11, s78 offen sc1\n\t" \
  "buffer_load_dwordx4 v[220:223], v36, %11, s79 offen sc1\n\t" \
  "s_waitcnt vmcnt(0)\n\t" \
  "s_branch MK_EA1_%=\n\t" \
  "MK_EBR_%=:\n\t" \
  "s_sub_u32 s44, s44, 1\n\t" \
  "s_cbranch_scc1 MK_EBD_%=\n\t" \
  "s_sleep 2\n\t" \
  "buffer_load_dwordx4 v[224:227], %1, %11, s69 offen offset:0 sc1\n\t" \
  "buffer_load_dwordx4 v[228:231], %1, %11, s69 offen offset:1024 sc1\n\t" \
  "buffer_load_dwordx4 v[232:235], %1, %11, s70 offen offset:0 sc1\n\t" \
  "buffer_load_dwordx4 v[236:239], %1, %11, s70 offen offset:1024 sc1\n\t" \
  "buffer_load_dwordx4 v[40:43], %1, %11, s35 offen offset:0 sc1\n\t" \
  "buffer_load_dwordx4 v[44:47], %1, %11, s35 offen offset:1024 sc1\n\t" \
  "s_waitcnt vmcnt(0)\n\t" \
  "s_branch MK_EB1_%=\n\t" \
  "MK_SLOW0_%=:\n\t" \
  "s_nop 15\n\t" \
  "v_max3_f32 v38, v192, v193, v194\n\t" \
  "v_max3_f32 v38, v38, v195, v200\n\t" \
  "v_max3_f32 v38, v38, v201, v202\n\t" \
  "v_max_f32_e32 v38, v38, v203\n\t" \
  "v_mov_b32_e32 v252, v38\n\t" \
  "s_nop 1\n\t" \
  "v_permlane16_swap_b32_e32 v38, v252\n\t" \
  "s_nop 0\n\t" \
  "v_max_f32_e32 v38, v38, v252\n\t" \
  "v_mov_b32_e32 v252, v38\n\t" \
  "s_nop 1\n\t" \
  "v_permlane32_swap_b32_e32 v38, v252\n\t" \
  "s_nop 0\n\t" \
  "v_max_f32_e32 v38, v38, v252\n\t" \
  "v_mov_b32_e32 v253, v250\n\t" \
  "v_mov_b32_e32 v252, v253\n\t" \
  "s_nop 1\n\t" \
  "v_permlane16_swap_b32_e32 v253, v252\n\t" \
  "s_nop 0\n\t" \
  "v_add_f32_e32 v253, v253, v252\n\t" \
  "v_mov_b32_e32 v252, v253\n\t" \
  "s_nop 1\n\t" \
  "v_permlane32_swap_b32_e32 v253, v252\n\t" \
  "s_nop 0\n\t" \
  "v_add_f32_e32 v253, v253, v252\n\t" \
  "v_log_f32_e32 v253, v253\n\t" \
  "s_nop 0\n\t" \
  "v_max3_f32 v38, v38, v253, 0\n\t" \
  "v_sub_f32_e32 v192, v192, v38\n\t" \
  "v_sub_f32_e32 v193, v193, v38\n\t" \
  "v_sub_f32_e32 v194, v194, v38\n\t" \
  "v_sub_f32_e32 v195, v195, v38\n\t" \
  "v_sub_f32_e32 v200, v200, v38\n\t" \
  "v_sub_f32_e32 v201, v201, v38\n\t" \
  "v_sub_f32_e32 v202, v202, v38\n\t" \
  "v_sub_f32_e32 v203, v203, v38\n\t" \
  "v_sub_f32_e32 v208, v208, v38\n\t" \
  "v_sub_f32_e32 v209, v209, v38\n\t" \
  "v_sub_f32_e32 v210, v210, v38\n\t" \
  "v_sub_f32_e32 v211, v211, v38\n\t" \
  "v_sub_f32_e32 v216, v216, v38\n\t" \
  "v_sub_f32_e32 v217, v217, v38\n\t" \
  "v_sub_f32_e32 v218, v218, v38\n\t" \
  "v_sub_f32_e32 v219, v219, v38\n\t" \
  "v_sub_f32_e32 v240, v240, v38\n\t" \
  "v_sub_f32_e32 v241, v241, v38\n\t" \
  "v_sub_f32_e32 v242, v242, v38\n\t" \
  "v_sub_f32_e32 v243, v243, v38\n\t" \
  "v_exp_f32_e64 v38, -v38\n\t" \
  "s_nop 0\n\t" \
  "v_mul_f32_e32 v250, v250, v38\n\t" \
  "v_accvgpr_read_b32 v252, a0\n\t" \
  "v_mul_f32_e32 v252, v252, v38\n\t" \
  "v_accvgpr_write_b32 a0, v252\n\t" \
  "v_accvgpr_read_b32 v253, a1\n\t" \
  "v_mul_f32_e32 v253, v253, v38\n\t" \
  "v_accvgpr_write_b32 a1, v253\n\t" \
  "v_accvgpr_read_b32 v254, a2\n\t" \
  "v_mul_f32_e32 v254, v254, v38\n\t" \
  "v_accvgpr_write_b32 a2, v254\n\t" \
  "v_accvgpr_read_b32 v255, a3\n\t" \
  "v_mul_f32_e32 v255, v255, v38\n\t" \
  "v_accvgpr_write_b32 a3, v255\n\t" \
  "v_accvgpr_read_b32 v252, a8\n\t" \
  "v_mul_f32_e32 v252, v252, v38\n\t" \
  "v_accvgpr_write_b32 a8, v252\n\t" \
  "v_accvgpr_read_b32 v253, a9\n\t" \
  "v_mul_f32_e32 v253, v253, v38\n\t" \
  "v_accvgpr_write_b32 a9, v253\n\t" \
  "v_accvgpr_read_b32 v254, a10\n\t" \
  "v_mul_f32_e32 v254, v254, v38\n\t" \
  "v_accvgpr_write_b32 a10, v254\n\t" \
  "v_accvgpr_read_b32 v255, a11\n\t" \
  "v_mul_f32_e32 v255, v255, v38\n\t" \
  "v_accvgpr_write_b32 a11, v255\n\t" \
  "v_accvgpr_read_b32 v252, a16\n\t" \
  "v_mul_f32_e32 v252, v252, v38\n\t" \
  "v_accvgpr_write_b32 a16, v252\n\t" \
  "v_accvgpr_read_b32 v253, a17\n\t" \
  "v_mul_f32_e32 v253, v253, v38\n\t" \
  "v_accvgpr_write_b32 a17, v253\n\t" \
  "v_accvgpr_read_b32 v254, a18\n\t" \
  "v_mul_f32_e32 v254, v254, v38\n\t" \
  "v_accvgpr_write_b32 a18, v254\n\t" \
  "v_accvgpr_read_b32 v255, a19\n\t" \
  "v_mul_f32_e32 v255, v255, v38\n\t" \
  "v_accvgpr_write_b32 a19, v255\n\t" \
  "v_accvgpr_read_b32 v252, a24\n\t" \
  "v_mul_f32_e32 v252, v252, v38\n\t" \
  "v_accvgpr_write_b32 a24, v252\n\t" \
  "v_accvgpr_read_b32 v253, a25\n\t" \
  "v_mul_f32_e32 v253, v253, v38\n\t" \
  "v_accvgpr_write_b32 a25, v253\n\t" \
  "v_accvgpr_read_b32 v254, a26\n\t" \
  "v_mul_f32_e32 v254, v254, v38\n\t" \
  "v_accvgpr_write_b32 a26, v254\n\t" \
  "v_accvgpr_read_b32 v255, a27\n\t" \
  "v_mul_f32_e32 v255, v255, v38\n\t" \
  "v_accvgpr_write_b32 a27, v255\n\t" \
  "v_accvgpr_read_b32 v252, a32\n\t" \
  "v_mul_f32_e32 v252, v252, v38\n\t" \
  "v_accvgpr_write_b32 a32, v252\n\t" \
  "v_accvgpr_read_b32 v253, a33\n\t" \
  "v_mul_f32_e32 v253, v253, v38\n\t" \
  "v_accvgpr_write_b32 a33, v253\n\t" \
  "v_accvgpr_read_b32 v254, a34\n\t" \
  "v_mul_f32_e32 v254, v254, v38\n\t" \
  "v_accvgpr_write_b32 a34, v254\n\t" \
  "v_accvgpr_read_b32 v255, a35\n\t" \
  "v_mul_f32_e32 v255, v255, v38\n\t" \
  "v_accvgpr_write_b32 a35, v255\n\t" \
  "v_accvgpr_read_b32 v252, a40\n\t" \
  "v_mul_f32_e32 v252, v252, v38\n\t" \
  "v_accvgpr_write_b32 a40, v252\n\t" \
  "v_accvgpr_read_b32 v253, a41\n\t" \
  "v_mul_f32_e32 v253, v253, v38\n\t" \
  "v_accvgpr_write_b32 a41, v253\n\t" \
  "v_accvgpr_read_b32 v254, a42\n\t" \
  "v_mul_f32_e32 v254, v254, v38\n\t" \
  "v_accvgpr_write_b32 a42, v254\n\t" \
  "v_accvgpr_read_b32 v255, a43\n\t" \
  "v_mul_f32_e32 v255, v255, v38\n\t" \
  "v_accvgpr_write_b32 a43, v255\n\t" \
  "v_accvgpr_read_b32 v252, a48\n\t" \
  "v_mul_f32_e32 v252, v252, v38\n\t" \
  "v_accvgpr_write_b32 a48, v252\n\t" \
  "v_accvgpr_read_b32 v253, a49\n\t" \
  "v_mul_f32_e32 v253, v253, v38\n\t" \
  "v_accvgpr_write_b32 a49, v253\n\t" \
  "v_accvgpr_read_b32 v254, a50\n\t" \
  "v_mul_f32_e32 v254, v254, v38\n\t" \
  "v_accvgpr_write_b32 a50, v254\n\t" \
  "v_accvgpr_read_b32 v255, a51\n\t" \
  "v_mul_f32_e32 v255, v255, v38\n\t" \
  "v_accvgpr_write_b32 a51, v255\n\t" \
  "v_accvgpr_read_b32 v252, a56\n\t" \
  "v_mul_f32_e32 v252, v252, v38\n\t" \
  "v_accvgpr_write_b32 a56, v252\n\t" \
  "v_accvgpr_read_b32 v253, a57\n\t" \
  "v_mul_f32_e32 v253, v253, v38\n\t" \
  "v_accvgpr_write_b32 a57, v253\n\t" \
  "v_accvgpr_read_b32 v254, a58\n\t" \
  "v_mul_f32_e32 v254, v254, v38\n\t" \
  "v_accvgpr_write_b32 a58, v254\n\t" \
  "v_accvgpr_read_b32 v255, a59\n\t" \
  "v_mul_f32_e32 v255, v255, v38\n\t" \
  "v_accvgpr_write_b32 a59, v255\n\t" \
  "v_accvgpr_read_b32 v252, a64\n\t" \
  "v_mul_f32_e32 v252, v252, v38\n\t" \
  "v_accvgpr_write_b32 a64, v252\n\t" \
  "v_accvgpr_read_b32 v253, a65\n\t" \
  "v_mul_f32_e32 v253, v253, v38\n\t" \
  "v_accvgpr_write_b32 a65, v253\n\t" \
  "v_accvgpr_read_b32 v254, a66\n\t" \
  "v_mul_f32_e32 v254, v254, v38\n\t" \
  "v_accvgpr_write_b32 a66, v254\n\t" \
  "v_accvgpr_read_b32 v255, a67\n\t" \
  "v_mul_f32_e32 v255, v255, v38\n\t" \
  "v_accvgpr_write_b32 a67, v255\n\t" \
  "v_accvgpr_read_b32 v252, a72\n\t" \
  "v_mul_f32_e32 v252, v252, v38\n\t" \
  "v_accvgpr_write_b32 a72, v252\n\t" \
  "v_accvgpr_read_b32 v253, a73\n\t" \
  "v_mul_f32_e32 v253, v253, v38\n\t" \
  "v_accvgpr_write_b32 a73, v253\n\t" \
  "v_accvgpr_read_b32 v254, a74\n\t" \
  "v_mul_f32_e32 v254, v254, v38\n\t" \
  "v_accvgpr_write_b32 a74, v254\n\t" \
  "v_accvgpr_read_b32 v255, a75\n\t" \
  "v_mul_f32_e32 v255, v255, v38\n\t" \
  "v_accvgpr_write_b32 a75, v255\n\t" \
  "v_accvgpr_read_b32 v252, a80\n\t" \
  "v_mul_f32_e32 v252, v252, v38\n\t" \
  "v_accvgpr_write_b32 a80, v252\n\t" \
  "v_accvgpr_read_b32 v253, a81\n\t" \
  "v_mul_f32_e32 v253, v253, v38\n\t" \
  "v_accvgpr_write_b32 a81, v253\n\t" \
  "v_accvgpr_read_b32 v254, a82\n\t" \
  "v_mul_f32_e32 v254, v254, v38\n\t" \
  "v_accvgpr_write_b32 a82, v254\n\t" \
  "v_accvgpr_read_b32 v255, a83\n\t" \
  "v_mul_f32_e32 v255, v255, v38\n\t" \
  "v_accvgpr_write_b32 a83, v255\n\t" \
  "v_accvgpr_read_b32 v252, a88\n\t" \
  "v_mul_f32_e32 v252, v252, v38\n\t" \
  "v_accvgpr_write_b32 a88, v252\n\t" \
  "v_accvgpr_read_b32 v253, a89\n\t" \
  "v_mul_f32_e32 v253, v253, v38\n\t" \
  "v_accvgpr_write_b32 a89, v253\n\t" \
  "v_accvgpr_read_b32 v254, a90\n\t" \
  "v_mul_f32_e32 v254, v254, v38\n\t" \
  "v_accvgpr_write_b32 a90, v254\n\t" \
  "v_accvgpr_read_b32 v255, a91\n\t" \
  "v_mul_f32_e32 v255, v255, v38\n\t" \
  "v_accvgpr_write_b32 a91, v255\n\t" \
  "v_accvgpr_read_b32 v252, a96\n\t" \
  "v_mul_f32_e32 v252, v252, v38\n\t" \
  "v_accvgpr_write_b32 a96, v252\n\t" \
  "v_accvgpr_read_b32 v253, a97\n\t" \
  "v_mul_f32_e32 v253, v253, v38\n\t" \
  "v_accvgpr_write_b32 a97, v253\n\t" \
  "v_accvgpr_read_b32 v254, a98\n\t" \
  "v_mul_f32_e32 v254, v254, v38\n\t" \
  "v_accvgpr_write_b32 a98, v254\n\t" \
  "v_accvgpr_read_b32 v255, a99\n\t" \
  "v_mul_f32_e32 v255, v255, v38\n\t" \
  "v_accvgpr_write_b32 a99, v255\n\t" \
  "v_accvgpr_read_b32 v252, a104\n\t" \
  "v_mul_f32_e32 v252, v252, v38\n\t" \
  "v_accvgpr_write_b32 a104, v252\n\t" \
  "v_accvgpr_read_b32 v253, a105\n\t" \
  "v_mul_f32_e32 v253, v253, v38\n\t" \
  "v_accvgpr_write_b32 a105, v253\n\t" \
  "v_accvgpr_read_b32 v254, a106\n\t" \
  "v_mul_f32_e32 v254, v254, v38\n\t" \
  "v_accvgpr_write_b32 a106, v254\n\t" \
  "v_accvgpr_read_b32 v255, a107\n\t" \
  "v_mul_f32_e32 v255, v255, v38\n\t" \
  "v_accvgpr_write_b32 a107, v255\n\t" \
  "v_accvgpr_read_b32 v252, a112\n\t" \
  "v_mul_f32_e32 v252, v252, v38\n\t" \
  "v_accvgpr_write_b32 a112, v252\n\t" \
  "v_accvgpr_read_b32 v253, a113\n\t" \
  "v_mul_f32_e32 v253, v253, v38\n\t" \
  "v_accvgpr_write_b32 a113, v253\n\t" \
  "v_accvgpr_read_b32 v254, a114\n\t" \
  "v_mul_f32_e32 v254, v254, v38\n\t" \
  "v_accvgpr_write_b32 a114, v254\n\t" \
  "v_accvgpr_read_b32 v255, a115\n\t" \
  "v_mul_f32_e32 v255, v255, v38\n\t" \
  "v_accvgpr_write_b32 a115, v255\n\t" \
  "v_accvgpr_read_b32 v252, a120\n\t" \
  "v_mul_f32_e32 v252, v252, v38\n\t" \
  "v_accvgpr_write_b32 a120, v252\n\t" \
  "v_accvgpr_read_b32 v253, a121\n\t" \
  "v_mul_f32_e32 v253, v253, v38\n\t" \
  "v_accvgpr_write_b32 a121, v253\n\t" \
  "v_accvgpr_read_b32 v254, a122\n\t" \
  "v_mul_f32_e32 v254, v254, v38\n\t" \
  "v_accvgpr_write_b32 a122, v254\n\t" \
  "v_accvgpr_read_b32 v255, a123\n\t" \
  "v_mul_f32_e32 v255, v255, v38\n\t" \
  "v_accvgpr_write_b32 a123, v255\n\t" \
  "v_exp_f32_e32 v252, v192\n\t" \
  "v_exp_f32_e32 v253, v193\n\t" \
  "v_add_f32_e32 v248, v250, v252\n\t" \
  "v_add_f32_e32 v248, v248, v253\n\t" \
  "v_cvt_pk_bf16_f32 v224, v252, v253\n\t" \
  "v_exp_f32_e32 v254, v194\n\t" \
  "v_exp_f32_e32 v255, v195\n\t" \
  "v_add_f32_e32 v248, v248, v254\n\t" \
  "v_add_f32_e32 v248, v248, v255\n\t" \
  "v_cvt_pk_bf16_f32 v225, v254, v255\n\t" \
  "v_exp_f32_e32 v252, v200\n\t" \
  "v_exp_f32_e32 v253, v201\n\t" \
  "v_add_f32_e32 v248, v248, v252\n\t" \
  "v_add_f32_e32 v248, v248, v253\n\t" \
  "v_cvt_pk_bf16_f32 v226, v252, v253\n\t" \
  "v_exp_f32_e32 v254, v202\n\t" \
  "v_exp_f32_e32 v255, v203\n\t" \
  "v_add_f32_e32 v248, v248, v254\n\t" \
  "v_add_f32_e32 v248, v248, v255\n\t" \
  "v_cvt_pk_bf16_f32 v227, v254, v255\n\t" \
  "MK_SLOW0B_%=:\n\t" \
  "s_nop 15\n\t" \
  "v_max3_f32 v38, v196, v197, v198\n\t" \
  "v_max3_f32 v38, v38, v199, v204\n\t" \
  "v_max3_f32 v38, v38, v205, v206\n\t" \
  "v_max_f32_e32 v38, v38, v207\n\t" \
  "v_mov_b32_e32 v252, v38\n\t" \
  "s_nop 1\n\t" \
  "v_permlane16_swap_b32_e32 v38, v252\n\t" \
  "s_nop 0\n\t" \
  "v_max_f32_e32 v38, v38, v252\n\t" \
  "v_mov_b32_e32 v252, v38\n\t" \
  "s_nop 1\n\t" \
  "v_permlane32_swap_b32_e32 v38, v252\n\t" \
  "s_nop 0\n\t" \
  "v_max_f32_e32 v38, v38, v252\n\t" \
  "v_mov_b32_e32 v253, v251\n\t" \
  "v_mov_b32_e32 v252, v253\n\t" \
  "s_nop 1\n\t" \
  "v_permlane16_swap_b32_e32 v253, v252\n\t" \
  "s_nop 0\n\t" \
  "v_add_f32_e32 v253, v253, v252\n\t" \
  "v_mov_b32_e32 v252, v253\n\t" \
  "s_nop 1\n\t" \
  "v_permlane32_swap_b32_e32 v253, v252\n\t" \
  "s_nop 0\n\t" \
  "v_add_f32_e32 v253, v253, v252\n\t" \
  "v_log_f32_e32 v253, v253\n\t" \
  "s_nop 0\n\t" \
  "v_max3_f32 v38, v38, v253, 0\n\t" \
  "v_sub_f32_e32 v196, v196, v38\n\t" \
  "v_sub_f32_e32 v197, v197, v38\n\t" \
  "v_sub_f32_e32 v198, v198, v38\n\t" \
  "v_sub_f32_e32 v199, v199, v38\n\t" \
  "v_sub_f32_e32 v204, v204, v38\n\t" \
  "v_sub_f32_e32 v205, v205, v38\n\t" \
  "v_sub_f32_e32 v206, v206, v38\n\t" \
  "v_sub_f32_e32 v207, v207, v38\n\t" \
  "v_sub_f32_e32 v212, v212, v38\n\t" \
  "v_sub_f32_e32 v213, v213, v38\n\t" \
  "v_sub_f32_e32 v214, v214, v38\n\t" \
  "v_sub_f32_e32 v215, v215, v38\n\t" \
  "v_sub_f32_e32 v220, v220, v38\n\t" \
  "v_sub_f32_e32 v221, v221, v38\n\t" \
  "v_sub_f32_e32 v222, v222, v38\n\t" \
  "v_sub_f32_e32 v223, v223, v38\n\t" \
  "v_sub_f32_e32 v244, v244, v38\n\t" \
  "v_sub_f32_e32 v245, v245, v38\n\t" \
  "v_sub_f32_e32 v246, v246, v38\n\t" \
  "v_sub_f32_e32 v247, v247, v38\n\t" \
  "v_exp_f32_e64 v38, -v38\n\t" \
  "s_nop 0\n\t" \
  "v_mul_f32_e32 v251, v251, v38\n\t" \
  "v_accvgpr_read_b32 v252, a4\n\t" \
  "v_mul_f32_e32 v252, v252, v38\n\t" \
  "v_accvgpr_write_b32 a4, v252\n\t" \
  "v_accvgpr_read_b32 v253, a5\n\t" \
  "v_mul_f32_e32 v253, v253, v38\n\t" \
  "v_accvgpr_write_b32 a5, v253\n\t" \
  "v_accvgpr_read_b32 v254, a6\n\t" \
  "v_mul_f32_e32 v254, v254, v38\n\t" \
  "v_accvgpr_write_b32 a6, v254\n\t" \
  "v_accvgpr_read_b32 v255, a7\n\t" \
  "v_mul_f32_e32 v255, v255, v38\n\t" \
  "v_accvgpr_write_b32 a7, v255\n\t" \
  "v_accvgpr_read_b32 v252, a12\n\t" \
  "v_mul_f32_e32 v252, v252, v38\n\t" \
  "v_accvgpr_write_b32 a12, v252\n\t" \
  "v_accvgpr_read_b32 v253, a13\n\t" \
  "v_mul_f32_e32 v253, v253, v38\n\t" \
  "v_accvgpr_write_b32 a13, v253\n\t" \
  "v_accvgpr_read_b32 v254, a14\n\t" \
  "v_mul_f32_e32 v254, v254, v38\n\t" \
  "v_accvgpr_write_b32 a14, v254\n\t" \
  "v_accvgpr_read_b32 v255, a15\n\t" \
  "v_mul_f32_e32 v255, v255, v38\n\t" \
  "v_accvgpr_write_b32 a15, v255\n\t" \
  "v_accvgpr_read_b32 v252, a20\n\t" \
  "v_mul_f32_e32 v252, v252, v38\n\t" \
  "v_accvgpr_write_b32 a20, v252\n\t" \
  "v_accvgpr_read_b32 v253, a21\n\t" \
  "v_mul_f32_e32 v253, v253, v38\n\t" \
  "v_accvgpr_write_b32 a21, v253\n\t" \
  "v_accvgpr_read_b32 v254, a22\n\t" \
  "v_mul_f32_e32 v254, v254, v38\n\t" \
  "v_accvgpr_write_b32 a22, v254\n\t" \
  "v_accvgpr_read_b32 v255, a23\n\t" \
  "v_mul_f32_e32 v255, v255, v38\n\t" \
  "v_accvgpr_write_b32 a23, v255\n\t" \
  "v_accvgpr_read_b32 v252, a28\n\t" \
  "v_mul_f32_e32 v252, v252, v38\n\t" \
  "v_accvgpr_write_b32 a28, v252\n\t" \
  "v_accvgpr_read_b32 v253, a29\n\t" \
  "v_mul_f32_e32 v253, v253, v38\n\t" \
  "v_accvgpr_write_b32 a29, v253\n\t" \
  "v_accvgpr_read_b32 v254, a30\n\t" \
  "v_mul_f32_e32 v254, v254, v38\n\t" \
  "v_accvgpr_write_b32 a30, v254\n\t" \
  "v_accvgpr_read_b32 v255, a31\n\t" \
  "v_mul_f32_e32 v255, v255, v38\n\t" \
  "v_accvgpr_write_b32 a31, v255\n\t" \
  "v_accvgpr_read_b32 v252, a36\n\t" \
  "v_mul_f32_e32 v252, v252, v38\n\t" \
  "v_accvgpr_write_b32 a36, v252\n\t" \
  "v_accvgpr_read_b32 v253, a37\n\t" \
  "v_mul_f32_e32 v253, v253, v38\n\t" \
  "v_accvgpr_write_b32 a37, v253\n\t" \
  "v_accvgpr_read_b32 v254, a38\n\t" \
  "v_mul_f32_e32 v254, v254, v38\n\t" \
  "v_accvgpr_write_b32 a38, v254\n\t" \
  "v_accvgpr_read_b32 v255, a39\n\t" \
  "v_mul_f32_e32 v255, v255, v38\n\t" \
  "v_accvgpr_write_b32 a39, v255\n\t" \
  "v_accvgpr_read_b32 v252, a44\n\t" \
  "v_mul_f32_e32 v252, v252, v38\n\t" \
  "v_accvgpr_write_b32 a44, v252\n\t" \
  "v_accvgpr_read_b32 v253, a45\n\t" \
  "v_mul_f32_e32 v253, v253, v38\n\t" \
  "v_accvgpr_write_b32 a45, v253\n\t" \
  "v_accvgpr_read_b32 v254, a46\n\t" \
  "v_mul_f32_e32 v254, v254, v38\n\t" \
  "v_accvgpr_write_b32 a46, v254\n\t" \
  "v_accvgpr_read_b32 v255, a47\n\t" \
  "v_mul_f32_e32 v255, v255, v38\n\t" \
  "v_accvgpr_write_b32 a47, v255\n\t" \
  "v_accvgpr_read_b32 v252, a52\n\t" \
  "v_mul_f32_e32 v252, v252, v38\n\t" \
  "v_accvgpr_write_b32 a52, v252\n\t" \
  "v_accvgpr_read_b32 v253, a53\n\t" \
  "v_mul_f32_e32 v253, v253, v38\n\t" \
  "v_accvgpr_write_b32 a53, v253\n\t" \
  "v_accvgpr_read_b32 v254, a54\n\t" \
  "v_mul_f32_e32 v254, v254, v38\n\t" \
  "v_accvgpr_write_b32 a54, v254\n\t" \
  "v_accvgpr_read_b32 v255, a55\n\t" \
  "v_mul_f32_e32 v255, v255, v38\n\t" \
  "v_accvgpr_write_b32 a55, v255\n\t" \
  "v_accvgpr_read_b32 v252, a60\n\t" \
  "v_mul_f32_e32 v252, v252, v38\n\t" \
  "v_accvgpr_write_b32 a60, v252\n\t" \
  "v_accvgpr_read_b32 v253, a61\n\t" \
  "v_mul_f32_e32 v253, v253, v38\n\t" \
  "v_accvgpr_write_b32 a61, v253\n\t" \
  "v_accvgpr_read_b32 v254, a62\n\t" \
  "v_mul_f32_e32 v254, v254, v38\n\t" \
  "v_accvgpr_write_b32 a62, v254\n\t" \
  "v_accvgpr_read_b32 v255, a63\n\t" \
  "v_mul_f32_e32 v255, v255, v38\n\t" \
  "v_accvgpr_write_b32 a63, v255\n\t" \
  "v_accvgpr_read_b32 v252, a68\n\t" \
  "v_mul_f32_e32 v252, v252, v38\n\t" \
  "v_accvgpr_write_b32 a68, v252\n\t" \
  "v_accvgpr_read_b32 v253, a69\n\t" \
  "v_mul_f32_e32 v253, v253, v38\n\t" \
  "v_accvgpr_write_b32 a69, v253\n\t" \
  "v_accvgpr_read_b32 v254, a70\n\t" \
  "v_mul_f32_e32 v254, v254, v38\n\t" \
  "v_accvgpr_write_b32 a70, v254\n\t" \
  "v_accvgpr_read_b32 v255, a71\n\t" \
  "v_mul_f32_e32 v255, v255, v38\n\t" \
  "v_accvgpr_write_b32 a71, v255\n\t" \
  "v_accvgpr_read_b32 v252, a76\n\t" \
  "v_mul_f32_e32 v252, v252, v38\n\t" \
  "v_accvgpr_write_b32 a76, v252\n\t" \
  "v_accvgpr_read_b32 v253, a77\n\t" \
  "v_mul_f32_e32 v253, v253, v38\n\t" \
  "v_accvgpr_write_b32 a77, v253\n\t" \
  "v_accvgpr_read_b32 v254, a78\n\t" \
  "v_mul_f32_e32 v254, v254, v38\n\t" \
  "v_accvgpr_write_b32 a78, v254\n\t" \
  "v_accvgpr_read_b32 v255, a79\n\t" \
  "v_mul_f32_e32 v255, v255, v38\n\t" \
  "v_accvgpr_write_b32 a79, v255\n\t" \
  "v_accvgpr_read_b32 v252, a84\n\t" \
  "v_mul_f32_e32 v252, v252, v38\n\t" \
  "v_accvgpr_write_b32 a84, v252\n\t" \
  "v_accvgpr_read_b32 v253, a85\n\t" \
  "v_mul_f32_e32 v253, v253, v38\n\t" \
  "v_accvgpr_write_b32 a85, v253\n\t" \
  "v_accvgpr_read_b32 v254, a86\n\t" \
  "v_mul_f32_e32 v254, v254, v38\n\t" \
  "v_accvgpr_write_b32 a86, v254\n\t" \
  "v_accvgpr_read_b32 v255, a87\n\t" \
  "v_mul_f32_e32 v255, v255, v38\n\t" \
  "v_accvgpr_write_b32 a87, v255\n\t" \
  "v_accvgpr_read_b32 v252, a92\n\t" \
  "v_mul_f32_e32 v252, v252, v38\n\t" \
  "v_accvgpr_write_b32 a92, v252\n\t" \
  "v_accvgpr_read_b32 v253, a93\n\t" \
  "v_mul_f32_e32 v253, v253, v38\n\t" \
  "v_accvgpr_write_b32 a93, v253\n\t" \
  "v_accvgpr_read_b32 v254, a94\n\t" \
  "v_mul_f32_e32 v254, v254, v38\n\t" \
  "v_accvgpr_write_b32 a94, v254\n\t" \
  "v_accvgpr_read_b32 v255, a95\n\t" \
  "v_mul_f32_e32 v255, v255, v38\n\t" \
  "v_accvgpr_write_b32 a95, v255\n\t" \
  "v_accvgpr_read_b32 v252, a100\n\t" \
  "v_mul_f32_e32 v252, v252, v38\n\t" \
  "v_accvgpr_write_b32 a100, v252\n\t" \
  "v_accvgpr_read_b32 v253, a101\n\t" \
  "v_mul_f32_e32 v253, v253, v38\n\t" \
  "v_accvgpr_write_b32 a101, v253\n\t" \
  "v_accvgpr_read_b32 v254, a102\n\t" \
  "v_mul_f32_e32 v254, v254, v38\n\t" \
  "v_accvgpr_write_b32 a102, v254\n\t" \
  "v_accvgpr_read_b32 v255, a103\n\t" \
  "v_mul_f32_e32 v255, v255, v38\n\t" \
  "v_accvgpr_write_b32 a103, v255\n\t" \
  "v_accvgpr_read_b32 v252, a108\n\t" \
  "v_mul_f32_e32 v252, v252, v38\n\t" \
  "v_accvgpr_write_b32 a108, v252\n\t" \
  "v_accvgpr_read_b32 v253, a109\n\t" \
  "v_mul_f32_e32 v253, v253, v38\n\t" \
  "v_accvgpr_write_b32 a109, v253\n\t" \
  "v_accvgpr_read_b32 v254, a110\n\t" \
  "v_mul_f32_e32 v254, v254, v38\n\t" \
  "v_accvgpr_write_b32 a110, v254\n\t" \
  "v_accvgpr_read_b32 v255, a111\n\t" \
  "v_mul_f32_e32 v255, v255, v38\n\t" \
  "v_accvgpr_write_b32 a111, v255\n\t" \
  "v_accvgpr_read_b32 v252, a116\n\t" \
  "v_mul_f32_e32 v252, v252, v38\n\t" \
  "v_accvgpr_write_b32 a116, v252\n\t" \
  "v_accvgpr_read_b32 v253, a117\n\t" \
  "v_mul_f32_e32 v253, v253, v38\n\t" \
  "v_accvgpr_write_b32 a117, v253\n\t" \
  "v_accvgpr_read_b32 v254, a118\n\t" \
  "v_mul_f32_e32 v254, v254, v38\n\t" \
  "v_accvgpr_write_b32 a118, v254\n\t" \
  "v_accvgpr_read_b32 v255, a119\n\t" \
  "v_mul_f32_e32 v255, v255, v38\n\t" \
  "v_accvgpr_write_b32 a119, v255\n\t" \
  "v_accvgpr_read_b32 v252, a124\n\t" \
  "v_mul_f32_e32 v252, v252, v38\n\t" \
  "v_accvgpr_write_b32 a124, v252\n\t" \
  "v_accvgpr_read_b32 v253, a125\n\t" \
  "v_mul_f32_e32 v253, v253, v38\n\t" \
  "v_accvgpr_write_b32 a125, v253\n\t" \
  "v_accvgpr_read_b32 v254, a126\n\t" \
  "v_mul_f32_e32 v254, v254, v38\n\t" \
  "v_accvgpr_write_b32 a126, v254\n\t" \
  "v_accvgpr_read_b32 v255, a127\n\t" \
  "v_mul_f32_e32 v255, v255, v38\n\t" \
  "v_accvgpr_write_b32 a127, v255\n\t" \
  "v_exp_f32_e32 v252, v196\n\t" \
  "v_exp_f32_e32 v253, v197\n\t" \
  "v_add_f32_e32 v249, v251, v252\n\t" \
  "v_add_f32_e32 v249, v249, v253\n\t" \
  "v_cvt_pk_bf16_f32 v228, v252, v253\n\t" \
  "v_exp_f32_e32 v254, v198\n\t" \
  "v_exp_f32_e32 v255, v199\n\t" \
  "v_add_f32_e32 v249, v249, v254\n\t" \
  "v_add_f32_e32 v249, v249, v255\n\t" \
  "v_cvt_pk_bf16_f32 v229, v254, v255\n\t" \
  "v_exp_f32_e32 v252, v204\n\t" \
  "v_exp_f32_e32 v253, v205\n\t" \
  "v_add_f32_e32 v249, v249, v252\n\t" \
  "v_add_f32_e32 v249, v249, v253\n\t" \
  "v_cvt_pk_bf16_f32 v230, v252, v253\n\t" \
  "v_exp_f32_e32 v254, v206\n\t" \
  "v_exp_f32_e32 v255, v207\n\t" \
  "v_add_f32_e32 v249, v249, v254\n\t" \
  "v_add_f32_e32 v249, v249, v255\n\t" \
  "v_cvt_pk_bf16_f32 v231, v254, v255\n\t" \
  "s_nop 1\n\t" \
  "s_cmp_eq_u32 s42, 0\n\t" \
  "s_cbranch_scc1 MK_R0_0_%=\n\t" \
  "s_cmp_eq_u32 s42, 1\n\t" \
  "s_cbranch_scc1 MK_R0_1_%=\n\t" \
  "s_cmp_eq_u32 s42, 2\n\t" \
  "s_cbranch_scc1 MK_R0_2_%=\n\t" \
  "s_cmp_eq_u32 s42, 3\n\t" \
  "s_cbranch_scc1 MK_R0_3_%=\n\t" \
  "s_branch MK_R0_4_%=\n\t" \
  "MK_SLOW1_%=:\n\t" \
  "s_nop 15\n\t" \
  "v_max3_f32 v38, v208, v209, v210\n\t" \
  "v_max3_f32 v38, v38, v211, v216\n\t" \
  "v_max3_f32 v38, v38, v217, v218\n\t" \
  "v_max_f32_e32 v38, v38, v219\n\t" \
  "v_mov_b32_e32 v252, v38\n\t" \
  "s_nop 1\n\t" \
  "v_permlane16_swap_b32_e32 v38, v252\n\t" \
  "s_nop 0\n\t" \
  "v_max_f32_e32 v38, v38, v252\n\t" \
  "v_mov_b32_e32 v252, v38\n\t" \
  "s_nop 1\n\t" \
  "v_permlane32_swap_b32_e32 v38, v252\n\t" \
  "s_nop 0\n\t" \
  "v_max_f32_e32 v38, v38, v252\n\t" \
  "v_mov_b32_e32 v253, v248\n\t" \
  "v_mov_b32_e32 v252, v253\n\t" \
  "s_nop 1\n\t" \
  "v_permlane16_swap_b32_e32 v253, v252\n\t" \
  "s_nop 0\n\t" \
  "v_add_f32_e32 v253, v253, v252\n\t" \
  "v_mov_b32_e32 v252, v253\n\t" \
  "s_nop 1\n\t" \
  "v_permlane32_swap_b32_e32 v253, v252\n\t" \
  "s_nop 0\n\t" \
  "v_add_f32_e32 v253, v253, v252\n\t" \
  "v_log_f32_e32 v253, v253\n\t" \
  "s_nop 0\n\t" \
  "v_max3_f32 v38, v38, v253, 0\n\t" \
  "v_sub_f32_e32 v192, v192, v38\n\t" \
  "v_sub_f32_e32 v193, v193, v38\n\t" \
  "v_sub_f32_e32 v194, v194, v38\n\t" \
  "v_sub_f32_e32 v195, v195, v38\n\t" \
  "v_sub_f32_e32 v200, v200, v38\n\t" \
  "v_sub_f32_e32 v201, v201, v38\n\t" \
  "v_sub_f32_e32 v202, v202, v38\n\t" \
  "v_sub_f32_e32 v203, v203, v38\n\t" \
  "v_sub_f32_e32 v208, v208, v38\n\t" \
  "v_sub_f32_e32 v209, v209, v38\n\t" \
  "v_sub_f32_e32 v210, v210, v38\n\t" \
  "v_sub_f32_e32 v211, v211, v38\n\t" \
  "v_sub_f32_e32 v216, v216, v38\n\t" \
  "v_sub_f32_e32 v217, v217, v38\n\t" \
  "v_sub_f32_e32 v218, v218, v38\n\t" \
  "v_sub_f32_e32 v219, v219, v38\n\t" \
  "v_sub_f32_e32 v240, v240, v38\n\t" \
  "v_sub_f32_e32 v241, v241, v38\n\t" \
  "v_sub_f32_e32 v242, v242, v38\n\t" \
  "v_sub_f32_e32 v243, v243, v38\n\t" \
  "v_exp_f32_e64 v38, -v38\n\t" \
  "s_nop 0\n\t" \
  "v_mul_f32_e32 v248, v248, v38\n\t" \
  "v_accvgpr_read_b32 v252, a0\n\t" \
  "v_mul_f32_e32 v252, v252, v38\n\t" \
  "v_accvgpr_write_b32 a0, v252\n\t" \
  "v_accvgpr_read_b32 v253, a1\n\t" \
  "v_mul_f32_e32 v253, v253, v38\n\t" \
  "v_accvgpr_write_b32 a1, v253\n\t" \
  "v_accvgpr_read_b32 v254, a2\n\t" \
  "v_mul_f32_e32 v254, v254, v38\n\t" \
  "v_accvgpr_write_b32 a2, v254\n\t" \
  "v_accvgpr_read_b32 v255, a3\n\t" \
  "v_mul_f32_e32 v255, v255, v38\n\t" \
  "v_accvgpr_write_b32 a3, v255\n\t" \
  "v_accvgpr_read_b32 v252, a8\n\t" \
  "v_mul_f32_e32 v252, v252, v38\n\t" \
  "v_accvgpr_write_b32 a8, v252\n\t" \
  "v_accvgpr_read_b32 v253, a9\n\t" \
  "v_mul_f32_e32 v253, v253, v38\n\t" \
  "v_accvgpr_write_b32 a9, v253\n\t" \
  "v_accvgpr_read_b32 v254, a10\n\t" \
  "v_mul_f32_e32 v254, v254, v38\n\t" \
  "v_accvgpr_write_b32 a10, v254\n\t" \
  "v_accvgpr_read_b32 v255, a11\n\t" \
  "v_mul_f32_e32 v255, v255, v38\n\t" \
  "v_accvgpr_write_b32 a11, v255\n\t" \
  "v_accvgpr_read_b32 v252, a16\n\t" \
  "v_mul_f32_e32 v252, v252, v38\n\t" \
  "v_accvgpr_write_b32 a16, v252\n\t" \
  "v_accvgpr_read_b32 v253, a17\n\t" \
  "v_mul_f32_e32 v253, v253, v38\n\t" \
  "v_accvgpr_write_b32 a17, v253\n\t" \
  "v_accvgpr_read_b32 v254, a18\n\t" \
  "v_mul_f32_e32 v254, v254, v38\n\t" \
  "v_accvgpr_write_b32 a18, v254\n\t" \
  "v_accvgpr_read_b32 v255, a19\n\t" \
  "v_mul_f32_e32 v255, v255, v38\n\t" \
  "v_accvgpr_write_b32 a19, v255\n\t" \
  "v_accvgpr_read_b32 v252, a24\n\t" \
  "v_mul_f32_e32 v252, v252, v38\n\t" \
  "v_accvgpr_write_b32 a24, v252\n\t" \
  "v_accvgpr_read_b32 v253, a25\n\t" \
  "v_mul_f32_e32 v253, v253, v38\n\t" \
  "v_accvgpr_write_b32 a25, v253\n\t" \
  "v_accvgpr_read_b32 v254, a26\n\t" \
  "v_mul_f32_e32 v254, v254, v38\n\t" \
  "v_accvgpr_write_b32 a26, v254\n\t" \
  "v_accvgpr_read_b32 v255, a27\n\t" \
  "v_mul_f32_e32 v255, v255, v38\n\t" \
  "v_accvgpr_write_b32 a27, v255\n\t" \
  "v_accvgpr_read_b32 v252, a32\n\t" \
  "v_mul_f32_e32 v252, v252, v38\n\t" \
  "v_accvgpr_write_b32 a32, v252\n\t" \
  "v_accvgpr_read_b32 v253, a33\n\t" \
  "v_mul_f32_e32 v253, v253, v38\n\t" \
  "v_accvgpr_write_b32 a33, v253\n\t" \
  "v_accvgpr_read_b32 v254, a34\n\t" \
  "v_mul_f32_e32 v254, v254, v38\n\t" \
  "v_accvgpr_write_b32 a34, v254\n\t" \
  "v_accvgpr_read_b32 v255, a35\n\t" \
  "v_mul_f32_e32 v255, v255, v38\n\t" \
  "v_accvgpr_write_b32 a35, v255\n\t" \
  "v_accvgpr_read_b32 v252, a40\n\t" \
  "v_mul_f32_e32 v252, v252, v38\n\t" \
  "v_accvgpr_write_b32 a40, v252\n\t" \
  "v_accvgpr_read_b32 v253, a41\n\t" \
  "v_mul_f32_e32 v253, v253, v38\n\t" \
  "v_accvgpr_write_b32 a41, v253\n\t" \
  "v_accvgpr_read_b32 v254, a42\n\t" \
  "v_mul_f32_e32 v254, v254, v38\n\t" \
  "v_accvgpr_write_b32 a42, v254\n\t" \
  "v_accvgpr_read_b32 v255, a43\n\t" \
  "v_mul_f32_e32 v255, v255, v38\n\t" \
  "v_accvgpr_write_b32 a43, v255\n\t" \
  "v_accvgpr_read_b32 v252, a48\n\t" \
  "v_mul_f32_e32 v252, v252, v38\n\t" \
  "v_accvgpr_write_b32 a48, v252\n\t" \
  "v_accvgpr_read_b32 v253, a49\n\t" \
  "v_mul_f32_e32 v253, v253, v38\n\t" \
  "v_accvgpr_write_b32 a49, v253\n\t" \
  "v_accvgpr_read_b32 v254, a50\n\t" \
  "v_mul_f32_e32 v254, v254, v38\n\t" \
  "v_accvgpr_write_b32 a50, v254\n\t" \
  "v_accvgpr_read_b32 v255, a51\n\t" \
  "v_mul_f32_e32 v255, v255, v38\n\t" \
  "v_accvgpr_write_b32 a51, v255\n\t" \
  "v_accvgpr_read_b32 v252, a56\n\t" \
  "v_mul_f32_e32 v252, v252, v38\n\t" \
  "v_accvgpr_write_b32 a56, v252\n\t" \
  "v_accvgpr_read_b32 v253, a57\n\t" \
  "v_mul_f32_e32 v253, v253, v38\n\t" \
  "v_accvgpr_write_b32 a57, v253\n\t" \
  "v_accvgpr_read_b32 v254, a58\n\t" \
  "v_mul_f32_e32 v254, v254, v38\n\t" \
  "v_accvgpr_write_b32 a58, v254\n\t" \
  "v_accvgpr_read_b32 v255, a59\n\t" \
  "v_mul_f32_e32 v255, v255, v38\n\t" \
  "v_accvgpr_write_b32 a59, v255\n\t" \
  "v_accvgpr_read_b32 v252, a64\n\t" \
  "v_mul_f32_e32 v252, v252, v38\n\t" \
  "v_accvgpr_write_b32 a64, v252\n\t" \
  "v_accvgpr_read_b32 v253, a65\n\t" \
  "v_mul_f32_e32 v253, v253, v38\n\t" \
  "v_accvgpr_write_b32 a65, v253\n\t" \
  "v_accvgpr_read_b32 v254, a66\n\t" \
  "v_mul_f32_e32 v254, v254, v38\n\t" \
  "v_accvgpr_write_b32 a66, v254\n\t" \
  "v_accvgpr_read_b32 v255, a67\n\t" \
  "v_mul_f32_e32 v255, v255, v38\n\t" \
  "v_accvgpr_write_b32 a67, v255\n\t" \
  "v_accvgpr_read_b32 v252, a72\n\t" \
  "v_mul_f32_e32 v252, v252, v38\n\t" \
  "v_accvgpr_write_b32 a72, v252\n\t" \
  "v_accvgpr_read_b32 v253, a73\n\t" \
  "v_mul_f32_e32 v253, v253, v38\n\t" \
  "v_accvgpr_write_b32 a73, v253\n\t" \
  "v_accvgpr_read_b32 v254, a74\n\t" \
  "v_mul_f32_e32 v254, v254, v38\n\t" \
  "v_accvgpr_write_b32 a74, v254\n\t" \
  "v_accvgpr_read_b32 v255, a75\n\t" \
  "v_mul_f32_e32 v255, v255, v38\n\t" \
  "v_accvgpr_write_b32 a75, v255\n\t" \
  "v_accvgpr_read_b32 v252, a80\n\t" \
  "v_mul_f32_e32 v252, v252, v38\n\t" \
  "v_accvgpr_write_b32 a80, v252\n\t" \
  "v_accvgpr_read_b32 v253, a81\n\t" \
  "v_mul_f32_e32 v253, v253, v38\n\t" \
  "v_accvgpr_write_b32 a81, v253\n\t" \
  "v_accvgpr_read_b32 v254, a82\n\t" \
  "v_mul_f32_e32 v254, v254, v38\n\t" \
  "v_accvgpr_write_b32 a82, v254\n\t" \
  "v_accvgpr_read_b32 v255, a83\n\t" \
  "v_mul_f32_e32 v255, v255, v38\n\t" \
  "v_accvgpr_write_b32 a83, v255\n\t" \
  "v_accvgpr_read_b32 v252, a88\n\t" \
  "v_mul_f32_e32 v252, v252, v38\n\t" \
  "v_accvgpr_write_b32 a88, v252\n\t" \
  "v_accvgpr_read_b32 v253, a89\n\t" \
  "v_mul_f32_e32 v253, v253, v38\n\t" \
  "v_accvgpr_write_b32 a89, v253\n\t" \
  "v_accvgpr_read_b32 v254, a90\n\t" \
  "v_mul_f32_e32 v254, v254, v38\n\t" \
  "v_accvgpr_write_b32 a90, v254\n\t" \
  "v_accvgpr_read_b32 v255, a91\n\t" \
  "v_mul_f32_e32 v255, v255, v38\n\t" \
  "v_accvgpr_write_b32 a91, v255\n\t" \
  "v_accvgpr_read_b32 v252, a96\n\t" \
  "v_mul_f32_e32 v252, v252, v38\n\t" \
  "v_accvgpr_write_b32 a96, v252\n\t" \
  "v_accvgpr_read_b32 v253, a97\n\t" \
  "v_mul_f32_e32 v253, v253, v38\n\t" \
  "v_accvgpr_write_b32 a97, v253\n\t" \
  "v_accvgpr_read_b32 v254, a98\n\t" \
  "v_mul_f32_e32 v254, v254, v38\n\t" \
  "v_accvgpr_write_b32 a98, v254\n\t" \
  "v_accvgpr_read_b32 v255, a99\n\t" \
  "v_mul_f32_e32 v255, v255, v38\n\t" \
  "v_accvgpr_write_b32 a99, v255\n\t" \
  "v_accvgpr_read_b32 v252, a104\n\t" \
  "v_mul_f32_e32 v252, v252, v38\n\t" \
  "v_accvgpr_write_b32 a104, v252\n\t" \
  "v_accvgpr_read_b32 v253, a105\n\t" \
  "v_mul_f32_e32 v253, v253, v38\n\t" \
  "v_accvgpr_write_b32 a105, v253\n\t" \
  "v_accvgpr_read_b32 v254, a106\n\t" \
  "v_mul_f32_e32 v254, v254, v38\n\t" \
  "v_accvgpr_write_b32 a106, v254\n\t" \
  "v_accvgpr_read_b32 v255, a107\n\t" \
  "v_mul_f32_e32 v255, v255, v38\n\t" \
  "v_accvgpr_write_b32 a107, v255\n\t" \
  "v_accvgpr_read_b32 v252, a112\n\t" \
  "v_mul_f32_e32 v252, v252, v38\n\t" \
  "v_accvgpr_write_b32 a112, v252\n\t" \
  "v_accvgpr_read_b32 v253, a113\n\t" \
  "v_mul_f32_e32 v253, v253, v38\n\t" \
  "v_accvgpr_write_b32 a113, v253\n\t" \
  "v_accvgpr_read_b32 v254, a114\n\t" \
  "v_mul_f32_e32 v254, v254, v38\n\t" \
  "v_accvgpr_write_b32 a114, v254\n\t" \
  "v_accvgpr_read_b32 v255, a115\n\t" \
  "v_mul_f32_e32 v255, v255, v38\n\t" \
  "v_accvgpr_write_b32 a115, v255\n\t" \
  "v_accvgpr_read_b32 v252, a120\n\t" \
  "v_mul_f32_e32 v252, v252, v38\n\t" \
  "v_accvgpr_write_b32 a120, v252\n\t" \
  "v_accvgpr_read_b32 v253, a121\n\t" \
  "v_mul_f32_e32 v253, v253, v38\n\t" \
  "v_accvgpr_write_b32 a121, v253\n\t" \
  "v_accvgpr_read_b32 v254, a122\n\t" \
  "v_mul_f32_e32 v254, v254, v38\n\t" \
  "v_accvgpr_write_b32 a122, v254\n\t" \
  "v_accvgpr_read_b32 v255, a123\n\t" \
  "v_mul_f32_e32 v255, v255, v38\n\t" \
  "v_accvgpr_write_b32 a123, v255\n\t" \
  "v_exp_f32_e32 v252, v208\n\t" \
  "v_exp_f32_e32 v253, v209\n\t" \
  "v_add_f32_e32 v250, v248, v252\n\t" \
  "v_add_f32_e32 v250, v250, v253\n\t" \
  "v_cvt_pk_bf16_f32 v232, v252, v253\n\t" \
  "v_exp_f32_e32 v254, v210\n\t" \
  "v_exp_f32_e32 v255, v211\n\t" \
  "v_add_f32_e32 v250, v250, v254\n\t" \
  "v_add_f32_e32 v250, v250, v255\n\t" \
  "v_cvt_pk_bf16_f32 v233, v254, v255\n\t" \
  "v_exp_f32_e32 v252, v216\n\t" \
  "v_exp_f32_e32 v253, v217\n\t" \
  "v_add_f32_e32 v250, v250, v252\n\t" \
  "v_add_f32_e32 v250, v250, v253\n\t" \
  "v_cvt_pk_bf16_f32 v234, v252, v253\n\t" \
  "v_exp_f32_e32 v254, v218\n\t" \
  "v_exp_f32_e32 v255, v219\n\t" \
  "v_add_f32_e32 v250, v250, v254\n\t" \
  "v_add_f32_e32 v250, v250, v255\n\t" \
  "v_cvt_pk_bf16_f32 v235, v254, v255\n\t" \
  "MK_SLOW1B_%=:\n\t" \
  "s_nop 15\n\t" \
  "v_max3_f32 v38, v212, v213, v214\n\t" \
  "v_max3_f32 v38, v38, v215, v220\n\t" \
  "v_max3_f32 v38, v38, v221, v222\n\t" \
  "v_max_f32_e32 v38, v38, v223\n\t" \
  "v_mov_b32_e32 v252, v38\n\t" \
  "s_nop 1\n\t" \
  "v_permlane16_swap_b32_e32 v38, v252\n\t" \
  "s_nop 0\n\t" \
  "v_max_f32_e32 v38, v38, v252\n\t" \
  "v_mov_b32_e32 v252, v38\n\t" \
  "s_nop 1\n\t" \
  "v_permlane32_swap_b32_e32 v38, v252\n\t" \
  "s_nop 0\n\t" \
  "v_max_f32_e32 v38, v38, v252\n\t" \
  "v_mov_b32_e32 v253, v249\n\t" \
  "v_mov_b32_e32 v252, v253\n\t" \
  "s_nop 1\n\t" \
  "v_permlane16_swap_b32_e32 v253, v252\n\t" \
  "s_nop 0\n\t" \
  "v_add_f32_e32 v253, v253, v252\n\t" \
  "v_mov_b32_e32 v252, v253\n\t" \
  "s_nop 1\n\t" \
  "v_permlane32_swap_b32_e32 v253, v252\n\t" \
  "s_nop 0\n\t" \
  "v_add_f32_e32 v253, v253, v252\n\t" \
  "v_log_f32_e32 v253, v253\n\t" \
  "s_nop 0\n\t" \
  "v_max3_f32 v38, v38, v253, 0\n\t" \
  "v_sub_f32_e32 v196, v196, v38\n\t" \
  "v_sub_f32_e32 v197, v197, v38\n\t" \
  "v_sub_f32_e32 v198, v198, v38\n\t" \
  "v_sub_f32_e32 v199, v199, v38\n\t" \
  "v_sub_f32_e32 v204, v204, v38\n\t" \
  "v_sub_f32_e32 v205, v205, v38\n\t" \
  "v_sub_f32_e32 v206, v206, v38\n\t" \
  "v_sub_f32_e32 v207, v207, v38\n\t" \
  "v_sub_f32_e32 v212, v212, v38\n\t" \
  "v_sub_f32_e32 v213, v213, v38\n\t" \
  "v_sub_f32_e32 v214, v214, v38\n\t" \
  "v_sub_f32_e32 v215, v215, v38\n\t" \
  "v_sub_f32_e32 v220, v220, v38\n\t" \
  "v_sub_f32_e32 v221, v221, v38\n\t" \
  "v_sub_f32_e32 v222, v222, v38\n\t" \
  "v_sub_f32_e32 v223, v223, v38\n\t" \
  "v_sub_f32_e32 v244, v244, v38\n\t" \
  "v_sub_f32_e32 v245, v245, v38\n\t" \
  "v_sub_f32_e32 v246, v246, v38\n\t" \
  "v_sub_f32_e32 v247, v247, v38\n\t" \
  "v_exp_f32_e64 v38, -v38\n\t" \
  "s_nop 0\n\t" \
  "v_mul_f32_e32 v249, v249, v38\n\t" \
  "v_accvgpr_read_b32 v252, a4\n\t" \
  "v_mul_f32_e32 v252, v252, v38\n\t" \
  "v_accvgpr_write_b32 a4, v252\n\t" \
  "v_accvgpr_read_b32 v253, a5\n\t" \
  "v_mul_f32_e32 v253, v253, v38\n\t" \
  "v_accvgpr_write_b32 a5, v253\n\t" \
  "v_accvgpr_read_b32 v254, a6\n\t" \
  "v_mul_f32_e32 v254, v254, v38\n\t" \
  "v_accvgpr_write_b32 a6, v254\n\t" \
  "v_accvgpr_read_b32 v255, a7\n\t" \
  "v_mul_f32_e32 v255, v255, v38\n\t" \
  "v_accvgpr_write_b32 a7, v255\n\t" \
  "v_accvgpr_read_b32 v252, a12\n\t" \
  "v_mul_f32_e32 v252, v252, v38\n\t" \
  "v_accvgpr_write_b32 a12, v252\n\t" \
  "v_accvgpr_read_b32 v253, a13\n\t" \
  "v_mul_f32_e32 v253, v253, v38\n\t" \
  "v_accvgpr_write_b32 a13, v253\n\t" \
  "v_accvgpr_read_b32 v254, a14\n\t" \
  "v_mul_f32_e32 v254, v254, v38\n\t" \
  "v_accvgpr_write_b32 a14, v254\n\t" \
  "v_accvgpr_read_b32 v255, a15\n\t" \
  "v_mul_f32_e32 v255, v255, v38\n\t" \
  "v_accvgpr_write_b32 a15, v255\n\t" \
  "v_accvgpr_read_b32 v252, a20\n\t" \
  "v_mul_f32_e32 v252, v252, v38\n\t" \
  "v_accvgpr_write_b32 a20, v252\n\t" \
  "v_accvgpr_read_b32 v253, a21\n\t" \
  "v_mul_f32_e32 v253, v253, v38\n\t" \
  "v_accvgpr_write_b32 a21, v253\n\t" \
  "v_accvgpr_read_b32 v254, a22\n\t" \
  "v_mul_f32_e32 v254, v254, v38\n\t" \
  "v_accvgpr_write_b32 a22, v254\n\t" \
  "v_accvgpr_read_b32 v255, a23\n\t" \
  "v_mul_f32_e32 v255, v255, v38\n\t" \
  "v_accvgpr_write_b32 a23, v255\n\t" \
  "v_accvgpr_read_b32 v252, a28\n\t" \
  "v_mul_f32_e32 v252, v252, v38\n\t" \
  "v_accvgpr_write_b32 a28, v252\n\t" \
  "v_accvgpr_read_b32 v253, a29\n\t" \
  "v_mul_f32_e32 v253, v253, v38\n\t" \
  "v_accvgpr_write_b32 a29, v253\n\t" \
  "v_accvgpr_read_b32 v254, a30\n\t" \
  "v_mul_f32_e32 v254, v254, v38\n\t" \
  "v_accvgpr_write_b32 a30, v254\n\t" \
  "v_accvgpr_read_b32 v255, a31\n\t" \
  "v_mul_f32_e32 v255, v255, v38\n\t" \
  "v_accvgpr_write_b32 a31, v255\n\t" \
  "v_accvgpr_read_b32 v252, a36\n\t" \
  "v_mul_f32_e32 v252, v252, v38\n\t" \
  "v_accvgpr_write_b32 a36, v252\n\t" \
  "v_accvgpr_read_b32 v253, a37\n\t" \
  "v_mul_f32_e32 v253, v253, v38\n\t" \
  "v_accvgpr_write_b32 a37, v253\n\t" \
  "v_accvgpr_read_b32 v254, a38\n\t" \
  "v_mul_f32_e32 v254, v254, v38\n\t" \
  "v_accvgpr_write_b32 a38, v254\n\t" \
  "v_accvgpr_read_b32 v255, a39\n\t" \
  "v_mul_f32_e32 v255, v255, v38\n\t" \
  "v_accvgpr_write_b32 a39, v255\n\t" \
  "v_accvgpr_read_b32 v252, a44\n\t" \
  "v_mul_f32_e32 v252, v252, v38\n\t" \
  "v_accvgpr_write_b32 a44, v252\n\t" \
  "v_accvgpr_read_b32 v253, a45\n\t" \
  "v_mul_f32_e32 v253, v253, v38\n\t" \
  "v_accvgpr_write_b32 a45, v253\n\t" \
  "v_accvgpr_read_b32 v254, a46\n\t" \
  "v_mul_f32_e32 v254, v254, v38\n\t" \
  "v_accvgpr_write_b32 a46, v254\n\t" \
  "v_accvgpr_read_b32 v255, a47\n\t" \
  "v_mul_f32_e32 v255, v255, v38\n\t" \
  "v_accvgpr_write_b32 a47, v255\n\t" \
  "v_accvgpr_read_b32 v252, a52\n\t" \
  "v_mul_f32_e32 v252, v252, v38\n\t" \
  "v_accvgpr_write_b32 a52, v252\n\t" \
  "v_accvgpr_read_b32 v253, a53\n\t" \
  "v_mul_f32_e32 v253, v253, v38\n\t" \
  "v_accvgpr_write_b32 a53, v253\n\t" \
  "v_accvgpr_read_b32 v254, a54\n\t" \
  "v_mul_f32_e32 v254, v254, v38\n\t" \
  "v_accvgpr_write_b32 a54, v254\n\t" \
  "v_accvgpr_read_b32 v255, a55\n\t" \
  "v_mul_f32_e32 v255, v255, v38\n\t" \
  "v_accvgpr_write_b32 a55, v255\n\t" \
  "v_accvgpr_read_b32 v252, a60\n\t" \
  "v_mul_f32_e32 v252, v252, v38\n\t" \
  "v_accvgpr_write_b32 a60, v252\n\t" \
  "v_accvgpr_read_b32 v253, a61\n\t" \
  "v_mul_f32_e32 v253, v253, v38\n\t" \
  "v_accvgpr_write_b32 a61, v253\n\t" \
  "v_accvgpr_read_b32 v254, a62\n\t" \
  "v_mul_f32_e32 v254, v254, v38\n\t" \
  "v_accvgpr_write_b32 a62, v254\n\t" \
  "v_accvgpr_read_b32 v255, a63\n\t" \
  "v_mul_f32_e32 v255, v255, v38\n\t" \
  "v_accvgpr_write_b32 a63, v255\n\t" \
  "v_accvgpr_read_b32 v252, a68\n\t" \
  "v_mul_f32_e32 v252, v252, v38\n\t" \
  "v_accvgpr_write_b32 a68, v252\n\t" \
  "v_accvgpr_read_b32 v253, a69\n\t" \
  "v_mul_f32_e32 v253, v253, v38\n\t" \
  "v_accvgpr_write_b32 a69, v253\n\t" \
  "v_accvgpr_read_b32 v254, a70\n\t" \
  "v_mul_f32_e32 v254, v254, v38\n\t" \
  "v_accvgpr_write_b32 a70, v254\n\t" \
  "v_accvgpr_read_b32 v255, a71\n\t" \
  "v_mul_f32_e32 v255, v255, v38\n\t" \
  "v_accvgpr_write_b32 a71, v255\n\t" \
  "v_accvgpr_read_b32 v252, a76\n\t" \
  "v_mul_f32_e32 v252, v252, v38\n\t" \
  "v_accvgpr_write_b32 a76, v252\n\t" \
  "v_accvgpr_read_b32 v253, a77\n\t" \
  "v_mul_f32_e32 v253, v253, v38\n\t" \
  "v_accvgpr_write_b32 a77, v253\n\t" \
  "v_accvgpr_read_b32 v254, a78\n\t" \
  "v_mul_f32_e32 v254, v254, v38\n\t" \
  "v_accvgpr_write_b32 a78, v254\n\t" \
  "v_accvgpr_read_b32 v255, a79\n\t" \
  "v_mul_f32_e32 v255, v255, v38\n\t" \
  "v_accvgpr_write_b32 a79, v255\n\t" \
  "v_accvgpr_read_b32 v252, a84\n\t" \
  "v_mul_f32_e32 v252, v252, v38\n\t" \
  "v_accvgpr_write_b32 a84, v252\n\t" \
  "v_accvgpr_read_b32 v253, a85\n\t" \
  "v_mul_f32_e32 v253, v253, v38\n\t" \
  "v_accvgpr_write_b32 a85, v253\n\t" \
  "v_accvgpr_read_b32 v254, a86\n\t" \
  "v_mul_f32_e32 v254, v254, v38\n\t" \
  "v_accvgpr_write_b32 a86, v254\n\t" \
  "v_accvgpr_read_b32 v255, a87\n\t" \
  "v_mul_f32_e32 v255, v255, v38\n\t" \
  "v_accvgpr_write_b32 a87, v255\n\t" \
  "v_accvgpr_read_b32 v252, a92\n\t" \
  "v_mul_f32_e32 v252, v252, v38\n\t" \
  "v_accvgpr_write_b32 a92, v252\n\t" \
  "v_accvgpr_read_b32 v253, a93\n\t" \
  "v_mul_f32_e32 v253, v253, v38\n\t" \
  "v_accvgpr_write_b32 a93, v253\n\t" \
  "v_accvgpr_read_b32 v254, a94\n\t" \
  "v_mul_f32_e32 v254, v254, v38\n\t" \
  "v_accvgpr_write_b32 a94, v254\n\t" \
  "v_accvgpr_read_b32 v255, a95\n\t" \
  "v_mul_f32_e32 v255, v255, v38\n\t" \
  "v_accvgpr_write_b32 a95, v255\n\t" \
  "v_accvgpr_read_b32 v252, a100\n\t" \
  "v_mul_f32_e32 v252, v252, v38\n\t" \
  "v_accvgpr_write_b32 a100, v252\n\t" \
  "v_accvgpr_read_b32 v253, a101\n\t" \
  "v_mul_f32_e32 v253, v253, v38\n\t" \
  "v_accvgpr_write_b32 a101, v253\n\t" \
  "v_accvgpr_read_b32 v254, a102\n\t" \
  "v_mul_f32_e32 v254, v254, v38\n\t" \
  "v_accvgpr_write_b32 a102, v254\n\t" \
  "v_accvgpr_read_b32 v255, a103\n\t" \
  "v_mul_f32_e32 v255, v255, v38\n\t" \
  "v_accvgpr_write_b32 a103, v255\n\t" \
  "v_accvgpr_read_b32 v252, a108\n\t" \
  "v_mul_f32_e32 v252, v252, v38\n\t" \
  "v_accvgpr_write_b32 a108, v252\n\t" \
  "v_accvgpr_read_b32 v253, a109\n\t" \
  "v_mul_f32_e32 v253, v253, v38\n\t" \
  "v_accvgpr_write_b32 a109, v253\n\t" \
  "v_accvgpr_read_b32 v254, a110\n\t" \
  "v_mul_f32_e32 v254, v254, v38\n\t" \
  "v_accvgpr_write_b32 a110, v254\n\t" \
  "v_accvgpr_read_b32 v255, a111\n\t" \
  "v_mul_f32_e32 v255, v255, v38\n\t" \
  "v_accvgpr_write_b32 a111, v255\n\t" \
  "v_accvgpr_read_b32 v252, a116\n\t" \
  "v_mul_f32_e32 v252, v252, v38\n\t" \
  "v_accvgpr_write_b32 a116, v252\n\t" \
  "v_accvgpr_read_b32 v253, a117\n\t" \
  "v_mul_f32_e32 v253, v253, v38\n\t" \
  "v_accvgpr_write_b32 a117, v253\n\t" \
  "v_accvgpr_read_b32 v254, a118\n\t" \
  "v_mul_f32_e32 v254, v254, v38\n\t" \
  "v_accvgpr_write_b32 a118, v254\n\t" \
  "v_accvgpr_read_b32 v255, a119\n\t" \
  "v_mul_f32_e32 v255, v255, v38\n\t" \
  "v_accvgpr_write_b32 a119, v255\n\t" \
  "v_accvgpr_read_b32 v252, a124\n\t" \
  "v_mul_f32_e32 v252, v252, v38\n\t" \
  "v_accvgpr_write_b32 a124, v252\n\t" \
  "v_accvgpr_read_b32 v253, a125\n\t" \
  "v_mul_f32_e32 v253, v253, v38\n\t" \
  "v_accvgpr_write_b32 a125, v253\n\t" \
  "v_accvgpr_read_b32 v254, a126\n\t" \
  "v_mul_f32_e32 v254, v254, v38\n\t" \
  "v_accvgpr_write_b32 a126, v254\n\t" \
  "v_accvgpr_read_b32 v255, a127\n\t" \
  "v_mul_f32_e32 v255, v255, v38\n\t" \
  "v_accvgpr_write_b32 a127, v255\n\t" \
  "v_exp_f32_e32 v252, v212\n\t" \
  "v_exp_f32_e32 v253, v213\n\t" \
  "v_add_f32_e32 v251, v249, v252\n\t" \
  "v_add_f32_e32 v251, v251, v253\n\t" \
  "v_cvt_pk_bf16_f32 v236, v252, v253\n\t" \
  "v_exp_f32_e32 v254, v214\n\t" \
  "v_exp_f32_e32 v255, v215\n\t" \
  "v_add_f32_e32 v251, v251, v254\n\t" \
  "v_add_f32_e32 v251, v251, v255\n\t" \
  "v_cvt_pk_bf16_f32 v237, v254, v255\n\t" \
  "v_exp_f32_e32 v252, v220\n\t" \
  "v_exp_f32_e32 v253, v221\n\t" \
  "v_add_f32_e32 v251, v251, v252\n\t" \
  "v_add_f32_e32 v251, v251, v253\n\t" \
  "v_cvt_pk_bf16_f32 v238, v252, v253\n\t" \
  "v_exp_f32_e32 v254, v222\n\t" \
  "v_exp_f32_e32 v255, v223\n\t" \
  "v_add_f32_e32 v251, v251, v254\n\t" \
  "v_add_f32_e32 v251, v251, v255\n\t" \
  "v_cvt_pk_bf16_f32 v239, v254, v255\n\t" \
  "s_nop 1\n\t" \
  "s_cmp_eq_u32 s42, 0\n\t" \
  "s_cbranch_scc1 MK_R1_0_%=\n\t" \
  "s_cmp_eq_u32 s42, 1\n\t" \
  "s_cbranch_scc1 MK_R1_1_%=\n\t" \
  "s_cmp_eq_u32 s42, 2\n\t" \
  "s_cbranch_scc1 MK_R1_2_%=\n\t" \
  "s_cmp_eq_u32 s42, 3\n\t" \
  "s_cbranch_scc1 MK_R1_3_%=\n\t" \
  "s_branch MK_R1_4_%=\n\t" \
  "MK_END_%=:\n\t"
constexpr int NA = 36;
constexpr int NA_UNITS = NA;
DI unsigned plo(const void* p) { return (unsigned)(unsigned long long)p; }
DI unsigned phi(const void* p) { return (unsigned)((unsigned long long)p >> 32) & 0xffffu; }
constexpr int HKD = 5;
constexpr int GRP = 1;
constexpr int NMAJ = 12;
constexpr int wo_pos(int q4, int st, int nt) {
  const int j = nt >= 4 ? nt - 4 : nt == q4 ? 6 : 3 + (nt < q4 ? nt : nt - 1);
  return st < 14 ? st * 7 + j : j == 6 ? 290 + st - 14 : st < 46 - NMAJ ? 98 + (st - 14) * 6 + j : 98 + 6 * (32 - NMAJ) + j * NMAJ + st - (46 - NMAJ);
}
constexpr int hpos(int h) { return (h & ~3) + ((h & 1) << 1) + ((h >> 1) & 1); }
struct Tab { unsigned v[4][64]; };
constexpr Tab make_tab(int b, int wid) {
  Tab T{};
  const int xcd = b & 7, jj = b >> 3;
  const int rb1 = jj & 7, cq = jj >> 3, head1 = xcd + 8 * (cq & 1), wk = wid & 1, wd = wid >> 1; const bool isv = cq >= 2;
  const int head = xcd + 8 * (jj & 1), role = b >> 4, pi = role & 7; const bool isB = role < 8;
  const int nb1 = 4 * (16 - pi) > NA_UNITS ? 4 * (16 - pi) - NA_UNITS : 0;
  const int nseg = (isB && nb1 > 0) ? 2 : 1;
  const int blk0 = (isB && nb1 == 0) ? pi : 15 - pi, u00 = isB ? 0 : nb1, n0 = isB ? (nb1 > 0 ? nb1 : 4 * (pi + 1)) : 4 * (16 - pi) - nb1;
  const int blkf = isB ? pi : 15 - pi;
  const int kq = xcd & 3, rb3 = GRP ? (jj >> 4) * 8 + (xcd >> 2) * 4 + (jj & 3) : (xcd >> 2) * 8 + (jj & 7), cb = GRP ? (jj >> 2) & 3 : jj >> 3;
  const bool merge = !isB && nb1 > 0;
  for (int lane = 0; lane < 64; ++lane) {
    int c = lane, u0 = u00, n = n0, hh = 4; const int len0 = 12 + n0, len1 = nseg == 2 ? 1 + 4 * (pi + 1) : 0; unsigned t0 = 0, t1 = 0;
    if (c >= len0 + len1) { c -= len0 + len1; t0 = c < 11 ? WS_WO + (kq * 4 + cb) * (322 * 1024) + c * 32768 : OOB; t1 = c < 10 ? t0 + 16384 : OOB; }
    else {
      if (c >= len0) { c -= len0 - 11; u0 = 0; n = 4 * (pi + 1); }
      if (c < 7) { t0 = WS_WQ + (head * 7 + c) * 32768; t1 = t0 + 16384; }
      else if (c < 7 + hh) { const int k = c - 7; t0 = head < 14 ? WS_W1 + head * (14 * 8192) + k * 32768 : OOB; t1 = (k == 3 || head >= 14) ? OOB : t0 + 16384; }
      else { const int k = c - 7 - hh; t0 = k == n ? OOB : WS_KF + (head * 64 + u0 + k) * 16384; t1 = k == 0 ? OOB : WS_VF + (head * 64 + u0 + k - 1) * 16384; }
    }
    T.v[0][lane] = t0; T.v[1][lane] = t1;
  }
  unsigned* p = T.v[2];
  for (int seg = 0; seg < 2; ++seg) {
    const int blk = seg ? pi : blk0, u0 = seg ? 0 : u00; const bool partial = nseg == 2 && seg == 0;
    p[8 * seg] = seg ? 4 * (pi + 1) : n0; p[8 * seg + 2] = WS_XN + (blkf * 8 + wid) * 14336;
    p[8 * seg + 4] = (partial ? (1 << 20) : blk * 128 - 32 * u0) + wid * 16; p[8 * seg + 5] = WS_AF + ((blk * 8 + wid) * AKS + hpos(head) * 8) * 1024;
    p[8 * seg + 7] = (partial ? 1 : 0) + (merge ? 2 : 0);
    { const int nn = seg ? 4 * (pi + 1) : n0, room = (nn - (partial ? 2 : 6)) / 2 - 4, lim = seg ? 1 : HKD, want = (role * 5 + (jj & 1) * 3) % (HKD + 1), d = want > lim ? lim : want;
      p[8 * seg + 1] = (seg == 0) == (n0 >= (nseg == 2 ? 10 : 14)) ? 4 + (d > room ? room : d) : 0; }
  }
  const unsigned a3 = WS_AF + (rb3 * 8 + wid * 2) * AKS * 1024, ex = WS_P3 + (rb3 * 4 + cb) * 16 * 14336;
  p[16] = wid * 4096; p[17] = merge ? PO_WAVE : 0u; p[18] = FL_PO + (head * 8 + pi) * 64; p[19] = head * 256;
  p[21] = WS_PO + ((head * 8 + pi) * 4 + wid) * PO_WAVE; p[22] = (head < 14 ? WS_AF + (blkf * 8 * AKS + 128 + head * 4) * 1024 : 0x60000000u) + wid * AKS * 1024;
  p[20] = merge ? p[21] + 17408 : 0x60000000u; p[23] = merge ? p[18] + 4 : 0x60000000u;
  p[30] = FL_PREP + b * 4; p[31] = FL_PREP; p[32] = isv ? 1 : 0;
  p[33] = WS_WQ + head * (7 * 32768); p[34] = FL_PREP2 + b * 4;
  p[35] = isv ? WS_VF + (head1 * 64 + rb1 * 8 + wk * 4) * 16384 + wd * 8192 : WS_KF + (head1 * 128 + rb1 * 16 + wk * 8) * 8192 + wd * 4096;
  p[36] = isv ? 1024 : 8192; p[37] = isv ? 16384 : 1024;
  p[40] = FL_KV + head1 * 64 + (rb1 + (isv ? 8 : 0)) * 4; p[41] = FL_KV + head * 64; p[42] = FL_CTX + (blkf * 16 + hpos(head)) * 4; p[43] = FL_PREP2;
  p[45] = a3 + (128 + 14 * kq) * 1024; p[46] = a3 + 32 * kq * 1024; p[47] = FL_CTX + rb3 * 64 + kq * 16;
  p[48] = ex + (wid * 4 + kq) * 14336 + 8192; p[49] = ex + kq * 4 * 14336 + (8 + wid) * 1024; p[50] = ((rb3 * 128 + kq * 32) * DM + cb * 112 + 64) * 2; p[57] = (cb * 112 + 64 + wd * 16) * 2;
  for (int i = 0; i < 3; ++i) { const int o = i < kq ? i : i + 1; p[24 + i] = ex + (o * 4 + kq) * 14336 + wid * 2048; p[27 + i] = ex + (kq * 4 + o) * 14336 + wid * 2048; }
  p[38] = ((rb3 * 128 + wid * 32) * DM + cb * 112 + kq * 16) * 2; p[39] = p[38] + 16 * DM * 2; p[51] = (cb * 112 + kq * 16) * 2; p[52] = (16 * wk * DM + 16 * wd) * 2;
  p[60] = b == 0 ? WS_FL : 0x60000000u; p[61] = wd ? 0 : 4096;
  unsigned* r = T.v[3];
  { const int tl = 1 + jj % 6; r[9] = (1u << tl) | (4u << tl) | (1u << 10) | (1u << 12); r[10] = tl; }
  for (int t = 0; t < 3; ++t) {
    int u = b + 256 * t; unsigned off = 0x60000000u, ld = 0, d0 = 0x60000000u;
    if (u < 448) { const int ks = u >> 5, nb = (u & 31) * 4 + wid; ld = NH * HD * 2; off = ks * 32 * ld + nb * 64; d0 = WS_WQ + ((nb >> 3) * 224 + (ks < 10 ? ks * 16 + (nb & 7) * 2 : 160 + (nb & 7) * 8 + (ks - 10) * 2)) * 1024; }
    else if (u < 644) { u -= 448; const int ks = u / 14, j = u - ks * 14; ld = FF * 2; off = ks * 32 * ld + (j * 4 + wid) * 64; d0 = WS_W1 + (j * 112 + wid * 28 + ks * 2) * 1024; }
    r[3 * t] = off; r[3 * t + 1] = ld; r[3 * t + 2] = d0;
  }
  for (int k = 0; k < 3; ++k) {
    int u = b + 256 * k; const bool ok = u < 644; if (!ok) u = 0;
    const bool w2m = u >= 448; if (w2m) u -= 448;
    const int kk = u / 7, nb = (u - kk * 7) * 2 + wk, ks = 2 * kk + wd, q4 = w2m ? ks / 14 : ks >> 5, st = w2m ? ks - 14 * q4 : 14 + ((hpos(ks >> 3) * 8 + (ks & 7)) & 31);
    r[32 + 3 * k] = ok ? ks * 32 * DM * 2 + nb * 64 + (w2m ? 1 : 0) : 0x60000000u;
    for (int h = 0; h < 2; ++h) { const int nt = 2 * nb + h, c0 = nt / 7; r[33 + 3 * k + h] = ok ? WS_WO + ((q4 * 4 + c0) * 322 + wo_pos(q4, st, nt - 7 * c0)) * 1024 : 0x60000000u; }
  }
  return T;
}
template <int B> struct TabV { static constexpr Tab v = make_tab(B >> 2, B & 3); };
struct Tabs { Tab t[1024]; };
template <int... Bs> constexpr Tabs make_tabs(ilist<Bs...>) { return Tabs{{TabV<Bs>::v...}}; }
__device__ const Tabs g_tabs = make_tabs(iseq<1024>{});

__global__ __launch_bounds__(256, 1) void fused_block(const u16* __restrict__ x, const u16* __restrict__ ckv, const u16* __restrict__ wq, const u16* __restrict__ wuk, const u16* __restrict__ wuv,
    const u16* __restrict__ wo, const u16* __restrict__ w1, const u16* __restrict__ b1, const u16* __restrict__ w2, const u16* __restrict__ b2, u16* __restrict__ out, char* __restrict__ ws) {
  __shared__ __attribute__((aligned(1024))) char lds[163840];
  const int tid = threadIdx.x, lane = tid & 63, wid = __builtin_amdgcn_readfirstlane(tid >> 6);
    const int b = blockIdx.x, xcd = b & 7, jj = b >> 3;
  const unsigned lds0w = (unsigned)(size_t)(lds_char*)lds + wid * 4096;
  const int rb1 = jj & 7, cq = jj >> 3, head1 = xcd + 8 * (cq & 1), wk = wid & 1, wd = wid >> 1;
  const bool isv = cq >= 2;
  const u16* wkv = isv ? wuv : wuk;
  const srd_t srdC = mksrd(ckv, SEQ * DC * 2), srdW = mksrd(wkv, DC * NH * HD * 2), srd = mksrd(ws, NREC);
  const unsigned cvb = (32 * wid + (lane >> 3)) * 2048 + (((lane & 7) ^ (lane >> 4)) * 16);
  const unsigned wvb = (8 * wid + (lane >> 5)) * 8192 + (((lane & 31) ^ (((lane >> 5) << 1) | ((wid & 1) << 3))) * 16);
  unsigned cv[4], wv[4];
#pragma unroll
  for (int q = 0; q < 4; ++q) { cv[q] = (cvb ^ ((q & 1) << 6)) + q * 15360; wv[q] = (wvb ^ ((q & 1) << 6)) + q * 15360; }
  const unsigned sc0 = rb1 * 256 * 2048, sw0 = head1 * 512;
#define K1_DC(slot, half, t) dma4(lds0w + 65536 + (slot) * 32768 + (half) * 16384, cv, srdC, sc0 + (half) * 128 * 2048 + (t) * 128)
#define K1_DW(u) dma4(lds0w + (u) * 16384, wv, srdW, sw0 + (u) * 32 * 8192)
  K1_DW(0); K1_DC(0, 0, 0); K1_DC(0, 1, 0);
  const int lrow = b * 8 + (wid & 1) * 4, lj = lane >> 2;
  asm volatile("buffer_load_dwordx4 v[40:43], %0, %1, 0 offen\n\tbuffer_load_dwordx4 v[44:47], %0, %1, 0 offen offset:32\n\tbuffer_load_dwordx4 v[48:51], %0, %1, 0 offen offset:448\n\tbuffer_load_dwordx4 v[52:55], %0, %1, 0 offen offset:480"
               ::"v"(lane < 56 ? (lane & 3) * (DM * 2) + (lj >> 1) * 64 + (lj & 1) * 16 : 0xf000u), "s"(mksrd(x + lrow * DM, 4 * DM * 2))
               : "memory", "v40", "v41", "v42", "v43", "v44", "v45", "v46", "v47", "v48", "v49", "v50", "v51", "v52", "v53", "v54", "v55");
  K1_DW(1); K1_DC(1, 0, 1); K1_DC(1, 1, 1); K1_DW(2);
  const int c16 = lane & 15, g4 = lane >> 4;
  u32x4 blo, bhi;
#pragma unroll
  for (int d = 0; d < 4; ++d) { const int k0 = 8 * g4 + 2 * d;
    blo[d] = (k0 == c16 ? 0x3f80u : 0u) | (k0 + 1 == c16 ? 0x3f800000u : 0u); bhi[d] = (k0 == c16 + 16 ? 0x3f80u : 0u) | (k0 + 1 == c16 + 16 ? 0x3f800000u : 0u); }
  const unsigned cab = 65536 + (128 * wk + c16) * 128 + ((g4 ^ ((c16 >> 1) & 7)) * 16);
  const unsigned wab = (8 * g4 + (c16 >> 2)) * 512 + ((16 * wd + ((lane & 3) >> 1)) ^ (((c16 >> 2) << 1) | ((g4 & 1) << 3))) * 16 + 8 * (lane & 1);
  const unsigned vout = ((16 * (wid & 1) + c16) * DM + 16 * (wid >> 1) + 4 * g4) * 2, vout3 = wid < 2 ? vout : OOB;
  unsigned ptrs = 0, t0 = 0;
#define SETP(L, V) asm("v_writelane_b32 %0, %1, %2" : "+v"(ptrs) : "s"((unsigned)(V)), "i"(L))
#define SETQ(L, P) SETP(L, plo(P)); SETP(L + 1, phi(P))
  SETQ(0, ckv); SETQ(2, wkv); SETQ(4, b1); SETQ(6, x); SETQ(8, out); SETQ(10, b2); SETQ(12, ws); SETQ(14, wq); SETQ(16, w1); SETQ(18, wo); SETQ(20, w2);
  SETP(22, sc0); SETP(23, sw0); SETP(24, cq >> 1); SETP(25, lds0w); SETP(26, (b * 4 + wid) * 4096); SETP(27, (jj * 4 + wid) * 1024);
  const unsigned vxl = 0;
  const unsigned vxs = (lane < 56 && wid < 2) ? WS_XN + ((lrow >> 4) * 14 + (lj >> 1)) * 1024 + ((lrow >> 2) & 3) * 256 + (lj & 1) * 64 + (lane & 3) * 16 : 0x60000000u;
  const unsigned vxf = (c16 >> 2) * 256 + (g4 & 1) * 128 + (g4 >> 1) * 64 + (c16 & 3) * 16;
  static_assert(WS_FL == 102080512, "keep the generator's copy in step");
  asm volatile("s_nop 1\n\t" MK_ASM : "+v"(t0) : "v"(lane * 16), "v"(c16 - 4 * g4), "v"(cvb), "v"(wvb), "v"(cab), "v"(wab), "v"(blo), "v"(bhi), "v"(vout), "v"(vout3), "s"(srd),
               "v"(tid == 0 ? 0u : 0x60000000u), "v"(ptrs), "v"(&g_tabs.t[b * 4 + wid].v[0][lane]), "v"(vxl), "v"(vxs), "v"(vxf)
               : "memory", "vcc", "scc", "v27", "v28", "v29", "v30", "v31", ACLOB, VSCLOB);
}
extern "C" void kernel_launch(void* const* d_in, const int* in_sizes, int n_in, void* d_out, int out_size, void* d_ws, size_t ws_size, hipStream_t stream) {
  fused_block<<<256, 256, 0, stream>>>((const u16*)d_in[0], (const u16*)d_in[1], (const u16*)d_in[2], (const u16*)d_in[3], (const u16*)d_in[4], (const u16*)d_in[5],
                                       (const u16*)d_in[7], (const u16*)d_in[8], (const u16*)d_in[9], (const u16*)d_in[10], (u16*)d_out, (char*)d_ws);
}
```
